```python
import torch

def _softmax(x, axis=-1):
    return torch.softmax(x, dim=axis)
def _gelu(x):
    return torch.nn.functional.gelu(x, approximate="tanh")
def _silu(x):
    return torch.nn.functional.silu(x)
def _sigmoid(x):
    return torch.sigmoid(x)
def _layernorm(x, axis=-1, eps=1e-5):
    m = x.mean(dim=axis, keepdim=True)
    v = x.var(dim=axis, keepdim=True, unbiased=False)
    return (x - m) / torch.sqrt(v + eps)
def _scatter_add(tgt, idx, src, axis=0):
    return tgt.clone().index_add_(axis, idx, src)
def _segment_sum(src, seg, n):
    out = torch.zeros((n,) + src.shape[1:], dtype=src.dtype)
    return out.index_add_(0, seg, src)
def _pad(x, pw):
    flat = tuple(v for pair in reversed(pw) for v in pair)
    return torch.nn.functional.pad(x, flat)

kernel_name = 'gated_mix__linear_attn_block__ssm_conv_h3__prompt'

def setup_inputs():

    _w = (lambda a: (lambda t: t.to(torch.bfloat16) if t.is_floating_point() else t)(torch.as_tensor(a)))
    def _A_setup():
        B, S, D = 1, 2048, 640
        return {
            "x": _w((lambda *s: torch.randn(*s, dtype=torch.bfloat16) * 0.05)(B, S, D)),
            "wqkv": _w((lambda *s: torch.randn(*s, dtype=torch.bfloat16) * 1.6)(D, 3 * D)),
            "wo": _w((lambda *s: torch.randn(*s, dtype=torch.bfloat16) * 0.1)(D, D)),
        }
    _A_in = _A_setup()

    def _B_setup():
        B, S, D = 1, 2048, 640
        ks = 4
        return {
            "q": _w((lambda *s: torch.randn(*s, dtype=torch.bfloat16) * 0.05)(B, S, D)),
            "k": _w((lambda *s: torch.randn(*s, dtype=torch.bfloat16) * 0.8)(B, S, D)),
            "v": _w((lambda *s: torch.randn(*s, dtype=torch.bfloat16) * 0.8)(B, S, D)),
            "shift_k": _w((lambda *s: torch.randn(*s, dtype=torch.bfloat16) * 0.8)(D, ks) * 0.5),
            "ssm_k": _w((lambda *s: torch.randn(*s, dtype=torch.bfloat16) * 0.8)(D, S) * (1.0 / S ** 0.5)),
            "ssm_D": _w((lambda *s: torch.randn(*s, dtype=torch.bfloat16) * 0.8)(D)),
        }
    _B_in = _B_setup()

    return {
        "A_x": _A_in["x"],
        "A_wqkv": _A_in["wqkv"],
        "A_wo": _A_in["wo"],
        "B_q": _B_in["q"],
        "B_k": _B_in["k"],
        "B_v": _B_in["v"],
        "B_shift_k": _B_in["shift_k"],
        "B_ssm_k": _B_in["ssm_k"],
        "B_ssm_D": _B_in["ssm_D"],
        "gate_w": _w((lambda *s: torch.randn(*s, dtype=torch.bfloat16) * 0.05)(640) * 16.0),
    }

def reference(A_x, A_wqkv, A_wo, B_q, B_k, B_v, B_shift_k, B_ssm_k, B_ssm_D, gate_w):
    def _A_ref(x, wqkv, wo):
        B, S, D = x.shape
        H = 32; Dh = D // H
        qkv = (lambda x, *d: x.permute(*d))((x @ wqkv).reshape(B, S, 3, H, Dh), 2, 0, 3, 1, 4)
        q, k, v = qkv[0], qkv[1], qkv[2]
        phi = lambda t: torch.where(t > 0, t + 1, torch.exp(t)) + 1e-6
        pq, pk = phi(q), phi(k)
        kv = torch.einsum("bhsd,bhse->bhde", pk, v)
        z = torch.einsum("bhsd->bhd", pk)
        num = torch.einsum("bhqd,bhde->bhqe", pq, kv)
        den = torch.einsum("bhqd,bhd->bhq", pq, z)[..., None] + 1e-6
        o = num / den
        return (lambda x, *d: x.permute(*d))(o, 0, 2, 1, 3).reshape(B, S, D) @ wo

    def _B_ref(q, k, v, shift_k, ssm_k, ssm_D):
        B, S, D = q.shape
        ks = shift_k.shape[-1]
        k_pad = _pad(k, ((0, 0), (ks - 1, 0), (0, 0)))
        k_shift = torch.zeros_like(k)
        for t in range(ks):
            k_shift = k_shift + k_pad[:, t:t + S, :] * shift_k[:, ks - 1 - t][None, None, :]
        kv = k_shift * v
        L = 2 * S
        kv_pad = _pad(kv, ((0, 0), (0, S), (0, 0)))
        kf_pad = _pad(ssm_k, ((0, 0), (0, S)))
        kv_f = (lambda x, axes=(-2,-1): torch.fft.rfft2(x.float(), dim=axes))(kv_pad[:, None, :, :], axes=(1, 2))
        kf_f = (lambda x, axes=(-2,-1): torch.fft.rfft2(x.float(), dim=axes))(kf_pad[:, None, :], axes=(1, 2))
        kf_f = (lambda x, *d: x.permute(*d))(kf_f, 1, 2, 0)
        y_f = kv_f * kf_f[None, :, :, :]
        y = (lambda x, s, axes=(-2,-1): torch.fft.irfft2(x, s=s, dim=axes).to(torch.bfloat16))(y_f, (1, L), axes=(1, 2))[:, 0, :S, :]
        y = y + kv * ssm_D[None, None, :]
        return q * y

    x = A_x
    g = _sigmoid((x * gate_w).sum(-1, keepdims=True))
    a = _A_ref(x, A_wqkv, A_wo)
    b = _B_ref(x, B_k, B_v, B_shift_k, B_ssm_k, B_ssm_D)
    return g * a + (1 - g) * b
```

```cpp
#include <hip/hip_runtime.h>
#include <hip/hip_bf16.h>
typedef unsigned short u16;
__device__ __forceinline__ float bf2f(u16 v){ return __uint_as_float(((unsigned)v)<<16); }
__device__ __forceinline__ u16 f2bf(float f){ unsigned u=__float_as_uint(f); u += 0x7fffu + ((u>>16)&1u); return (u16)(u>>16); }
__device__ __forceinline__ float rb(float f){ return bf2f(f2bf(f)); }
#define S 2048
#define D 640
#define H 32
#define DH 20
__global__ void k_qkv(const u16* x, const u16* w, u16* qkv){
  int n = blockIdx.x*blockDim.x+threadIdx.x; int s = blockIdx.y;
  if(n>=3*D) return;
  float acc=0; for(int k=0;k<D;k++) acc += bf2f(x[s*D+k])*bf2f(w[k*3*D+n]);
  qkv[s*3*D+n]=f2bf(acc);
}
__device__ __forceinline__ float phi(float t){ float r = t>0? rb(t+1.f) : rb(__expf(t)); return rb(r+1e-6f); }
__global__ void k_kv(const u16* qkv, float* kv, float* z){
  int h=blockIdx.x; int t=threadIdx.x;
  if(t<400){ int d=t/20,e=t%20; float acc=0; for(int s=0;s<S;s++) acc += phi(bf2f(qkv[s*3*D+D+h*DH+d]))*bf2f(qkv[s*3*D+2*D+h*DH+e]); kv[h*400+t]=rb(acc);}
  else if(t<420){ int d=t-400; float acc=0; for(int s=0;s<S;s++) acc += phi(bf2f(qkv[s*3*D+D+h*DH+d])); z[h*20+d]=rb(acc);}
}
__global__ void k_o(const u16* qkv, const float* kv, const float* z, u16* o){
  int s=blockIdx.x; int c=threadIdx.x; int h=c/DH,e=c%DH;
  float num=0,den=0; for(int d=0;d<DH;d++){ float pq=phi(bf2f(qkv[s*3*D+h*DH+d])); num+=pq*kv[h*400+d*20+e]; den+=pq*z[h*20+d]; }
  num=rb(num); den=rb(rb(den)+1e-6f); o[s*D+c]=f2bf(num/den);
}
__global__ void k_u(const u16* k,const u16* v,const u16* sk,u16* u){
  int s=blockIdx.x; int c=threadIdx.x; float acc=0;
  for(int t=0;t<4;t++){ int ss=s+t-3; float kv = ss>=0? bf2f(k[ss*D+c]):0.f; acc = rb(acc + rb(kv*bf2f(sk[c*4+3-t]))); }
  u[s*D+c]=f2bf(acc*bf2f(v[s*D+c]));
}
__global__ void k_final(const u16* x,const u16* o,const u16* wo,const u16* u,const u16* sk,const u16* sD,const u16* gw,u16* out){
  int s=blockIdx.x; int c=threadIdx.x;
  __shared__ float red[D];
  red[c]=rb(bf2f(x[s*D+c])*bf2f(gw[c])); __syncthreads();
  float gs=0; for(int i=0;i<D;i++) gs+=red[i]; gs=rb(gs); float g=rb(1.f/(1.f+__expf(-gs)));
  float a=0; for(int k=0;k<D;k++) a+=bf2f(o[s*D+k])*bf2f(wo[k*D+c]); a=rb(a);
  float y=0; for(int t=0;t<=s;t++) y+=bf2f(sk[c*S+(s-t)])*bf2f(u[t*D+c]); y=rb(y);
  y=rb(y+rb(bf2f(u[s*D+c])*bf2f(sD[c]))); float b=rb(bf2f(x[s*D+c])*y);
  out[s*D+c]=f2bf(rb(g*a)+rb(rb(1.f-g)*b));
}
extern "C" void kernel_launch(void* const* d_in, const int* in_sizes, int n_in, void* d_out, int out_size, void* d_ws, size_t ws_size, hipStream_t stream) {
  const u16* x=(const u16*)d_in[0]; const u16* wqkv=(const u16*)d_in[1]; const u16* wo=(const u16*)d_in[2];
  const u16* k=(const u16*)d_in[4]; const u16* v=(const u16*)d_in[5]; const u16* shk=(const u16*)d_in[6];
  const u16* sk=(const u16*)d_in[7]; const u16* sD=(const u16*)d_in[8]; const u16* gw=(const u16*)d_in[9];
  char* ws=(char*)d_ws;
  u16* qkv=(u16*)ws; u16* o=(u16*)(ws+(8<<20)); u16* u=(u16*)(ws+(12<<20)); float* kv=(float*)(ws+(16<<20)); float* z=(float*)(ws+(17<<20));
  k_qkv<<<dim3((3*D+255)/256,S),256,0,stream>>>(x,wqkv,qkv);
  k_kv<<<H,448,0,stream>>>(qkv,kv,z);
  k_o<<<S,D,0,stream>>>(qkv,kv,z,o);
  k_u<<<S,D,0,stream>>>(k,v,shk,u);
  k_final<<<S,D,0,stream>>>(x,o,wo,u,sk,sD,gw,(u16*)d_out);
}
```

```cpp
#include <hip/hip_runtime.h>
#include <stdint.h>

typedef unsigned short u16;
typedef unsigned int u32;
typedef __bf16 bf16x8 __attribute__((ext_vector_type(8)));
typedef __bf16 bf16x2 __attribute__((ext_vector_type(2)));
typedef float f32x4 __attribute__((ext_vector_type(4)));
typedef float f32x2 __attribute__((ext_vector_type(2)));
typedef u32 u32x4 __attribute__((ext_vector_type(4)));
typedef u32 u32x2 __attribute__((ext_vector_type(2)));
typedef short s16x4 __attribute__((ext_vector_type(4)));
#define LAS __attribute__((address_space(3)))
#define GAS __attribute__((address_space(1)))
#define DI __device__ __forceinline__

constexpr int S = 2048, D = 640;

constexpr size_t WS_UT = 0;
constexpr size_t WS_YT = WS_UT + 2621440;
constexpr size_t WS_PK0 = WS_YT + 2621440;
constexpr size_t WS_V0 = WS_PK0 + 2097152;
constexpr size_t WS_X = WS_V0 + 2097152;
constexpr size_t WS_P2 = WS_X + 1048576;
constexpr size_t WS_O = WS_P2 + 2621440;
constexpr size_t WS_WT = WS_O + 2621440;
constexpr size_t WS_G = WS_WT + 819200;
constexpr size_t WS_STAMP = WS_G + 8192;

#define STAMP(kern, idx, which) do {} while (0)

DI u32 pk2(float a, float b) { f32x2 t = {a, b}; return __builtin_bit_cast(u32, __builtin_convertvector(t, bf16x2)); }
DI float blo(u32 u) { return __uint_as_float(u << 16); }
DI float bhi(u32 u) { return __uint_as_float(u & 0xffff0000u); }
DI float rbf(float f) { return blo(pk2(f, f)); }
DI bf16x8 asbf(u32x4 v) { return __builtin_bit_cast(bf16x8, v); }
DI f32x4 mfma16(u32x4 a, u32x4 b, f32x4 c) { return __builtin_amdgcn_mfma_f32_16x16x32_bf16(asbf(a), asbf(b), c, 0, 0, 0); }

DI void glds16(const void* sbase, u32 voff, u32 lds) {
    u32 keep;
    asm volatile("s_mov_b32 %0, m0\n\ts_mov_b32 m0, %3\n\ts_nop 0\n\tglobal_load_lds_dwordx4 %1, %2\n\ts_mov_b32 m0, %0"
                 : "=&s"(keep) : "v"(voff), "s"(sbase), "s"(lds) : "memory");
}
DI u32x4 lds128(LAS char* sm, u32 a) { return *(LAS u32x4*)(sm + a); }
DI u32x2 lds64(LAS char* sm, u32 a) { return *(LAS u32x2*)(sm + a); }
DI u32x2 ldstr(LAS char* sm, u32 a) { return __builtin_bit_cast(u32x2, __builtin_amdgcn_ds_read_tr16_b64_v4i16((LAS s16x4*)(sm + a))); }
#define VMCNT(n) asm volatile("s_waitcnt vmcnt(" #n ")" ::: "memory")
#define LGKM0() asm volatile("s_waitcnt lgkmcnt(0)" ::: "memory")
#define BAR() do { asm volatile("" ::: "memory"); __builtin_amdgcn_s_barrier(); asm volatile("" ::: "memory"); } while (0)

DI float phi(float t) {
    float e = __builtin_amdgcn_exp2f(fminf(t, 0.f) * 1.4426950408889634f);
    return e + fmaxf(t, 0.f) + 1e-6f;
}

constexpr int K1_STAGE = 32768;
constexpr int K1_LDS = 4 * K1_STAGE;
constexpr int P0_K = 3 * K1_STAGE, P0_V = P0_K + 9216, P0_OUT = P0_V + 8192;

DI void p0_issue(const u16* kin, const u16* vin, int tile, int wave, int lane) {
    const int cg = tile % 10, rg = tile / 10, s0 = rg * 64;
    {
        int r = s0 - 4 + wave * 8 + (lane >> 3); r = r < 0 ? 0 : r;
        glds16(kin, (u32)(r * 1280 + cg * 128 + (lane & 7) * 16), P0_K + wave * 1024);
        int r2 = s0 + wave * 8 + (lane >> 3);
        glds16(vin, (u32)(r2 * 1280 + cg * 128 + (lane & 7) * 16), P0_V + wave * 1024);
    }
    if (wave == 0) {
        int r = s0 + 60 + (lane >> 3); r = r > S - 1 ? S - 1 : r;
        glds16(kin, (u32)(r * 1280 + cg * 128 + (lane & 7) * 16), P0_K + 8 * 1024);
    }
}
DI void p0_finish(LAS char* sm, u32x2 w4, u16* UT, int tile, int wave, int lane, int tid) {
    const int cg = tile % 10, rg = tile / 10, s0 = rg * 64;
    const int G = wave * 4 + (lane >> 4), cb = G & 3, o = G >> 2, i = lane & 15, q = i >> 2, p = i & 3;
    const u32 ka = P0_K + (8 * o + q) * 128 + 32 * cb + 8 * p;
    const u32 va = P0_V + (8 * o + q) * 128 + 32 * cb + 8 * p;
    u32x2 k0 = ldstr(sm, ka), k1 = ldstr(sm, ka + 512), k2 = ldstr(sm, ka + 1024);
    u32x2 v0 = ldstr(sm, va), v1 = ldstr(sm, va + 512);
    if (s0 == 0 && o == 0) { k0.x = 0; k0.y = 0; }
    float kk[12] = {blo(k0.x), bhi(k0.x), blo(k0.y), bhi(k0.y), blo(k1.x), bhi(k1.x), blo(k1.y), bhi(k1.y), blo(k2.x), bhi(k2.x), blo(k2.y), bhi(k2.y)};
    float vv[8] = {blo(v0.x), bhi(v0.x), blo(v0.y), bhi(v0.y), blo(v1.x), bhi(v1.x), blo(v1.y), bhi(v1.y)};
    const float w0 = blo(w4.x), w1 = bhi(w4.x), w2 = blo(w4.y), w3 = bhi(w4.y);
    float u[8];
#pragma unroll
    for (int t = 0; t < 8; t++) {
        float ks = kk[t + 1] * w3;
        ks = fmaf(kk[t + 2], w2, ks); ks = fmaf(kk[t + 3], w1, ks); ks = fmaf(kk[t + 4], w0, ks);
        u[t] = ks * vv[t];
    }
    const int ch = 16 * cb + i;
    u32x4 r = {pk2(u[0], u[1]), pk2(u[2], u[3]), pk2(u[4], u[5]), pk2(u[6], u[7])};
    *(LAS u32x4*)(sm + P0_OUT + ch * 128 + ((o ^ (ch & 7)) * 16)) = r;
    LGKM0(); BAR();
    const int ch2 = tid >> 3, o2 = tid & 7;
    u32x4 t4 = lds128(sm, P0_OUT + ch2 * 128 + ((o2 ^ (ch2 & 7)) * 16));
    *(GAS u32x4*)((GAS char*)UT + (size_t)(cg * 64 + ch2) * 4096 + (s0 + 8 * o2) * 2) = t4;
}
DI u32x2 p0_weights(const u16* shk, int tile, int wave, int lane) {
    const int cg = tile % 10, cb = (wave * 4 + (lane >> 4)) & 3;
    return *(const GAS u32x2*)((const GAS char*)shk + (cg * 64 + 16 * cb + (lane & 15)) * 8);
}

template <bool QT>
DI void k1_loop(LAS char* sm, const u16* x, const u16* w, const u32 (&vx)[2], const u32 (&vw)[2], int wave, u32 xa0, const u32 (&wb)[2][2], f32x4 (&acc)[4][2]) {
#pragma unroll 1
    for (int c = 0; c < 10; c++) {
        if (c < 8) VMCNT(8); else if (c == 8) VMCNT(4); else VMCNT(0);
        BAR();
        if (c < 7) {
            const u32 st = ((c + 3) & 3) * K1_STAGE;
            const char* xb = (const char*)x + (c + 3) * 128; const char* wp = (const char*)w + (size_t)(c + 3) * 64 * 3840;
            glds16(xb, vx[0], st + (2 * wave) * 1024); glds16(xb, vx[1], st + (2 * wave + 1) * 1024);
            glds16(wp, vw[0], st + 16384 + (2 * wave) * 1024); glds16(wp, vw[1], st + 16384 + (2 * wave + 1) * 1024);
        }
        const u32 sb = (c & 3) * K1_STAGE;
#pragma unroll
        for (int ks = 0; ks < 2; ks++) {
            u32x4 xa[4], wf[2];
            const u32 xaddr = sb + (xa0 ^ (ks * 64));
#pragma unroll
            for (int i = 0; i < 4; i++) xa[i] = lds128(sm, xaddr + i * 2048);
#pragma unroll
            for (int jj = 0; jj < 2; jj++) {
                u32x2 lo = ldstr(sm, sb + wb[jj][0] + ks * 8192), hi = ldstr(sm, sb + wb[jj][1] + ks * 8192);
                wf[jj] = (u32x4){lo.x, lo.y, hi.x, hi.y};
            }
#pragma unroll
            for (int i = 0; i < 4; i++)
#pragma unroll
                for (int jj = 0; jj < 2; jj++) acc[i][jj] = QT ? mfma16(wf[jj], xa[i], acc[i][jj]) : mfma16(xa[i], wf[jj], acc[i][jj]);
        }
        LGKM0();
    }
}

__global__ __launch_bounds__(512, 2) void k1_qkv(const u16* __restrict__ x, const u16* __restrict__ w, const u16* __restrict__ kin, const u16* __restrict__ vin,
                                                  const u16* __restrict__ shk, const u16* __restrict__ gw, char* __restrict__ ws) {
    extern __shared__ __attribute__((aligned(16))) char smem[];
    LAS char* sm = (LAS char*)smem;
    const int tid = threadIdx.x, lane = tid & 63, wave = __builtin_amdgcn_readfirstlane(tid >> 6);
    const int b = blockIdx.x, xcd = b & 7, j = b >> 3;
    u16* UT = (u16*)(ws + WS_UT);
    STAMP(0, b, 0);
    if (j >= 30) {
        const int m = xcd * 2 + (j - 30);
        {
            const int sub = lane & 15, r4 = lane >> 4;
            u32x4 gq[5];
#pragma unroll
            for (int jj = 0; jj < 5; jj++) gq[jj] = *(const GAS u32x4*)((const GAS char*)gw + (sub + 16 * jj) * 16);
            float* G = (float*)(ws + WS_G);
            u32x4 xr[4][5];
#pragma unroll
            for (int it = 0; it < 4; it++)
#pragma unroll
                for (int jj = 0; jj < 5; jj++) xr[it][jj] = *(const GAS u32x4*)((const GAS char*)x + (size_t)(128 * m + 16 * wave + 4 * it + r4) * 1280 + (sub + 16 * jj) * 16);
#pragma unroll
            for (int it = 0; it < 4; it++) {
                float a = 0.f;
#pragma unroll
                for (int jj = 0; jj < 5; jj++)
#pragma unroll
                    for (int e = 0; e < 4; e++) { a = fmaf(blo(xr[it][jj][e]), blo(gq[jj][e]), a); a = fmaf(bhi(xr[it][jj][e]), bhi(gq[jj][e]), a); }
                a += __shfl_xor(a, 1); a += __shfl_xor(a, 2); a += __shfl_xor(a, 4); a += __shfl_xor(a, 8);
                a = rbf(a);
                const float g = rbf(__builtin_amdgcn_rcpf(1.f + __builtin_amdgcn_exp2f(-1.4426950408889634f * a)));
                if (sub == 0) G[128 * m + 16 * wave + 4 * it + r4] = g;
            }
        }
#pragma unroll 1
        for (int t = 0; t < 5; t++) {
            const int tile = 240 + 5 * m + t;
            p0_issue(kin, vin, tile, wave, lane);
            const u32x2 w4 = p0_weights(shk, tile, wave, lane);
            VMCNT(0); BAR();
            p0_finish(sm, w4, UT, tile, wave, lane, tid);
            LGKM0(); BAR();
        }
        STAMP(0, b, 1);
        return;
    }
    const int rt = 2 * xcd + j / 15, cbk = j % 15, tile = xcd * 30 + j;
    const u32x2 w4 = p0_weights(shk, tile, wave, lane);
    p0_issue(kin, vin, tile, wave, lane);
    u32 vx[2], vw[2];
#pragma unroll
    for (int n2 = 0; n2 < 2; n2++) {
        const int n = 2 * wave + n2;
        const int row = 8 * n + (lane >> 3), ch = (lane & 7) ^ ((row >> 1) & 7);
        vx[n2] = (u32)((rt * 128 + row) * 1280 + ch * 16);
        const int kr = 4 * n + (lane >> 4), f = ((kr & 3) << 2) | ((kr >> 2) & 3), cw = (lane & 15) ^ f;
        vw[n2] = (u32)(kr * 3840 + cbk * 256 + cw * 16);
    }
#pragma unroll
    for (int c = 0; c < 3; c++) {
        const u32 st = c * K1_STAGE;
        const char* xb = (const char*)x + c * 128; const char* wp = (const char*)w + (size_t)c * 64 * 3840;
        glds16(xb, vx[0], st + (2 * wave) * 1024); glds16(xb, vx[1], st + (2 * wave + 1) * 1024);
        glds16(wp, vw[0], st + 16384 + (2 * wave) * 1024); glds16(wp, vw[1], st + 16384 + (2 * wave + 1) * 1024);
    }
    VMCNT(12); BAR();
    p0_finish(sm, w4, UT, tile, wave, lane, tid);
    LGKM0();
    STAMP(0, b, 2);
    const int wm = wave >> 2, wn = wave & 3, fr = lane & 15, g = lane >> 4;
    const u32 xa0 = (64 * wm + fr) * 128 + ((g ^ (fr >> 1)) * 16);
    u32 wb[2][2];
#pragma unroll
    for (int jj = 0; jj < 2; jj++)
#pragma unroll
        for (int t = 0; t < 2; t++) {
            const int q = fr >> 2, p = lane & 3, row = 8 * g + 4 * t + q, ch = 4 * wn + 2 * jj + (p >> 1), f = (q << 2) | ((2 * g + t) & 3);
            wb[jj][t] = 16384 + row * 256 + ((ch ^ f) * 16) + 8 * (p & 1);
        }
    f32x4 acc[4][2];
#pragma unroll
    for (int i = 0; i < 4; i++) { acc[i][0] = (f32x4){0, 0, 0, 0}; acc[i][1] = (f32x4){0, 0, 0, 0}; }
    if (cbk < 5) {
        k1_loop<true>(sm, x, w, vx, vw, wave, xa0, wb, acc);
        STAMP(0, b, 3);
        const int unit = ((g & 1) << 1) | (g >> 1), U = 16 * cbk + 4 * wn + unit, pr = U / 5, u5 = U - 5 * pr;
        GAS char* P2 = (GAS char*)(ws + WS_P2);
#pragma unroll
        for (int i = 0; i < 4; i++) {
            const u32 x0 = pk2(phi(acc[i][0][0]), phi(acc[i][0][1])), y0 = pk2(phi(acc[i][0][2]), phi(acc[i][0][3]));
            const u32 x1 = pk2(phi(acc[i][1][0]), phi(acc[i][1][1])), y1 = pk2(phi(acc[i][1][2]), phi(acc[i][1][3]));
            auto rx = __builtin_amdgcn_permlane16_swap(x0, x1, false, false);
            auto ry = __builtin_amdgcn_permlane16_swap(y0, y1, false, false);
            const int sb16 = rt * 8 + 4 * wm + i;
            *(GAS u32x4*)(P2 + (size_t)(((pr * 128 + sb16) * 5 + u5) * 128 + fr * 8) * 2) = (u32x4){rx[0], ry[0], rx[1], ry[1]};
        }
    } else {
        k1_loop<false>(sm, x, w, vx, vw, wave, xa0, wb, acc);
        STAMP(0, b, 3);
        const bool isk = cbk < 10;
        const int j5 = isk ? cbk - 5 : cbk - 10;
#pragma unroll
        for (int jj = 0; jj < 2; jj++) {
            const int cc = 128 * j5 + 32 * wn + 16 * jj + fr, h = cc / 20, d = cc - 20 * h;
            const int sb32 = rt * 4 + 2 * wm;
            GAS char* dst; u32 step;
            if (d < 16) { dst = (GAS char*)(ws + (isk ? WS_PK0 : WS_V0)) + (size_t)(((h * 64 + sb32) * 4 + g) * 128 + d * 8) * 2; step = 1024; }
            else { dst = (GAS char*)(ws + WS_X) + (size_t)(((h * 64 + sb32) * 4 + g) * 64 + (d - 16 + (isk ? 0 : 4)) * 8) * 2; step = 512; }
#pragma unroll
            for (int z = 0; z < 2; z++) {
                f32x4 a = acc[2 * z][jj], c2 = acc[2 * z + 1][jj];
                if (isk) {
#pragma unroll
                    for (int r = 0; r < 4; r++) { a[r] = phi(a[r]); c2[r] = phi(c2[r]); }
                }
                *(GAS u32x4*)(dst + z * step) = (u32x4){pk2(a[0], a[1]), pk2(a[2], a[3]), pk2(c2[0], c2[1]), pk2(c2[2], c2[3])};
            }
        }
    }
    STAMP(0, b, 1);
}

constexpr int CV_PITCH = 4160;
constexpr int CV_U = 4 * CV_PITCH;
constexpr int CV_WAVE = CV_U + 4608;
constexpr int K2_LDS = 4 * CV_WAVE;

DI void conv_wave(LAS char* sm0, int wave, int lane, int c, const u16* __restrict__ hk, const u16* __restrict__ sD, char* __restrict__ ws) {
    LAS char* sm = sm0 + wave * CV_WAVE;
    const u32 ldsb = wave * CV_WAVE;
    {
        const char* ub = ws + WS_UT + (size_t)c * 4096;
#pragma unroll
        for (int z = 0; z < 4; z++) glds16(ub, z * 1024 + lane * 16, ldsb + CV_U + 512 + z * 1024);
    }
    const GAS char* hb = (const GAS char*)hk + (size_t)c * 4096;
    u32x4 hd[4]; u32x2 hm[4];
#pragma unroll
    for (int z = 0; z < 4; z++) {
        const int i = lane + 64 * z;
        hd[z] = *(const GAS u32x4*)(hb + i * 16);
        hm[z] = *(const GAS u32x2*)(hb + (i > 0 ? i * 16 - 8 : 0));
    }
    const float Dc = blo((u32)sD[c]);
    if (lane < 32) *(LAS u32x4*)(sm + CV_U + lane * 16) = (u32x4){0, 0, 0, 0};
    if (lane < 8) *(LAS u32x4*)(sm + (lane >> 1) * CV_PITCH + (lane & 1) * 16) = (u32x4){0, 0, 0, 0};
#pragma unroll
    for (int z = 0; z < 4; z++) {
        const int i = lane + 64 * z;
        u32 m2 = hm[z].x, m1 = hm[z].y;
        if (i == 0) { m2 = 0; m1 = 0; }
        const u32 d0 = hd[z].x, d1 = hd[z].y, d2 = hd[z].z, d3 = hd[z].w;
        const u32 om2 = __builtin_amdgcn_alignbit(m1, m2, 16), om1 = __builtin_amdgcn_alignbit(d0, m1, 16), o0 = __builtin_amdgcn_alignbit(d1, d0, 16),
                  o1 = __builtin_amdgcn_alignbit(d2, d1, 16), o2 = __builtin_amdgcn_alignbit(d3, d2, 16);
        LAS char* p = sm + 32 + i * 16;
        *(LAS u32x4*)(p) = (u32x4){d0, d1, d2, d3};
        *(LAS u32x4*)(p + CV_PITCH) = (u32x4){om1, o0, o1, o2};
        *(LAS u32x4*)(p + 2 * CV_PITCH) = (u32x4){m1, d0, d1, d2};
        *(LAS u32x4*)(p + 3 * CV_PITCH) = (u32x4){om2, om1, o0, o1};
    }
    VMCNT(0); LGKM0();
    const int fr = lane & 15, g = lane >> 4, q = fr >> 2, p = lane & 3;
    const u32 fa = q * CV_PITCH + 2 * (32 - 8 * g + 4 * p);
    const u32 ga = CV_U + 480 + 32 * fr + 16 * g;
    f32x4 Y[8];
#pragma unroll
    for (int i = 0; i < 8; i++) Y[i] = (f32x4){0, 0, 0, 0};
#pragma unroll 1
    for (int k0 = 0; k0 < 8; k0++) {
        u32x4 F[8], G[8];
#pragma unroll
        for (int k1 = 0; k1 < 8; k1++) {
            const u32 a = fa + 64 * k0 + 512 * k1;
            const u32x2 lo = ldstr(sm, a), hi = ldstr(sm, a - 8);
            F[k1] = (u32x4){lo.x, lo.y, hi.x, hi.y};
            G[k1] = lds128(sm, ga - 64 * k0 + 512 * k1);
        }
#pragma unroll
        for (int e = 0; e < 8; e++)
#pragma unroll
            for (int k1 = 0; k1 + e < 8; k1++) Y[k1 + e] = mfma16(F[k1], G[e], Y[k1 + e]);
    }
    LGKM0();
#pragma unroll
    for (int ib = 0; ib < 8; ib++)
        *(LAS u32x2*)(sm + 512 * ib + 32 * fr + 8 * g) = (u32x2){pk2(Y[ib][0], Y[ib][1]), pk2(Y[ib][2], Y[ib][3])};
    LGKM0();
    GAS char* yo = (GAS char*)(ws + WS_YT) + (size_t)c * 4096;
#pragma unroll
    for (int z = 0; z < 4; z++) {
        const u32x4 y8 = lds128(sm, z * 1024 + lane * 16), u8 = lds128(sm, CV_U + 512 + z * 1024 + lane * 16);
        u32x4 r;
#pragma unroll
        for (int e = 0; e < 4; e++)
            r[e] = pk2(blo(y8[e]) + rbf(blo(u8[e]) * Dc), bhi(y8[e]) + rbf(bhi(u8[e]) * Dc));
        *(GAS u32x4*)(yo + z * 1024 + lane * 16) = r;
    }
}

DI void kvo_block(LAS char* sm, int wave, int lane, int tid, int h, int half, char* __restrict__ ws) {
    const int fr = lane & 15, g = lane >> 4;
    const GAS char* pk0 = (const GAS char*)(ws + WS_PK0) + (size_t)(h * 64 + 16 * wave) * 1024 + lane * 16;
    const GAS char* v0 = (const GAS char*)(ws + WS_V0) + (size_t)(h * 64 + 16 * wave) * 1024 + lane * 16;
    const GAS char* xx = (const GAS char*)(ws + WS_X) + (size_t)(h * 64 + 16 * wave) * 512 + (g * 8 + (fr & 7)) * 16;
    u32x4 A[16], B[16], X[16];
#pragma unroll
    for (int i = 0; i < 16; i++) { A[i] = *(const GAS u32x4*)(pk0 + i * 1024); B[i] = *(const GAS u32x4*)(v0 + i * 1024); X[i] = *(const GAS u32x4*)(xx + i * 512); }
    const int odd = h & 1, pr = h >> 1;
    const int u5 = (g < 3 ? g : 2) + 2 * odd;
    const int sb0 = half * 64 + wave * 16;
    const GAS char* pq = (const GAS char*)(ws + WS_P2) + (size_t)(((pr * 128 + sb0) * 5 + u5) * 128 + fr * 8) * 2;
    u32x4 Q[16];
#pragma unroll
    for (int i = 0; i < 16; i++) Q[i] = *(const GAS u32x4*)(pq + (size_t)i * 1280);
    f32x4 a00 = {0, 0, 0, 0}, a0x = a00, ax0 = a00, axx = a00;
    const u32 cst = fr == 8 ? 0x3F803F80u : 0u;
#pragma unroll
    for (int i = 0; i < 16; i++) {
        u32x4 xv = X[i];
        if (fr >= 8) xv = (u32x4){cst, cst, cst, cst};
        a00 = mfma16(A[i], B[i], a00); a0x = mfma16(A[i], xv, a0x); ax0 = mfma16(xv, B[i], ax0); axx = mfma16(xv, xv, axx);
    }
    *(LAS f32x4*)(sm + ((wave * 4 + 0) * 64 + lane) * 16) = a00;
    *(LAS f32x4*)(sm + ((wave * 4 + 1) * 64 + lane) * 16) = a0x;
    *(LAS f32x4*)(sm + ((wave * 4 + 2) * 64 + lane) * 16) = ax0;
    *(LAS f32x4*)(sm + ((wave * 4 + 3) * 64 + lane) * 16) = axx;
    __syncthreads();
    {
        const int tile = wave;
        f32x4 s = *(LAS f32x4*)(sm + ((0 * 4 + tile) * 64 + lane) * 16);
#pragma unroll
        for (int w2 = 1; w2 < 4; w2++) s += *(LAS f32x4*)(sm + ((w2 * 4 + tile) * 64 + lane) * 16);
        const int e = (tile & 1) ? (fr == 8 ? 20 : 12 + fr) : fr;
        const bool colok = (tile & 1) ? (fr >= 4 && fr <= 8) : true;
        const bool rowok = (tile & 2) ? (g == 0) : true;
        if (colok && rowok) {
#pragma unroll
            for (int r = 0; r < 4; r++) {
                const int d = (tile & 2) ? 16 + r : 4 * g + r;
                *(LAS u16*)(sm + 16384 + (d * 24 + e) * 2) = (u16)(pk2(s[r], s[r]) & 0xffffu);
            }
        }
    }
    __syncthreads();
    u32x4 A0, A1[4], A2;
    {
        u32 t0[8], t1[8], t2[8];
#pragma unroll
        for (int jx = 0; jx < 8; jx++) {
            const int d = 8 * g + jx - 4 * odd;
            const bool ok = g < 3 && d >= 0 && d < 20;
            const int dd = ok ? d : 0;
            const u32 v0_ = *(LAS u16*)(sm + 16384 + (dd * 24 + fr) * 2), v1_ = *(LAS u16*)(sm + 16384 + (dd * 24 + 16 + (fr & 3)) * 2), v2_ = *(LAS u16*)(sm + 16384 + (dd * 24 + 20) * 2);
            t0[jx] = ok ? v0_ : 0u; t1[jx] = ok ? v1_ : 0u; t2[jx] = ok ? v2_ : 0u;
        }
#pragma unroll
        for (int e = 0; e < 4; e++) {
            A0[e] = t0[2 * e] | (t0[2 * e + 1] << 16); A2[e] = t2[2 * e] | (t2[2 * e + 1] << 16);
            const u32 v1 = t1[2 * e] | (t1[2 * e + 1] << 16);
#pragma unroll
            for (int z = 0; z < 4; z++) A1[z][e] = ((fr >> 2) == z) ? v1 : 0u;
        }
    }
    GAS char* O = (GAS char*)(ws + WS_O);
    const int cg0 = 5 * h + g, cg4 = 5 * h + 4;
    const u32 off0 = (u32)((((cg0 >> 3) * 4 + ((cg0 >> 1) & 3)) * 128 + fr * 8 + (cg0 & 1) * 4) * 2);
    const u32 off4 = (u32)((((cg4 >> 3) * 4 + ((cg4 >> 1) & 3)) * 128 + fr * 8 + (cg4 & 1) * 4) * 2);
    const f32x4 zero = {0, 0, 0, 0};
#pragma unroll
    for (int i4 = 0; i4 < 4; i4++) {
        f32x4 n1 = zero; float rden[4];
#pragma unroll
        for (int z = 0; z < 4; z++) {
            const int i = 4 * i4 + z;
            const f32x4 n0 = mfma16(A0, Q[i], zero);
            const f32x4 dn = mfma16(A2, Q[i], zero);
            n1 = mfma16(A1[z], Q[i], n1);
            const float den = rbf(rbf(dn[0]) + 1e-6f);
            const float rd = __builtin_amdgcn_rcpf(den);
            rden[z] = rd;
            const u32x2 o = {pk2(rbf(n0[0]) * rd, rbf(n0[1]) * rd), pk2(rbf(n0[2]) * rd, rbf(n0[3]) * rd)};
            *(GAS u32x2*)(O + (size_t)(sb0 + i) * 20480 + off0) = o;
        }
        const float rd = g == 0 ? rden[0] : g == 1 ? rden[1] : g == 2 ? rden[2] : rden[3];
        const u32x2 o = {pk2(rbf(n1[0]) * rd, rbf(n1[1]) * rd), pk2(rbf(n1[2]) * rd, rbf(n1[3]) * rd)};
        *(GAS u32x2*)(O + (size_t)(sb0 + 4 * i4 + g) * 20480 + off4) = o;
    }
}

DI void wt_block(LAS char* sm, int wave, int lane, int kap, const u16* __restrict__ wo, char* __restrict__ ws) {
    const char* src = (const char*)wo + (size_t)kap * 32 * 1280;
#pragma unroll
    for (int i = 0; i < 10; i++) glds16(src, (wave * 10 + i) * 1024 + lane * 16, (wave * 10 + i) * 1024);
    VMCNT(0); BAR();
    const int fr = lane & 15, g = lane >> 4, q = fr >> 2, p = lane & 3;
    GAS char* WT = (GAS char*)(ws + WS_WT);
#pragma unroll
    for (int i = 0; i < 10; i++) {
        const int cb = wave * 10 + i;
        const u32 a = (8 * g + q) * 1280 + cb * 32 + 8 * p;
        const u32x2 lo = ldstr(sm, a), hi = ldstr(sm, a + 4 * 1280);
        *(GAS u32x4*)(WT + (size_t)(cb * 20 + kap) * 1024 + lane * 16) = (u32x4){lo.x, lo.y, hi.x, hi.y};
    }
}

__global__ __launch_bounds__(256, 1) void k2_mid(const u16* __restrict__ hk, const u16* __restrict__ sD, const u16* __restrict__ wo, char* __restrict__ ws) {
    extern __shared__ __attribute__((aligned(16))) char smem[];
    LAS char* sm = (LAS char*)smem;
    const int tid = threadIdx.x, lane = tid & 63, wave = __builtin_amdgcn_readfirstlane(tid >> 6);
    const int b = blockIdx.x;
    STAMP(1, b, 0);
    if (b < 160) conv_wave(sm, wave, lane, 4 * b + wave, hk, sD, ws);
    else if (b < 224) kvo_block(sm, wave, lane, tid, (b - 160) >> 1, (b - 160) & 1, ws);
    else wt_block(sm, wave, lane, b - 224, wo, ws);
    STAMP(1, b, 1);
}

constexpr int K3_A = 0;
constexpr int K3_YY = 102400;
constexpr int K3_Y2 = K3_YY + 10240;
constexpr int K3_LDS = K3_Y2 + 64 * 176;
constexpr int RED_PITCH = 336;

__global__ __launch_bounds__(256, 1) void k3_out(const u16* __restrict__ x, const char* __restrict__ wsr, u16* __restrict__ out, char* __restrict__ ws) {
    extern __shared__ __attribute__((aligned(16))) char smem[];
    LAS char* sm = (LAS char*)smem;
    const int tid = threadIdx.x, lane = tid & 63, wave = __builtin_amdgcn_readfirstlane(tid >> 6);
    const int b = blockIdx.x, xcd = b & 7, j = b >> 3, st = 4 * xcd + (j >> 3), ct = j & 7;
    const int kh = wave & 1, sh = wave >> 1, fr = lane & 15, g = lane >> 4;
    STAMP(2, b, 0);
    const char* wt = wsr + WS_WT + (size_t)ct * 102400;
    const GAS char* ob = (const GAS char*)(wsr + WS_O) + (size_t)((st * 4 + 2 * sh) * 20 + 10 * kh) * 1024 + lane * 16;
    u32x4 B[2][10];
#pragma unroll
    for (int grp = 0; grp < 2; grp++) {
#pragma unroll
        for (int n = 0; n < 13; n++) {
            const int id = 4 * n + wave;
            if (id < 50) {
                const int cb = id % 5, kl = (id / 5) % 5, hf = id / 25, kap = 10 * hf + 5 * grp + kl;
                glds16(wt, (u32)((cb * 20 + kap) * 1024 + lane * 16), (u32)((cb * 20 + kap) * 1024));
            }
        }
#pragma unroll
        for (int kl = 0; kl < 5; kl++) { B[0][5 * grp + kl] = *(const GAS u32x4*)(ob + (5 * grp + kl) * 1024); B[1][5 * grp + kl] = *(const GAS u32x4*)(ob + 20480 + (5 * grp + kl) * 1024); }
    }
    {
        const char* yt = wsr + WS_YT + (size_t)(ct * 80) * 4096 + st * 128;
#pragma unroll
        for (int n = 0; n < 3; n++) { const int id = 4 * n + wave; if (id < 10) glds16(yt, (u32)((id * 8 + (lane >> 3)) * 4096 + (lane & 7) * 16), K3_YY + id * 1024); }
    }
    u32x4 xv[3]; float gv[3];
#pragma unroll
    for (int r = 0; r < 3; r++) {
        int it = tid + 256 * r; it = it < 640 ? it : 639;
        const int s = it / 10, u = it - 10 * s;
        xv[r] = *(const GAS u32x4*)((const GAS char*)x + (size_t)(st * 64 + s) * 1280 + ct * 160 + u * 16);
        gv[r] = ((const GAS float*)(wsr + WS_G))[st * 64 + s];
    }
    VMCNT(0); BAR();
    f32x4 acc[5][2];
#pragma unroll
    for (int cb = 0; cb < 5; cb++) { acc[cb][0] = (f32x4){0, 0, 0, 0}; acc[cb][1] = (f32x4){0, 0, 0, 0}; }
#pragma unroll
    for (int kl = 0; kl < 10; kl++)
#pragma unroll
        for (int cb = 0; cb < 5; cb++) {
            const u32x4 a = lds128(sm, (cb * 20 + 10 * kh + kl) * 1024 + lane * 16);
            acc[cb][0] = mfma16(a, B[0][kl], acc[cb][0]); acc[cb][1] = mfma16(a, B[1][kl], acc[cb][1]);
        }
    if (kh == 0) {
        const int q = fr >> 2, p = lane & 3;
#pragma unroll
        for (int cb = 0; cb < 5; cb++)
#pragma unroll
            for (int z = 0; z < 2; z++) {
                const int sb = 2 * sh + z;
                const u32x2 t = ldstr(sm, K3_YY + (16 * cb + 4 * g + q) * 128 + (16 * sb + 4 * p) * 2);
                *(LAS u32x2*)(sm + K3_Y2 + (16 * sb + fr) * 176 + (16 * cb + 4 * g) * 2) = t;
            }
    }
    LGKM0(); BAR();
#pragma unroll
    for (int cb = 0; cb < 5; cb++)
#pragma unroll
        for (int z = 0; z < 2; z++)
            *(LAS f32x4*)(sm + kh * (64 * RED_PITCH) + (16 * (2 * sh + z) + fr) * RED_PITCH + (16 * cb + 4 * g) * 4) = acc[cb][z];
    LGKM0(); BAR();
#pragma unroll
    for (int r = 0; r < 3; r++) {
        const int it = tid + 256 * r;
        if (it < 640) {
            const int s = it / 10, u = it - 10 * s;
            const u32 ra = s * RED_PITCH + u * 32;
            const f32x4 p0 = *(LAS f32x4*)(sm + ra), p1 = *(LAS f32x4*)(sm + ra + 16), q0 = *(LAS f32x4*)(sm + 64 * RED_PITCH + ra), q1 = *(LAS f32x4*)(sm + 64 * RED_PITCH + ra + 16);
            const u32x4 y8 = lds128(sm, K3_Y2 + s * 176 + u * 16);
            const float gg = gv[r], omg = rbf(1.f - gg);
            float o[8];
#pragma unroll
            for (int e = 0; e < 4; e++) {
                const float a0 = rbf((e < 2 ? p0[2 * e] + q0[2 * e] : p1[2 * e - 4] + q1[2 * e - 4])), a1 = rbf((e < 2 ? p0[2 * e + 1] + q0[2 * e + 1] : p1[2 * e - 3] + q1[2 * e - 3]));
                const float b0 = rbf(blo(xv[r][e]) * blo(y8[e])), b1 = rbf(bhi(xv[r][e]) * bhi(y8[e]));
                o[2 * e] = rbf(gg * a0) + rbf(omg * b0); o[2 * e + 1] = rbf(gg * a1) + rbf(omg * b1);
            }
            *(GAS u32x4*)((GAS char*)out + (size_t)(st * 64 + s) * 1280 + ct * 160 + u * 16) = (u32x4){pk2(o[0], o[1]), pk2(o[2], o[3]), pk2(o[4], o[5]), pk2(o[6], o[7])};
        }
    }
    STAMP(2, b, 1);
}

extern "C" void kernel_launch(void* const* d_in, const int* in_sizes, int n_in, void* d_out, int out_size, void* d_ws, size_t ws_size, hipStream_t stream) {
    const u16* x = (const u16*)d_in[0]; const u16* wqkv = (const u16*)d_in[1]; const u16* wo = (const u16*)d_in[2];
    const u16* kin = (const u16*)d_in[4]; const u16* vin = (const u16*)d_in[5]; const u16* shk = (const u16*)d_in[6];
    const u16* hk = (const u16*)d_in[7]; const u16* sD = (const u16*)d_in[8]; const u16* gw = (const u16*)d_in[9];
    char* ws = (char*)d_ws;
    (void)hipFuncSetAttribute((const void*)k1_qkv, hipFuncAttributeMaxDynamicSharedMemorySize, K1_LDS);
    (void)hipFuncSetAttribute((const void*)k2_mid, hipFuncAttributeMaxDynamicSharedMemorySize, K2_LDS);
    (void)hipFuncSetAttribute((const void*)k3_out, hipFuncAttributeMaxDynamicSharedMemorySize, K3_LDS);
    k1_qkv<<<256, 512, K1_LDS, stream>>>(x, wqkv, kin, vin, shk, gw, ws);
    k2_mid<<<244, 256, K2_LDS, stream>>>(hk, sD, wo, ws);
    k3_out<<<256, 256, K3_LDS, stream>>>(x, ws, (u16*)d_out, ws);
}
```

```cpp
#include <hip/hip_runtime.h>
#include <stdint.h>

typedef unsigned short u16;
typedef unsigned int u32;
typedef __bf16 bf16x8 __attribute__((ext_vector_type(8)));
typedef __bf16 bf16x2 __attribute__((ext_vector_type(2)));
typedef float f32x4 __attribute__((ext_vector_type(4)));
typedef float f32x2 __attribute__((ext_vector_type(2)));
typedef float f32x16 __attribute__((ext_vector_type(16)));
typedef u32 u32x4 __attribute__((ext_vector_type(4)));
typedef u32 u32x2 __attribute__((ext_vector_type(2)));
typedef short s16x4 __attribute__((ext_vector_type(4)));
#define LAS __attribute__((address_space(3)))
#define GAS __attribute__((address_space(1)))
#define DI __device__ __forceinline__
typedef __amdgpu_buffer_rsrc_t rsrc_t;

constexpr int S = 2048, D = 640;

constexpr u32 WS_UT = 0;
constexpr u32 WS_YT = WS_UT + 2621440;
constexpr u32 WS_PK0 = WS_YT + 2621440;
constexpr u32 WS_V0 = WS_PK0 + 2097152;
constexpr u32 WS_X = WS_V0 + 2097152;
constexpr u32 WS_P2 = WS_X + 1048576;
constexpr u32 WS_O = WS_P2 + 2621440;
constexpr u32 WS_G = WS_O + 2621440;
constexpr u32 WS_FLAG = WS_G + 8192;
constexpr u32 F_P0 = 0, F_G1 = 320, F_GATE = 560, F_O = 576, F_Y = 640, F_EPOCH = 800, F_TMO = 801;
constexpr u32 WS_STAMP = WS_FLAG + 832 * 64;
constexpr u32 WS_BYTES = WS_STAMP + 256 * 32 * 16;

#define TS(w, id) do {} while (0)

DI u32 pk2(float a, float b) { f32x2 t = {a, b}; return __builtin_bit_cast(u32, __builtin_convertvector(t, bf16x2)); }
DI float blo(u32 u) { return __uint_as_float(u << 16); }
DI float bhi(u32 u) { return __uint_as_float(u & 0xffff0000u); }
DI float rbf(float f) { return blo(pk2(f, f)); }
DI bf16x8 asbf(u32x4 v) { return __builtin_bit_cast(bf16x8, v); }
DI f32x4 mfma16(u32x4 a, u32x4 b, f32x4 c) { return __builtin_amdgcn_mfma_f32_16x16x32_bf16(asbf(a), asbf(b), c, 0, 0, 0); }
DI f32x16 mfma32(u32x4 a, u32x4 b, f32x16 c) { return __builtin_amdgcn_mfma_f32_32x32x16_bf16(asbf(a), asbf(b), c, 0, 0, 0); }

DI void glds16(const void* sbase, u32 voff, u32 lds) {
    asm volatile("s_mov_b32 m0, %2\n\ts_nop 0\n\tglobal_load_lds_dwordx4 %0, %1" :: "v"(voff), "s"(sbase), "s"(lds) : "memory");
}
DI void gld64(u32x2& d, const void* sbase, u32 voff) { asm volatile("global_load_dwordx2 %0, %1, %2" : "=v"(d) : "v"(voff), "s"(sbase) : "memory"); }
DI void gld128(u32x4& d, const void* sbase, u32 voff) { asm volatile("global_load_dwordx4 %0, %1, %2" : "=v"(d) : "v"(voff), "s"(sbase) : "memory"); }
DI void gld128c(u32x4& d, const void* sbase, u32 voff) { asm volatile("global_load_dwordx4 %0, %1, %2 sc1" : "=v"(d) : "v"(voff), "s"(sbase) : "memory"); }
DI void gld32c(u32& d, const void* sbase, u32 voff) { asm volatile("global_load_dword %0, %1, %2 sc1" : "=v"(d) : "v"(voff), "s"(sbase) : "memory"); }
#define PIN(x) asm volatile("" : "+v"(x))
#define SB() __builtin_amdgcn_sched_barrier(0)
DI u32x4 ld128c(rsrc_t r, u32 off) { return __builtin_amdgcn_raw_buffer_load_b128(r, off, 0, 16); }
DI u32 ld32c(rsrc_t r, u32 off) { return __builtin_amdgcn_raw_buffer_load_b32(r, off, 0, 16); }
DI void st128c(rsrc_t r, u32 off, u32x4 v) { __builtin_amdgcn_raw_buffer_store_b128(v, r, off, 0, 16); }
DI void st64c(rsrc_t r, u32 off, u32x2 v) { __builtin_amdgcn_raw_buffer_store_b64(v, r, off, 0, 16); }
DI void st32c(rsrc_t r, u32 off, u32 v) { __builtin_amdgcn_raw_buffer_store_b32(v, r, off, 0, 16); }
DI u32x4 lds128(LAS char* sm, u32 a) { return *(LAS u32x4*)(sm + a); }
DI u32x2 lds64(LAS char* sm, u32 a) { return *(LAS u32x2*)(sm + a); }
DI u32x2 ldstr(LAS char* sm, u32 a) { return __builtin_bit_cast(u32x2, __builtin_amdgcn_ds_read_tr16_b64_v4i16((LAS s16x4*)(sm + a))); }
#define VMCNT(n) asm volatile("s_waitcnt vmcnt(" #n ")" ::: "memory")
#define LGKM0() asm volatile("s_waitcnt lgkmcnt(0)" ::: "memory")
#define BAR() do { asm volatile("" ::: "memory"); __builtin_amdgcn_s_barrier(); asm volatile("" ::: "memory"); } while (0)
#define MEMF() asm volatile("" ::: "memory")

DI void set_flag(rsrc_t r, u32 idx, u32 tag) { st32c(r, WS_FLAG + idx * 64, tag); }
DI void wait_flags(rsrc_t r, int idx0, int idx1, u32 tag) {
    const unsigned long long t0 = __builtin_amdgcn_s_memrealtime();
    for (;;) {
        u32 a = tag, b = tag;
        if (idx0 >= 0) a = ld32c(r, WS_FLAG + idx0 * 64);
        if (idx1 >= 0) b = ld32c(r, WS_FLAG + idx1 * 64);
        if (__all(a == tag && b == tag)) break;
        if (__builtin_amdgcn_s_memrealtime() - t0 > 2000000ull) { set_flag(r, F_TMO, tag); break; }
        __builtin_amdgcn_s_sleep(1);
    }
    MEMF();
}
DI void publish_last(rsrc_t r, LAS char* sm, u32 cnt, u32 nw, u32 flag, u32 tag, int lane) {
    VMCNT(0);
    if (lane == 0) {
        const u32 old = __hip_atomic_fetch_add((LAS u32*)(sm + cnt), 1u, __ATOMIC_RELAXED, __HIP_MEMORY_SCOPE_WORKGROUP);
        if (old == nw - 1) set_flag(r, flag, tag);
    }
    MEMF();
}

DI u32 phi2(float a, float b) {
    const f32x2 t = {a, b}, s = t * 1.4426950408889634f;
    const float ea = __builtin_amdgcn_fmed3f(__builtin_amdgcn_exp2f(s.x), 0.f, 1.f), eb = __builtin_amdgcn_fmed3f(__builtin_amdgcn_exp2f(s.y), 0.f, 1.f);
    const float ma = __int_as_float(max(__float_as_int(a), 0)), mb = __int_as_float(max(__float_as_int(b), 0));
    const f32x2 r = ((f32x2){ea, eb} + (f32x2){ma, mb}) + 1e-6f;
    return pk2(r.x, r.y);
}

constexpr int K1_STAGE = 32768;
constexpr int P0_K = 131072, P0_V = P0_K + 9216, P0_OUT = P0_V + 8192;
constexpr int P0_K2 = 3 * K1_STAGE, P0_OUT2 = P0_K2 + 17408;
constexpr int L_CNT = 156672;
constexpr int CV_PITCH = 4160;
constexpr int CV_U = 4 * CV_PITCH;
constexpr int CV_WAVE = CV_U + 4608;
constexpr int CV_BASE = 65536;
constexpr int WO_HI = 65536;
constexpr int F_YY = 110592;
constexpr int F_Y2 = F_YY + 10240;
constexpr int KO_RED = 110592;
constexpr int KO_KV = KO_RED + 32768;
constexpr int RED_PITCH = 336;
constexpr int LDS_BYTES = 160 * 1024;
DI constexpr u32 wo_off(int kap) { return kap < 12 ? kap * 5120 : WO_HI + (kap - 12) * 5120; }

DI void p0_issue(const u16* kin, const u16* vin, int tile, int lw, int lane, u32 base) {
    const int cg = tile % 10, rg = tile / 10, s0 = rg * 64;
    const u32 col = cg * 128 + (lane & 7) * 16;
#pragma unroll
    for (int z = 0; z < 2; z++) {
        const int n = 2 * lw + z;
        int r = s0 - 4 + n * 8 + (lane >> 3); r = r < 0 ? 0 : r;
        glds16(kin, (u32)(r * 1280) + col, base + n * 1024);
        glds16(vin, (u32)((s0 + n * 8 + (lane >> 3)) * 1280) + col, base + 9216 + n * 1024);
    }
    if (lw == 0) {
        int r = s0 + 60 + (lane >> 3); r = r > S - 1 ? S - 1 : r;
        glds16(kin, (u32)(r * 1280) + col, base + 8 * 1024);
    }
}
DI void p0_compute(LAS char* sm, u32x2 w4, int tile, int wave, int lane, u32 base, u32 outb) {
    const int s0 = (tile / 10) * 64;
    const int G = wave * 4 + (lane >> 4), cb = G & 3, i = lane & 15, q = i >> 2, p = i & 3;
    const float w0 = blo(w4.x), w1 = bhi(w4.x), w2 = blo(w4.y), w3 = bhi(w4.y);
    const int ch = 16 * cb + i;
    u32x2 kq[2][3], vq[2][2];
#pragma unroll
    for (int z = 0; z < 2; z++) {
        const int o = (G >> 2) + 4 * z;
        const u32 ka = base + (8 * o + q) * 128 + 32 * cb + 8 * p, va = ka + 9216;
        kq[z][0] = ldstr(sm, ka); kq[z][1] = ldstr(sm, ka + 512); kq[z][2] = ldstr(sm, ka + 1024);
        vq[z][0] = ldstr(sm, va); vq[z][1] = ldstr(sm, va + 512);
    }
#pragma unroll
    for (int z = 0; z < 2; z++) {
        const int o = (G >> 2) + 4 * z;
        u32x2 k0 = kq[z][0]; const u32x2 k1 = kq[z][1], k2 = kq[z][2], v0 = vq[z][0], v1 = vq[z][1];
        if (s0 == 0 && o == 0) { k0.x = 0; k0.y = 0; }
        const float kk[12] = {blo(k0.x), bhi(k0.x), blo(k0.y), bhi(k0.y), blo(k1.x), bhi(k1.x), blo(k1.y), bhi(k1.y), blo(k2.x), bhi(k2.x), blo(k2.y), bhi(k2.y)};
        const float vv[8] = {blo(v0.x), bhi(v0.x), blo(v0.y), bhi(v0.y), blo(v1.x), bhi(v1.x), blo(v1.y), bhi(v1.y)};
        float u[8];
#pragma unroll
        for (int t = 0; t < 8; t++) {
            float ks = kk[t + 1] * w3;
            ks = fmaf(kk[t + 2], w2, ks); ks = fmaf(kk[t + 3], w1, ks); ks = fmaf(kk[t + 4], w0, ks);
            u[t] = ks * vv[t];
        }
        *(LAS u32x4*)(sm + outb + ch * 128 + ((o ^ (ch & 7)) * 16)) = (u32x4){pk2(u[0], u[1]), pk2(u[2], u[3]), pk2(u[4], u[5]), pk2(u[6], u[7])};
    }
}
DI void p0_store(LAS char* sm, rsrc_t rw, int tile, int t256, u32 outb) {
    const int cg = tile % 10, s0 = (tile / 10) * 64;
#pragma unroll
    for (int z = 0; z < 2; z++) {
        const int ch2 = (t256 >> 3) + 32 * z, o2 = t256 & 7;
        const u32x4 t4 = lds128(sm, outb + ch2 * 128 + ((o2 ^ (ch2 & 7)) * 16));
        st128c(rw, WS_UT + (cg * 64 + ch2) * 4096 + (s0 + 8 * o2) * 2, t4);
    }
}
DI void p0_weights(u32x2& w4, const u16* shk, int tile, int wave, int lane) {
    const int cg = tile % 10, cb = (wave * 4 + (lane >> 4)) & 3;
    gld64(w4, shk, (u32)((cg * 64 + 16 * cb + (lane & 15)) * 8));
}

struct K1Set { u32x4 xa[2][2]; u32x2 wl[2][2], wh[2][2]; };
struct K1Addr { u32 x[2][2]; u32 w[2][2]; };
template <bool QT, int WO, int XO>
DI void k1_phase(LAS char* sm, f32x16 (&acc)[2][2], const K1Set& cur, K1Set& nxt, const u32 (&xb)[2], const u32 (&wb)[2][2]) {
#pragma unroll
    for (int n = 0; n < 8; n++) {
        const int ks = n >> 2, i = (n >> 1) & 1, jj = n & 1;
        const u32x4 wf = {cur.wl[ks][jj].x, cur.wl[ks][jj].y, cur.wh[ks][jj].x, cur.wh[ks][jj].y};
        acc[i][jj] = QT ? mfma32(wf, cur.xa[ks][i], acc[i][jj]) : mfma32(cur.xa[ks][i], wf, acc[i][jj]);
        if (n == 0) { nxt.wl[0][0] = ldstr(sm, wb[0][0] + WO); nxt.wh[0][0] = ldstr(sm, wb[0][1] + WO); }
        if (n == 1) { nxt.xa[0][0] = lds128(sm, xb[0] + XO); nxt.wl[0][1] = ldstr(sm, wb[1][0] + WO); }
        if (n == 2) { nxt.wh[0][1] = ldstr(sm, wb[1][1] + WO); nxt.xa[0][1] = lds128(sm, xb[0] + XO + 4096); }
        if (n == 3) { nxt.wl[1][0] = ldstr(sm, wb[0][0] + WO + 4096); nxt.wh[1][0] = ldstr(sm, wb[0][1] + WO + 4096); }
        if (n == 4) { nxt.xa[1][0] = lds128(sm, xb[1] + XO); nxt.wl[1][1] = ldstr(sm, wb[1][0] + WO + 4096); }
        if (n == 5) { nxt.wh[1][1] = ldstr(sm, wb[1][1] + WO + 4096); nxt.xa[1][1] = lds128(sm, xb[1] + XO + 4096); }
        SB();
    }
}
template <bool QT, int ST>
DI void k1_chunk(LAS char* sm, f32x16 (&acc)[2][2], K1Set& s0, K1Set& s1, const K1Addr& a0, const K1Addr& a1, bool bar, bool drain) {
    constexpr int SN = (ST + 1) & 3;
    const K1Addr& ac = ST < 2 ? a0 : a1; const K1Addr& an = SN < 2 ? a0 : a1;
    k1_phase<QT, (ST & 1) * K1_STAGE + 8192, (ST & 1) * K1_STAGE + 16384>(sm, acc, s0, s1, ac.x[1], ac.w);
    if (drain) VMCNT(0);
    if (bar) { LGKM0(); BAR(); }
    k1_phase<QT, (SN & 1) * K1_STAGE, (SN & 1) * K1_STAGE + 16384>(sm, acc, s1, s0, an.x[0], an.w);
}
template <bool QT>
DI void k1_loop(LAS char* sm, const K1Addr& a0, f32x16 (&acc)[2][2], rsrc_t rw, int tile, int tile2, u32 tag, int wave, int lane) {
    K1Addr a1;
#pragma unroll
    for (int i = 0; i < 2; i++)
#pragma unroll
        for (int z = 0; z < 2; z++) { a1.x[i][z] = a0.x[i][z] + 65536; a1.w[i][z] = a0.w[i][z] + 65536; }
    K1Set s0, s1;
#pragma unroll
    for (int ks = 0; ks < 2; ks++)
#pragma unroll
        for (int jj = 0; jj < 2; jj++) {
            s0.wl[ks][jj] = ldstr(sm, a0.w[jj][0] + ks * 4096); s0.wh[ks][jj] = ldstr(sm, a0.w[jj][1] + ks * 4096);
            s0.xa[ks][jj] = lds128(sm, a0.x[0][ks] + 16384 + jj * 4096);
        }
    SB();
#pragma unroll 1
    for (int c = 0; c < 8; c += 4) {
        k1_chunk<QT, 0>(sm, acc, s0, s1, a0, a1, true, false);
        k1_chunk<QT, 1>(sm, acc, s0, s1, a0, a1, true, false);
        k1_chunk<QT, 2>(sm, acc, s0, s1, a0, a1, true, false);
        k1_chunk<QT, 3>(sm, acc, s0, s1, a0, a1, true, c == 0);
        if (c == 0 && wave == 0 && lane < 2 && (lane == 0 || tile2 >= 0)) set_flag(rw, F_P0 + (lane == 0 ? tile : tile2), tag);
    }
    k1_chunk<QT, 0>(sm, acc, s0, s1, a0, a1, true, false);
    k1_chunk<QT, 1>(sm, acc, s0, s1, a0, a1, false, false);
}

DI void wo_issue(const u16* wo, int ct, int k0, int k1, int w, int nw, int lane) {
    u32 vo[5];
#pragma unroll
    for (int i = 0; i < 5; i++) { const int n = 64 * i + lane, rho = n / 10, c = n - 10 * rho, row = (rho & 19) | ((rho & 4) << 1) | ((rho & 8) >> 1); vo[i] = (u32)(row * 1280 + ct * 160 + c * 16); }
#pragma unroll 1
    for (int kap = k0 + w; kap < k1; kap += nw) {
        const char* src = (const char*)wo + (size_t)kap * 40960;
        const u32 dst = kap < 12 ? kap * 5120 : WO_HI + (kap - 12) * 5120;
#pragma unroll
        for (int i = 0; i < 5; i++) glds16(src, vo[i], dst + i * 1024);
    }
}

struct CvSet { u32x2 fl[8], fh[8]; u32x4 g[8]; };
DI void cv_load(LAS char* sm, CvSet& s, u32 fa, u32 ga) {
#pragma unroll
    for (int k1 = 0; k1 < 8; k1++) { s.fl[k1] = ldstr(sm, fa + 512 * k1); s.fh[k1] = ldstr(sm, fa + 512 * k1 - 8); s.g[k1] = lds128(sm, ga + 512 * k1); }
}
DI void cv_phase(LAS char* sm, f32x4 (&Y)[8], const CvSet& cur, CvSet& nxt, u32 fa, u32 ga) {
    int n = 0;
#pragma unroll
    for (int e = 0; e < 8; e++)
#pragma unroll
        for (int k1 = 0; k1 + e < 8; k1++) {
            const u32x4 F = {cur.fl[k1].x, cur.fl[k1].y, cur.fh[k1].x, cur.fh[k1].y};
            Y[k1 + e] = mfma16(F, cur.g[e], Y[k1 + e]);
            if (n == 0) nxt.g[0] = lds128(sm, ga);
            else if (n <= 16) { const int r = (n - 1) >> 1; if ((n - 1) & 1) nxt.fh[r] = ldstr(sm, fa + 512 * r - 8); else nxt.fl[r] = ldstr(sm, fa + 512 * r); }
            else if (n < 24) nxt.g[n - 16] = lds128(sm, ga + 512 * (n - 16));
            n++;
            SB();
        }
}
struct CvIn { u32x4 hd[4]; u32x2 hm[4]; u32x4 u[4]; };
DI void conv_fetch_h(CvIn& in, const u16* hk, int c, int lane) {
    const char* hb = (const char*)hk + (size_t)c * 4096;
#pragma unroll
    for (int z = 0; z < 4; z++) {
        const int i = lane + 64 * z;
        gld128(in.hd[z], hb, (u32)(i * 16));
        gld64(in.hm[z], hb, (u32)(i > 0 ? i * 16 - 8 : 0));
    }
}
DI void conv_run(LAS char* sm0, CvIn& in, float Dc, int w, int lane, int c, rsrc_t rw, char* ws) {
    LAS char* sm = sm0 + CV_BASE + w * CV_WAVE;
    VMCNT(4);
#pragma unroll
    for (int z = 0; z < 4; z++) { PIN(in.hd[z]); PIN(in.hm[z]); }
    if (lane < 32) *(LAS u32x4*)(sm + CV_U + lane * 16) = (u32x4){0, 0, 0, 0};
    if (lane < 8) *(LAS u32x4*)(sm + (lane >> 1) * CV_PITCH + (lane & 1) * 16) = (u32x4){0, 0, 0, 0};
#pragma unroll
    for (int z = 0; z < 4; z++) {
        const int i = lane + 64 * z;
        u32 m2 = in.hm[z].x, m1 = in.hm[z].y;
        if (i == 0) { m2 = 0; m1 = 0; }
        const u32 d0 = in.hd[z].x, d1 = in.hd[z].y, d2 = in.hd[z].z, d3 = in.hd[z].w;
        const u32 om2 = __builtin_amdgcn_alignbit(m1, m2, 16), om1 = __builtin_amdgcn_alignbit(d0, m1, 16), o0 = __builtin_amdgcn_alignbit(d1, d0, 16),
                  o1 = __builtin_amdgcn_alignbit(d2, d1, 16), o2 = __builtin_amdgcn_alignbit(d3, d2, 16);
        LAS char* p = sm + 32 + i * 16;
        *(LAS u32x4*)(p) = (u32x4){d0, d1, d2, d3};
        *(LAS u32x4*)(p + CV_PITCH) = (u32x4){om1, o0, o1, o2};
        *(LAS u32x4*)(p + 2 * CV_PITCH) = (u32x4){m1, d0, d1, d2};
        *(LAS u32x4*)(p + 3 * CV_PITCH) = (u32x4){om2, om1, o0, o1};
    }
    VMCNT(0);
#pragma unroll
    for (int z = 0; z < 4; z++) { PIN(in.u[z]); *(LAS u32x4*)(sm + CV_U + 512 + z * 1024 + lane * 16) = in.u[z]; }
    LGKM0();
    TS(4, 9);
    const int fr = lane & 15, g = lane >> 4, q = fr >> 2, p = lane & 3;
    const u32 fa = q * CV_PITCH + 2 * (32 - 8 * g + 4 * p);
    const u32 ga = CV_U + 480 + 32 * fr + 16 * g;
    f32x4 Y[8];
#pragma unroll
    for (int i = 0; i < 8; i++) Y[i] = (f32x4){0, 0, 0, 0};
    CvSet A, B;
    cv_load(sm, A, fa, ga);
    SB();
#pragma unroll 1
    for (int k0 = 0; k0 < 8; k0 += 2) {
        cv_phase(sm, Y, A, B, fa + 64 * (k0 + 1), ga - 64 * (k0 + 1));
        cv_phase(sm, Y, B, A, fa + 64 * (k0 + 2), ga - 64 * (k0 + 2));
    }
    TS(4, 10);
    {
        u32x2 u4[8];
#pragma unroll
        for (int ib = 0; ib < 8; ib++) u4[ib] = lds64(sm, CV_U + 512 + 512 * ib + 32 * fr + 8 * g);
#pragma unroll
        for (int ib = 0; ib < 8; ib++) {
            const u32x2 r = {pk2(fmaf(blo(u4[ib].x), Dc, Y[ib][0]), fmaf(bhi(u4[ib].x), Dc, Y[ib][1])), pk2(fmaf(blo(u4[ib].y), Dc, Y[ib][2]), fmaf(bhi(u4[ib].y), Dc, Y[ib][3]))};
            *(LAS u32x2*)(sm + 512 * ib + 32 * fr + 8 * g) = r;
        }
    }
    LGKM0();
    u32x4 y8[4];
#pragma unroll
    for (int z = 0; z < 4; z++) y8[z] = lds128(sm, z * 1024 + lane * 16);
#pragma unroll
    for (int z = 0; z < 4; z++) st128c(rw, WS_YT + c * 4096 + z * 1024 + lane * 16, y8[z]);
}

DI void kvo_block(LAS char* sm, int wave, int lane, int h, int half, rsrc_t rw, const u16* wo, int ct, char* ws) {
    const int fr = lane & 15, g = lane >> 4;
    const u32 pk0 = WS_PK0 + (h * 64 + 8 * wave) * 1024 + lane * 16, v0 = pk0 + (WS_V0 - WS_PK0);
    const u32 xx = WS_X + (h * 64 + 8 * wave) * 512 + (g * 8 + (fr & 7)) * 16;
    u32x4 A[8], B[8], X[8], Q[8];
#pragma unroll
    for (int i = 0; i < 8; i++) { A[i] = ld128c(rw, pk0 + i * 1024); B[i] = ld128c(rw, v0 + i * 1024); X[i] = ld128c(rw, xx + i * 512); }
    const int odd = h & 1, pr = h >> 1;
    const int u5 = (g < 3 ? g : 2) + 2 * odd;
    const int sb0 = half * 64 + wave * 8;
    const u32 pq = WS_P2 + (((pr * 128 + sb0) * 5 + u5) * 128 + fr * 8) * 2;
#pragma unroll
    for (int i = 0; i < 8; i++) Q[i] = ld128c(rw, pq + i * 1280);
    MEMF(); SB();
    f32x4 a00 = {0, 0, 0, 0}, a0x = a00, ax0 = a00, axx = a00;
    const u32 cst = fr == 8 ? 0x3F803F80u : 0u;
#pragma unroll
    for (int i = 0; i < 8; i++) {
        u32x4 xv = X[i];
        if (fr >= 8) xv = (u32x4){cst, cst, cst, cst};
        a00 = mfma16(A[i], B[i], a00); a0x = mfma16(A[i], xv, a0x); ax0 = mfma16(xv, B[i], ax0); axx = mfma16(xv, xv, axx);
    }
    TS(0, 9);
    *(LAS f32x4*)(sm + KO_RED + ((wave * 4 + 0) * 64 + lane) * 16) = a00;
    *(LAS f32x4*)(sm + KO_RED + ((wave * 4 + 1) * 64 + lane) * 16) = a0x;
    *(LAS f32x4*)(sm + KO_RED + ((wave * 4 + 2) * 64 + lane) * 16) = ax0;
    *(LAS f32x4*)(sm + KO_RED + ((wave * 4 + 3) * 64 + lane) * 16) = axx;
    if (wave >= 4 && (wave - 4) * 64 + lane < 189) *(LAS u32x2*)(sm + KO_KV + ((wave - 4) * 64 + lane) * 8) = (u32x2){0, 0};
    LGKM0(); BAR();
    if (wave < 4) {
        const int tile = wave;
        f32x4 s = *(LAS f32x4*)(sm + KO_RED + (tile * 64 + lane) * 16);
#pragma unroll
        for (int w2 = 1; w2 < 8; w2++) s += *(LAS f32x4*)(sm + KO_RED + ((w2 * 4 + tile) * 64 + lane) * 16);
        const int e = (tile & 1) ? 12 + fr : fr;
        const bool colok = (tile & 1) ? (fr >= 4 && fr <= 8) : true;
        const bool rowok = (tile & 2) ? (g == 0) : true;
        const int d0 = (tile & 2) ? 16 : 4 * g;
        if (colok && rowok) *(LAS u32x2*)(sm + KO_KV + e * 72 + (d0 + 4) * 2) = (u32x2){pk2(s[0], s[1]), pk2(s[2], s[3])};
    }
    LGKM0(); BAR();
    u32x4 A0, A1[4], A2;
    {
        const u32 dofs = KO_KV + (8 * g - 4 * odd + 4) * 2;
        const u32 r0 = (4 * odd + fr) * 72, r1 = ((odd ? 0 : 16) + (fr & 3)) * 72;
        const u32x2 p0 = lds64(sm, dofs + r0), p1 = lds64(sm, dofs + r0 + 8);
        const u32x2 q0 = lds64(sm, dofs + r1), q1 = lds64(sm, dofs + r1 + 8);
        const u32x2 z0 = lds64(sm, dofs + 20 * 72), z1 = lds64(sm, dofs + 20 * 72 + 8);
        A0 = (u32x4){p0.x, p0.y, p1.x, p1.y}; A2 = (u32x4){z0.x, z0.y, z1.x, z1.y};
#pragma unroll
        for (int z = 0; z < 4; z++) { const bool m = (fr >> 2) == z; A1[z] = (u32x4){m ? q0.x : 0u, m ? q0.y : 0u, m ? q1.x : 0u, m ? q1.y : 0u}; }
    }
    TS(0, 10);
    const int U0 = (5 * h + odd) >> 1;
    const int Uw = U0 + (g >> 1);
    const u32 offA = WS_O + (((Uw >> 2) * 4 + (Uw & 3)) * 128 + fr * 8) * 2 + (g & 1) * 20480;
    const int cgx = 5 * h + (odd ? 0 : 4), Ux = cgx >> 1;
    const u32 offX = WS_O + (((Ux >> 2) * 4 + (Ux & 3)) * 128 + fr * 8 + (cgx & 1) * 4) * 2;
    const f32x4 zero = {0, 0, 0, 0};
#pragma unroll
    for (int i4 = 0; i4 < 2; i4++) {
        f32x4 n1 = zero; float rden[4]; u32x2 ov[4];
#pragma unroll
        for (int z = 0; z < 4; z++) {
            const int i = 4 * i4 + z;
            const f32x4 n0 = mfma16(A0, Q[i], zero);
            const f32x4 dn = mfma16(A2, Q[i], zero);
            n1 = mfma16(A1[z], Q[i], n1);
            const float rd = __builtin_amdgcn_rcpf(dn[0] + 1e-6f);
            rden[z] = rd;
            ov[z] = (u32x2){pk2(n0[0] * rd, n0[1] * rd), pk2(n0[2] * rd, n0[3] * rd)};
        }
#pragma unroll
        for (int z = 0; z < 4; z += 2) {
            auto rx = __builtin_amdgcn_permlane16_swap(ov[z].x, ov[z + 1].x, false, false);
            auto ry = __builtin_amdgcn_permlane16_swap(ov[z].y, ov[z + 1].y, false, false);
            st128c(rw, offA + (sb0 + 4 * i4 + z) * 20480, (u32x4){rx[0], ry[0], rx[1], ry[1]});
        }
        const float rd = g == 0 ? rden[0] : g == 1 ? rden[1] : g == 2 ? rden[2] : rden[3];
        st64c(rw, offX + (sb0 + 4 * i4 + g) * 20480, (u32x2){pk2(n1[0] * rd, n1[1] * rd), pk2(n1[2] * rd, n1[3] * rd)});
    }
}

DI void gate_block(int wave, int lane, int m, const u16* x, const u16* gw, rsrc_t rw) {
    const int sub = lane & 15, r4 = lane >> 4;
    u32x4 gq[5], xr[4][5];
#pragma unroll
    for (int jj = 0; jj < 5; jj++) gq[jj] = *(const GAS u32x4*)((const GAS char*)gw + (sub + 16 * jj) * 16);
#pragma unroll
    for (int it = 0; it < 4; it++)
#pragma unroll
        for (int jj = 0; jj < 5; jj++) xr[it][jj] = *(const GAS u32x4*)((const GAS char*)x + (size_t)(128 * m + 16 * wave + 4 * it + r4) * 1280 + (sub + 16 * jj) * 16);
    MEMF(); SB();
#pragma unroll
    for (int it = 0; it < 4; it++) {
        float a = 0.f;
#pragma unroll
        for (int jj = 0; jj < 5; jj++)
#pragma unroll
            for (int e = 0; e < 4; e++) { a = fmaf(blo(xr[it][jj][e]), blo(gq[jj][e]), a); a = fmaf(bhi(xr[it][jj][e]), bhi(gq[jj][e]), a); }
        a += __shfl_xor(a, 1); a += __shfl_xor(a, 2); a += __shfl_xor(a, 4); a += __shfl_xor(a, 8);
        a = rbf(a);
        const float g = rbf(__builtin_amdgcn_rcpf(1.f + __builtin_amdgcn_exp2f(-1.4426950408889634f * a)));
        if (sub == 0) st32c(rw, WS_G + (128 * m + 16 * wave + 4 * it + r4) * 4, __float_as_uint(g));
    }
}

DI void final_block(LAS char* sm, int wave, int lane, int tid, int st, int ct, const u16* x, u16* out, rsrc_t rw, u32 tag, char* ws) {
    const int fr = lane & 15, g = lane >> 4;
    u32x4 xv[2];
#pragma unroll
    for (int r = 0; r < 2; r++) {
        int it = tid + 512 * r; it = it < 640 ? it : 639;
        const int s = it / 10, u = it - 10 * s;
        xv[r] = *(const GAS u32x4*)((const GAS char*)x + (size_t)(st * 64 + s) * 1280 + ct * 160 + u * 16);
    }
    if (wave == 0) {
        const int idx = lane < 32 ? (int)F_O + 2 * lane + (st >> 4) : lane < 52 ? (int)F_Y + 20 * ct + (lane - 32) : lane == 52 ? (int)F_GATE + (st >> 1) : -1;
        wait_flags(rw, idx, -1, tag);
    }
    VMCNT(0); BAR();
    TS(0, 17);
    u32 gv[2];
#pragma unroll
    for (int r = 0; r < 2; r++) { int it = tid + 512 * r; it = it < 640 ? it : 639; gv[r] = ld32c(rw, WS_G + (st * 64 + it / 10) * 4); }
    if (wave < 4) {
        const int kh = wave & 1, sh = wave >> 1;
        const u32 ob = WS_O + ((st * 4 + 2 * sh) * 20 + 10 * kh) * 1024 + lane * 16;
        u32x4 B[2][10];
#pragma unroll
        for (int kl = 0; kl < 10; kl++) { B[0][kl] = ld128c(rw, ob + kl * 1024); B[1][kl] = ld128c(rw, ob + 20480 + kl * 1024); }
        MEMF(); SB();
        f32x4 acc[5][2];
#pragma unroll
        for (int cb = 0; cb < 5; cb++) { acc[cb][0] = (f32x4){0, 0, 0, 0}; acc[cb][1] = (f32x4){0, 0, 0, 0}; }
        const u32 aoff = (16 * (g >> 1) + 4 * (g & 1) + (fr >> 2)) * 160 + (lane & 3) * 8;
        u32x2 al[2][5], ah[2][5];
#define F_READ(buf, kl) do { const u32 b0_ = kh ? wo_off(10 + (kl)) : wo_off(kl); \
            _Pragma("unroll") for (int cb = 0; cb < 5; cb++) { al[buf][cb] = ldstr(sm, b0_ + aoff + cb * 32); ah[buf][cb] = ldstr(sm, b0_ + aoff + cb * 32 + 1280); } } while (0)
        F_READ(0, 0);
#pragma unroll
        for (int kl = 0; kl < 10; kl++) {
            if (kl < 9) F_READ((kl + 1) & 1, kl + 1);
            SB();
#pragma unroll
            for (int cb = 0; cb < 5; cb++) {
                const u32x4 a = {al[kl & 1][cb].x, al[kl & 1][cb].y, ah[kl & 1][cb].x, ah[kl & 1][cb].y};
                acc[cb][0] = mfma16(a, B[0][kl], acc[cb][0]); acc[cb][1] = mfma16(a, B[1][kl], acc[cb][1]);
            }
            SB();
        }
        TS(0, 18);
        LGKM0(); BAR();
#pragma unroll
        for (int cb = 0; cb < 5; cb++)
#pragma unroll
            for (int z = 0; z < 2; z++)
                *(LAS f32x4*)(sm + kh * (64 * RED_PITCH) + (16 * (2 * sh + z) + fr) * RED_PITCH + (16 * cb + 4 * g) * 4) = acc[cb][z];
    } else {
        const int t = tid - 256;
        u32x4 yv[3];
#pragma unroll
        for (int r = 0; r < 3; r++) { int id = t + 256 * r; id = id < 640 ? id : 639; yv[r] = ld128c(rw, WS_YT + (80 * ct + (id >> 3)) * 4096 + st * 128 + (id & 7) * 16); }
#pragma unroll
        for (int r = 0; r < 3; r++) { const int id = t + 256 * r; if (id < 640) *(LAS u32x4*)(sm + F_YY + id * 16) = yv[r]; }
        LGKM0(); BAR();
        const int sb = wave - 4, q = fr >> 2, p = lane & 3;
        u32x2 tt[5];
#pragma unroll
        for (int cb = 0; cb < 5; cb++) tt[cb] = ldstr(sm, F_YY + (16 * cb + 4 * g + q) * 128 + (16 * sb + 4 * p) * 2);
#pragma unroll
        for (int cb = 0; cb < 5; cb++) *(LAS u32x2*)(sm + F_Y2 + (16 * sb + fr) * 176 + (16 * cb + 4 * g) * 2) = tt[cb];
    }
    LGKM0(); BAR();
#pragma unroll
    for (int r = 0; r < 2; r++) {
        const int it = tid + 512 * r;
        if (it < 640) {
            const int s = it / 10, u = it - 10 * s;
            const u32 ra = s * RED_PITCH + u * 32;
            const f32x4 p0 = *(LAS f32x4*)(sm + ra), p1 = *(LAS f32x4*)(sm + ra + 16), q0 = *(LAS f32x4*)(sm + 64 * RED_PITCH + ra), q1 = *(LAS f32x4*)(sm + 64 * RED_PITCH + ra + 16);
            const u32x4 y8 = lds128(sm, F_Y2 + s * 176 + u * 16);
            const float gg = __uint_as_float(gv[r]), omg = 1.f - gg;
            float o[8];
#pragma unroll
            for (int e = 0; e < 4; e++) {
                const float a0 = (e < 2 ? p0[2 * e] + q0[2 * e] : p1[2 * e - 4] + q1[2 * e - 4]), a1 = (e < 2 ? p0[2 * e + 1] + q0[2 * e + 1] : p1[2 * e - 3] + q1[2 * e - 3]);
                const float b0 = omg * blo(xv[r][e]), b1 = omg * bhi(xv[r][e]);
                o[2 * e] = fmaf(b0, blo(y8[e]), gg * a0); o[2 * e + 1] = fmaf(b1, bhi(y8[e]), gg * a1);
            }
            *(GAS u32x4*)((GAS char*)out + (size_t)(st * 64 + s) * 1280 + ct * 160 + u * 16) = (u32x4){pk2(o[0], o[1]), pk2(o[2], o[3]), pk2(o[4], o[5]), pk2(o[6], o[7])};
        }
    }
}

__global__ __launch_bounds__(512, 2) void fused_kernel(const u16* __restrict__ x, const u16* __restrict__ w, const u16* __restrict__ wo, const u16* __restrict__ kin,
                                                        const u16* __restrict__ vin, const u16* __restrict__ shk, const u16* __restrict__ hk, const u16* __restrict__ sD,
                                                        const u16* __restrict__ gw, u16* __restrict__ out, char* ws) {
    extern __shared__ __attribute__((aligned(16))) char smem[];
    LAS char* sm = (LAS char*)smem;
    const int tid = threadIdx.x, lane = tid & 63, wave = __builtin_amdgcn_readfirstlane(tid >> 6);
    const int b = blockIdx.x, xcd = b & 7, j = b >> 3;
    const rsrc_t rw = __builtin_amdgcn_make_buffer_rsrc(ws, 0, WS_BYTES, 0x00020000);
    u32 tag;
    asm volatile("s_load_dword %0, %1, 0x0\n\ts_waitcnt lgkmcnt(0)" : "=s"(tag) : "s"(ws + WS_FLAG + F_EPOCH * 64) : "memory");
    tag += 1u;
    if (tid < 4) *(LAS u32*)(sm + L_CNT + tid * 4) = 0u;
    TS(0, 0);
    const int st = 4 * xcd + (j >> 3), ct = j & 7;
    int ko = -1;
    bool is_conv = false;

    if (j >= 30) {
        const int m = xcd * 2 + (j - 30);
        ko = m;
        gate_block(wave, lane, m, x, gw, rw);
        VMCNT(0); LGKM0(); BAR();
        if (tid == 0) set_flag(rw, F_GATE + m, tag);
        TS(0, 3);
    } else {
        const int rt = 2 * xcd + j / 15, cbk = j % 15, tile = xcd * 30 + j;
        is_conv = cbk < 10;
        int tile2 = -1;
        if (!is_conv) { const int vi = rt * 5 + (cbk - 10); ko = vi < 48 ? 16 + vi : -2 - (vi - 48); tile2 = 240 + vi; }
        if (wave >= 4) {
            const int lw = wave - 4;
            p0_issue(kin, vin, tile, lw, lane, P0_K);
            if (tile2 >= 0) p0_issue(kin, vin, tile2, lw, lane, P0_K2);
            u32 vx[4], vw[4];
#pragma unroll
            for (int n2 = 0; n2 < 4; n2++) {
                const int n = 4 * lw + n2;
                const int row = 8 * n + (lane >> 3), ch = (lane & 7) ^ ((row >> 1) & 7);
                vx[n2] = (u32)((rt * 128 + row) * 1280 + ch * 16);
                const int kr = 4 * n + (lane >> 4), f = ((kr & 3) << 2) | ((kr >> 2) & 3), cw = (lane & 15) ^ f;
                vw[n2] = (u32)(kr * 3840 + cbk * 256 + cw * 16);
            }
#define K1_ISSUE(c) do { const u32 st_ = ((c) & 3) * K1_STAGE + 4 * lw * 1024; const char* xb_ = (const char*)x + (c) * 128; const char* wp_ = (const char*)w + (size_t)(c) * 64 * 3840; \
            _Pragma("unroll") for (int n2 = 0; n2 < 4; n2++) { glds16(xb_, vx[n2], st_ + 16384 + n2 * 1024); glds16(wp_, vw[n2], st_ + n2 * 1024); } } while (0)
            K1_ISSUE(0); K1_ISSUE(1);
            VMCNT(16); BAR();
            K1_ISSUE(2);
            VMCNT(16); BAR();
            VMCNT(8); BAR();
            K1_ISSUE(3); K1_ISSUE(4);
#pragma unroll 1
            for (int c = 1; c < 6; c++) {
                VMCNT(16); BAR();
                K1_ISSUE(c + 4);
            }
            const int cv = rt * 10 + cbk, c = 4 * cv + lw;
            if (is_conv) {
                CvIn in; u32 fl;
                gld32c(fl, ws, WS_FLAG + (F_P0 + (lane & 31) * 10 + (cv >> 4)) * 64);
                conv_fetch_h(in, hk, c, lane);
                VMCNT(25); BAR();
                VMCNT(17); BAR();
                VMCNT(8); PIN(fl);
                if (!__all(fl == tag)) wait_flags(rw, F_P0 + (lane & 31) * 10 + (cv >> 4), -1, tag);
#pragma unroll
                for (int z = 0; z < 4; z++) gld128c(in.u[z], ws, WS_UT + c * 4096 + z * 1024 + lane * 16);
                BAR();
                TS(4, 8);
                const float Dc = blo((u32)sD[c]);
                conv_run(sm, in, Dc, lw, lane, c, rw, ws);
                publish_last(rw, sm, L_CNT + 4, 4, F_Y + cv, tag, lane);
                TS(4, 11);
            } else {
                VMCNT(16); BAR();
                VMCNT(8); BAR();
                VMCNT(0); BAR();
            }
        } else {
            u32x2 w4, w4b; p0_weights(w4, shk, tile, wave, lane);
            p0_weights(w4b, shk, tile2 >= 0 ? tile2 : tile, wave, lane);
            const int wm = wave >> 1, wn = wave & 1, r32 = lane & 31, hh = lane >> 5;
            K1Addr a0;
#pragma unroll
            for (int ks = 0; ks < 2; ks++) {
                a0.x[0][ks] = (64 * wm + r32) * 128 + (((2 * ks + hh) ^ ((r32 >> 1) & 7)) * 16);
                a0.x[1][ks] = a0.x[0][ks] ^ 64;
            }
#pragma unroll
            for (int jj = 0; jj < 2; jj++)
#pragma unroll
                for (int t = 0; t < 2; t++) {
                    const int blk = (lane >> 4) & 1, q = (lane & 15) >> 2, p = lane & 3, row = 8 * hh + 4 * t + q, ch = 8 * wn + 4 * jj + 2 * blk + (p >> 1);
                    const int f = ((row & 3) << 2) | ((row >> 2) & 3);
                    a0.w[jj][t] = row * 256 + ((ch ^ f) * 16) + 8 * (p & 1);
                }
            f32x16 acc[2][2];
#pragma unroll
            for (int i = 0; i < 2; i++)
#pragma unroll
                for (int jj = 0; jj < 2; jj++)
#pragma unroll
                    for (int e = 0; e < 16; e++) acc[i][jj][e] = 0.f;
            VMCNT(0); PIN(w4); PIN(w4b); BAR();
            p0_compute(sm, w4, tile, wave, lane, P0_K, P0_OUT);
            if (tile2 >= 0) p0_compute(sm, w4b, tile2, wave, lane, P0_K2, P0_OUT2);
            LGKM0(); BAR();
            p0_store(sm, rw, tile, tid, P0_OUT);
            if (tile2 >= 0) p0_store(sm, rw, tile2, tid, P0_OUT2);
            TS(0, 1);
            if (cbk < 5) {
                k1_loop<true>(sm, a0, acc, rw, tile, tile2, tag, wave, lane);
                TS(0, 2);
#pragma unroll
                for (int jj = 0; jj < 2; jj++)
#pragma unroll
                    for (int ap = 0; ap < 2; ap++) {
                        const int U = 16 * cbk + 8 * wn + 4 * jj + 2 * ap + hh, pr = U / 5, u5 = U - 5 * pr;
                        const u32 dst = WS_P2 + (((pr * 128 + rt * 8 + 4 * wm + (r32 >> 4)) * 5 + u5) * 128 + (r32 & 15) * 8) * 2;
#pragma unroll
                        for (int i = 0; i < 2; i++) {
                            const f32x16& a = acc[i][jj];
                            const u32 x0 = phi2(a[8 * ap], a[8 * ap + 1]), y0 = phi2(a[8 * ap + 2], a[8 * ap + 3]);
                            const u32 x1 = phi2(a[8 * ap + 4], a[8 * ap + 5]), y1 = phi2(a[8 * ap + 6], a[8 * ap + 7]);
                            auto rx = __builtin_amdgcn_permlane32_swap(x0, x1, false, false);
                            auto ry = __builtin_amdgcn_permlane32_swap(y0, y1, false, false);
                            st128c(rw, dst + i * 2560, (u32x4){rx[0], ry[0], rx[1], ry[1]});
                        }
                    }
            } else {
                k1_loop<false>(sm, a0, acc, rw, tile, tile2, tag, wave, lane);
                TS(0, 2);
                const bool isk = cbk < 10;
                const int j5 = isk ? cbk - 5 : cbk - 10;
                u32 dst[2], gstep[2];
#pragma unroll
                for (int jj = 0; jj < 2; jj++) {
                    const int cc = 128 * j5 + 64 * wn + 32 * jj + r32, h = cc / 20, d = cc - 20 * h;
                    const int sb32 = rt * 4 + 2 * wm;
                    const bool lo = d < 16;
                    dst[jj] = lo ? (isk ? WS_PK0 : WS_V0) + (((h * 64 + sb32) * 4 + hh) * 128 + d * 8) * 2
                                 : WS_X + (((h * 64 + sb32) * 4 + hh) * 64 + (d - 16 + (isk ? 0 : 4)) * 8) * 2;
                    gstep[jj] = lo ? 512 : 256;
                }
                if (isk) {
#pragma unroll
                    for (int jj = 0; jj < 2; jj++)
#pragma unroll
                        for (int i = 0; i < 2; i++)
#pragma unroll
                            for (int z = 0; z < 2; z++) {
                                const f32x16& a = acc[i][jj];
                                st128c(rw, dst[jj] + (2 * i + z) * gstep[jj],
                                       (u32x4){phi2(a[8 * z], a[8 * z + 1]), phi2(a[8 * z + 2], a[8 * z + 3]), phi2(a[8 * z + 4], a[8 * z + 5]), phi2(a[8 * z + 6], a[8 * z + 7])});
                            }
                } else {
#pragma unroll
                    for (int jj = 0; jj < 2; jj++)
#pragma unroll
                        for (int i = 0; i < 2; i++)
#pragma unroll
                            for (int z = 0; z < 2; z++) {
                                const f32x16& a = acc[i][jj];
                                st128c(rw, dst[jj] + (2 * i + z) * gstep[jj],
                                       (u32x4){pk2(a[8 * z], a[8 * z + 1]), pk2(a[8 * z + 2], a[8 * z + 3]), pk2(a[8 * z + 4], a[8 * z + 5]), pk2(a[8 * z + 6], a[8 * z + 7])});
                            }
                }
            }
            LGKM0();
            publish_last(rw, sm, L_CNT, 4, F_G1 + tile, tag, lane);
            TS(0, 3);
            if (is_conv) {
                while (*(volatile LAS u32*)(sm + L_CNT) < 4u) __builtin_amdgcn_s_sleep(1);
                MEMF();
                wo_issue(wo, ct, 0, 12, wave, 4, lane);
            }
        }
    }
    if (!is_conv) {
        LGKM0(); BAR();
        if (ko >= 0) {
            const int h = ko >> 1, half = ko & 1;
            wo_issue(wo, ct, 0, 12, wave, 8, lane);
            if (wave == 0) {
                const int j0 = (20 * h) >> 7, j1 = (20 * h + 19) >> 7;
                const int l16 = lane & 15, sel = lane >> 4;
                const int i0 = (int)F_G1 + 15 * l16 + (sel < 2 ? 5 : 10) + ((sel & 1) ? j1 : j0);
                const int i1 = lane < 16 ? (int)F_G1 + 15 * (8 * half + (lane & 7)) + (lane < 8 ? j0 : j1) : -1;
                wait_flags(rw, i0, i1, tag);
            }
            BAR();
            TS(0, 8);
            kvo_block(sm, wave, lane, h, half, rw, wo, ct, ws);
            VMCNT(0); BAR();
            if (tid == 0) set_flag(rw, F_O + ko, tag);
            TS(0, 11);
            wo_issue(wo, ct, 12, 20, wave, 8, lane);
        } else {
            wo_issue(wo, ct, 0, 20, wave, 8, lane);
            if (ko == -2 && wave == 0) {
                wait_flags(rw, (int)F_G1 + lane, (int)F_G1 + 64 + lane, tag);
                wait_flags(rw, (int)F_G1 + 128 + lane, (int)F_G1 + 192 + lane, tag);
                if (lane == 0) set_flag(rw, F_EPOCH, tag);
            }
        }
    } else {
        VMCNT(0); BAR();
        if (wave >= 4) wo_issue(wo, ct, 12, 20, wave - 4, 4, lane);
    }
    TS(0, 16);
    final_block(sm, wave, lane, tid, st, ct, x, out, rw, tag, ws);
    TS(0, 19);
}

extern "C" void kernel_launch(void* const* d_in, const int* in_sizes, int n_in, void* d_out, int out_size, void* d_ws, size_t ws_size, hipStream_t stream) {
    const u16* x = (const u16*)d_in[0]; const u16* wqkv = (const u16*)d_in[1]; const u16* wo = (const u16*)d_in[2];
    const u16* kin = (const u16*)d_in[4]; const u16* vin = (const u16*)d_in[5]; const u16* shk = (const u16*)d_in[6];
    const u16* hk = (const u16*)d_in[7]; const u16* sD = (const u16*)d_in[8]; const u16* gw = (const u16*)d_in[9];
    (void)hipFuncSetAttribute((const void*)fused_kernel, hipFuncAttributeMaxDynamicSharedMemorySize, LDS_BYTES);
    fused_kernel<<<256, 512, LDS_BYTES, stream>>>(x, wqkv, wo, kin, vin, shk, hk, sD, gw, (u16*)d_out, (char*)d_ws);
}
```

```cpp
#include <hip/hip_runtime.h>
#include <stdint.h>

typedef unsigned short u16;
typedef unsigned int u32;
typedef __bf16 bf16x8 __attribute__((ext_vector_type(8)));
typedef __bf16 bf16x2 __attribute__((ext_vector_type(2)));
typedef float f32x4 __attribute__((ext_vector_type(4)));
typedef float f32x2 __attribute__((ext_vector_type(2)));
typedef float f32x16 __attribute__((ext_vector_type(16)));
typedef u32 u32x4 __attribute__((ext_vector_type(4)));
typedef u32 u32x2 __attribute__((ext_vector_type(2)));
typedef short s16x4 __attribute__((ext_vector_type(4)));
#define LAS __attribute__((address_space(3)))
#define GAS __attribute__((address_space(1)))
#define DI __device__ __forceinline__
typedef __amdgpu_buffer_rsrc_t rsrc_t;

constexpr int S = 2048, D = 640;

constexpr u32 WS_UT = 0;
constexpr u32 WS_YT = WS_UT + 2621440;
constexpr u32 WS_PK0 = WS_YT + 2621440;
constexpr u32 WS_V0 = WS_PK0 + 2097152;
constexpr u32 WS_X = WS_V0 + 2097152;
constexpr u32 WS_P2 = WS_X + 1048576;
constexpr u32 WS_O = WS_P2 + 2621440;
constexpr u32 WS_G = WS_O + 2621440;
constexpr u32 WS_FLAG = WS_G + 8192;
constexpr u32 F_P0 = 0, F_G1 = 320, F_GATE = 560, F_O = 576, F_Y = 640, F_EPOCH = 800, F_TMO = 801;
constexpr u32 WS_STAMP = WS_FLAG + 832 * 64;
constexpr u32 WS_BYTES = WS_STAMP + 256 * 32 * 16;

#define TS(w, id) do {} while (0)

DI u32 pk2(float a, float b) { f32x2 t = {a, b}; return __builtin_bit_cast(u32, __builtin_convertvector(t, bf16x2)); }
DI float blo(u32 u) { return __uint_as_float(u << 16); }
DI float bhi(u32 u) { return __uint_as_float(u & 0xffff0000u); }
DI float rbf(float f) { return blo(pk2(f, f)); }
DI bf16x8 asbf(u32x4 v) { return __builtin_bit_cast(bf16x8, v); }
DI f32x4 mfma16(u32x4 a, u32x4 b, f32x4 c) { return __builtin_amdgcn_mfma_f32_16x16x32_bf16(asbf(a), asbf(b), c, 0, 0, 0); }
DI f32x16 mfma32(u32x4 a, u32x4 b, f32x16 c) { return __builtin_amdgcn_mfma_f32_32x32x16_bf16(asbf(a), asbf(b), c, 0, 0, 0); }

DI void glds16(const void* sbase, u32 voff, u32 lds) {
    asm volatile("s_mov_b32 m0, %2\n\ts_nop 0\n\tglobal_load_lds_dwordx4 %0, %1" :: "v"(voff), "s"(sbase), "s"(lds) : "memory");
}
DI void gld64(u32x2& d, const void* sbase, u32 voff) { asm volatile("global_load_dwordx2 %0, %1, %2" : "=v"(d) : "v"(voff), "s"(sbase) : "memory"); }
DI void gld128(u32x4& d, const void* sbase, u32 voff) { asm volatile("global_load_dwordx4 %0, %1, %2" : "=v"(d) : "v"(voff), "s"(sbase) : "memory"); }
DI void gld128c(u32x4& d, const void* sbase, u32 voff) { asm volatile("global_load_dwordx4 %0, %1, %2 sc1" : "=v"(d) : "v"(voff), "s"(sbase) : "memory"); }
DI void gld32c(u32& d, const void* sbase, u32 voff) { asm volatile("global_load_dword %0, %1, %2 sc1" : "=v"(d) : "v"(voff), "s"(sbase) : "memory"); }
#define PIN(x) asm volatile("" : "+v"(x))
#define SB() __builtin_amdgcn_sched_barrier(0)
DI u32x4 ld128c(rsrc_t r, u32 off) { return __builtin_amdgcn_raw_buffer_load_b128(r, off, 0, 16); }
DI u32 ld32c(rsrc_t r, u32 off) { return __builtin_amdgcn_raw_buffer_load_b32(r, off, 0, 16); }
DI void st128c(rsrc_t r, u32 off, u32x4 v) { __builtin_amdgcn_raw_buffer_store_b128(v, r, off, 0, 16); }
DI void st64c(rsrc_t r, u32 off, u32x2 v) { __builtin_amdgcn_raw_buffer_store_b64(v, r, off, 0, 16); }
DI void st32c(rsrc_t r, u32 off, u32 v) { __builtin_amdgcn_raw_buffer_store_b32(v, r, off, 0, 16); }
DI u32x4 lds128(LAS char* sm, u32 a) { return *(LAS u32x4*)(sm + a); }
DI u32x2 lds64(LAS char* sm, u32 a) { return *(LAS u32x2*)(sm + a); }
DI u32x2 ldstr(LAS char* sm, u32 a) { return __builtin_bit_cast(u32x2, __builtin_amdgcn_ds_read_tr16_b64_v4i16((LAS s16x4*)(sm + a))); }
#define VMCNT(n) asm volatile("s_waitcnt vmcnt(" #n ")" ::: "memory")
#define LGKM0() asm volatile("s_waitcnt lgkmcnt(0)" ::: "memory")
#define BAR() do { asm volatile("" ::: "memory"); __builtin_amdgcn_s_barrier(); asm volatile("" ::: "memory"); } while (0)
#define MEMF() asm volatile("" ::: "memory")

DI void set_flag(rsrc_t r, u32 idx, u32 tag) { st32c(r, WS_FLAG + idx * 64, tag); }
DI void wait_flags(rsrc_t r, int idx0, int idx1, u32 tag) {
    const unsigned long long t0 = __builtin_amdgcn_s_memrealtime();
    for (;;) {
        u32 a = tag, b = tag;
        if (idx0 >= 0) a = ld32c(r, WS_FLAG + idx0 * 64);
        if (idx1 >= 0) b = ld32c(r, WS_FLAG + idx1 * 64);
        if (__all(a == tag && b == tag)) break;
        if (__builtin_amdgcn_s_memrealtime() - t0 > 2000000ull) { set_flag(r, F_TMO, tag); break; }
        __builtin_amdgcn_s_sleep(1);
    }
    MEMF();
}
DI void publish_last(rsrc_t r, LAS char* sm, u32 cnt, u32 nw, u32 flag, u32 tag, int lane) {
    VMCNT(0);
    if (lane == 0) {
        const u32 old = __hip_atomic_fetch_add((LAS u32*)(sm + cnt), 1u, __ATOMIC_RELAXED, __HIP_MEMORY_SCOPE_WORKGROUP);
        if (old == nw - 1) set_flag(r, flag, tag);
    }
    MEMF();
}

DI u32 phi2(float a, float b) {
    const f32x2 t = {a, b}, s = t * 1.4426950408889634f;
    const float ea = __builtin_amdgcn_fmed3f(__builtin_amdgcn_exp2f(s.x), 0.f, 1.f), eb = __builtin_amdgcn_fmed3f(__builtin_amdgcn_exp2f(s.y), 0.f, 1.f);
    const float ma = __int_as_float(max(__float_as_int(a), 0)), mb = __int_as_float(max(__float_as_int(b), 0));
    const f32x2 r = ((f32x2){ea, eb} + (f32x2){ma, mb}) + 1e-6f;
    return pk2(r.x, r.y);
}

constexpr int K1_STAGE = 32768;
constexpr int P0_K = 131072, P0_V = P0_K + 9216, P0_OUT = P0_V + 8192;
constexpr int P0_K2 = 3 * K1_STAGE, P0_OUT2 = P0_K2 + 17408;
constexpr int L_CNT = 156672;
constexpr int CV_PITCH = 4160;
constexpr int CV_U = 4 * CV_PITCH;
constexpr int CV_WAVE = CV_U + 4608;
constexpr int CV_BASE = 65536;
constexpr int WO_HI = 65536;
constexpr int F_YY = 110592;
constexpr int F_Y2 = F_YY + 10240;
constexpr int KO_RED = 110592;
constexpr int KO_KV = KO_RED + 32768;
constexpr int RED_PITCH = 336;
constexpr int LDS_BYTES = 160 * 1024;
DI constexpr u32 wo_off(int kap) { return kap < 12 ? kap * 5120 : WO_HI + (kap - 12) * 5120; }

DI void p0_issue(const u16* kin, const u16* vin, int tile, int lw, int lane, u32 base) {
    const int cg = tile % 10, rg = tile / 10, s0 = rg * 64;
    const u32 col = cg * 128 + (lane & 7) * 16;
#pragma unroll
    for (int z = 0; z < 2; z++) {
        const int n = 2 * lw + z;
        int r = s0 - 4 + n * 8 + (lane >> 3); r = r < 0 ? 0 : r;
        glds16(kin, (u32)(r * 1280) + col, base + n * 1024);
        glds16(vin, (u32)((s0 + n * 8 + (lane >> 3)) * 1280) + col, base + 9216 + n * 1024);
    }
    if (lw == 0) {
        int r = s0 + 60 + (lane >> 3); r = r > S - 1 ? S - 1 : r;
        glds16(kin, (u32)(r * 1280) + col, base + 8 * 1024);
    }
}
DI void p0_compute(LAS char* sm, u32x2 w4, int tile, int wave, int lane, u32 base, u32 outb) {
    const int s0 = (tile / 10) * 64;
    const int G = wave * 4 + (lane >> 4), cb = G & 3, i = lane & 15, q = i >> 2, p = i & 3;
    const float w0 = blo(w4.x), w1 = bhi(w4.x), w2 = blo(w4.y), w3 = bhi(w4.y);
    const int ch = 16 * cb + i;
    u32x2 kq[2][3], vq[2][2];
#pragma unroll
    for (int z = 0; z < 2; z++) {
        const int o = (G >> 2) + 4 * z;
        const u32 ka = base + (8 * o + q) * 128 + 32 * cb + 8 * p, va = ka + 9216;
        kq[z][0] = ldstr(sm, ka); kq[z][1] = ldstr(sm, ka + 512); kq[z][2] = ldstr(sm, ka + 1024);
        vq[z][0] = ldstr(sm, va); vq[z][1] = ldstr(sm, va + 512);
    }
#pragma unroll
    for (int z = 0; z < 2; z++) {
        const int o = (G >> 2) + 4 * z;
        u32x2 k0 = kq[z][0]; const u32x2 k1 = kq[z][1], k2 = kq[z][2], v0 = vq[z][0], v1 = vq[z][1];
        if (s0 == 0 && o == 0) { k0.x = 0; k0.y = 0; }
        const float kk[12] = {blo(k0.x), bhi(k0.x), blo(k0.y), bhi(k0.y), blo(k1.x), bhi(k1.x), blo(k1.y), bhi(k1.y), blo(k2.x), bhi(k2.x), blo(k2.y), bhi(k2.y)};
        const float vv[8] = {blo(v0.x), bhi(v0.x), blo(v0.y), bhi(v0.y), blo(v1.x), bhi(v1.x), blo(v1.y), bhi(v1.y)};
        float u[8];
#pragma unroll
        for (int t = 0; t < 8; t++) {
            float ks = kk[t + 1] * w3;
            ks = fmaf(kk[t + 2], w2, ks); ks = fmaf(kk[t + 3], w1, ks); ks = fmaf(kk[t + 4], w0, ks);
            u[t] = ks * vv[t];
        }
        *(LAS u32x4*)(sm + outb + ch * 128 + ((o ^ (ch & 7)) * 16)) = (u32x4){pk2(u[0], u[1]), pk2(u[2], u[3]), pk2(u[4], u[5]), pk2(u[6], u[7])};
    }
}
DI void p0_store(LAS char* sm, rsrc_t rw, int tile, int t256, u32 outb) {
    const int cg = tile % 10, s0 = (tile / 10) * 64;
#pragma unroll
    for (int z = 0; z < 2; z++) {
        const int ch2 = (t256 >> 3) + 32 * z, o2 = t256 & 7;
        const u32x4 t4 = lds128(sm, outb + ch2 * 128 + ((o2 ^ (ch2 & 7)) * 16));
        st128c(rw, WS_UT + (cg * 64 + ch2) * 4096 + (s0 + 8 * o2) * 2, t4);
    }
}
DI void p0_weights(u32x2& w4, const u16* shk, int tile, int wave, int lane) {
    const int cg = tile % 10, cb = (wave * 4 + (lane >> 4)) & 3;
    gld64(w4, shk, (u32)((cg * 64 + 16 * cb + (lane & 15)) * 8));
}

struct K1Set { u32x4 xa[2][2]; u32x2 wl[2][2], wh[2][2]; };
struct K1Addr { u32 x[2][2]; u32 w[2][2]; };
template <bool QT, int WO, int XO>
DI void k1_phase(LAS char* sm, f32x16 (&acc)[2][2], const K1Set& cur, K1Set& nxt, const u32 (&xb)[2], const u32 (&wb)[2][2]) {
#pragma unroll
    for (int n = 0; n < 8; n++) {
        const int ks = n >> 2, i = (n >> 1) & 1, jj = n & 1;
        const u32x4 wf = {cur.wl[ks][jj].x, cur.wl[ks][jj].y, cur.wh[ks][jj].x, cur.wh[ks][jj].y};
        acc[i][jj] = QT ? mfma32(wf, cur.xa[ks][i], acc[i][jj]) : mfma32(cur.xa[ks][i], wf, acc[i][jj]);
        if (n == 0) { nxt.wl[0][0] = ldstr(sm, wb[0][0] + WO); nxt.wh[0][0] = ldstr(sm, wb[0][1] + WO); }
        if (n == 1) { nxt.xa[0][0] = lds128(sm, xb[0] + XO); nxt.wl[0][1] = ldstr(sm, wb[1][0] + WO); }
        if (n == 2) { nxt.wh[0][1] = ldstr(sm, wb[1][1] + WO); nxt.xa[0][1] = lds128(sm, xb[0] + XO + 4096); }
        if (n == 3) { nxt.wl[1][0] = ldstr(sm, wb[0][0] + WO + 4096); nxt.wh[1][0] = ldstr(sm, wb[0][1] + WO + 4096); }
        if (n == 4) { nxt.xa[1][0] = lds128(sm, xb[1] + XO); nxt.wl[1][1] = ldstr(sm, wb[1][0] + WO + 4096); }
        if (n == 5) { nxt.wh[1][1] = ldstr(sm, wb[1][1] + WO + 4096); nxt.xa[1][1] = lds128(sm, xb[1] + XO + 4096); }
        SB();
    }
}
template <bool QT, int ST>
DI void k1_chunk(LAS char* sm, f32x16 (&acc)[2][2], K1Set& s0, K1Set& s1, const K1Addr& a0, const K1Addr& a1, bool bar, bool drain) {
    constexpr int SN = (ST + 1) & 3;
    const K1Addr& ac = ST < 2 ? a0 : a1; const K1Addr& an = SN < 2 ? a0 : a1;
    k1_phase<QT, (ST & 1) * K1_STAGE + 8192, (ST & 1) * K1_STAGE + 16384>(sm, acc, s0, s1, ac.x[1], ac.w);
    if (drain) VMCNT(0);
    if (bar) { LGKM0(); BAR(); }
    k1_phase<QT, (SN & 1) * K1_STAGE, (SN & 1) * K1_STAGE + 16384>(sm, acc, s1, s0, an.x[0], an.w);
}
template <bool QT>
DI void k1_loop(LAS char* sm, const K1Addr& a0, f32x16 (&acc)[2][2], rsrc_t rw, int tile, int tile2, u32 tag, int wave, int lane) {
    K1Addr a1;
#pragma unroll
    for (int i = 0; i < 2; i++)
#pragma unroll
        for (int z = 0; z < 2; z++) { a1.x[i][z] = a0.x[i][z] + 65536; a1.w[i][z] = a0.w[i][z] + 65536; }
    K1Set s0, s1;
#pragma unroll
    for (int ks = 0; ks < 2; ks++)
#pragma unroll
        for (int jj = 0; jj < 2; jj++) {
            s0.wl[ks][jj] = ldstr(sm, a0.w[jj][0] + ks * 4096); s0.wh[ks][jj] = ldstr(sm, a0.w[jj][1] + ks * 4096);
            s0.xa[ks][jj] = lds128(sm, a0.x[0][ks] + 16384 + jj * 4096);
        }
    SB();
#pragma unroll 1
    for (int c = 0; c < 8; c += 4) {
        k1_chunk<QT, 0>(sm, acc, s0, s1, a0, a1, true, false);
        k1_chunk<QT, 1>(sm, acc, s0, s1, a0, a1, true, false);
        k1_chunk<QT, 2>(sm, acc, s0, s1, a0, a1, true, false);
        k1_chunk<QT, 3>(sm, acc, s0, s1, a0, a1, true, c == 0);
        if (c == 0 && wave == 0 && lane < 2 && (lane == 0 || tile2 >= 0)) set_flag(rw, F_P0 + (lane == 0 ? tile : tile2), tag);
    }
    k1_chunk<QT, 0>(sm, acc, s0, s1, a0, a1, true, false);
    k1_chunk<QT, 1>(sm, acc, s0, s1, a0, a1, false, false);
}

DI void wo_issue(const u16* wo, int ct, int k0, int k1, int w, int nw, int lane) {
    u32 vo[5];
#pragma unroll
    for (int i = 0; i < 5; i++) { const int n = 64 * i + lane, rho = n / 10, c = n - 10 * rho, row = (rho & 19) | ((rho & 4) << 1) | ((rho & 8) >> 1); vo[i] = (u32)(row * 1280 + ct * 160 + c * 16); }
#pragma unroll 1
    for (int kap = k0 + w; kap < k1; kap += nw) {
        const char* src = (const char*)wo + (size_t)kap * 40960;
        const u32 dst = kap < 12 ? kap * 5120 : WO_HI + (kap - 12) * 5120;
#pragma unroll
        for (int i = 0; i < 5; i++) glds16(src, vo[i], dst + i * 1024);
    }
}

struct CvSet { u32x2 fl[8], fh[8]; u32x4 g[8]; };
DI void cv_load(LAS char* sm, CvSet& s, u32 fa, u32 ga) {
#pragma unroll
    for (int k1 = 0; k1 < 8; k1++) { s.fl[k1] = ldstr(sm, fa + 512 * k1); s.fh[k1] = ldstr(sm, fa + 512 * k1 - 8); s.g[k1] = lds128(sm, ga + 512 * k1); }
}
DI void cv_phase(LAS char* sm, f32x4 (&Y)[8], const CvSet& cur, CvSet& nxt, u32 fa, u32 ga) {
    int n = 0;
#pragma unroll
    for (int e = 0; e < 8; e++)
#pragma unroll
        for (int k1 = 0; k1 + e < 8; k1++) {
            const u32x4 F = {cur.fl[k1].x, cur.fl[k1].y, cur.fh[k1].x, cur.fh[k1].y};
            Y[k1 + e] = mfma16(F, cur.g[e], Y[k1 + e]);
            if (n == 0) nxt.g[0] = lds128(sm, ga);
            else if (n <= 16) { const int r = (n - 1) >> 1; if ((n - 1) & 1) nxt.fh[r] = ldstr(sm, fa + 512 * r - 8); else nxt.fl[r] = ldstr(sm, fa + 512 * r); }
            else if (n < 24) nxt.g[n - 16] = lds128(sm, ga + 512 * (n - 16));
            n++;
            SB();
        }
}
struct CvIn { u32x4 hd[4]; u32x2 hm[4]; u32x4 u[4]; };
DI void conv_fetch_h(CvIn& in, const u16* hk, int c, int lane) {
    const char* hb = (const char*)hk + (size_t)c * 4096;
#pragma unroll
    for (int z = 0; z < 4; z++) {
        const int i = lane + 64 * z;
        gld128(in.hd[z], hb, (u32)(i * 16));
        gld64(in.hm[z], hb, (u32)(i > 0 ? i * 16 - 8 : 0));
    }
}
DI void conv_run(LAS char* sm0, CvIn& in, float Dc, int w, int lane, int c, rsrc_t rw, char* ws) {
    LAS char* sm = sm0 + CV_BASE + w * CV_WAVE;
    VMCNT(4);
#pragma unroll
    for (int z = 0; z < 4; z++) { PIN(in.hd[z]); PIN(in.hm[z]); }
    if (lane < 32) *(LAS u32x4*)(sm + CV_U + lane * 16) = (u32x4){0, 0, 0, 0};
    if (lane < 8) *(LAS u32x4*)(sm + (lane >> 1) * CV_PITCH + (lane & 1) * 16) = (u32x4){0, 0, 0, 0};
#pragma unroll
    for (int z = 0; z < 4; z++) {
        const int i = lane + 64 * z;
        u32 m2 = in.hm[z].x, m1 = in.hm[z].y;
        if (i == 0) { m2 = 0; m1 = 0; }
        const u32 d0 = in.hd[z].x, d1 = in.hd[z].y, d2 = in.hd[z].z, d3 = in.hd[z].w;
        const u32 om2 = __builtin_amdgcn_alignbit(m1, m2, 16), om1 = __builtin_amdgcn_alignbit(d0, m1, 16), o0 = __builtin_amdgcn_alignbit(d1, d0, 16),
                  o1 = __builtin_amdgcn_alignbit(d2, d1, 16), o2 = __builtin_amdgcn_alignbit(d3, d2, 16);
        LAS char* p = sm + 32 + i * 16;
        *(LAS u32x4*)(p) = (u32x4){d0, d1, d2, d3};
        *(LAS u32x4*)(p + CV_PITCH) = (u32x4){om1, o0, o1, o2};
        *(LAS u32x4*)(p + 2 * CV_PITCH) = (u32x4){m1, d0, d1, d2};
        *(LAS u32x4*)(p + 3 * CV_PITCH) = (u32x4){om2, om1, o0, o1};
    }
    VMCNT(0);
#pragma unroll
    for (int z = 0; z < 4; z++) { PIN(in.u[z]); *(LAS u32x4*)(sm + CV_U + 512 + z * 1024 + lane * 16) = in.u[z]; }
    LGKM0();
    TS(4, 9);
    const int fr = lane & 15, g = lane >> 4, q = fr >> 2, p = lane & 3;
    const u32 fa = q * CV_PITCH + 2 * (32 - 8 * g + 4 * p);
    const u32 ga = CV_U + 480 + 32 * fr + 16 * g;
    f32x4 Y[8];
#pragma unroll
    for (int i = 0; i < 8; i++) Y[i] = (f32x4){0, 0, 0, 0};
    CvSet A, B;
    cv_load(sm, A, fa, ga);
    SB();
#pragma unroll 1
    for (int k0 = 0; k0 < 8; k0 += 2) {
        cv_phase(sm, Y, A, B, fa + 64 * (k0 + 1), ga - 64 * (k0 + 1));
        cv_phase(sm, Y, B, A, fa + 64 * (k0 + 2), ga - 64 * (k0 + 2));
    }
    TS(4, 10);
    {
        u32x2 u4[8];
#pragma unroll
        for (int ib = 0; ib < 8; ib++) u4[ib] = lds64(sm, CV_U + 512 + 512 * ib + 32 * fr + 8 * g);
#pragma unroll
        for (int ib = 0; ib < 8; ib++) {
            const u32x2 r = {pk2(fmaf(blo(u4[ib].x), Dc, Y[ib][0]), fmaf(bhi(u4[ib].x), Dc, Y[ib][1])), pk2(fmaf(blo(u4[ib].y), Dc, Y[ib][2]), fmaf(bhi(u4[ib].y), Dc, Y[ib][3]))};
            *(LAS u32x2*)(sm + 512 * ib + 32 * fr + 8 * g) = r;
        }
    }
    LGKM0();
    u32x4 y8[4];
#pragma unroll
    for (int z = 0; z < 4; z++) y8[z] = lds128(sm, z * 1024 + lane * 16);
#pragma unroll
    for (int z = 0; z < 4; z++) st128c(rw, WS_YT + c * 4096 + z * 1024 + lane * 16, y8[z]);
}

DI void kvo_block(LAS char* sm, int wave, int lane, int h, int half, rsrc_t rw, const u16* wo, int ct, char* ws) {
    const int fr = lane & 15, g = lane >> 4;
    const u32 pk0 = WS_PK0 + (h * 64 + 8 * wave) * 1024 + lane * 16, v0 = pk0 + (WS_V0 - WS_PK0);
    const u32 xx = WS_X + (h * 64 + 8 * wave) * 512 + (g * 8 + (fr & 7)) * 16;
    u32x4 A[8], B[8], X[8], Q[8];
#pragma unroll
    for (int i = 0; i < 8; i++) { A[i] = ld128c(rw, pk0 + i * 1024); B[i] = ld128c(rw, v0 + i * 1024); X[i] = ld128c(rw, xx + i * 512); }
    const int odd = h & 1, pr = h >> 1;
    const int u5 = (g < 3 ? g : 2) + 2 * odd;
    const int sb0 = half * 64 + wave * 8;
    const u32 pq = WS_P2 + (((pr * 128 + sb0) * 5 + u5) * 128 + fr * 8) * 2;
#pragma unroll
    for (int i = 0; i < 8; i++) Q[i] = ld128c(rw, pq + i * 1280);
    MEMF(); SB();
    f32x4 a00 = {0, 0, 0, 0}, a0x = a00, ax0 = a00, axx = a00;
    const u32 cst = fr == 8 ? 0x3F803F80u : 0u;
#pragma unroll
    for (int i = 0; i < 8; i++) {
        u32x4 xv = X[i];
        if (fr >= 8) xv = (u32x4){cst, cst, cst, cst};
        a00 = mfma16(A[i], B[i], a00); a0x = mfma16(A[i], xv, a0x); ax0 = mfma16(xv, B[i], ax0); axx = mfma16(xv, xv, axx);
    }
    TS(0, 9);
    *(LAS f32x4*)(sm + KO_RED + ((wave * 4 + 0) * 64 + lane) * 16) = a00;
    *(LAS f32x4*)(sm + KO_RED + ((wave * 4 + 1) * 64 + lane) * 16) = a0x;
    *(LAS f32x4*)(sm + KO_RED + ((wave * 4 + 2) * 64 + lane) * 16) = ax0;
    *(LAS f32x4*)(sm + KO_RED + ((wave * 4 + 3) * 64 + lane) * 16) = axx;
    if (wave >= 4 && (wave - 4) * 64 + lane < 189) *(LAS u32x2*)(sm + KO_KV + ((wave - 4) * 64 + lane) * 8) = (u32x2){0, 0};
    LGKM0(); BAR();
    if (wave < 4) {
        const int tile = wave;
        f32x4 s = *(LAS f32x4*)(sm + KO_RED + (tile * 64 + lane) * 16);
#pragma unroll
        for (int w2 = 1; w2 < 8; w2++) s += *(LAS f32x4*)(sm + KO_RED + ((w2 * 4 + tile) * 64 + lane) * 16);
        const int e = (tile & 1) ? 12 + fr : fr;
        const bool colok = (tile & 1) ? (fr >= 4 && fr <= 8) : true;
        const bool rowok = (tile & 2) ? (g == 0) : true;
        const int d0 = (tile & 2) ? 16 : 4 * g;
        if (colok && rowok) *(LAS u32x2*)(sm + KO_KV + e * 72 + (d0 + 4) * 2) = (u32x2){pk2(s[0], s[1]), pk2(s[2], s[3])};
    }
    LGKM0(); BAR();
    u32x4 A0, A1[4], A2;
    {
        const u32 dofs = KO_KV + (8 * g - 4 * odd + 4) * 2;
        const u32 r0 = (4 * odd + fr) * 72, r1 = ((odd ? 0 : 16) + (fr & 3)) * 72;
        const u32x2 p0 = lds64(sm, dofs + r0), p1 = lds64(sm, dofs + r0 + 8);
        const u32x2 q0 = lds64(sm, dofs + r1), q1 = lds64(sm, dofs + r1 + 8);
        const u32x2 z0 = lds64(sm, dofs + 20 * 72), z1 = lds64(sm, dofs + 20 * 72 + 8);
        A0 = (u32x4){p0.x, p0.y, p1.x, p1.y}; A2 = (u32x4){z0.x, z0.y, z1.x, z1.y};
#pragma unroll
        for (int z = 0; z < 4; z++) { const bool m = (fr >> 2) == z; A1[z] = (u32x4){m ? q0.x : 0u, m ? q0.y : 0u, m ? q1.x : 0u, m ? q1.y : 0u}; }
    }
    TS(0, 10);
    const int U0 = (5 * h + odd) >> 1;
    const int Uw = U0 + (g >> 1);
    const u32 offA = WS_O + (((Uw >> 2) * 4 + (Uw & 3)) * 128 + fr * 8) * 2 + (g & 1) * 20480;
    const int cgx = 5 * h + (odd ? 0 : 4), Ux = cgx >> 1;
    const u32 offX = WS_O + (((Ux >> 2) * 4 + (Ux & 3)) * 128 + fr * 8 + (cgx & 1) * 4) * 2;
    const f32x4 zero = {0, 0, 0, 0};
#pragma unroll
    for (int i4 = 0; i4 < 2; i4++) {
        f32x4 n1 = zero; float rden[4]; u32x2 ov[4];
#pragma unroll
        for (int z = 0; z < 4; z++) {
            const int i = 4 * i4 + z;
            const f32x4 n0 = mfma16(A0, Q[i], zero);
            const f32x4 dn = mfma16(A2, Q[i], zero);
            n1 = mfma16(A1[z], Q[i], n1);
            const float rd = __builtin_amdgcn_rcpf(dn[0] + 1e-6f);
            rden[z] = rd;
            ov[z] = (u32x2){pk2(n0[0] * rd, n0[1] * rd), pk2(n0[2] * rd, n0[3] * rd)};
        }
#pragma unroll
        for (int z = 0; z < 4; z += 2) {
            auto rx = __builtin_amdgcn_permlane16_swap(ov[z].x, ov[z + 1].x, false, false);
            auto ry = __builtin_amdgcn_permlane16_swap(ov[z].y, ov[z + 1].y, false, false);
            st128c(rw, offA + (sb0 + 4 * i4 + z) * 20480, (u32x4){rx[0], ry[0], rx[1], ry[1]});
        }
        const float rd = g == 0 ? rden[0] : g == 1 ? rden[1] : g == 2 ? rden[2] : rden[3];
        st64c(rw, offX + (sb0 + 4 * i4 + g) * 20480, (u32x2){pk2(n1[0] * rd, n1[1] * rd), pk2(n1[2] * rd, n1[3] * rd)});
    }
}

DI void gate_block(int wave, int lane, int m, const u16* x, const u16* gw, rsrc_t rw) {
    const int sub = lane & 15, r4 = lane >> 4;
    u32x4 gq[5], xr[4][5];
#pragma unroll
    for (int jj = 0; jj < 5; jj++) gq[jj] = *(const GAS u32x4*)((const GAS char*)gw + (sub + 16 * jj) * 16);
#pragma unroll
    for (int it = 0; it < 4; it++)
#pragma unroll
        for (int jj = 0; jj < 5; jj++) xr[it][jj] = *(const GAS u32x4*)((const GAS char*)x + (size_t)(128 * m + 16 * wave + 4 * it + r4) * 1280 + (sub + 16 * jj) * 16);
    MEMF(); SB();
#pragma unroll
    for (int it = 0; it < 4; it++) {
        float a = 0.f;
#pragma unroll
        for (int jj = 0; jj < 5; jj++)
#pragma unroll
            for (int e = 0; e < 4; e++) { a = fmaf(blo(xr[it][jj][e]), blo(gq[jj][e]), a); a = fmaf(bhi(xr[it][jj][e]), bhi(gq[jj][e]), a); }
        a += __shfl_xor(a, 1); a += __shfl_xor(a, 2); a += __shfl_xor(a, 4); a += __shfl_xor(a, 8);
        a = rbf(a);
        const float g = rbf(__builtin_amdgcn_rcpf(1.f + __builtin_amdgcn_exp2f(-1.4426950408889634f * a)));
        if (sub == 0) st32c(rw, WS_G + (128 * m + 16 * wave + 4 * it + r4) * 4, __float_as_uint(g));
    }
}

DI void final_block(LAS char* sm, int wave, int lane, int tid, int st, int ct, const u16* x, u16* out, rsrc_t rw, u32 tag, char* ws) {
    const int fr = lane & 15, g = lane >> 4;
    u32x4 xv[2];
#pragma unroll
    for (int r = 0; r < 2; r++) {
        int it = tid + 512 * r; it = it < 640 ? it : 639;
        const int s = it / 10, u = it - 10 * s;
        xv[r] = *(const GAS u32x4*)((const GAS char*)x + (size_t)(st * 64 + s) * 1280 + ct * 160 + u * 16);
    }
    if (wave == 0) {
        const int idx = lane < 32 ? (int)F_O + 2 * lane + (st >> 4) : lane < 52 ? (int)F_Y + 20 * ct + (lane - 32) : lane == 52 ? (int)F_GATE + (st >> 1) : -1;
        wait_flags(rw, idx, -1, tag);
    }
    VMCNT(0); BAR();
    TS(0, 17);
    u32 gv[2];
#pragma unroll
    for (int r = 0; r < 2; r++) { int it = tid + 512 * r; it = it < 640 ? it : 639; gv[r] = ld32c(rw, WS_G + (st * 64 + it / 10) * 4); }
    if (wave < 4) {
        const int kh = wave & 1, sh = wave >> 1;
        const u32 ob = WS_O + ((st * 4 + 2 * sh) * 20 + 10 * kh) * 1024 + lane * 16;
        u32x4 B[2][10];
#pragma unroll
        for (int kl = 0; kl < 10; kl++) { B[0][kl] = ld128c(rw, ob + kl * 1024); B[1][kl] = ld128c(rw, ob + 20480 + kl * 1024); }
        MEMF(); SB();
        f32x4 acc[5][2];
#pragma unroll
        for (int cb = 0; cb < 5; cb++) { acc[cb][0] = (f32x4){0, 0, 0, 0}; acc[cb][1] = (f32x4){0, 0, 0, 0}; }
        const u32 aoff = (16 * (g >> 1) + 4 * (g & 1) + (fr >> 2)) * 160 + (lane & 3) * 8;
        u32x2 al[2][5], ah[2][5];
#define F_READ(buf, kl) do { const u32 b0_ = kh ? wo_off(10 + (kl)) : wo_off(kl); \
            _Pragma("unroll") for (int cb = 0; cb < 5; cb++) { al[buf][cb] = ldstr(sm, b0_ + aoff + cb * 32); ah[buf][cb] = ldstr(sm, b0_ + aoff + cb * 32 + 1280); } } while (0)
        F_READ(0, 0);
#pragma unroll
        for (int kl = 0; kl < 10; kl++) {
            if (kl < 9) F_READ((kl + 1) & 1, kl + 1);
            SB();
#pragma unroll
            for (int cb = 0; cb < 5; cb++) {
                const u32x4 a = {al[kl & 1][cb].x, al[kl & 1][cb].y, ah[kl & 1][cb].x, ah[kl & 1][cb].y};
                acc[cb][0] = mfma16(a, B[0][kl], acc[cb][0]); acc[cb][1] = mfma16(a, B[1][kl], acc[cb][1]);
            }
            SB();
        }
        TS(0, 18);
        LGKM0(); BAR();
#pragma unroll
        for (int cb = 0; cb < 5; cb++)
#pragma unroll
            for (int z = 0; z < 2; z++)
                *(LAS f32x4*)(sm + kh * (64 * RED_PITCH) + (16 * (2 * sh + z) + fr) * RED_PITCH + (16 * cb + 4 * g) * 4) = acc[cb][z];
    } else {
        const int t = tid - 256;
        u32x4 yv[3];
#pragma unroll
        for (int r = 0; r < 3; r++) { int id = t + 256 * r; id = id < 640 ? id : 639; yv[r] = ld128c(rw, WS_YT + (80 * ct + (id >> 3)) * 4096 + st * 128 + (id & 7) * 16); }
#pragma unroll
        for (int r = 0; r < 3; r++) { const int id = t + 256 * r; if (id < 640) *(LAS u32x4*)(sm + F_YY + id * 16) = yv[r]; }
        LGKM0(); BAR();
        const int sb = wave - 4, q = fr >> 2, p = lane & 3;
        u32x2 tt[5];
#pragma unroll
        for (int cb = 0; cb < 5; cb++) tt[cb] = ldstr(sm, F_YY + (16 * cb + 4 * g + q) * 128 + (16 * sb + 4 * p) * 2);
#pragma unroll
        for (int cb = 0; cb < 5; cb++) *(LAS u32x2*)(sm + F_Y2 + (16 * sb + fr) * 176 + (16 * cb + 4 * g) * 2) = tt[cb];
    }
    LGKM0(); BAR();
#pragma unroll
    for (int r = 0; r < 2; r++) {
        const int it = tid + 512 * r;
        if (it < 640) {
            const int s = it / 10, u = it - 10 * s;
            const u32 ra = s * RED_PITCH + u * 32;
            const f32x4 p0 = *(LAS f32x4*)(sm + ra), p1 = *(LAS f32x4*)(sm + ra + 16), q0 = *(LAS f32x4*)(sm + 64 * RED_PITCH + ra), q1 = *(LAS f32x4*)(sm + 64 * RED_PITCH + ra + 16);
            const u32x4 y8 = lds128(sm, F_Y2 + s * 176 + u * 16);
            const float gg = __uint_as_float(gv[r]), omg = 1.f - gg;
            float o[8];
#pragma unroll
            for (int e = 0; e < 4; e++) {
                const float a0 = (e < 2 ? p0[2 * e] + q0[2 * e] : p1[2 * e - 4] + q1[2 * e - 4]), a1 = (e < 2 ? p0[2 * e + 1] + q0[2 * e + 1] : p1[2 * e - 3] + q1[2 * e - 3]);
                const float b0 = omg * blo(xv[r][e]), b1 = omg * bhi(xv[r][e]);
                o[2 * e] = fmaf(b0, blo(y8[e]), gg * a0); o[2 * e + 1] = fmaf(b1, bhi(y8[e]), gg * a1);
            }
            *(GAS u32x4*)((GAS char*)out + (size_t)(st * 64 + s) * 1280 + ct * 160 + u * 16) = (u32x4){pk2(o[0], o[1]), pk2(o[2], o[3]), pk2(o[4], o[5]), pk2(o[6], o[7])};
        }
    }
}

__global__ __launch_bounds__(512, 2) void fused_kernel(const u16* __restrict__ x, const u16* __restrict__ w, const u16* __restrict__ wo, const u16* __restrict__ kin,
                                                        const u16* __restrict__ vin, const u16* __restrict__ shk, const u16* __restrict__ hk, const u16* __restrict__ sD,
                                                        const u16* __restrict__ gw, u16* __restrict__ out, char* ws) {
    extern __shared__ __attribute__((aligned(16))) char smem[];
    LAS char* sm = (LAS char*)smem;
    const int tid = threadIdx.x, lane = tid & 63, wave = __builtin_amdgcn_readfirstlane(tid >> 6);
    const int b = blockIdx.x, xcd = b & 7, j = b >> 3;
    const rsrc_t rw = __builtin_amdgcn_make_buffer_rsrc(ws, 0, WS_BYTES, 0x00020000);
    u32 tag;
    asm volatile("s_load_dword %0, %1, 0x0\n\ts_waitcnt lgkmcnt(0)" : "=s"(tag) : "s"(ws + WS_FLAG + F_EPOCH * 64) : "memory");
    tag += 1u;
    if (tid < 4) *(LAS u32*)(sm + L_CNT + tid * 4) = 0u;
    TS(0, 0);
    if (tid == 0) { const unsigned long long t0_ = __builtin_amdgcn_s_memrealtime(); st64c(rw, WS_STAMP + blockIdx.x * 8, __builtin_bit_cast(u32x2, t0_)); }
    const int st = 4 * xcd + (j >> 3), ct = j & 7;
    int ko = -1;
    bool is_conv = false;

    if (j >= 30) {
        const int m = xcd * 2 + (j - 30);
        ko = m;
        gate_block(wave, lane, m, x, gw, rw);
        VMCNT(0); LGKM0(); BAR();
        if (tid == 0) set_flag(rw, F_GATE + m, tag);
        TS(0, 3);
    } else {
        const int rt = 2 * xcd + j / 15, cbk = j % 15, tile = xcd * 30 + j;
        is_conv = cbk < 10;
        int tile2 = -1;
        if (!is_conv) { const int vi = rt * 5 + (cbk - 10); ko = vi < 48 ? 16 + vi : -2 - (vi - 48); tile2 = 240 + vi; }
        if (wave >= 4) {
            const int lw = wave - 4;
            p0_issue(kin, vin, tile, lw, lane, P0_K);
            if (tile2 >= 0) p0_issue(kin, vin, tile2, lw, lane, P0_K2);
            u32 vx[4], vw[4];
#pragma unroll
            for (int n2 = 0; n2 < 4; n2++) {
                const int n = 4 * lw + n2;
                const int row = 8 * n + (lane >> 3), ch = (lane & 7) ^ ((row >> 1) & 7);
                vx[n2] = (u32)((rt * 128 + row) * 1280 + ch * 16);
                const int kr = 4 * n + (lane >> 4), f = ((kr & 3) << 2) | ((kr >> 2) & 3), cw = (lane & 15) ^ f;
                vw[n2] = (u32)(kr * 3840 + cbk * 256 + cw * 16);
            }
#define K1_ISSUE(c) do { const u32 st_ = ((c) & 3) * K1_STAGE + 4 * lw * 1024; const char* xb_ = (const char*)x + (c) * 128; const char* wp_ = (const char*)w + (size_t)(c) * 64 * 3840; \
            _Pragma("unroll") for (int n2 = 0; n2 < 4; n2++) { glds16(xb_, vx[n2], st_ + 16384 + n2 * 1024); glds16(wp_, vw[n2], st_ + n2 * 1024); } } while (0)
            K1_ISSUE(0); K1_ISSUE(1);
            VMCNT(16); BAR();
            K1_ISSUE(2);
            VMCNT(16); BAR();
            VMCNT(8); BAR();
            K1_ISSUE(3); K1_ISSUE(4);
#pragma unroll 1
            for (int c = 1; c < 6; c++) {
                VMCNT(16); BAR();
                K1_ISSUE(c + 4);
            }
            const int cv = rt * 10 + cbk, c = 4 * cv + lw;
            if (is_conv) {
                CvIn in; u32 fl;
                gld32c(fl, ws, WS_FLAG + (F_P0 + (lane & 31) * 10 + (cv >> 4)) * 64);
                conv_fetch_h(in, hk, c, lane);
                VMCNT(25); BAR();
                VMCNT(17); BAR();
                VMCNT(8); PIN(fl);
                if (!__all(fl == tag)) wait_flags(rw, F_P0 + (lane & 31) * 10 + (cv >> 4), -1, tag);
#pragma unroll
                for (int z = 0; z < 4; z++) gld128c(in.u[z], ws, WS_UT + c * 4096 + z * 1024 + lane * 16);
                BAR();
                TS(4, 8);
                const float Dc = blo((u32)sD[c]);
                conv_run(sm, in, Dc, lw, lane, c, rw, ws);
                publish_last(rw, sm, L_CNT + 4, 4, F_Y + cv, tag, lane);
                TS(4, 11);
            } else {
                VMCNT(16); BAR();
                VMCNT(8); BAR();
                VMCNT(0); BAR();
            }
        } else {
            u32x2 w4, w4b; p0_weights(w4, shk, tile, wave, lane);
            p0_weights(w4b, shk, tile2 >= 0 ? tile2 : tile, wave, lane);
            const int wm = wave >> 1, wn = wave & 1, r32 = lane & 31, hh = lane >> 5;
            K1Addr a0;
#pragma unroll
            for (int ks = 0; ks < 2; ks++) {
                a0.x[0][ks] = (64 * wm + r32) * 128 + (((2 * ks + hh) ^ ((r32 >> 1) & 7)) * 16);
                a0.x[1][ks] = a0.x[0][ks] ^ 64;
            }
#pragma unroll
            for (int jj = 0; jj < 2; jj++)
#pragma unroll
                for (int t = 0; t < 2; t++) {
                    const int blk = (lane >> 4) & 1, q = (lane & 15) >> 2, p = lane & 3, row = 8 * hh + 4 * t + q, ch = 8 * wn + 4 * jj + 2 * blk + (p >> 1);
                    const int f = ((row & 3) << 2) | ((row >> 2) & 3);
                    a0.w[jj][t] = row * 256 + ((ch ^ f) * 16) + 8 * (p & 1);
                }
            f32x16 acc[2][2];
#pragma unroll
            for (int i = 0; i < 2; i++)
#pragma unroll
                for (int jj = 0; jj < 2; jj++)
#pragma unroll
                    for (int e = 0; e < 16; e++) acc[i][jj][e] = 0.f;
            VMCNT(0); PIN(w4); PIN(w4b); BAR();
            p0_compute(sm, w4, tile, wave, lane, P0_K, P0_OUT);
            if (tile2 >= 0) p0_compute(sm, w4b, tile2, wave, lane, P0_K2, P0_OUT2);
            LGKM0(); BAR();
            p0_store(sm, rw, tile, tid, P0_OUT);
            if (tile2 >= 0) p0_store(sm, rw, tile2, tid, P0_OUT2);
            TS(0, 1);
            if (cbk < 5) {
                k1_loop<true>(sm, a0, acc, rw, tile, tile2, tag, wave, lane);
                TS(0, 2);
#pragma unroll
                for (int jj = 0; jj < 2; jj++)
#pragma unroll
                    for (int ap = 0; ap < 2; ap++) {
                        const int U = 16 * cbk + 8 * wn + 4 * jj + 2 * ap + hh, pr = U / 5, u5 = U - 5 * pr;
                        const u32 dst = WS_P2 + (((pr * 128 + rt * 8 + 4 * wm + (r32 >> 4)) * 5 + u5) * 128 + (r32 & 15) * 8) * 2;
#pragma unroll
                        for (int i = 0; i < 2; i++) {
                            const f32x16& a = acc[i][jj];
                            const u32 x0 = phi2(a[8 * ap], a[8 * ap + 1]), y0 = phi2(a[8 * ap + 2], a[8 * ap + 3]);
                            const u32 x1 = phi2(a[8 * ap + 4], a[8 * ap + 5]), y1 = phi2(a[8 * ap + 6], a[8 * ap + 7]);
                            auto rx = __builtin_amdgcn_permlane32_swap(x0, x1, false, false);
                            auto ry = __builtin_amdgcn_permlane32_swap(y0, y1, false, false);
                            st128c(rw, dst + i * 2560, (u32x4){rx[0], ry[0], rx[1], ry[1]});
                        }
                    }
            } else {
                k1_loop<false>(sm, a0, acc, rw, tile, tile2, tag, wave, lane);
                TS(0, 2);
                const bool isk = cbk < 10;
                const int j5 = isk ? cbk - 5 : cbk - 10;
                u32 dst[2], gstep[2];
#pragma unroll
                for (int jj = 0; jj < 2; jj++) {
                    const int cc = 128 * j5 + 64 * wn + 32 * jj + r32, h = cc / 20, d = cc - 20 * h;
                    const int sb32 = rt * 4 + 2 * wm;
                    const bool lo = d < 16;
                    dst[jj] = lo ? (isk ? WS_PK0 : WS_V0) + (((h * 64 + sb32) * 4 + hh) * 128 + d * 8) * 2
                                 : WS_X + (((h * 64 + sb32) * 4 + hh) * 64 + (d - 16 + (isk ? 0 : 4)) * 8) * 2;
                    gstep[jj] = lo ? 512 : 256;
                }
                if (isk) {
#pragma unroll
                    for (int jj = 0; jj < 2; jj++)
#pragma unroll
                        for (int i = 0; i < 2; i++)
#pragma unroll
                            for (int z = 0; z < 2; z++) {
                                const f32x16& a = acc[i][jj];
                                st128c(rw, dst[jj] + (2 * i + z) * gstep[jj],
                                       (u32x4){phi2(a[8 * z], a[8 * z + 1]), phi2(a[8 * z + 2], a[8 * z + 3]), phi2(a[8 * z + 4], a[8 * z + 5]), phi2(a[8 * z + 6], a[8 * z + 7])});
                            }
                } else {
#pragma unroll
                    for (int jj = 0; jj < 2; jj++)
#pragma unroll
                        for (int i = 0; i < 2; i++)
#pragma unroll
                            for (int z = 0; z < 2; z++) {
                                const f32x16& a = acc[i][jj];
                                st128c(rw, dst[jj] + (2 * i + z) * gstep[jj],
                                       (u32x4){pk2(a[8 * z], a[8 * z + 1]), pk2(a[8 * z + 2], a[8 * z + 3]), pk2(a[8 * z + 4], a[8 * z + 5]), pk2(a[8 * z + 6], a[8 * z + 7])});
                            }
                }
            }
            LGKM0();
            publish_last(rw, sm, L_CNT, 4, F_G1 + tile, tag, lane);
            TS(0, 3);
            if (is_conv) {
                while (*(volatile LAS u32*)(sm + L_CNT) < 4u) __builtin_amdgcn_s_sleep(1);
                MEMF();
                wo_issue(wo, ct, 0, 12, wave, 4, lane);
            }
        }
    }
    if (!is_conv) {
        LGKM0(); BAR();
        if (ko >= 0) {
            const int h = ko >> 1, half = ko & 1;
            wo_issue(wo, ct, 0, 12, wave, 8, lane);
            if (wave == 0) {
                const int j0 = (20 * h) >> 7, j1 = (20 * h + 19) >> 7;
                const int l16 = lane & 15, sel = lane >> 4;
                const int i0 = (int)F_G1 + 15 * l16 + (sel < 2 ? 5 : 10) + ((sel & 1) ? j1 : j0);
                const int i1 = lane < 16 ? (int)F_G1 + 15 * (8 * half + (lane & 7)) + (lane < 8 ? j0 : j1) : -1;
                wait_flags(rw, i0, i1, tag);
            }
            BAR();
            TS(0, 8);
            kvo_block(sm, wave, lane, h, half, rw, wo, ct, ws);
            VMCNT(0); BAR();
            if (tid == 0) set_flag(rw, F_O + ko, tag);
            TS(0, 11);
            wo_issue(wo, ct, 12, 20, wave, 8, lane);
        } else {
            wo_issue(wo, ct, 0, 20, wave, 8, lane);
            if (ko == -2 && wave == 0) {
                wait_flags(rw, (int)F_G1 + lane, (int)F_G1 + 64 + lane, tag);
                wait_flags(rw, (int)F_G1 + 128 + lane, (int)F_G1 + 192 + lane, tag);
                if (lane == 0) set_flag(rw, F_EPOCH, tag);
            }
        }
    } else {
        VMCNT(0); BAR();
        if (wave >= 4) wo_issue(wo, ct, 12, 20, wave - 4, 4, lane);
    }
    TS(0, 16);
    final_block(sm, wave, lane, tid, st, ct, x, out, rw, tag, ws);

    BAR();
    if (blockIdx.x == 83 && wave == 0) {
        unsigned long long mn = ~0ull, mx = 0;
        for (int q = 0; q < 4; q++) { const u32x2 v_ = __builtin_amdgcn_raw_buffer_load_b64(rw, WS_STAMP + (q * 64 + lane) * 8, 0, 16); const unsigned long long t_ = __builtin_bit_cast(unsigned long long, v_); mn = t_ < mn ? t_ : mn; mx = t_ > mx ? t_ : mx; }
        for (int o = 1; o < 64; o <<= 1) { const unsigned long long a_ = __shfl_xor(mn, o), b_ = __shfl_xor(mx, o); mn = a_ < mn ? a_ : mn; mx = b_ > mx ? b_ : mx; }
        const unsigned long long now_ = __builtin_amdgcn_s_memrealtime();
        const unsigned long long d_ = mx - mn;
        const unsigned long long e_ = now_ + 2 * d_;
        while (__builtin_amdgcn_s_memrealtime() < e_) __builtin_amdgcn_s_sleep(1);
    }
    TS(0, 19);
}

extern "C" void kernel_launch(void* const* d_in, const int* in_sizes, int n_in, void* d_out, int out_size, void* d_ws, size_t ws_size, hipStream_t stream) {
    const u16* x = (const u16*)d_in[0]; const u16* wqkv = (const u16*)d_in[1]; const u16* wo = (const u16*)d_in[2];
    const u16* kin = (const u16*)d_in[4]; const u16* vin = (const u16*)d_in[5]; const u16* shk = (const u16*)d_in[6];
    const u16* hk = (const u16*)d_in[7]; const u16* sD = (const u16*)d_in[8]; const u16* gw = (const u16*)d_in[9];
    (void)hipFuncSetAttribute((const void*)fused_kernel, hipFuncAttributeMaxDynamicSharedMemorySize, LDS_BYTES);
    fused_kernel<<<256, 512, LDS_BYTES, stream>>>(x, wqkv, wo, kin, vin, shk, hk, sD, gw, (u16*)d_out, (char*)d_ws);
}
```

```cpp
#include <hip/hip_runtime.h>
#include <stdint.h>

typedef unsigned short u16;
typedef unsigned int u32;
typedef __bf16 bf16x8 __attribute__((ext_vector_type(8)));
typedef __bf16 bf16x2 __attribute__((ext_vector_type(2)));
typedef float f32x4 __attribute__((ext_vector_type(4)));
typedef float f32x2 __attribute__((ext_vector_type(2)));
typedef float f32x16 __attribute__((ext_vector_type(16)));
typedef u32 u32x4 __attribute__((ext_vector_type(4)));
typedef u32 u32x2 __attribute__((ext_vector_type(2)));
typedef short s16x4 __attribute__((ext_vector_type(4)));
#define LAS __attribute__((address_space(3)))
#define GAS __attribute__((address_space(1)))
#define DI __device__ __forceinline__
typedef __amdgpu_buffer_rsrc_t rsrc_t;

constexpr int S = 2048, D = 640;
#define ROT_ON 1

constexpr u32 WS_UT = 0;
constexpr u32 WS_YT = WS_UT + 2621440;
constexpr u32 WS_PK0 = WS_YT + 2621440;
constexpr u32 WS_V0 = WS_PK0 + 2097152;
constexpr u32 WS_X = WS_V0 + 2097152;
constexpr u32 WS_P2 = WS_X + 1048576;
constexpr u32 WS_O = WS_P2 + 2621440;
constexpr u32 WS_G = WS_O + 2621440;
constexpr u32 WS_FLAG = WS_G + 8192;
constexpr u32 F_P0 = 0;
constexpr u32 F_G1 = 320;
constexpr u32 F_GATE = 560;
constexpr u32 F_O = 576;
constexpr u32 F_Y = 640;
constexpr u32 F_EPOCH = 896, F_TMO = 928;
constexpr u32 WS_STAMP = WS_FLAG + 4096;
constexpr u32 WS_BYTES = WS_STAMP + 256 * 32 * 16;

#define TS(w, id) do {} while (0)

DI u32 pk2(float a, float b) { f32x2 t = {a, b}; return __builtin_bit_cast(u32, __builtin_convertvector(t, bf16x2)); }
DI float blo(u32 u) { return __uint_as_float(u << 16); }
DI float bhi(u32 u) { return __uint_as_float(u & 0xffff0000u); }
DI float rbf(float f) { return blo(pk2(f, f)); }
DI bf16x8 asbf(u32x4 v) { return __builtin_bit_cast(bf16x8, v); }
DI f32x4 mfma16(u32x4 a, u32x4 b, f32x4 c) { return __builtin_amdgcn_mfma_f32_16x16x32_bf16(asbf(a), asbf(b), c, 0, 0, 0); }
DI f32x16 mfma32(u32x4 a, u32x4 b, f32x16 c) { return __builtin_amdgcn_mfma_f32_32x32x16_bf16(asbf(a), asbf(b), c, 0, 0, 0); }

DI void glds16(const void* sbase, u32 voff, u32 lds) {
    asm volatile("s_mov_b32 m0, %2\n\ts_nop 0\n\tglobal_load_lds_dwordx4 %0, %1" :: "v"(voff), "s"(sbase), "s"(lds) : "memory");
}
DI void gld64(u32x2& d, const void* sbase, u32 voff) { asm volatile("global_load_dwordx2 %0, %1, %2" : "=v"(d) : "v"(voff), "s"(sbase) : "memory"); }
DI void gld128(u32x4& d, const void* sbase, u32 voff) { asm volatile("global_load_dwordx4 %0, %1, %2" : "=v"(d) : "v"(voff), "s"(sbase) : "memory"); }
DI void gld128c(u32x4& d, const void* sbase, u32 voff) { asm volatile("global_load_dwordx4 %0, %1, %2 sc1" : "=v"(d) : "v"(voff), "s"(sbase) : "memory"); }
DI void gld32c(u32& d, const void* sbase, u32 voff) { asm volatile("global_load_dword %0, %1, %2 sc1" : "=v"(d) : "v"(voff), "s"(sbase) : "memory"); }
#define PIN(x) asm volatile("" : "+v"(x))
#define SB() __builtin_amdgcn_sched_barrier(0)
DI u32x4 ld128c(rsrc_t r, u32 off) { return __builtin_amdgcn_raw_buffer_load_b128(r, off, 0, 16); }
DI u32 ld32c(rsrc_t r, u32 off) { return __builtin_amdgcn_raw_buffer_load_b32(r, off, 0, 16); }
DI void st128c(rsrc_t r, u32 off, u32x4 v) { __builtin_amdgcn_raw_buffer_store_b128(v, r, off, 0, 16); }
DI void st64c(rsrc_t r, u32 off, u32x2 v) { __builtin_amdgcn_raw_buffer_store_b64(v, r, off, 0, 16); }
DI void st32c(rsrc_t r, u32 off, u32 v) { __builtin_amdgcn_raw_buffer_store_b32(v, r, off, 0, 16); }
DI u32x4 lds128(LAS char* sm, u32 a) { return *(LAS u32x4*)(sm + a); }
DI u32x2 lds64(LAS char* sm, u32 a) { return *(LAS u32x2*)(sm + a); }
DI u32x2 ldstr(LAS char* sm, u32 a) { return __builtin_bit_cast(u32x2, __builtin_amdgcn_ds_read_tr16_b64_v4i16((LAS s16x4*)(sm + a))); }
#define VMCNT(n) asm volatile("s_waitcnt vmcnt(" #n ")" ::: "memory")
#define LGKM0() asm volatile("s_waitcnt lgkmcnt(0)" ::: "memory")
#define BAR() do { asm volatile("" ::: "memory"); __builtin_amdgcn_s_barrier(); asm volatile("" ::: "memory"); } while (0)
#define MEMF() asm volatile("" ::: "memory")

DI void set_flag(rsrc_t r, u32 idx, u32 tag) { st32c(r, WS_FLAG + idx * 4, tag); }
DI void wait_flags(rsrc_t r, int idx0, int idx1, u32 tag) {
    const unsigned long long t0 = __builtin_amdgcn_s_memrealtime();
    for (u32 it = 1;; it++) {
        u32 a = tag, b = tag;
        if (idx0 >= 0) a = ld32c(r, WS_FLAG + idx0 * 4);
        if (idx1 >= 0) b = ld32c(r, WS_FLAG + idx1 * 4);
        if (__all(a == tag && b == tag)) break;
        if ((it & 63u) == 0u && __builtin_amdgcn_s_memrealtime() - t0 > 2000000ull) { set_flag(r, F_TMO, tag); break; }
    }
    MEMF();
}
DI void lds_arrive(LAS char* sm, u32 cnt, int lane) { LGKM0(); if (lane == 0) __hip_atomic_fetch_add((LAS u32*)(sm + cnt), 1u, __ATOMIC_RELAXED, __HIP_MEMORY_SCOPE_WORKGROUP); MEMF(); }
DI void lds_wait(LAS char* sm, u32 cnt, u32 n) { MEMF(); while (*(volatile LAS u32*)(sm + cnt) < n) __builtin_amdgcn_s_sleep(0); MEMF(); }
DI void publish_last(rsrc_t r, LAS char* sm, u32 cnt, u32 nw, u32 flag, u32 tag, int lane) {
    VMCNT(0);
    if (lane == 0) {
        const u32 old = __hip_atomic_fetch_add((LAS u32*)(sm + cnt), 1u, __ATOMIC_RELAXED, __HIP_MEMORY_SCOPE_WORKGROUP);
        if (old == nw - 1) set_flag(r, flag, tag);
    }
    MEMF();
}

DI u32 phi2(float a, float b) {
    const f32x2 t = {a, b}, s = t * 1.4426950408889634f;
    const float ea = __builtin_amdgcn_fmed3f(__builtin_amdgcn_exp2f(s.x), 0.f, 1.f), eb = __builtin_amdgcn_fmed3f(__builtin_amdgcn_exp2f(s.y), 0.f, 1.f);
    const float ma = __int_as_float(max(__float_as_int(a), 0)), mb = __int_as_float(max(__float_as_int(b), 0));
    const f32x2 r = ((f32x2){ea, eb} + (f32x2){ma, mb}) + 1e-6f;
    return pk2(r.x, r.y);
}

constexpr int K1_STAGE = 32768;
constexpr int P0_K = 131072, P0_V = P0_K + 9216, P0_OUT = P0_V + 8192;
constexpr int L_CNT = 156672;
constexpr int C_G1 = L_CNT, C_Y = L_CNT + 4, C_P0A = L_CNT + 8, C_P0B = L_CNT + 12, C_P0C = L_CNT + 16, C_FY = L_CNT + 20, C_FY2 = L_CNT + 24, C_FO = L_CNT + 28, C_FR = L_CNT + 32;
constexpr int CV_PITCH = 4160;
constexpr int CV_U = 4 * CV_PITCH;
constexpr int CV_WAVE = CV_U + 4608;
constexpr int CV_BASE = 65536;
constexpr int WO_HI = 65536;
constexpr int F_YY = 110592;
constexpr int F_Y2 = F_YY + 10240;
constexpr int F_GG = F_Y2 + 11264;
constexpr int KO_RED = 110592;
constexpr int KO_KV = KO_RED + 32768;
constexpr int RED_PITCH = 336;
constexpr int LDS_BYTES = 160 * 1024;
DI u32 p0_flag(int tile) { return F_P0 + (tile % 10) * 32 + tile / 10; }
DI constexpr u32 wo_off(int kap) { return kap < 12 ? kap * 5120 : WO_HI + (kap - 12) * 5120; }

DI void p0_issue(const u16* kin, const u16* vin, int tile, int lw, int lane, u32 base) {
    const int cg = tile % 10, rg = tile / 10, s0 = rg * 64;
    const u32 col = cg * 128 + (lane & 7) * 16;
#pragma unroll
    for (int z = 0; z < 2; z++) {
        const int n = 2 * lw + z;
        int r = s0 - 4 + n * 8 + (lane >> 3); r = r < 0 ? 0 : r;
        glds16(kin, (u32)(r * 1280) + col, base + n * 1024);
        glds16(vin, (u32)((s0 + n * 8 + (lane >> 3)) * 1280) + col, base + 9216 + n * 1024);
    }
    {
        int r = s0 + 60 + (lane >> 3); r = r > S - 1 ? S - 1 : r;
        glds16(kin, (u32)(r * 1280) + col, base + 8 * 1024);
    }
}
DI void p0_compute(LAS char* sm, u32x2 w4, int tile, int wave, int lane, u32 base, u32 outb) {
    const int s0 = (tile / 10) * 64;
    const int G = wave * 4 + (lane >> 4), cb = G & 3, i = lane & 15, q = i >> 2, p = i & 3;
    const float w0 = blo(w4.x), w1 = bhi(w4.x), w2 = blo(w4.y), w3 = bhi(w4.y);
    const int ch = 16 * cb + i;
    u32x2 kq[2][3], vq[2][2];
#pragma unroll
    for (int z = 0; z < 2; z++) {
        const int o = (G >> 2) + 4 * z;
        const u32 ka = base + (8 * o + q) * 128 + 32 * cb + 8 * p, va = ka + 9216;
        kq[z][0] = ldstr(sm, ka); kq[z][1] = ldstr(sm, ka + 512); kq[z][2] = ldstr(sm, ka + 1024);
        vq[z][0] = ldstr(sm, va); vq[z][1] = ldstr(sm, va + 512);
    }
#pragma unroll
    for (int z = 0; z < 2; z++) {
        const int o = (G >> 2) + 4 * z;
        u32x2 k0 = kq[z][0]; const u32x2 k1 = kq[z][1], k2 = kq[z][2], v0 = vq[z][0], v1 = vq[z][1];
        if (s0 == 0 && o == 0) { k0.x = 0; k0.y = 0; }
        const float kk[12] = {blo(k0.x), bhi(k0.x), blo(k0.y), bhi(k0.y), blo(k1.x), bhi(k1.x), blo(k1.y), bhi(k1.y), blo(k2.x), bhi(k2.x), blo(k2.y), bhi(k2.y)};
        const float vv[8] = {blo(v0.x), bhi(v0.x), blo(v0.y), bhi(v0.y), blo(v1.x), bhi(v1.x), blo(v1.y), bhi(v1.y)};
        float u[8];
#pragma unroll
        for (int t = 0; t < 8; t++) {
            float ks = kk[t + 1] * w3;
            ks = fmaf(kk[t + 2], w2, ks); ks = fmaf(kk[t + 3], w1, ks); ks = fmaf(kk[t + 4], w0, ks);
            u[t] = ks * vv[t];
        }
        *(LAS u32x4*)(sm + outb + ch * 128 + ((o ^ (ch & 7)) * 16)) = (u32x4){pk2(u[0], u[1]), pk2(u[2], u[3]), pk2(u[4], u[5]), pk2(u[6], u[7])};
    }
}
DI void p0_store(LAS char* sm, rsrc_t rw, int tile, int t256, u32 outb) {
    const int cg = tile % 10, s0 = (tile / 10) * 64;
#pragma unroll
    for (int z = 0; z < 2; z++) {
        const int ch2 = (t256 >> 3) + 32 * z, o2 = t256 & 7;
        const u32x4 t4 = lds128(sm, outb + ch2 * 128 + ((o2 ^ (ch2 & 7)) * 16));
        st128c(rw, WS_UT + (cg * 64 + ch2) * 4096 + (s0 + 8 * o2) * 2, t4);
    }
}
DI void p0_weights(u32x2& w4, const u16* shk, int tile, int wave, int lane) {
    const int cg = tile % 10, cb = (wave * 4 + (lane >> 4)) & 3;
    gld64(w4, shk, (u32)((cg * 64 + 16 * cb + (lane & 15)) * 8));
}

struct K1Set { u32x4 xa[2][2]; u32x2 wl[2][2], wh[2][2]; };
struct K1Addr { u32 x[2][2]; u32 w[2][2]; };
template <bool QT, int WO, int XO>
DI void k1_phase(LAS char* sm, f32x16 (&acc)[2][2], const K1Set& cur, K1Set& nxt, const u32 (&xb)[2], const u32 (&wb)[2][2]) {
#pragma unroll
    for (int n = 0; n < 8; n++) {
        const int ks = n >> 2, i = (n >> 1) & 1, jj = n & 1;
        const u32x4 wf = {cur.wl[ks][jj].x, cur.wl[ks][jj].y, cur.wh[ks][jj].x, cur.wh[ks][jj].y};
        acc[i][jj] = QT ? mfma32(wf, cur.xa[ks][i], acc[i][jj]) : mfma32(cur.xa[ks][i], wf, acc[i][jj]);
        if (n == 0) { nxt.wl[0][0] = ldstr(sm, wb[0][0] + WO); nxt.wh[0][0] = ldstr(sm, wb[0][1] + WO); }
        if (n == 1) { nxt.xa[0][0] = lds128(sm, xb[0] + XO); nxt.wl[0][1] = ldstr(sm, wb[1][0] + WO); }
        if (n == 2) { nxt.wh[0][1] = ldstr(sm, wb[1][1] + WO); nxt.xa[0][1] = lds128(sm, xb[0] + XO + 4096); }
        if (n == 3) { nxt.wl[1][0] = ldstr(sm, wb[0][0] + WO + 4096); nxt.wh[1][0] = ldstr(sm, wb[0][1] + WO + 4096); }
        if (n == 4) { nxt.xa[1][0] = lds128(sm, xb[1] + XO); nxt.wl[1][1] = ldstr(sm, wb[1][0] + WO + 4096); }
        if (n == 5) { nxt.wh[1][1] = ldstr(sm, wb[1][1] + WO + 4096); nxt.xa[1][1] = lds128(sm, xb[1] + XO + 4096); }
        SB();
    }
}
template <bool QT, int ST>
DI void k1_chunk(LAS char* sm, f32x16 (&acc)[2][2], K1Set& s0, K1Set& s1, const K1Addr& a0, const K1Addr& a1, bool bar, bool drain = false) {
    constexpr int SN = (ST + 1) & 3;
    const K1Addr& ac = ST < 2 ? a0 : a1; const K1Addr& an = SN < 2 ? a0 : a1;
    k1_phase<QT, (ST & 1) * K1_STAGE + 8192, (ST & 1) * K1_STAGE + 16384>(sm, acc, s0, s1, ac.x[1], ac.w);
    if (drain) VMCNT(0);
    if (bar) { LGKM0(); BAR(); }
    k1_phase<QT, (SN & 1) * K1_STAGE, (SN & 1) * K1_STAGE + 16384>(sm, acc, s1, s0, an.x[0], an.w);
}
template <bool QT>
DI void k1_loop(LAS char* sm, const K1Addr& a0, f32x16 (&acc)[2][2], rsrc_t rw, u32 pflag, u32 tag, int wave, int lane) {
    K1Addr a1;
#pragma unroll
    for (int i = 0; i < 2; i++)
#pragma unroll
        for (int z = 0; z < 2; z++) { a1.x[i][z] = a0.x[i][z] + 65536; a1.w[i][z] = a0.w[i][z] + 65536; }
    K1Set s0, s1;
#pragma unroll
    for (int ks = 0; ks < 2; ks++)
#pragma unroll
        for (int jj = 0; jj < 2; jj++) {
            s0.wl[ks][jj] = ldstr(sm, a0.w[jj][0] + ks * 4096); s0.wh[ks][jj] = ldstr(sm, a0.w[jj][1] + ks * 4096);
            s0.xa[ks][jj] = lds128(sm, a0.x[0][ks] + 16384 + jj * 4096);
        }
    SB();
#pragma unroll 1
    for (int c = 0; c < 8; c += 4) {
        k1_chunk<QT, 0>(sm, acc, s0, s1, a0, a1, true);
        k1_chunk<QT, 1>(sm, acc, s0, s1, a0, a1, true);
        k1_chunk<QT, 2>(sm, acc, s0, s1, a0, a1, true);
        k1_chunk<QT, 3>(sm, acc, s0, s1, a0, a1, true, c == 0);
        if (c == 0 && wave == 0 && lane == 0) set_flag(rw, pflag, tag);
    }
    k1_chunk<QT, 0>(sm, acc, s0, s1, a0, a1, true);
    k1_chunk<QT, 1>(sm, acc, s0, s1, a0, a1, false);
}

DI void wo_issue(const u16* wo, int ct, int k0, int k1, int w, int nw, int lane) {
    u32 vo[5];
#pragma unroll
    for (int i = 0; i < 5; i++) { const int n = 64 * i + lane, rho = n / 10, c = n - 10 * rho, row = (rho & 19) | ((rho & 4) << 1) | ((rho & 8) >> 1); vo[i] = (u32)(row * 1280 + ct * 160 + c * 16); }
#pragma unroll 1
    for (int kap = k0 + w; kap < k1; kap += nw) {
        const char* src = (const char*)wo + (size_t)kap * 40960;
        const u32 dst = kap < 12 ? kap * 5120 : WO_HI + (kap - 12) * 5120;
#pragma unroll
        for (int i = 0; i < 5; i++) glds16(src, vo[i], dst + i * 1024);
    }
}

struct CvSet { u32x2 fl[8], fh[8]; u32x4 g[8]; };
DI void cv_load(LAS char* sm, CvSet& s, u32 fa, u32 ga) {
#pragma unroll
    for (int k1 = 0; k1 < 8; k1++) { s.fl[k1] = ldstr(sm, fa + 512 * k1 + 8); s.fh[k1] = ldstr(sm, fa + 512 * k1); s.g[k1] = lds128(sm, ga + 512 * k1); }
}
DI void cv_phase(LAS char* sm, f32x4 (&Y)[8], const CvSet& cur, CvSet& nxt, u32 fa, u32 ga) {
    PIN(fa); PIN(ga);
    SB();
    int n = 0;
#pragma unroll
    for (int e = 0; e < 8; e++)
#pragma unroll
        for (int k1 = 0; k1 + e < 8; k1++) {
            const u32x4 F = {cur.fl[k1].x, cur.fl[k1].y, cur.fh[k1].x, cur.fh[k1].y};
            Y[k1 + e] = mfma16(F, cur.g[e], Y[k1 + e]);
            if (n == 0) nxt.g[0] = lds128(sm, ga);
            else if (n <= 16) { const int r = (n - 1) >> 1; if ((n - 1) & 1) nxt.fh[r] = ldstr(sm, fa + 512 * r); else nxt.fl[r] = ldstr(sm, fa + 512 * r + 8); }
            else if (n < 24) nxt.g[n - 16] = lds128(sm, ga + 512 * (n - 16));
            n++;
            SB();
        }
}
struct CvIn { u32x4 hd[4]; u32x2 hm[4]; };
DI void conv_fetch_h(CvIn& in, const u16* hk, int c, int lane) {
    const char* hb = (const char*)hk + (size_t)c * 4096;
#pragma unroll
    for (int z = 0; z < 4; z++) {
        const int i = lane + 64 * z;
        gld128(in.hd[z], hb, (u32)(i * 16));
        gld64(in.hm[z], hb, (u32)(i > 0 ? i * 16 - 8 : 0));
    }
}
DI void conv_build_h(LAS char* sm0, CvIn& in, int w, int lane) {
    LAS char* sm = sm0 + CV_BASE + w * CV_WAVE;
#pragma unroll
    for (int z = 0; z < 4; z++) { PIN(in.hd[z]); PIN(in.hm[z]); }
    if (lane < 32) *(LAS u32x4*)(sm + CV_U + lane * 16) = (u32x4){0, 0, 0, 0};
    if (lane < 8) *(LAS u32x4*)(sm + (lane >> 1) * CV_PITCH + (lane & 1) * 16) = (u32x4){0, 0, 0, 0};
#pragma unroll
    for (int z = 0; z < 4; z++) {
        const int i = lane + 64 * z;
        u32 m2 = in.hm[z].x, m1 = in.hm[z].y;
        if (i == 0) { m2 = 0; m1 = 0; }
        const u32 d0 = in.hd[z].x, d1 = in.hd[z].y, d2 = in.hd[z].z, d3 = in.hd[z].w;
        const u32 om2 = __builtin_amdgcn_alignbit(m1, m2, 16), om1 = __builtin_amdgcn_alignbit(d0, m1, 16), o0 = __builtin_amdgcn_alignbit(d1, d0, 16),
                  o1 = __builtin_amdgcn_alignbit(d2, d1, 16), o2 = __builtin_amdgcn_alignbit(d3, d2, 16);
        LAS char* p = sm + 32 + i * 16;
        *(LAS u32x4*)(p) = (u32x4){d0, d1, d2, d3};
        *(LAS u32x4*)(p + CV_PITCH) = (u32x4){om1, o0, o1, o2};
        *(LAS u32x4*)(p + 2 * CV_PITCH) = (u32x4){m1, d0, d1, d2};
        *(LAS u32x4*)(p + 3 * CV_PITCH) = (u32x4){om2, om1, o0, o1};
    }
}
DI void conv_run(LAS char* sm0, float Dc, int w, int lane, int c, rsrc_t rw, char* ws) {
    LAS char* sm = sm0;
    LGKM0();
    TS(4, 9);
    const int fr = lane & 15, g = lane >> 4, q = fr >> 2, p = lane & 3;
    sm = sm0;
    const u32 fa = CV_BASE + w * CV_WAVE + q * CV_PITCH + 2 * (32 - 8 * g + 4 * p) - 8;
    const u32 ga = CV_BASE + w * CV_WAVE + CV_U + 480 + 32 * fr + 16 * g;
    f32x4 Y[8];
#pragma unroll
    for (int i = 0; i < 8; i++) Y[i] = (f32x4){0, 0, 0, 0};
    CvSet A, B;
    cv_load(sm, A, fa, ga);
    SB();
#pragma unroll 1
    for (int k0 = 0; k0 < 8; k0 += 2) {
        cv_phase(sm, Y, A, B, fa + 64 * (k0 + 1), ga - 64 * (k0 + 1));
        cv_phase(sm, Y, B, A, fa + 64 * (k0 + 2), ga - 64 * (k0 + 2));
    }
    TS(4, 10);
    sm = sm0 + CV_BASE + w * CV_WAVE;
    {
        u32x2 u4[8];
#pragma unroll
        for (int ib = 0; ib < 8; ib++) u4[ib] = lds64(sm, CV_U + 512 + 512 * ib + 32 * fr + 8 * g);
#pragma unroll
        for (int ib = 0; ib < 8; ib++) {
            const u32x2 r = {pk2(fmaf(blo(u4[ib].x), Dc, Y[ib][0]), fmaf(bhi(u4[ib].x), Dc, Y[ib][1])), pk2(fmaf(blo(u4[ib].y), Dc, Y[ib][2]), fmaf(bhi(u4[ib].y), Dc, Y[ib][3]))};
            *(LAS u32x2*)(sm + 512 * ib + 32 * fr + 8 * g) = r;
        }
    }
    LGKM0();
    u32x4 y8[4];
#pragma unroll
    for (int z = 0; z < 4; z++) y8[z] = lds128(sm, z * 1024 + lane * 16);
#pragma unroll
    for (int z = 0; z < 4; z++) st128c(rw, WS_YT + c * 4096 + z * 1024 + lane * 16, y8[z]);
}

DI void kvo_block(LAS char* sm, int wave, int lane, int h, int half, rsrc_t rw, const u16* wo, int ct, char* ws) {
    const int fr = lane & 15, g = lane >> 4;
    const u32 pk0 = WS_PK0 + (h * 64 + 8 * wave) * 1024 + lane * 16, v0 = pk0 + (WS_V0 - WS_PK0);
    const u32 xx = WS_X + (h * 32 + 4 * wave) * 1024 + lane * 16;
    u32x4 A[8], B[8], X[4], Q[8];
#pragma unroll
    for (int i = 0; i < 8; i++) { A[i] = ld128c(rw, pk0 + i * 1024); B[i] = ld128c(rw, v0 + i * 1024); if ((i & 1) == 0) X[i >> 1] = ld128c(rw, xx + (i >> 1) * 1024); }
    const int odd = h & 1, pr = h >> 1;
    const int u5 = (g < 3 ? g : 2) + 2 * odd;
    const int sb0 = half * 64 + wave * 8;
    const u32 pq = WS_P2 + (((pr * 128 + sb0) * 5 + u5) * 128 + fr * 8) * 2;
#pragma unroll
    for (int i = 0; i < 8; i++) Q[i] = ld128c(rw, pq + i * 1280);
    MEMF(); SB();
    f32x4 a00 = {0, 0, 0, 0}, a0x = a00, ax0 = a00, axx = a00;
    const u32 cst = fr == 8 ? 0x3F803F80u : 0u;
#pragma unroll
    for (int i = 0; i < 8; i++) {
        u32x4 xv = X[i >> 1];
        if (i & 1) {
#pragma unroll
            for (int e = 0; e < 4; e++) xv[e] = (u32)__builtin_amdgcn_update_dpp(0, (int)xv[e], 0x108, 0xf, 0xf, false);
        }
        if (fr >= 8) xv = (u32x4){cst, cst, cst, cst};
        a00 = mfma16(A[i], B[i], a00); a0x = mfma16(A[i], xv, a0x); ax0 = mfma16(xv, B[i], ax0); axx = mfma16(xv, xv, axx);
    }
    TS(0, 9);
    *(LAS f32x4*)(sm + KO_RED + ((wave * 4 + 0) * 64 + lane) * 16) = a00;
    *(LAS f32x4*)(sm + KO_RED + ((wave * 4 + 1) * 64 + lane) * 16) = a0x;
    *(LAS f32x4*)(sm + KO_RED + ((wave * 4 + 2) * 64 + lane) * 16) = ax0;
    *(LAS f32x4*)(sm + KO_RED + ((wave * 4 + 3) * 64 + lane) * 16) = axx;
    if (wave >= 4 && (wave - 4) * 64 + lane < 189) *(LAS u32x2*)(sm + KO_KV + ((wave - 4) * 64 + lane) * 8) = (u32x2){0, 0};
    LGKM0(); BAR();
    if (wave < 4) {
        const int tile = wave;
        f32x4 s = *(LAS f32x4*)(sm + KO_RED + (tile * 64 + lane) * 16);
#pragma unroll
        for (int w2 = 1; w2 < 8; w2++) s += *(LAS f32x4*)(sm + KO_RED + ((w2 * 4 + tile) * 64 + lane) * 16);
        const int e = (tile & 1) ? 12 + fr : fr;
        const bool colok = (tile & 1) ? (fr >= 4 && fr <= 8) : true;
        const bool rowok = (tile & 2) ? (g == 0) : true;
        const int d0 = (tile & 2) ? 16 : 4 * g;
        if (colok && rowok) *(LAS u32x2*)(sm + KO_KV + e * 72 + (d0 + 4) * 2) = (u32x2){pk2(s[0], s[1]), pk2(s[2], s[3])};
    }
    LGKM0(); BAR();
    u32x4 A0, A1[4], A2;
    {
        const u32 dofs = KO_KV + (8 * g - 4 * odd + 4) * 2;
        const u32 r0 = (4 * odd + fr) * 72, r1 = ((odd ? 0 : 16) + (fr & 3)) * 72;
        const u32x2 p0 = lds64(sm, dofs + r0), p1 = lds64(sm, dofs + r0 + 8);
        const u32x2 q0 = lds64(sm, dofs + r1), q1 = lds64(sm, dofs + r1 + 8);
        const u32x2 z0 = lds64(sm, dofs + 20 * 72), z1 = lds64(sm, dofs + 20 * 72 + 8);
        A0 = (u32x4){p0.x, p0.y, p1.x, p1.y}; A2 = (u32x4){z0.x, z0.y, z1.x, z1.y};
#pragma unroll
        for (int z = 0; z < 4; z++) { const bool m = (fr >> 2) == z; A1[z] = (u32x4){m ? q0.x : 0u, m ? q0.y : 0u, m ? q1.x : 0u, m ? q1.y : 0u}; }
    }
    TS(0, 10);
    const int U0 = (5 * h + odd) >> 1;
    const int Uw = U0 + (g >> 1);
    const u32 offA = WS_O + (((Uw >> 2) * 4 + (Uw & 3)) * 128 + fr * 8) * 2 + (g & 1) * 20480;
    const int cgx = 5 * h + (odd ? 0 : 4), Ux = cgx >> 1;
    const u32 offX = WS_O + (((Ux >> 2) * 4 + (Ux & 3)) * 128 + fr * 8 + (cgx & 1) * 4) * 2;
    const f32x4 zero = {0, 0, 0, 0};
#pragma unroll
    for (int i4 = 0; i4 < 2; i4++) {
        f32x4 n1 = zero; float rden[4]; u32x2 ov[4];
#pragma unroll
        for (int z = 0; z < 4; z++) {
            const int i = 4 * i4 + z;
            const f32x4 n0 = mfma16(A0, Q[i], zero);
            const f32x4 dn = mfma16(A2, Q[i], zero);
            n1 = mfma16(A1[z], Q[i], n1);
            const float rd = __builtin_amdgcn_rcpf(dn[0] + 1e-6f);
            rden[z] = rd;
            ov[z] = (u32x2){pk2(n0[0] * rd, n0[1] * rd), pk2(n0[2] * rd, n0[3] * rd)};
        }
#pragma unroll
        for (int z = 0; z < 4; z += 2) {
            auto rx = __builtin_amdgcn_permlane16_swap(ov[z].x, ov[z + 1].x, false, false);
            auto ry = __builtin_amdgcn_permlane16_swap(ov[z].y, ov[z + 1].y, false, false);
            st128c(rw, offA + (sb0 + 4 * i4 + z) * 20480, (u32x4){rx[0], ry[0], rx[1], ry[1]});
        }
        const float rd = g == 0 ? rden[0] : g == 1 ? rden[1] : g == 2 ? rden[2] : rden[3];
        st64c(rw, offX + (sb0 + 4 * i4 + g) * 20480, (u32x2){pk2(n1[0] * rd, n1[1] * rd), pk2(n1[2] * rd, n1[3] * rd)});
    }
}

DI void gate_block(int wave, int lane, int m, const u16* x, const u16* gw, rsrc_t rw) {
    const int sub = lane & 15, r4 = lane >> 4;
    u32x4 gq[5], xr[4][5];
#pragma unroll
    for (int jj = 0; jj < 5; jj++) gq[jj] = *(const GAS u32x4*)((const GAS char*)gw + (sub + 16 * jj) * 16);
#pragma unroll
    for (int it = 0; it < 4; it++)
#pragma unroll
        for (int jj = 0; jj < 5; jj++) xr[it][jj] = *(const GAS u32x4*)((const GAS char*)x + (size_t)(128 * m + 16 * wave + 4 * it + r4) * 1280 + (sub + 16 * jj) * 16);
    MEMF(); SB();
#pragma unroll
    for (int it = 0; it < 4; it++) {
        float a = 0.f;
#pragma unroll
        for (int jj = 0; jj < 5; jj++)
#pragma unroll
            for (int e = 0; e < 4; e++) { a = fmaf(blo(xr[it][jj][e]), blo(gq[jj][e]), a); a = fmaf(bhi(xr[it][jj][e]), bhi(gq[jj][e]), a); }
        a += __shfl_xor(a, 1); a += __shfl_xor(a, 2); a += __shfl_xor(a, 4); a += __shfl_xor(a, 8);
        a = rbf(a);
        const float g = rbf(__builtin_amdgcn_rcpf(1.f + __builtin_amdgcn_exp2f(-1.4426950408889634f * a)));
        if (sub == 0) st32c(rw, WS_G + (128 * m + 16 * wave + 4 * it + r4) * 4, __float_as_uint(g));
    }
}

DI void final_block(LAS char* sm, int wave, int lane, int tid, int st, int ct, const u16* x, u16* out, rsrc_t rw, u32 tag, char* ws) {
    const int fr = lane & 15, g = lane >> 4;
    u32x4 xv[2];
#pragma unroll
    for (int r = 0; r < 2; r++) {
        int it = tid + 512 * r; it = it < 640 ? it : 639;
        const int s = it / 10, u = it - 10 * s;
        xv[r] = *(const GAS u32x4*)((const GAS char*)x + (size_t)(st * 64 + s) * 1280 + ct * 160 + u * 16);
    }
    VMCNT(0); BAR();
    if (wave < 4) {
        if (wave == 0) { wait_flags(rw, lane < 32 ? (int)F_O + (st >> 4) * 32 + lane : -1, -1, tag); lds_arrive(sm, C_FO, lane); }
        else lds_wait(sm, C_FO, 1);
        TS(0, 17);
        const int kh = wave & 1, sh = wave >> 1;
        const u32 ob = WS_O + ((st * 4 + 2 * sh) * 20 + 10 * kh) * 1024 + lane * 16;
        u32x4 B[2][10];
#pragma unroll
        for (int kl = 0; kl < 10; kl++) { B[0][kl] = ld128c(rw, ob + kl * 1024); B[1][kl] = ld128c(rw, ob + 20480 + kl * 1024); }
        MEMF(); SB();
        f32x4 acc[5][2];
#pragma unroll
        for (int cb = 0; cb < 5; cb++) { acc[cb][0] = (f32x4){0, 0, 0, 0}; acc[cb][1] = (f32x4){0, 0, 0, 0}; }
        const u32 aoff = (16 * (g >> 1) + 4 * (g & 1) + (fr >> 2)) * 160 + (lane & 3) * 8;
        u32x2 al[2][5], ah[2][5];
#define F_READ(buf, kl) do { const u32 b0_ = kh ? wo_off(10 + (kl)) : wo_off(kl); \
            _Pragma("unroll") for (int cb = 0; cb < 5; cb++) { al[buf][cb] = ldstr(sm, b0_ + aoff + cb * 32); ah[buf][cb] = ldstr(sm, b0_ + aoff + cb * 32 + 1280); } } while (0)
        F_READ(0, 0);
#pragma unroll
        for (int kl = 0; kl < 10; kl++) {
            if (kl < 9) F_READ((kl + 1) & 1, kl + 1);
            SB();
#pragma unroll
            for (int cb = 0; cb < 5; cb++) {
                const u32x4 a = {al[kl & 1][cb].x, al[kl & 1][cb].y, ah[kl & 1][cb].x, ah[kl & 1][cb].y};
                acc[cb][0] = mfma16(a, B[0][kl], acc[cb][0]); acc[cb][1] = mfma16(a, B[1][kl], acc[cb][1]);
            }
            SB();
        }
        TS(0, 18);
        lds_arrive(sm, C_FR, lane); lds_wait(sm, C_FR, 4);
#pragma unroll
        for (int cb = 0; cb < 5; cb++)
#pragma unroll
            for (int z = 0; z < 2; z++)
                *(LAS f32x4*)(sm + kh * (64 * RED_PITCH) + (16 * (2 * sh + z) + fr) * RED_PITCH + (16 * cb + 4 * g) * 4) = acc[cb][z];
    } else {
        if (wave == 4) { wait_flags(rw, lane < 20 ? (int)F_Y + ct * 32 + lane : lane == 32 ? (int)F_GATE + (st >> 1) : -1, -1, tag); lds_arrive(sm, C_FY, lane); }
        else lds_wait(sm, C_FY, 1);
        const int t = tid - 256;
        u32x4 yv[3];
#pragma unroll
        for (int r = 0; r < 3; r++) { int id = t + 256 * r; id = id < 640 ? id : 639; yv[r] = ld128c(rw, WS_YT + (80 * ct + (id >> 3)) * 4096 + st * 128 + (id & 7) * 16); }
#pragma unroll
        for (int r = 0; r < 3; r++) { const int id = t + 256 * r; if (id < 640) *(LAS u32x4*)(sm + F_YY + id * 16) = yv[r]; }
        if (wave == 4) *(LAS u32*)(sm + F_GG + lane * 4) = ld32c(rw, WS_G + (st * 64 + lane) * 4);
        lds_arrive(sm, C_FY2, lane); lds_wait(sm, C_FY2, 4);
        const int sb = wave - 4, q = fr >> 2, p = lane & 3;
        u32x2 tt[5];
#pragma unroll
        for (int cb = 0; cb < 5; cb++) tt[cb] = ldstr(sm, F_YY + (16 * cb + 4 * g + q) * 128 + (16 * sb + 4 * p) * 2);
#pragma unroll
        for (int cb = 0; cb < 5; cb++) *(LAS u32x2*)(sm + F_Y2 + (16 * sb + fr) * 176 + (16 * cb + 4 * g) * 2) = tt[cb];
    }
    LGKM0(); BAR();
#pragma unroll
    for (int r = 0; r < 2; r++) {
        const int it = tid + 512 * r;
        if (it < 640) {
            const int s = it / 10, u = it - 10 * s;
            const u32 ra = s * RED_PITCH + u * 32;
            const f32x4 p0 = *(LAS f32x4*)(sm + ra), p1 = *(LAS f32x4*)(sm + ra + 16), q0 = *(LAS f32x4*)(sm + 64 * RED_PITCH + ra), q1 = *(LAS f32x4*)(sm + 64 * RED_PITCH + ra + 16);
            const u32x4 y8 = lds128(sm, F_Y2 + s * 176 + u * 16);
            const float gg = *(LAS float*)(sm + F_GG + s * 4), omg = 1.f - gg;
            float o[8];
#pragma unroll
            for (int e = 0; e < 4; e++) {
                const float a0 = (e < 2 ? p0[2 * e] + q0[2 * e] : p1[2 * e - 4] + q1[2 * e - 4]), a1 = (e < 2 ? p0[2 * e + 1] + q0[2 * e + 1] : p1[2 * e - 3] + q1[2 * e - 3]);
                const float b0 = omg * blo(xv[r][e]), b1 = omg * bhi(xv[r][e]);
                o[2 * e] = fmaf(b0, blo(y8[e]), gg * a0); o[2 * e + 1] = fmaf(b1, bhi(y8[e]), gg * a1);
            }
            *(GAS u32x4*)((GAS char*)out + (size_t)(st * 64 + s) * 1280 + ct * 160 + u * 16) = (u32x4){pk2(o[0], o[1]), pk2(o[2], o[3]), pk2(o[4], o[5]), pk2(o[6], o[7])};
        }
    }
}

__global__ __launch_bounds__(512, 2) void fused_kernel(const u16* __restrict__ x, const u16* __restrict__ w, const u16* __restrict__ wo, const u16* __restrict__ kin,
                                                        const u16* __restrict__ vin, const u16* __restrict__ shk, const u16* __restrict__ hk, const u16* __restrict__ sD,
                                                        const u16* __restrict__ gw, u16* __restrict__ out, char* ws) {
    extern __shared__ __attribute__((aligned(16))) char smem[];
    LAS char* sm = (LAS char*)smem;
    const int tid = threadIdx.x, lane = tid & 63, wave = __builtin_amdgcn_readfirstlane(tid >> 6);
    const int b = blockIdx.x, xcd = b & 7, j = b >> 3;
    const rsrc_t rw = __builtin_amdgcn_make_buffer_rsrc(ws, 0, WS_BYTES, 0x00020000);
    u32 tag;
    asm volatile("s_load_dword %0, %1, 0x0\n\ts_waitcnt lgkmcnt(0)" : "=s"(tag) : "s"(ws + WS_FLAG + F_EPOCH * 4) : "memory");
    tag += 1u;
    if (tid < 16) *(LAS u32*)(sm + L_CNT + tid * 4) = 0u;
    TS(0, 0);
    const int st = 4 * xcd + (j >> 3), ct = j & 7;
    int ko = -1;
    bool is_conv = false;

    if (j >= 30) {
        const int m = xcd * 2 + (j - 30);
        ko = -100;
        {
            const int grp = wave >> 2, w4i = wave & 3;
            u32x2 w4[3];
#pragma unroll
            for (int t = 0; t < 3; t++) { const int tt = 2 * t + grp < 5 ? 2 * t + grp : 4; p0_weights(w4[t], shk, 240 + 5 * m + tt, w4i, lane); }
#pragma unroll
            for (int t = 0; t < 3; t++) if (2 * t + grp < 5) p0_issue(kin, vin, 240 + 5 * m + 2 * t + grp, w4i, lane, (2 * t + grp) * 17408);
            VMCNT(0);
#pragma unroll
            for (int t = 0; t < 3; t++) PIN(w4[t]);
            BAR();
#pragma unroll
            for (int t = 0; t < 3; t++)
                if (2 * t + grp < 5) p0_compute(sm, w4[t], 240 + 5 * m + 2 * t + grp, w4i, lane, (2 * t + grp) * 17408, 90112 + (2 * t + grp) * 8192);
            LGKM0(); BAR();
            {
                u32x4 t4[3][2];
#pragma unroll
                for (int t = 0; t < 3; t++)
#pragma unroll
                    for (int z = 0; z < 2; z++) { const int ch2 = ((tid & 255) >> 3) + 32 * z, o2 = tid & 7, tt = 2 * t + grp < 5 ? 2 * t + grp : 4; t4[t][z] = lds128(sm, 90112 + tt * 8192 + ch2 * 128 + ((o2 ^ (ch2 & 7)) * 16)); }
#pragma unroll
                for (int t = 0; t < 3; t++)
                    if (2 * t + grp < 5) {
                        const int tile = 240 + 5 * m + 2 * t + grp, cg = tile % 10, s0 = (tile / 10) * 64;
#pragma unroll
                        for (int z = 0; z < 2; z++) { const int ch2 = ((tid & 255) >> 3) + 32 * z, o2 = tid & 7; st128c(rw, WS_UT + (cg * 64 + ch2) * 4096 + (s0 + 8 * o2) * 2, t4[t][z]); }
                    }
            }
        }
        VMCNT(0); BAR();
        if (tid < 5) set_flag(rw, p0_flag(240 + 5 * m + tid), tag);
        TS(0, 1);
        gate_block(wave, lane, m, x, gw, rw);
        VMCNT(0); LGKM0(); BAR();
        if (tid == 0) set_flag(rw, F_GATE + m, tag);
        TS(0, 3);
    } else {
        const int rt = 2 * xcd + j / 15, cbk = j % 15, tile = xcd * 30 + j;
        is_conv = cbk < 10;
        if (!is_conv) { const int vi = rt * 5 + (cbk - 10); ko = vi < 64 ? vi : -2 - (vi - 64); }
        if (wave >= 4) {
            const int lw = wave - 4;
            p0_issue(kin, vin, tile, lw, lane, P0_K);
            u32 vx[4], vw[4];
#pragma unroll
            for (int n2 = 0; n2 < 4; n2++) {
                const int n = 4 * lw + n2;
                const int row = 8 * n + (lane >> 3), ch = (lane & 7) ^ ((row >> 1) & 7);
                vx[n2] = (u32)((rt * 128 + row) * 1280 + ch * 16);
                const int kr = 4 * n + (lane >> 4), f = ((kr & 3) << 2) | ((kr >> 2) & 3), cw = (lane & 15) ^ f;
                vw[n2] = (u32)(kr * 3840 + cbk * 256 + cw * 16);
            }
            const int rot = (rt + cbk) % 10;
#define K1_ISSUE(c) do { const u32 st_ = ((c) & 3) * K1_STAGE + 4 * lw * 1024; int kc_ = (c) + ROT_ON * rot; kc_ = kc_ >= 10 ? kc_ - 10 : kc_; \
            const char* xb_ = (const char*)x + kc_ * 128; const char* wp_ = (const char*)w + (size_t)kc_ * 64 * 3840; \
            _Pragma("unroll") for (int n2 = 0; n2 < 4; n2++) { glds16(xb_, vx[n2], st_ + 16384 + n2 * 1024); glds16(wp_, vw[n2], st_ + n2 * 1024); } } while (0)
            K1_ISSUE(0);
            VMCNT(8); BAR();
            K1_ISSUE(1); K1_ISSUE(2);
            VMCNT(16); BAR();
            K1_ISSUE(3);
#pragma unroll 1
            for (int c = 0; c < 6; c++) {
                VMCNT(16); BAR();
                K1_ISSUE(c + 4);
            }
            const int cv = rt * 10 + cbk, c = 4 * cv + lw;
            if (is_conv) {
                CvIn in; u32 fl; u32x4 u[4];
                const int fidx = (int)F_P0 + (cv >> 4) * 32 + (lane & 31);
                conv_fetch_h(in, hk, c, lane);
                VMCNT(24); BAR();
                VMCNT(16); BAR();
                gld32c(fl, ws, WS_FLAG + fidx * 4);
                VMCNT(9); BAR();
                TS(4, 4);
                VMCNT(0); PIN(fl);
                const bool ready = __all(fl == tag);
                if (ready) {
#pragma unroll
                    for (int z = 0; z < 4; z++) gld128c(u[z], ws, WS_UT + c * 4096 + z * 1024 + lane * 16);
                }
                TS(4, 5);
                conv_build_h(sm, in, lw, lane);
                TS(4, 6);
                if (!ready) {
                    wait_flags(rw, fidx, -1, tag);
#pragma unroll
                    for (int z = 0; z < 4; z++) gld128c(u[z], ws, WS_UT + c * 4096 + z * 1024 + lane * 16);
                }
                TS(4, 7);
                VMCNT(0);
#pragma unroll
                for (int z = 0; z < 4; z++) { PIN(u[z]); *(LAS u32x4*)(sm + CV_BASE + lw * CV_WAVE + CV_U + 512 + z * 1024 + lane * 16) = u[z]; }
                const float Dc = blo((u32)sD[c]);
                TS(4, 8);
                conv_run(sm, Dc, lw, lane, c, rw, ws);
                publish_last(rw, sm, C_Y, 4, F_Y + (cv / 20) * 32 + cv % 20, tag, lane);
                TS(4, 11);
            } else {
                VMCNT(16); BAR();
                VMCNT(8); BAR();
                VMCNT(0); BAR();
            }
        } else {
            u32x2 w4; p0_weights(w4, shk, tile, wave, lane);
            const int wm = wave >> 1, wn = wave & 1, r32 = lane & 31, hh = lane >> 5;
            K1Addr a0;
#pragma unroll
            for (int ks = 0; ks < 2; ks++) {
                a0.x[0][ks] = (64 * wm + r32) * 128 + (((2 * ks + hh) ^ ((r32 >> 1) & 7)) * 16);
                a0.x[1][ks] = a0.x[0][ks] ^ 64;
            }
#pragma unroll
            for (int jj = 0; jj < 2; jj++)
#pragma unroll
                for (int t = 0; t < 2; t++) {
                    const int blk = (lane >> 4) & 1, q = (lane & 15) >> 2, p = lane & 3, row = 8 * hh + 4 * t + q, ch = 8 * wn + 4 * jj + 2 * blk + (p >> 1);
                    const int f = ((row & 3) << 2) | ((row >> 2) & 3);
                    a0.w[jj][t] = row * 256 + ((ch ^ f) * 16) + 8 * (p & 1);
                }
            f32x16 acc[2][2];
#pragma unroll
            for (int i = 0; i < 2; i++)
#pragma unroll
                for (int jj = 0; jj < 2; jj++)
#pragma unroll
                    for (int e = 0; e < 16; e++) acc[i][jj][e] = 0.f;
            TS(0, 20);
            VMCNT(0); PIN(w4); TS(0, 21); BAR();
            TS(0, 22);
            p0_compute(sm, w4, tile, wave, lane, P0_K, P0_OUT);
            LGKM0(); TS(0, 23); BAR();
            TS(0, 24);
            p0_store(sm, rw, tile, tid, P0_OUT);
            TS(0, 1);
            if (cbk < 5) {
                k1_loop<true>(sm, a0, acc, rw, p0_flag(tile), tag, wave, lane);
                __builtin_amdgcn_s_setprio(2);
                TS(0, 2);
#pragma unroll
                for (int jj = 0; jj < 2; jj++)
#pragma unroll
                    for (int ap = 0; ap < 2; ap++) {
                        const int U = 16 * cbk + 8 * wn + 4 * jj + 2 * ap + hh, pr = U / 5, u5 = U - 5 * pr;
                        const u32 dst = WS_P2 + (((pr * 128 + rt * 8 + 4 * wm + (r32 >> 4)) * 5 + u5) * 128 + (r32 & 15) * 8) * 2;
#pragma unroll
                        for (int i = 0; i < 2; i++) {
                            const f32x16& a = acc[i][jj];
                            const u32 x0 = phi2(a[8 * ap], a[8 * ap + 1]), y0 = phi2(a[8 * ap + 2], a[8 * ap + 3]);
                            const u32 x1 = phi2(a[8 * ap + 4], a[8 * ap + 5]), y1 = phi2(a[8 * ap + 6], a[8 * ap + 7]);
                            auto rx = __builtin_amdgcn_permlane32_swap(x0, x1, false, false);
                            auto ry = __builtin_amdgcn_permlane32_swap(y0, y1, false, false);
                            st128c(rw, dst + i * 2560, (u32x4){rx[0], ry[0], rx[1], ry[1]});
                        }
                    }
            } else {
                k1_loop<false>(sm, a0, acc, rw, p0_flag(tile), tag, wave, lane);
                __builtin_amdgcn_s_setprio(2);
                TS(0, 2);
                const bool isk = cbk < 10;
                const int j5 = isk ? cbk - 5 : cbk - 10;
                u32 dst[2], istep[2];
#pragma unroll
                for (int jj = 0; jj < 2; jj++) {
                    const int cc = 128 * j5 + 64 * wn + 32 * jj + r32, h = cc / 20, d = cc - 20 * h;
                    const int sb32 = rt * 4 + 2 * wm;
                    const bool lo = d < 16;
                    dst[jj] = lo ? (isk ? WS_PK0 : WS_V0) + (((h * 64 + sb32) * 4 + hh) * 128 + d * 8) * 2
                                 : WS_X + (((h * 32 + (sb32 >> 1)) * 4 + hh) * 128 + (d - 16 + (isk ? 0 : 4)) * 8) * 2;
                    istep[jj] = lo ? 1024 : 128;
                }
                if (isk) {
#pragma unroll
                    for (int jj = 0; jj < 2; jj++)
#pragma unroll
                        for (int i = 0; i < 2; i++)
#pragma unroll
                            for (int z = 0; z < 2; z++) {
                                const f32x16& a = acc[i][jj];
                                st128c(rw, dst[jj] + i * istep[jj] + z * 512,
                                       (u32x4){phi2(a[8 * z], a[8 * z + 1]), phi2(a[8 * z + 2], a[8 * z + 3]), phi2(a[8 * z + 4], a[8 * z + 5]), phi2(a[8 * z + 6], a[8 * z + 7])});
                            }
                } else {
#pragma unroll
                    for (int jj = 0; jj < 2; jj++)
#pragma unroll
                        for (int i = 0; i < 2; i++)
#pragma unroll
                            for (int z = 0; z < 2; z++) {
                                const f32x16& a = acc[i][jj];
                                st128c(rw, dst[jj] + i * istep[jj] + z * 512,
                                       (u32x4){pk2(a[8 * z], a[8 * z + 1]), pk2(a[8 * z + 2], a[8 * z + 3]), pk2(a[8 * z + 4], a[8 * z + 5]), pk2(a[8 * z + 6], a[8 * z + 7])});
                            }
                }
            }
            LGKM0();
            TS(0, 25);
            publish_last(rw, sm, C_G1, 4, F_G1 + cbk * 16 + rt, tag, lane);
            __builtin_amdgcn_s_setprio(0);
            TS(0, 3);
            if (is_conv) {
                lds_wait(sm, C_G1, 4);
                wo_issue(wo, ct, 0, 12, wave, 4, lane);
            }
        }
    }
    if (!is_conv) {
        LGKM0(); BAR();
        if (ko >= 0) {
            const int h = ko >> 1, half = ko & 1;
            wo_issue(wo, ct, 0, 12, wave, 8, lane);
            if (wave == 0) {
                const int j0 = (20 * h) >> 7, j1 = (20 * h + 19) >> 7;
                const int l16 = lane & 15, sel = lane >> 4;
                const int i0 = (int)F_G1 + 16 * ((sel < 2 ? 5 : 10) + ((sel & 1) ? j1 : j0)) + l16;
                const int i1 = lane < 16 ? (int)F_G1 + 16 * (lane < 8 ? j0 : j1) + 8 * half + (lane & 7) : -1;
                wait_flags(rw, i0, i1, tag);
            }
            BAR();
            TS(0, 8);
            kvo_block(sm, wave, lane, h, half, rw, wo, ct, ws);
            TS(0, 26);
            VMCNT(0); BAR();
            if (tid == 0) set_flag(rw, F_O + half * 32 + h, tag);
            TS(0, 11);
            wo_issue(wo, ct, 12, 20, wave, 8, lane);
        } else {
            wo_issue(wo, ct, 0, 20, wave, 8, lane);
            if (ko == -2 && wave == 0) {
                wait_flags(rw, (int)F_G1 + lane, (int)F_G1 + 64 + lane, tag);
                wait_flags(rw, (int)F_G1 + 128 + lane, (int)F_G1 + 192 + lane, tag);
                if (lane == 0) set_flag(rw, F_EPOCH, tag);
            }
        }
    } else {
        VMCNT(0); BAR();
        if (wave >= 4) wo_issue(wo, ct, 12, 20, wave - 4, 4, lane);
    }
    TS(0, 16);
    final_block(sm, wave, lane, tid, st, ct, x, out, rw, tag, ws);
    TS(0, 19);
}

extern "C" void kernel_launch(void* const* d_in, const int* in_sizes, int n_in, void* d_out, int out_size, void* d_ws, size_t ws_size, hipStream_t stream) {
    const u16* x = (const u16*)d_in[0]; const u16* wqkv = (const u16*)d_in[1]; const u16* wo = (const u16*)d_in[2];
    const u16* kin = (const u16*)d_in[4]; const u16* vin = (const u16*)d_in[5]; const u16* shk = (const u16*)d_in[6];
    const u16* hk = (const u16*)d_in[7]; const u16* sD = (const u16*)d_in[8]; const u16* gw = (const u16*)d_in[9];
    (void)hipFuncSetAttribute((const void*)fused_kernel, hipFuncAttributeMaxDynamicSharedMemorySize, LDS_BYTES);
    fused_kernel<<<256, 512, LDS_BYTES, stream>>>(x, wqkv, wo, kin, vin, shk, hk, sD, gw, (u16*)d_out, (char*)d_ws);
}
```

```cpp
#define FS 64
#define POLL_SLEEP 4
#include <hip/hip_runtime.h>
#include <stdint.h>

typedef unsigned short u16;
typedef unsigned int u32;
typedef __bf16 bf16x8 __attribute__((ext_vector_type(8)));
typedef __bf16 bf16x2 __attribute__((ext_vector_type(2)));
typedef float f32x4 __attribute__((ext_vector_type(4)));
typedef float f32x2 __attribute__((ext_vector_type(2)));
typedef float f32x16 __attribute__((ext_vector_type(16)));
typedef u32 u32x4 __attribute__((ext_vector_type(4)));
typedef u32 u32x2 __attribute__((ext_vector_type(2)));
typedef short s16x4 __attribute__((ext_vector_type(4)));
#define LAS __attribute__((address_space(3)))
#define GAS __attribute__((address_space(1)))
#define DI __device__ __forceinline__
typedef __amdgpu_buffer_rsrc_t rsrc_t;

constexpr int S = 2048, D = 640;
#define ROT_ON 1

constexpr u32 WS_UT = 0;
constexpr u32 WS_YT = WS_UT + 2621440;
constexpr u32 WS_PK0 = WS_YT + 2621440;
constexpr u32 WS_V0 = WS_PK0 + 2097152;
constexpr u32 WS_X = WS_V0 + 2097152;
constexpr u32 WS_P2 = WS_X + 1048576;
constexpr u32 WS_O = WS_P2 + 2621440;
constexpr u32 WS_G = WS_O + 2621440;
constexpr u32 WS_FLAG = WS_G + 8192;
constexpr u32 F_P0 = 0;
constexpr u32 F_G1 = 320;
constexpr u32 F_GATE = 560;
constexpr u32 F_O = 576;
constexpr u32 F_Y = 640;
constexpr u32 F_EPOCH = 896, F_TMO = 928;
constexpr u32 WS_STAMP = WS_FLAG + 1024 * FS;
constexpr u32 WS_BYTES = WS_STAMP + 256 * 32 * 16;

#define TS(w, id) do {} while (0)

DI u32 pk2(float a, float b) { f32x2 t = {a, b}; return __builtin_bit_cast(u32, __builtin_convertvector(t, bf16x2)); }
DI float blo(u32 u) { return __uint_as_float(u << 16); }
DI float bhi(u32 u) { return __uint_as_float(u & 0xffff0000u); }
DI float rbf(float f) { return blo(pk2(f, f)); }
DI bf16x8 asbf(u32x4 v) { return __builtin_bit_cast(bf16x8, v); }
DI f32x4 mfma16(u32x4 a, u32x4 b, f32x4 c) { return __builtin_amdgcn_mfma_f32_16x16x32_bf16(asbf(a), asbf(b), c, 0, 0, 0); }
DI f32x16 mfma32(u32x4 a, u32x4 b, f32x16 c) { return __builtin_amdgcn_mfma_f32_32x32x16_bf16(asbf(a), asbf(b), c, 0, 0, 0); }

DI void glds16(const void* sbase, u32 voff, u32 lds) {
    asm volatile("s_mov_b32 m0, %2\n\ts_nop 0\n\tglobal_load_lds_dwordx4 %0, %1" :: "v"(voff), "s"(sbase), "s"(lds) : "memory");
}
DI void gld64(u32x2& d, const void* sbase, u32 voff) { asm volatile("global_load_dwordx2 %0, %1, %2" : "=v"(d) : "v"(voff), "s"(sbase) : "memory"); }
DI void gld128(u32x4& d, const void* sbase, u32 voff) { asm volatile("global_load_dwordx4 %0, %1, %2" : "=v"(d) : "v"(voff), "s"(sbase) : "memory"); }
DI void gld128c(u32x4& d, const void* sbase, u32 voff) { asm volatile("global_load_dwordx4 %0, %1, %2 sc1" : "=v"(d) : "v"(voff), "s"(sbase) : "memory"); }
DI void gld32c(u32& d, const void* sbase, u32 voff) { asm volatile("global_load_dword %0, %1, %2 sc1" : "=v"(d) : "v"(voff), "s"(sbase) : "memory"); }
#define PIN(x) asm volatile("" : "+v"(x))
#define SB() __builtin_amdgcn_sched_barrier(0)
DI u32x4 ld128c(rsrc_t r, u32 off) { return __builtin_amdgcn_raw_buffer_load_b128(r, off, 0, 16); }
DI u32 ld32c(rsrc_t r, u32 off) { return __builtin_amdgcn_raw_buffer_load_b32(r, off, 0, 16); }
DI void st128c(rsrc_t r, u32 off, u32x4 v) { __builtin_amdgcn_raw_buffer_store_b128(v, r, off, 0, 16); }
DI void st64c(rsrc_t r, u32 off, u32x2 v) { __builtin_amdgcn_raw_buffer_store_b64(v, r, off, 0, 16); }
DI void st32c(rsrc_t r, u32 off, u32 v) { __builtin_amdgcn_raw_buffer_store_b32(v, r, off, 0, 16); }
DI u32x4 lds128(LAS char* sm, u32 a) { return *(LAS u32x4*)(sm + a); }
DI u32x2 lds64(LAS char* sm, u32 a) { return *(LAS u32x2*)(sm + a); }
DI u32x2 ldstr(LAS char* sm, u32 a) { return __builtin_bit_cast(u32x2, __builtin_amdgcn_ds_read_tr16_b64_v4i16((LAS s16x4*)(sm + a))); }
#define VMCNT(n) asm volatile("s_waitcnt vmcnt(" #n ")" ::: "memory")
#define LGKM0() asm volatile("s_waitcnt lgkmcnt(0)" ::: "memory")
#define BAR() do { asm volatile("" ::: "memory"); __builtin_amdgcn_s_barrier(); asm volatile("" ::: "memory"); } while (0)
#define MEMF() asm volatile("" ::: "memory")

DI void set_flag(rsrc_t r, u32 idx, u32 tag) { st32c(r, WS_FLAG + idx * FS, tag); }
DI void wait_flags(rsrc_t r, int idx0, int idx1, u32 tag) {
    const unsigned long long t0 = __builtin_amdgcn_s_memrealtime();
    for (u32 it = 1;; it++) {
        u32 a = tag, b = tag;
        if (idx0 >= 0) a = ld32c(r, WS_FLAG + idx0 * FS);
        if (idx1 >= 0) b = ld32c(r, WS_FLAG + idx1 * FS);
        if (__all(a == tag && b == tag)) break;
        if (POLL_SLEEP) __builtin_amdgcn_s_sleep(POLL_SLEEP);
        if ((it & 63u) == 0u && __builtin_amdgcn_s_memrealtime() - t0 > 2000000ull) { set_flag(r, F_TMO, tag); break; }
    }
    MEMF();
}
DI void lds_arrive(LAS char* sm, u32 cnt, int lane) { LGKM0(); if (lane == 0) __hip_atomic_fetch_add((LAS u32*)(sm + cnt), 1u, __ATOMIC_RELAXED, __HIP_MEMORY_SCOPE_WORKGROUP); MEMF(); }
DI void lds_wait(LAS char* sm, u32 cnt, u32 n) { MEMF(); while (*(volatile LAS u32*)(sm + cnt) < n) __builtin_amdgcn_s_sleep(0); MEMF(); }
DI void publish_last(rsrc_t r, LAS char* sm, u32 cnt, u32 nw, u32 flag, u32 tag, int lane) {
    VMCNT(0);
    if (lane == 0) {
        const u32 old = __hip_atomic_fetch_add((LAS u32*)(sm + cnt), 1u, __ATOMIC_RELAXED, __HIP_MEMORY_SCOPE_WORKGROUP);
        if (old == nw - 1) set_flag(r, flag, tag);
    }
    MEMF();
}

DI u32 phi2(float a, float b) {
    const f32x2 t = {a, b}, s = t * 1.4426950408889634f;
    const float ea = __builtin_amdgcn_fmed3f(__builtin_amdgcn_exp2f(s.x), 0.f, 1.f), eb = __builtin_amdgcn_fmed3f(__builtin_amdgcn_exp2f(s.y), 0.f, 1.f);
    const float ma = __int_as_float(max(__float_as_int(a), 0)), mb = __int_as_float(max(__float_as_int(b), 0));
    const f32x2 r = ((f32x2){ea, eb} + (f32x2){ma, mb}) + 1e-6f;
    return pk2(r.x, r.y);
}

constexpr int K1_STAGE = 32768;
constexpr int P0_K = 131072, P0_V = P0_K + 9216, P0_OUT = P0_V + 8192;
constexpr int L_CNT = 156672;
constexpr int C_G1 = L_CNT, C_Y = L_CNT + 4, C_P0A = L_CNT + 8, C_P0B = L_CNT + 12, C_P0C = L_CNT + 16, C_FY = L_CNT + 20, C_FY2 = L_CNT + 24, C_FO = L_CNT + 28, C_FR = L_CNT + 32;
constexpr int CV_PITCH = 4160;
constexpr int CV_U = 4 * CV_PITCH;
constexpr int CV_WAVE = CV_U + 4608;
constexpr int CV_BASE = 65536;
constexpr int WO_HI = 65536;
constexpr int F_YY = 110592;
constexpr int F_Y2 = F_YY + 10240;
constexpr int F_GG = F_Y2 + 11264;
constexpr int KO_RED = 110592;
constexpr int KO_KV = KO_RED + 32768;
constexpr int RED_PITCH = 336;
constexpr int LDS_BYTES = 160 * 1024;
DI u32 p0_flag(int tile) { return F_P0 + (tile % 10) * 32 + tile / 10; }
DI constexpr u32 wo_off(int kap) { return kap < 12 ? kap * 5120 : WO_HI + (kap - 12) * 5120; }

DI void p0_issue(const u16* kin, const u16* vin, int tile, int lw, int lane, u32 base) {
    const int cg = tile % 10, rg = tile / 10, s0 = rg * 64;
    const u32 col = cg * 128 + (lane & 7) * 16;
#pragma unroll
    for (int z = 0; z < 2; z++) {
        const int n = 2 * lw + z;
        int r = s0 - 4 + n * 8 + (lane >> 3); r = r < 0 ? 0 : r;
        glds16(kin, (u32)(r * 1280) + col, base + n * 1024);
        glds16(vin, (u32)((s0 + n * 8 + (lane >> 3)) * 1280) + col, base + 9216 + n * 1024);
    }
    {
        int r = s0 + 60 + (lane >> 3); r = r > S - 1 ? S - 1 : r;
        glds16(kin, (u32)(r * 1280) + col, base + 8 * 1024);
    }
}
DI void p0_compute(LAS char* sm, u32x2 w4, int tile, int wave, int lane, u32 base, u32 outb) {
    const int s0 = (tile / 10) * 64;
    const int G = wave * 4 + (lane >> 4), cb = G & 3, i = lane & 15, q = i >> 2, p = i & 3;
    const float w0 = blo(w4.x), w1 = bhi(w4.x), w2 = blo(w4.y), w3 = bhi(w4.y);
    const int ch = 16 * cb + i;
    u32x2 kq[2][3], vq[2][2];
#pragma unroll
    for (int z = 0; z < 2; z++) {
        const int o = (G >> 2) + 4 * z;
        const u32 ka = base + (8 * o + q) * 128 + 32 * cb + 8 * p, va = ka + 9216;
        kq[z][0] = ldstr(sm, ka); kq[z][1] = ldstr(sm, ka + 512); kq[z][2] = ldstr(sm, ka + 1024);
        vq[z][0] = ldstr(sm, va); vq[z][1] = ldstr(sm, va + 512);
    }
#pragma unroll
    for (int z = 0; z < 2; z++) {
        const int o = (G >> 2) + 4 * z;
        u32x2 k0 = kq[z][0]; const u32x2 k1 = kq[z][1], k2 = kq[z][2], v0 = vq[z][0], v1 = vq[z][1];
        if (s0 == 0 && o == 0) { k0.x = 0; k0.y = 0; }
        const float kk[12] = {blo(k0.x), bhi(k0.x), blo(k0.y), bhi(k0.y), blo(k1.x), bhi(k1.x), blo(k1.y), bhi(k1.y), blo(k2.x), bhi(k2.x), blo(k2.y), bhi(k2.y)};
        const float vv[8] = {blo(v0.x), bhi(v0.x), blo(v0.y), bhi(v0.y), blo(v1.x), bhi(v1.x), blo(v1.y), bhi(v1.y)};
        float u[8];
#pragma unroll
        for (int t = 0; t < 8; t++) {
            float ks = kk[t + 1] * w3;
            ks = fmaf(kk[t + 2], w2, ks); ks = fmaf(kk[t + 3], w1, ks); ks = fmaf(kk[t + 4], w0, ks);
            u[t] = ks * vv[t];
        }
        *(LAS u32x4*)(sm + outb + ch * 128 + ((o ^ (ch & 7)) * 16)) = (u32x4){pk2(u[0], u[1]), pk2(u[2], u[3]), pk2(u[4], u[5]), pk2(u[6], u[7])};
    }
}
DI void p0_store(LAS char* sm, rsrc_t rw, int tile, int t256, u32 outb) {
    const int cg = tile % 10, s0 = (tile / 10) * 64;
#pragma unroll
    for (int z = 0; z < 2; z++) {
        const int ch2 = (t256 >> 3) + 32 * z, o2 = t256 & 7;
        const u32x4 t4 = lds128(sm, outb + ch2 * 128 + ((o2 ^ (ch2 & 7)) * 16));
        st128c(rw, WS_UT + (cg * 64 + ch2) * 4096 + (s0 + 8 * o2) * 2, t4);
    }
}
DI void p0_weights(u32x2& w4, const u16* shk, int tile, int wave, int lane) {
    const int cg = tile % 10, cb = (wave * 4 + (lane >> 4)) & 3;
    gld64(w4, shk, (u32)((cg * 64 + 16 * cb + (lane & 15)) * 8));
}

struct K1Set { u32x4 xa[2][2]; u32x2 wl[2][2], wh[2][2]; };
struct K1Addr { u32 x[2][2]; u32 w[2][2]; };
template <bool QT, int WO, int XO>
DI void k1_phase(LAS char* sm, f32x16 (&acc)[2][2], const K1Set& cur, K1Set& nxt, const u32 (&xb)[2], const u32 (&wb)[2][2]) {
#pragma unroll
    for (int n = 0; n < 8; n++) {
        const int ks = n >> 2, i = (n >> 1) & 1, jj = n & 1;
        const u32x4 wf = {cur.wl[ks][jj].x, cur.wl[ks][jj].y, cur.wh[ks][jj].x, cur.wh[ks][jj].y};
        acc[i][jj] = QT ? mfma32(wf, cur.xa[ks][i], acc[i][jj]) : mfma32(cur.xa[ks][i], wf, acc[i][jj]);
        if (n == 0) { nxt.wl[0][0] = ldstr(sm, wb[0][0] + WO); nxt.wh[0][0] = ldstr(sm, wb[0][1] + WO); }
        if (n == 1) { nxt.xa[0][0] = lds128(sm, xb[0] + XO); nxt.wl[0][1] = ldstr(sm, wb[1][0] + WO); }
        if (n == 2) { nxt.wh[0][1] = ldstr(sm, wb[1][1] + WO); nxt.xa[0][1] = lds128(sm, xb[0] + XO + 4096); }
        if (n == 3) { nxt.wl[1][0] = ldstr(sm, wb[0][0] + WO + 4096); nxt.wh[1][0] = ldstr(sm, wb[0][1] + WO + 4096); }
        if (n == 4) { nxt.xa[1][0] = lds128(sm, xb[1] + XO); nxt.wl[1][1] = ldstr(sm, wb[1][0] + WO + 4096); }
        if (n == 5) { nxt.wh[1][1] = ldstr(sm, wb[1][1] + WO + 4096); nxt.xa[1][1] = lds128(sm, xb[1] + XO + 4096); }
        SB();
    }
}
template <bool QT, int ST>
DI void k1_chunk(LAS char* sm, f32x16 (&acc)[2][2], K1Set& s0, K1Set& s1, const K1Addr& a0, const K1Addr& a1, bool bar, bool drain = false) {
    constexpr int SN = (ST + 1) & 3;
    const K1Addr& ac = ST < 2 ? a0 : a1; const K1Addr& an = SN < 2 ? a0 : a1;
    k1_phase<QT, (ST & 1) * K1_STAGE + 8192, (ST & 1) * K1_STAGE + 16384>(sm, acc, s0, s1, ac.x[1], ac.w);
    if (drain) VMCNT(0);
    if (bar) { LGKM0(); BAR(); }
    k1_phase<QT, (SN & 1) * K1_STAGE, (SN & 1) * K1_STAGE + 16384>(sm, acc, s1, s0, an.x[0], an.w);
}
template <bool QT>
DI void k1_loop(LAS char* sm, const K1Addr& a0, f32x16 (&acc)[2][2], rsrc_t rw, u32 pflag, u32 tag, int wave, int lane) {
    K1Addr a1;
#pragma unroll
    for (int i = 0; i < 2; i++)
#pragma unroll
        for (int z = 0; z < 2; z++) { a1.x[i][z] = a0.x[i][z] + 65536; a1.w[i][z] = a0.w[i][z] + 65536; }
    K1Set s0, s1;
#pragma unroll
    for (int ks = 0; ks < 2; ks++)
#pragma unroll
        for (int jj = 0; jj < 2; jj++) {
            s0.wl[ks][jj] = ldstr(sm, a0.w[jj][0] + ks * 4096); s0.wh[ks][jj] = ldstr(sm, a0.w[jj][1] + ks * 4096);
            s0.xa[ks][jj] = lds128(sm, a0.x[0][ks] + 16384 + jj * 4096);
        }
    SB();
#pragma unroll 1
    for (int c = 0; c < 8; c += 4) {
        k1_chunk<QT, 0>(sm, acc, s0, s1, a0, a1, true);
        k1_chunk<QT, 1>(sm, acc, s0, s1, a0, a1, true);
        k1_chunk<QT, 2>(sm, acc, s0, s1, a0, a1, true);
        k1_chunk<QT, 3>(sm, acc, s0, s1, a0, a1, true, c == 0);
        if (c == 0 && wave == 0 && lane == 0) set_flag(rw, pflag, tag);
    }
    k1_chunk<QT, 0>(sm, acc, s0, s1, a0, a1, true);
    k1_chunk<QT, 1>(sm, acc, s0, s1, a0, a1, false);
}

DI void wo_issue(const u16* wo, int ct, int k0, int k1, int w, int nw, int lane) {
    u32 vo[5];
#pragma unroll
    for (int i = 0; i < 5; i++) { const int n = 64 * i + lane, rho = n / 10, c = n - 10 * rho, row = (rho & 19) | ((rho & 4) << 1) | ((rho & 8) >> 1); vo[i] = (u32)(row * 1280 + ct * 160 + c * 16); }
#pragma unroll 1
    for (int kap = k0 + w; kap < k1; kap += nw) {
        const char* src = (const char*)wo + (size_t)kap * 40960;
        const u32 dst = kap < 12 ? kap * 5120 : WO_HI + (kap - 12) * 5120;
#pragma unroll
        for (int i = 0; i < 5; i++) glds16(src, vo[i], dst + i * 1024);
    }
}

struct CvSet { u32x2 fl[8], fh[8]; u32x4 g[8]; };
DI void cv_load(LAS char* sm, CvSet& s, u32 fa, u32 ga) {
#pragma unroll
    for (int k1 = 0; k1 < 8; k1++) { s.fl[k1] = ldstr(sm, fa + 512 * k1 + 8); s.fh[k1] = ldstr(sm, fa + 512 * k1); s.g[k1] = lds128(sm, ga + 512 * k1); }
}
DI void cv_phase(LAS char* sm, f32x4 (&Y)[8], const CvSet& cur, CvSet& nxt, u32 fa, u32 ga) {
    PIN(fa); PIN(ga);
    SB();
    int n = 0;
#pragma unroll
    for (int e = 0; e < 8; e++)
#pragma unroll
        for (int k1 = 0; k1 + e < 8; k1++) {
            const u32x4 F = {cur.fl[k1].x, cur.fl[k1].y, cur.fh[k1].x, cur.fh[k1].y};
            Y[k1 + e] = mfma16(F, cur.g[e], Y[k1 + e]);
            if (n == 0) nxt.g[0] = lds128(sm, ga);
            else if (n <= 16) { const int r = (n - 1) >> 1; if ((n - 1) & 1) nxt.fh[r] = ldstr(sm, fa + 512 * r); else nxt.fl[r] = ldstr(sm, fa + 512 * r + 8); }
            else if (n < 24) nxt.g[n - 16] = lds128(sm, ga + 512 * (n - 16));
            n++;
            SB();
        }
}
struct CvIn { u32x4 hd[4]; u32x2 hm[4]; };
DI void conv_fetch_h(CvIn& in, const u16* hk, int c, int lane) {
    const char* hb = (const char*)hk + (size_t)c * 4096;
#pragma unroll
    for (int z = 0; z < 4; z++) {
        const int i = lane + 64 * z;
        gld128(in.hd[z], hb, (u32)(i * 16));
        gld64(in.hm[z], hb, (u32)(i > 0 ? i * 16 - 8 : 0));
    }
}
DI void conv_build_h(LAS char* sm0, CvIn& in, int w, int lane) {
    LAS char* sm = sm0 + CV_BASE + w * CV_WAVE;
#pragma unroll
    for (int z = 0; z < 4; z++) { PIN(in.hd[z]); PIN(in.hm[z]); }
    if (lane < 32) *(LAS u32x4*)(sm + CV_U + lane * 16) = (u32x4){0, 0, 0, 0};
    if (lane < 8) *(LAS u32x4*)(sm + (lane >> 1) * CV_PITCH + (lane & 1) * 16) = (u32x4){0, 0, 0, 0};
#pragma unroll
    for (int z = 0; z < 4; z++) {
        const int i = lane + 64 * z;
        u32 m2 = in.hm[z].x, m1 = in.hm[z].y;
        if (i == 0) { m2 = 0; m1 = 0; }
        const u32 d0 = in.hd[z].x, d1 = in.hd[z].y, d2 = in.hd[z].z, d3 = in.hd[z].w;
        const u32 om2 = __builtin_amdgcn_alignbit(m1, m2, 16), om1 = __builtin_amdgcn_alignbit(d0, m1, 16), o0 = __builtin_amdgcn_alignbit(d1, d0, 16),
                  o1 = __builtin_amdgcn_alignbit(d2, d1, 16), o2 = __builtin_amdgcn_alignbit(d3, d2, 16);
        LAS char* p = sm + 32 + i * 16;
        *(LAS u32x4*)(p) = (u32x4){d0, d1, d2, d3};
        *(LAS u32x4*)(p + CV_PITCH) = (u32x4){om1, o0, o1, o2};
        *(LAS u32x4*)(p + 2 * CV_PITCH) = (u32x4){m1, d0, d1, d2};
        *(LAS u32x4*)(p + 3 * CV_PITCH) = (u32x4){om2, om1, o0, o1};
    }
}
DI void conv_run(LAS char* sm0, float Dc, int w, int lane, int c, rsrc_t rw, char* ws) {
    LAS char* sm = sm0;
    LGKM0();
    TS(4, 9);
    const int fr = lane & 15, g = lane >> 4, q = fr >> 2, p = lane & 3;
    sm = sm0;
    const u32 fa = CV_BASE + w * CV_WAVE + q * CV_PITCH + 2 * (32 - 8 * g + 4 * p) - 8;
    const u32 ga = CV_BASE + w * CV_WAVE + CV_U + 480 + 32 * fr + 16 * g;
    f32x4 Y[8];
#pragma unroll
    for (int i = 0; i < 8; i++) Y[i] = (f32x4){0, 0, 0, 0};
    CvSet A, B;
    cv_load(sm, A, fa, ga);
    SB();
#pragma unroll 1
    for (int k0 = 0; k0 < 8; k0 += 2) {
        cv_phase(sm, Y, A, B, fa + 64 * (k0 + 1), ga - 64 * (k0 + 1));
        cv_phase(sm, Y, B, A, fa + 64 * (k0 + 2), ga - 64 * (k0 + 2));
    }
    TS(4, 10);
    sm = sm0 + CV_BASE + w * CV_WAVE;
    {
        u32x2 u4[8];
#pragma unroll
        for (int ib = 0; ib < 8; ib++) u4[ib] = lds64(sm, CV_U + 512 + 512 * ib + 32 * fr + 8 * g);
#pragma unroll
        for (int ib = 0; ib < 8; ib++) {
            const u32x2 r = {pk2(fmaf(blo(u4[ib].x), Dc, Y[ib][0]), fmaf(bhi(u4[ib].x), Dc, Y[ib][1])), pk2(fmaf(blo(u4[ib].y), Dc, Y[ib][2]), fmaf(bhi(u4[ib].y), Dc, Y[ib][3]))};
            *(LAS u32x2*)(sm + 512 * ib + 32 * fr + 8 * g) = r;
        }
    }
    LGKM0();
    u32x4 y8[4];
#pragma unroll
    for (int z = 0; z < 4; z++) y8[z] = lds128(sm, z * 1024 + lane * 16);
#pragma unroll
    for (int z = 0; z < 4; z++) st128c(rw, WS_YT + c * 4096 + z * 1024 + lane * 16, y8[z]);
}

DI void kvo_block(LAS char* sm, int wave, int lane, int h, int half, rsrc_t rw, const u16* wo, int ct, char* ws) {
    const int fr = lane & 15, g = lane >> 4;
    const u32 pk0 = WS_PK0 + (h * 64 + 8 * wave) * 1024 + lane * 16, v0 = pk0 + (WS_V0 - WS_PK0);
    const u32 xx = WS_X + (h * 32 + 4 * wave) * 1024 + lane * 16;
    u32x4 A[8], B[8], X[4], Q[8];
#pragma unroll
    for (int i = 0; i < 8; i++) { A[i] = ld128c(rw, pk0 + i * 1024); B[i] = ld128c(rw, v0 + i * 1024); if ((i & 1) == 0) X[i >> 1] = ld128c(rw, xx + (i >> 1) * 1024); }
    const int odd = h & 1, pr = h >> 1;
    const int u5 = (g < 3 ? g : 2) + 2 * odd;
    const int sb0 = half * 64 + wave * 8;
    const u32 pq = WS_P2 + (((pr * 128 + sb0) * 5 + u5) * 128 + fr * 8) * 2;
#pragma unroll
    for (int i = 0; i < 8; i++) Q[i] = ld128c(rw, pq + i * 1280);
    MEMF(); SB();
    f32x4 a00 = {0, 0, 0, 0}, a0x = a00, ax0 = a00, axx = a00;
    const u32 cst = fr == 8 ? 0x3F803F80u : 0u;
#pragma unroll
    for (int i = 0; i < 8; i++) {
        u32x4 xv = X[i >> 1];
        if (i & 1) {
#pragma unroll
            for (int e = 0; e < 4; e++) xv[e] = (u32)__builtin_amdgcn_update_dpp(0, (int)xv[e], 0x108, 0xf, 0xf, false);
        }
        if (fr >= 8) xv = (u32x4){cst, cst, cst, cst};
        a00 = mfma16(A[i], B[i], a00); a0x = mfma16(A[i], xv, a0x); ax0 = mfma16(xv, B[i], ax0); axx = mfma16(xv, xv, axx);
    }
    TS(0, 9);
    *(LAS f32x4*)(sm + KO_RED + ((wave * 4 + 0) * 64 + lane) * 16) = a00;
    *(LAS f32x4*)(sm + KO_RED + ((wave * 4 + 1) * 64 + lane) * 16) = a0x;
    *(LAS f32x4*)(sm + KO_RED + ((wave * 4 + 2) * 64 + lane) * 16) = ax0;
    *(LAS f32x4*)(sm + KO_RED + ((wave * 4 + 3) * 64 + lane) * 16) = axx;
    if (wave >= 4 && (wave - 4) * 64 + lane < 189) *(LAS u32x2*)(sm + KO_KV + ((wave - 4) * 64 + lane) * 8) = (u32x2){0, 0};
    LGKM0(); BAR();
    if (wave < 4) {
        const int tile = wave;
        f32x4 s = *(LAS f32x4*)(sm + KO_RED + (tile * 64 + lane) * 16);
#pragma unroll
        for (int w2 = 1; w2 < 8; w2++) s += *(LAS f32x4*)(sm + KO_RED + ((w2 * 4 + tile) * 64 + lane) * 16);
        const int e = (tile & 1) ? 12 + fr : fr;
        const bool colok = (tile & 1) ? (fr >= 4 && fr <= 8) : true;
        const bool rowok = (tile & 2) ? (g == 0) : true;
        const int d0 = (tile & 2) ? 16 : 4 * g;
        if (colok && rowok) *(LAS u32x2*)(sm + KO_KV + e * 72 + (d0 + 4) * 2) = (u32x2){pk2(s[0], s[1]), pk2(s[2], s[3])};
    }
    LGKM0(); BAR();
    u32x4 A0, A1[4], A2;
    {
        const u32 dofs = KO_KV + (8 * g - 4 * odd + 4) * 2;
        const u32 r0 = (4 * odd + fr) * 72, r1 = ((odd ? 0 : 16) + (fr & 3)) * 72;
        const u32x2 p0 = lds64(sm, dofs + r0), p1 = lds64(sm, dofs + r0 + 8);
        const u32x2 q0 = lds64(sm, dofs + r1), q1 = lds64(sm, dofs + r1 + 8);
        const u32x2 z0 = lds64(sm, dofs + 20 * 72), z1 = lds64(sm, dofs + 20 * 72 + 8);
        A0 = (u32x4){p0.x, p0.y, p1.x, p1.y}; A2 = (u32x4){z0.x, z0.y, z1.x, z1.y};
#pragma unroll
        for (int z = 0; z < 4; z++) { const bool m = (fr >> 2) == z; A1[z] = (u32x4){m ? q0.x : 0u, m ? q0.y : 0u, m ? q1.x : 0u, m ? q1.y : 0u}; }
    }
    TS(0, 10);
    const int U0 = (5 * h + odd) >> 1;
    const int Uw = U0 + (g >> 1);
    const u32 offA = WS_O + (((Uw >> 2) * 4 + (Uw & 3)) * 128 + fr * 8) * 2 + (g & 1) * 20480;
    const int cgx = 5 * h + (odd ? 0 : 4), Ux = cgx >> 1;
    const u32 offX = WS_O + (((Ux >> 2) * 4 + (Ux & 3)) * 128 + fr * 8 + (cgx & 1) * 4) * 2;
    const f32x4 zero = {0, 0, 0, 0};
#pragma unroll
    for (int i4 = 0; i4 < 2; i4++) {
        f32x4 n1 = zero; float rden[4]; u32x2 ov[4];
#pragma unroll
        for (int z = 0; z < 4; z++) {
            const int i = 4 * i4 + z;
            const f32x4 n0 = mfma16(A0, Q[i], zero);
            const f32x4 dn = mfma16(A2, Q[i], zero);
            n1 = mfma16(A1[z], Q[i], n1);
            const float rd = __builtin_amdgcn_rcpf(dn[0] + 1e-6f);
            rden[z] = rd;
            ov[z] = (u32x2){pk2(n0[0] * rd, n0[1] * rd), pk2(n0[2] * rd, n0[3] * rd)};
        }
#pragma unroll
        for (int z = 0; z < 4; z += 2) {
            auto rx = __builtin_amdgcn_permlane16_swap(ov[z].x, ov[z + 1].x, false, false);
            auto ry = __builtin_amdgcn_permlane16_swap(ov[z].y, ov[z + 1].y, false, false);
            st128c(rw, offA + (sb0 + 4 * i4 + z) * 20480, (u32x4){rx[0], ry[0], rx[1], ry[1]});
        }
        const float rd = g == 0 ? rden[0] : g == 1 ? rden[1] : g == 2 ? rden[2] : rden[3];
        st64c(rw, offX + (sb0 + 4 * i4 + g) * 20480, (u32x2){pk2(n1[0] * rd, n1[1] * rd), pk2(n1[2] * rd, n1[3] * rd)});
    }
}

DI void gate_block(int wave, int lane, int m, const u16* x, const u16* gw, rsrc_t rw) {
    const int sub = lane & 15, r4 = lane >> 4;
    u32x4 gq[5], xr[4][5];
#pragma unroll
    for (int jj = 0; jj < 5; jj++) gq[jj] = *(const GAS u32x4*)((const GAS char*)gw + (sub + 16 * jj) * 16);
#pragma unroll
    for (int it = 0; it < 4; it++)
#pragma unroll
        for (int jj = 0; jj < 5; jj++) xr[it][jj] = *(const GAS u32x4*)((const GAS char*)x + (size_t)(128 * m + 16 * wave + 4 * it + r4) * 1280 + (sub + 16 * jj) * 16);
    MEMF(); SB();
#pragma unroll
    for (int it = 0; it < 4; it++) {
        float a = 0.f;
#pragma unroll
        for (int jj = 0; jj < 5; jj++)
#pragma unroll
            for (int e = 0; e < 4; e++) { a = fmaf(blo(xr[it][jj][e]), blo(gq[jj][e]), a); a = fmaf(bhi(xr[it][jj][e]), bhi(gq[jj][e]), a); }
        a += __shfl_xor(a, 1); a += __shfl_xor(a, 2); a += __shfl_xor(a, 4); a += __shfl_xor(a, 8);
        a = rbf(a);
        const float g = rbf(__builtin_amdgcn_rcpf(1.f + __builtin_amdgcn_exp2f(-1.4426950408889634f * a)));
        if (sub == 0) st32c(rw, WS_G + (128 * m + 16 * wave + 4 * it + r4) * 4, __float_as_uint(g));
    }
}

DI void final_block(LAS char* sm, int wave, int lane, int tid, int st, int ct, const u16* x, u16* out, rsrc_t rw, u32 tag, char* ws) {
    const int fr = lane & 15, g = lane >> 4;
    u32x4 xv[2];
#pragma unroll
    for (int r = 0; r < 2; r++) {
        int it = tid + 512 * r; it = it < 640 ? it : 639;
        const int s = it / 10, u = it - 10 * s;
        xv[r] = *(const GAS u32x4*)((const GAS char*)x + (size_t)(st * 64 + s) * 1280 + ct * 160 + u * 16);
    }
    VMCNT(0); BAR();
    if (wave < 4) {
        if (wave == 0) { wait_flags(rw, lane < 32 ? (int)F_O + (st >> 4) * 32 + lane : -1, -1, tag); lds_arrive(sm, C_FO, lane); }
        else lds_wait(sm, C_FO, 1);
        TS(0, 17);
        const int kh = wave & 1, sh = wave >> 1;
        const u32 ob = WS_O + ((st * 4 + 2 * sh) * 20 + 10 * kh) * 1024 + lane * 16;
        u32x4 B[2][10];
#pragma unroll
        for (int kl = 0; kl < 10; kl++) { B[0][kl] = ld128c(rw, ob + kl * 1024); B[1][kl] = ld128c(rw, ob + 20480 + kl * 1024); }
        MEMF(); SB();
        f32x4 acc[5][2];
#pragma unroll
        for (int cb = 0; cb < 5; cb++) { acc[cb][0] = (f32x4){0, 0, 0, 0}; acc[cb][1] = (f32x4){0, 0, 0, 0}; }
        const u32 aoff = (16 * (g >> 1) + 4 * (g & 1) + (fr >> 2)) * 160 + (lane & 3) * 8;
        u32x2 al[2][5], ah[2][5];
#define F_READ(buf, kl) do { const u32 b0_ = kh ? wo_off(10 + (kl)) : wo_off(kl); \
            _Pragma("unroll") for (int cb = 0; cb < 5; cb++) { al[buf][cb] = ldstr(sm, b0_ + aoff + cb * 32); ah[buf][cb] = ldstr(sm, b0_ + aoff + cb * 32 + 1280); } } while (0)
        F_READ(0, 0);
#pragma unroll
        for (int kl = 0; kl < 10; kl++) {
            if (kl < 9) F_READ((kl + 1) & 1, kl + 1);
            SB();
#pragma unroll
            for (int cb = 0; cb < 5; cb++) {
                const u32x4 a = {al[kl & 1][cb].x, al[kl & 1][cb].y, ah[kl & 1][cb].x, ah[kl & 1][cb].y};
                acc[cb][0] = mfma16(a, B[0][kl], acc[cb][0]); acc[cb][1] = mfma16(a, B[1][kl], acc[cb][1]);
            }
            SB();
        }
        TS(0, 18);
        lds_arrive(sm, C_FR, lane); lds_wait(sm, C_FR, 4);
#pragma unroll
        for (int cb = 0; cb < 5; cb++)
#pragma unroll
            for (int z = 0; z < 2; z++)
                *(LAS f32x4*)(sm + kh * (64 * RED_PITCH) + (16 * (2 * sh + z) + fr) * RED_PITCH + (16 * cb + 4 * g) * 4) = acc[cb][z];
    } else {
        if (wave == 4) { wait_flags(rw, lane < 20 ? (int)F_Y + ct * 32 + lane : lane == 32 ? (int)F_GATE + (st >> 1) : -1, -1, tag); lds_arrive(sm, C_FY, lane); }
        else lds_wait(sm, C_FY, 1);
        const int t = tid - 256;
        u32x4 yv[3];
#pragma unroll
        for (int r = 0; r < 3; r++) { int id = t + 256 * r; id = id < 640 ? id : 639; yv[r] = ld128c(rw, WS_YT + (80 * ct + (id >> 3)) * 4096 + st * 128 + (id & 7) * 16); }
#pragma unroll
        for (int r = 0; r < 3; r++) { const int id = t + 256 * r; if (id < 640) *(LAS u32x4*)(sm + F_YY + id * 16) = yv[r]; }
        if (wave == 4) *(LAS u32*)(sm + F_GG + lane * 4) = ld32c(rw, WS_G + (st * 64 + lane) * 4);
        lds_arrive(sm, C_FY2, lane); lds_wait(sm, C_FY2, 4);
        const int sb = wave - 4, q = fr >> 2, p = lane & 3;
        u32x2 tt[5];
#pragma unroll
        for (int cb = 0; cb < 5; cb++) tt[cb] = ldstr(sm, F_YY + (16 * cb + 4 * g + q) * 128 + (16 * sb + 4 * p) * 2);
#pragma unroll
        for (int cb = 0; cb < 5; cb++) *(LAS u32x2*)(sm + F_Y2 + (16 * sb + fr) * 176 + (16 * cb + 4 * g) * 2) = tt[cb];
    }
    LGKM0(); BAR();
#pragma unroll
    for (int r = 0; r < 2; r++) {
        const int it = tid + 512 * r;
        if (it < 640) {
            const int s = it / 10, u = it - 10 * s;
            const u32 ra = s * RED_PITCH + u * 32;
            const f32x4 p0 = *(LAS f32x4*)(sm + ra), p1 = *(LAS f32x4*)(sm + ra + 16), q0 = *(LAS f32x4*)(sm + 64 * RED_PITCH + ra), q1 = *(LAS f32x4*)(sm + 64 * RED_PITCH + ra + 16);
            const u32x4 y8 = lds128(sm, F_Y2 + s * 176 + u * 16);
            const float gg = *(LAS float*)(sm + F_GG + s * 4), omg = 1.f - gg;
            float o[8];
#pragma unroll
            for (int e = 0; e < 4; e++) {
                const float a0 = (e < 2 ? p0[2 * e] + q0[2 * e] : p1[2 * e - 4] + q1[2 * e - 4]), a1 = (e < 2 ? p0[2 * e + 1] + q0[2 * e + 1] : p1[2 * e - 3] + q1[2 * e - 3]);
                const float b0 = omg * blo(xv[r][e]), b1 = omg * bhi(xv[r][e]);
                o[2 * e] = fmaf(b0, blo(y8[e]), gg * a0); o[2 * e + 1] = fmaf(b1, bhi(y8[e]), gg * a1);
            }
            *(GAS u32x4*)((GAS char*)out + (size_t)(st * 64 + s) * 1280 + ct * 160 + u * 16) = (u32x4){pk2(o[0], o[1]), pk2(o[2], o[3]), pk2(o[4], o[5]), pk2(o[6], o[7])};
        }
    }
}

__global__ __launch_bounds__(512, 2) void fused_kernel(const u16* __restrict__ x, const u16* __restrict__ w, const u16* __restrict__ wo, const u16* __restrict__ kin,
                                                        const u16* __restrict__ vin, const u16* __restrict__ shk, const u16* __restrict__ hk, const u16* __restrict__ sD,
                                                        const u16* __restrict__ gw, u16* __restrict__ out, char* ws) {
    extern __shared__ __attribute__((aligned(16))) char smem[];
    LAS char* sm = (LAS char*)smem;
    const int tid = threadIdx.x, lane = tid & 63, wave = __builtin_amdgcn_readfirstlane(tid >> 6);
    const int b = blockIdx.x, xcd = b & 7, j = b >> 3;
    const rsrc_t rw = __builtin_amdgcn_make_buffer_rsrc(ws, 0, WS_BYTES, 0x00020000);
    u32 tag;
    asm volatile("s_load_dword %0, %1, 0x0\n\ts_waitcnt lgkmcnt(0)" : "=s"(tag) : "s"(ws + WS_FLAG + F_EPOCH * FS) : "memory");
    tag += 1u;
    if (tid < 16) *(LAS u32*)(sm + L_CNT + tid * 4) = 0u;
    TS(0, 0);
    const int st = 4 * xcd + (j >> 3), ct = j & 7;
    int ko = -1;
    bool is_conv = false;

    if (j >= 30) {
        const int m = xcd * 2 + (j - 30);
        ko = -100;
        {
            const int grp = wave >> 2, w4i = wave & 3;
            u32x2 w4[3];
#pragma unroll
            for (int t = 0; t < 3; t++) { const int tt = 2 * t + grp < 5 ? 2 * t + grp : 4; p0_weights(w4[t], shk, 240 + 5 * m + tt, w4i, lane); }
#pragma unroll
            for (int t = 0; t < 3; t++) if (2 * t + grp < 5) p0_issue(kin, vin, 240 + 5 * m + 2 * t + grp, w4i, lane, (2 * t + grp) * 17408);
            VMCNT(0);
#pragma unroll
            for (int t = 0; t < 3; t++) PIN(w4[t]);
            BAR();
#pragma unroll
            for (int t = 0; t < 3; t++)
                if (2 * t + grp < 5) p0_compute(sm, w4[t], 240 + 5 * m + 2 * t + grp, w4i, lane, (2 * t + grp) * 17408, 90112 + (2 * t + grp) * 8192);
            LGKM0(); BAR();
            {
                u32x4 t4[3][2];
#pragma unroll
                for (int t = 0; t < 3; t++)
#pragma unroll
                    for (int z = 0; z < 2; z++) { const int ch2 = ((tid & 255) >> 3) + 32 * z, o2 = tid & 7, tt = 2 * t + grp < 5 ? 2 * t + grp : 4; t4[t][z] = lds128(sm, 90112 + tt * 8192 + ch2 * 128 + ((o2 ^ (ch2 & 7)) * 16)); }
#pragma unroll
                for (int t = 0; t < 3; t++)
                    if (2 * t + grp < 5) {
                        const int tile = 240 + 5 * m + 2 * t + grp, cg = tile % 10, s0 = (tile / 10) * 64;
#pragma unroll
                        for (int z = 0; z < 2; z++) { const int ch2 = ((tid & 255) >> 3) + 32 * z, o2 = tid & 7; st128c(rw, WS_UT + (cg * 64 + ch2) * 4096 + (s0 + 8 * o2) * 2, t4[t][z]); }
                    }
            }
        }
        VMCNT(0); BAR();
        if (tid < 5) set_flag(rw, p0_flag(240 + 5 * m + tid), tag);
        TS(0, 1);
        gate_block(wave, lane, m, x, gw, rw);
        VMCNT(0); LGKM0(); BAR();
        if (tid == 0) set_flag(rw, F_GATE + m, tag);
        TS(0, 3);
    } else {
        const int rt = 2 * xcd + j / 15, cbk = j % 15, tile = xcd * 30 + j;
        is_conv = cbk < 10;
        if (!is_conv) { const int vi = rt * 5 + (cbk - 10); ko = vi < 64 ? vi : -2 - (vi - 64); }
        if (wave >= 4) {
            const int lw = wave - 4;
            p0_issue(kin, vin, tile, lw, lane, P0_K);
            u32 vx[4], vw[4];
#pragma unroll
            for (int n2 = 0; n2 < 4; n2++) {
                const int n = 4 * lw + n2;
                const int row = 8 * n + (lane >> 3), ch = (lane & 7) ^ ((row >> 1) & 7);
                vx[n2] = (u32)((rt * 128 + row) * 1280 + ch * 16);
                const int kr = 4 * n + (lane >> 4), f = ((kr & 3) << 2) | ((kr >> 2) & 3), cw = (lane & 15) ^ f;
                vw[n2] = (u32)(kr * 3840 + cbk * 256 + cw * 16);
            }
            const int rot = (rt + cbk) % 10;
#define K1_ISSUE(c) do { const u32 st_ = ((c) & 3) * K1_STAGE + 4 * lw * 1024; int kc_ = (c) + ROT_ON * rot; kc_ = kc_ >= 10 ? kc_ - 10 : kc_; \
            const char* xb_ = (const char*)x + kc_ * 128; const char* wp_ = (const char*)w + (size_t)kc_ * 64 * 3840; \
            _Pragma("unroll") for (int n2 = 0; n2 < 4; n2++) { glds16(xb_, vx[n2], st_ + 16384 + n2 * 1024); glds16(wp_, vw[n2], st_ + n2 * 1024); } } while (0)
            K1_ISSUE(0);
            VMCNT(8); BAR();
            K1_ISSUE(1); K1_ISSUE(2);
            VMCNT(16); BAR();
            K1_ISSUE(3);
#pragma unroll 1
            for (int c = 0; c < 6; c++) {
                VMCNT(16); BAR();
                K1_ISSUE(c + 4);
            }
            const int cv = rt * 10 + cbk, c = 4 * cv + lw;
            if (is_conv) {
                CvIn in; u32 fl; u32x4 u[4];
                const int fidx = (int)F_P0 + (cv >> 4) * 32 + (lane & 31);
                conv_fetch_h(in, hk, c, lane);
                VMCNT(24); BAR();
                VMCNT(16); BAR();
                gld32c(fl, ws, WS_FLAG + fidx * FS);
                VMCNT(9); BAR();
                TS(4, 4);
                VMCNT(0); PIN(fl);
                const bool ready = __all(fl == tag);
                if (ready) {
#pragma unroll
                    for (int z = 0; z < 4; z++) gld128c(u[z], ws, WS_UT + c * 4096 + z * 1024 + lane * 16);
                }
                TS(4, 5);
                conv_build_h(sm, in, lw, lane);
                TS(4, 6);
                if (!ready) {
                    wait_flags(rw, fidx, -1, tag);
#pragma unroll
                    for (int z = 0; z < 4; z++) gld128c(u[z], ws, WS_UT + c * 4096 + z * 1024 + lane * 16);
                }
                TS(4, 7);
                VMCNT(0);
#pragma unroll
                for (int z = 0; z < 4; z++) { PIN(u[z]); *(LAS u32x4*)(sm + CV_BASE + lw * CV_WAVE + CV_U + 512 + z * 1024 + lane * 16) = u[z]; }
                const float Dc = blo((u32)sD[c]);
                TS(4, 8);
                conv_run(sm, Dc, lw, lane, c, rw, ws);
                publish_last(rw, sm, C_Y, 4, F_Y + (cv / 20) * 32 + cv % 20, tag, lane);
                TS(4, 11);
            } else {
                VMCNT(16); BAR();
                VMCNT(8); BAR();
                VMCNT(0); BAR();
            }
        } else {
            u32x2 w4; p0_weights(w4, shk, tile, wave, lane);
            const int wm = wave >> 1, wn = wave & 1, r32 = lane & 31, hh = lane >> 5;
            K1Addr a0;
#pragma unroll
            for (int ks = 0; ks < 2; ks++) {
                a0.x[0][ks] = (64 * wm + r32) * 128 + (((2 * ks + hh) ^ ((r32 >> 1) & 7)) * 16);
                a0.x[1][ks] = a0.x[0][ks] ^ 64;
            }
#pragma unroll
            for (int jj = 0; jj < 2; jj++)
#pragma unroll
                for (int t = 0; t < 2; t++) {
                    const int blk = (lane >> 4) & 1, q = (lane & 15) >> 2, p = lane & 3, row = 8 * hh + 4 * t + q, ch = 8 * wn + 4 * jj + 2 * blk + (p >> 1);
                    const int f = ((row & 3) << 2) | ((row >> 2) & 3);
                    a0.w[jj][t] = row * 256 + ((ch ^ f) * 16) + 8 * (p & 1);
                }
            f32x16 acc[2][2];
#pragma unroll
            for (int i = 0; i < 2; i++)
#pragma unroll
                for (int jj = 0; jj < 2; jj++)
#pragma unroll
                    for (int e = 0; e < 16; e++) acc[i][jj][e] = 0.f;
            TS(0, 20);
            VMCNT(0); PIN(w4); TS(0, 21); BAR();
            TS(0, 22);
            p0_compute(sm, w4, tile, wave, lane, P0_K, P0_OUT);
            LGKM0(); TS(0, 23); BAR();
            TS(0, 24);
            p0_store(sm, rw, tile, tid, P0_OUT);
            TS(0, 1);
            if (cbk < 5) {
                k1_loop<true>(sm, a0, acc, rw, p0_flag(tile), tag, wave, lane);
                __builtin_amdgcn_s_setprio(2);
                TS(0, 2);
#pragma unroll
                for (int jj = 0; jj < 2; jj++)
#pragma unroll
                    for (int ap = 0; ap < 2; ap++) {
                        const int U = 16 * cbk + 8 * wn + 4 * jj + 2 * ap + hh, pr = U / 5, u5 = U - 5 * pr;
                        const u32 dst = WS_P2 + (((pr * 128 + rt * 8 + 4 * wm + (r32 >> 4)) * 5 + u5) * 128 + (r32 & 15) * 8) * 2;
#pragma unroll
                        for (int i = 0; i < 2; i++) {
                            const f32x16& a = acc[i][jj];
                            const u32 x0 = phi2(a[8 * ap], a[8 * ap + 1]), y0 = phi2(a[8 * ap + 2], a[8 * ap + 3]);
                            const u32 x1 = phi2(a[8 * ap + 4], a[8 * ap + 5]), y1 = phi2(a[8 * ap + 6], a[8 * ap + 7]);
                            auto rx = __builtin_amdgcn_permlane32_swap(x0, x1, false, false);
                            auto ry = __builtin_amdgcn_permlane32_swap(y0, y1, false, false);
                            st128c(rw, dst + i * 2560, (u32x4){rx[0], ry[0], rx[1], ry[1]});
                        }
                    }
            } else {
                k1_loop<false>(sm, a0, acc, rw, p0_flag(tile), tag, wave, lane);
                __builtin_amdgcn_s_setprio(2);
                TS(0, 2);
                const bool isk = cbk < 10;
                const int j5 = isk ? cbk - 5 : cbk - 10;
                u32 dst[2], istep[2];
#pragma unroll
                for (int jj = 0; jj < 2; jj++) {
                    const int cc = 128 * j5 + 64 * wn + 32 * jj + r32, h = cc / 20, d = cc - 20 * h;
                    const int sb32 = rt * 4 + 2 * wm;
                    const bool lo = d < 16;
                    dst[jj] = lo ? (isk ? WS_PK0 : WS_V0) + (((h * 64 + sb32) * 4 + hh) * 128 + d * 8) * 2
                                 : WS_X + (((h * 32 + (sb32 >> 1)) * 4 + hh) * 128 + (d - 16 + (isk ? 0 : 4)) * 8) * 2;
                    istep[jj] = lo ? 1024 : 128;
                }
                if (isk) {
#pragma unroll
                    for (int jj = 0; jj < 2; jj++)
#pragma unroll
                        for (int i = 0; i < 2; i++)
#pragma unroll
                            for (int z = 0; z < 2; z++) {
                                const f32x16& a = acc[i][jj];
                                st128c(rw, dst[jj] + i * istep[jj] + z * 512,
                                       (u32x4){phi2(a[8 * z], a[8 * z + 1]), phi2(a[8 * z + 2], a[8 * z + 3]), phi2(a[8 * z + 4], a[8 * z + 5]), phi2(a[8 * z + 6], a[8 * z + 7])});
                            }
                } else {
#pragma unroll
                    for (int jj = 0; jj < 2; jj++)
#pragma unroll
                        for (int i = 0; i < 2; i++)
#pragma unroll
                            for (int z = 0; z < 2; z++) {
                                const f32x16& a = acc[i][jj];
                                st128c(rw, dst[jj] + i * istep[jj] + z * 512,
                                       (u32x4){pk2(a[8 * z], a[8 * z + 1]), pk2(a[8 * z + 2], a[8 * z + 3]), pk2(a[8 * z + 4], a[8 * z + 5]), pk2(a[8 * z + 6], a[8 * z + 7])});
                            }
                }
            }
            LGKM0();
            TS(0, 25);
            publish_last(rw, sm, C_G1, 4, F_G1 + cbk * 16 + rt, tag, lane);
            __builtin_amdgcn_s_setprio(0);
            TS(0, 3);
            if (is_conv) {
                lds_wait(sm, C_G1, 4);
                wo_issue(wo, ct, 0, 12, wave, 4, lane);
            }
        }
    }
    if (!is_conv) {
        LGKM0(); BAR();
        if (ko >= 0) {
            const int h = ko >> 1, half = ko & 1;
            wo_issue(wo, ct, 0, 12, wave, 8, lane);
            if (wave == 0) {
                const int j0 = (20 * h) >> 7, j1 = (20 * h + 19) >> 7;
                const int l16 = lane & 15, sel = lane >> 4;
                const int i0 = (int)F_G1 + 16 * ((sel < 2 ? 5 : 10) + ((sel & 1) ? j1 : j0)) + l16;
                const int i1 = lane < 16 ? (int)F_G1 + 16 * (lane < 8 ? j0 : j1) + 8 * half + (lane & 7) : -1;
                wait_flags(rw, i0, i1, tag);
            }
            BAR();
            TS(0, 8);
            kvo_block(sm, wave, lane, h, half, rw, wo, ct, ws);
            TS(0, 26);
            VMCNT(0); BAR();
            if (tid == 0) set_flag(rw, F_O + half * 32 + h, tag);
            TS(0, 11);
            wo_issue(wo, ct, 12, 20, wave, 8, lane);
        } else {
            wo_issue(wo, ct, 0, 20, wave, 8, lane);
            if (ko == -2 && wave == 0) {
                wait_flags(rw, (int)F_G1 + lane, (int)F_G1 + 64 + lane, tag);
                wait_flags(rw, (int)F_G1 + 128 + lane, (int)F_G1 + 192 + lane, tag);
                if (lane == 0) set_flag(rw, F_EPOCH, tag);
            }
        }
    } else {
        VMCNT(0); BAR();
        if (wave >= 4) wo_issue(wo, ct, 12, 20, wave - 4, 4, lane);
    }
    TS(0, 16);
    final_block(sm, wave, lane, tid, st, ct, x, out, rw, tag, ws);
    TS(0, 19);
}

extern "C" void kernel_launch(void* const* d_in, const int* in_sizes, int n_in, void* d_out, int out_size, void* d_ws, size_t ws_size, hipStream_t stream) {
    const u16* x = (const u16*)d_in[0]; const u16* wqkv = (const u16*)d_in[1]; const u16* wo = (const u16*)d_in[2];
    const u16* kin = (const u16*)d_in[4]; const u16* vin = (const u16*)d_in[5]; const u16* shk = (const u16*)d_in[6];
    const u16* hk = (const u16*)d_in[7]; const u16* sD = (const u16*)d_in[8]; const u16* gw = (const u16*)d_in[9];
    (void)hipFuncSetAttribute((const void*)fused_kernel, hipFuncAttributeMaxDynamicSharedMemorySize, LDS_BYTES);
    fused_kernel<<<256, 512, LDS_BYTES, stream>>>(x, wqkv, wo, kin, vin, shk, hk, sD, gw, (u16*)d_out, (char*)d_ws);
}
```

```cpp
#include <hip/hip_runtime.h>
#include <stdint.h>

typedef unsigned short u16;
typedef unsigned int u32;
typedef __bf16 bf16x8 __attribute__((ext_vector_type(8)));
typedef __bf16 bf16x2 __attribute__((ext_vector_type(2)));
typedef float f32x4 __attribute__((ext_vector_type(4)));
typedef float f32x2 __attribute__((ext_vector_type(2)));
typedef float f32x16 __attribute__((ext_vector_type(16)));
typedef u32 u32x4 __attribute__((ext_vector_type(4)));
typedef u32 u32x2 __attribute__((ext_vector_type(2)));
typedef short s16x4 __attribute__((ext_vector_type(4)));
#define LAS __attribute__((address_space(3)))
#define GAS __attribute__((address_space(1)))
#define DI __device__ __forceinline__
typedef __amdgpu_buffer_rsrc_t rsrc_t;

constexpr int S = 2048, D = 640;
#define FS 64
#define POLL_SLEEP 2
#define ROT_ON 1

constexpr u32 WS_UT = 0;
constexpr u32 WS_YT = WS_UT + 2621440;
constexpr u32 WS_PK0 = WS_YT + 2621440;
constexpr u32 WS_V0 = WS_PK0 + 2097152;
constexpr u32 WS_X = WS_V0 + 2097152;
constexpr u32 WS_P2 = WS_X + 1048576;
constexpr u32 WS_O = WS_P2 + 2621440;
constexpr u32 WS_G = WS_O + 2621440;
constexpr u32 WS_FLAG = WS_G + 8192;
constexpr u32 F_P0 = 0;
constexpr u32 F_G1 = 320;
constexpr u32 F_GATE = 560;
constexpr u32 F_O = 576;
constexpr u32 F_Y = 640;
constexpr u32 F_EPOCH = 896, F_TMO = 928;
constexpr u32 WS_STAMP = WS_FLAG + 1024 * FS;
constexpr u32 WS_BYTES = WS_STAMP + 256 * 32 * 16;

#define TS(w, id) do {} while (0)

DI u32 pk2(float a, float b) { f32x2 t = {a, b}; return __builtin_bit_cast(u32, __builtin_convertvector(t, bf16x2)); }
DI float blo(u32 u) { return __uint_as_float(u << 16); }
DI float bhi(u32 u) { return __uint_as_float(u & 0xffff0000u); }
DI float rbf(float f) { return blo(pk2(f, f)); }
DI bf16x8 asbf(u32x4 v) { return __builtin_bit_cast(bf16x8, v); }
DI f32x4 mfma16(u32x4 a, u32x4 b, f32x4 c) { return __builtin_amdgcn_mfma_f32_16x16x32_bf16(asbf(a), asbf(b), c, 0, 0, 0); }
DI f32x16 mfma32(u32x4 a, u32x4 b, f32x16 c) { return __builtin_amdgcn_mfma_f32_32x32x16_bf16(asbf(a), asbf(b), c, 0, 0, 0); }

DI void glds16(const void* sbase, u32 voff, u32 lds) {
    asm volatile("s_mov_b32 m0, %2\n\ts_nop 0\n\tglobal_load_lds_dwordx4 %0, %1" :: "v"(voff), "s"(sbase), "s"(lds) : "memory");
}
DI void gld64(u32x2& d, const void* sbase, u32 voff) { asm volatile("global_load_dwordx2 %0, %1, %2" : "=v"(d) : "v"(voff), "s"(sbase) : "memory"); }
DI void gld128(u32x4& d, const void* sbase, u32 voff) { asm volatile("global_load_dwordx4 %0, %1, %2" : "=v"(d) : "v"(voff), "s"(sbase) : "memory"); }
DI void gld128c(u32x4& d, const void* sbase, u32 voff) { asm volatile("global_load_dwordx4 %0, %1, %2 sc1" : "=v"(d) : "v"(voff), "s"(sbase) : "memory"); }
DI void gld32c(u32& d, const void* sbase, u32 voff) { asm volatile("global_load_dword %0, %1, %2 sc1" : "=v"(d) : "v"(voff), "s"(sbase) : "memory"); }
#define PIN(x) asm volatile("" : "+v"(x))
#define SB() __builtin_amdgcn_sched_barrier(0)
DI u32x4 ld128c(rsrc_t r, u32 off) { return __builtin_amdgcn_raw_buffer_load_b128(r, off, 0, 16); }
DI u32 ld32c(rsrc_t r, u32 off) { return __builtin_amdgcn_raw_buffer_load_b32(r, off, 0, 16); }
DI void st128c(rsrc_t r, u32 off, u32x4 v) { __builtin_amdgcn_raw_buffer_store_b128(v, r, off, 0, 16); }
DI void st64c(rsrc_t r, u32 off, u32x2 v) { __builtin_amdgcn_raw_buffer_store_b64(v, r, off, 0, 16); }
DI void st32c(rsrc_t r, u32 off, u32 v) { __builtin_amdgcn_raw_buffer_store_b32(v, r, off, 0, 16); }
DI u32x4 lds128(LAS char* sm, u32 a) { return *(LAS u32x4*)(sm + a); }
DI u32x2 lds64(LAS char* sm, u32 a) { return *(LAS u32x2*)(sm + a); }
DI u32x2 ldstr(LAS char* sm, u32 a) { return __builtin_bit_cast(u32x2, __builtin_amdgcn_ds_read_tr16_b64_v4i16((LAS s16x4*)(sm + a))); }
#define VMCNT(n) asm volatile("s_waitcnt vmcnt(" #n ")" ::: "memory")
#define LGKM0() asm volatile("s_waitcnt lgkmcnt(0)" ::: "memory")
#define BAR() do { asm volatile("" ::: "memory"); __builtin_amdgcn_s_barrier(); asm volatile("" ::: "memory"); } while (0)
#define MEMF() asm volatile("" ::: "memory")

DI void set_flag(rsrc_t r, u32 idx, u32 tag) { st32c(r, WS_FLAG + idx * FS, tag); }
DI void wait_flags(rsrc_t r, int idx0, int idx1, u32 tag) {
    const unsigned long long t0 = __builtin_amdgcn_s_memrealtime();
    for (u32 it = 1;; it++) {
        u32 a = tag, b = tag;
        if (idx0 >= 0) a = ld32c(r, WS_FLAG + idx0 * FS);
        if (idx1 >= 0) b = ld32c(r, WS_FLAG + idx1 * FS);
        if (__all(a == tag && b == tag)) break;
        if (POLL_SLEEP) __builtin_amdgcn_s_sleep(POLL_SLEEP);
        if ((it & 63u) == 0u && __builtin_amdgcn_s_memrealtime() - t0 > 2000000ull) { set_flag(r, F_TMO, tag); break; }
    }
    MEMF();
}
DI void lds_arrive(LAS char* sm, u32 cnt, int lane) { LGKM0(); if (lane == 0) __hip_atomic_fetch_add((LAS u32*)(sm + cnt), 1u, __ATOMIC_RELAXED, __HIP_MEMORY_SCOPE_WORKGROUP); MEMF(); }
DI void lds_wait(LAS char* sm, u32 cnt, u32 n) { MEMF(); while (*(volatile LAS u32*)(sm + cnt) < n) __builtin_amdgcn_s_sleep(0); MEMF(); }
DI void publish_last(rsrc_t r, LAS char* sm, u32 cnt, u32 nw, u32 flag, u32 tag, int lane) {
    VMCNT(0);
    if (lane == 0) {
        const u32 old = __hip_atomic_fetch_add((LAS u32*)(sm + cnt), 1u, __ATOMIC_RELAXED, __HIP_MEMORY_SCOPE_WORKGROUP);
        if (old == nw - 1) set_flag(r, flag, tag);
    }
    MEMF();
}

DI u32 phi2(float a, float b) {
    const f32x2 t = {a, b}, s = t * 1.4426950408889634f;
    const float ea = __builtin_amdgcn_fmed3f(__builtin_amdgcn_exp2f(s.x), 0.f, 1.f), eb = __builtin_amdgcn_fmed3f(__builtin_amdgcn_exp2f(s.y), 0.f, 1.f);
    const float ma = __int_as_float(max(__float_as_int(a), 0)), mb = __int_as_float(max(__float_as_int(b), 0));
    const f32x2 r = ((f32x2){ea, eb} + (f32x2){ma, mb}) + 1e-6f;
    return pk2(r.x, r.y);
}

constexpr int K1_STAGE = 32768;
constexpr int P0_K = 131072, P0_V = P0_K + 9216, P0_OUT = P0_V + 8192;
constexpr int L_CNT = 156672;
constexpr int C_G1 = L_CNT, C_Y = L_CNT + 4, C_P0A = L_CNT + 8, C_P0B = L_CNT + 12, C_P0C = L_CNT + 16, C_FY = L_CNT + 20, C_FY2 = L_CNT + 24, C_FO = L_CNT + 28, C_FR = L_CNT + 32;
constexpr int CV_PITCH = 4160;
constexpr int CV_U = 4 * CV_PITCH;
constexpr int CV_WAVE = CV_U + 4608;
constexpr int CV_BASE = 65536;
constexpr int WO_HI = 65536;
constexpr int F_YY = 110592;
constexpr int F_Y2 = F_YY + 10240;
constexpr int F_GG = F_Y2 + 11264;
constexpr int KO_RED = 110592;
constexpr int KO_KV = KO_RED + 32768;
constexpr int RED_PITCH = 336;
constexpr int LDS_BYTES = 160 * 1024;
DI u32 p0_flag(int tile) { return F_P0 + (tile % 10) * 32 + tile / 10; }
DI constexpr u32 wo_off(int kap) { return kap < 12 ? kap * 5120 : WO_HI + (kap - 12) * 5120; }

DI void p0_issue(const u16* kin, const u16* vin, int tile, int lw, int lane, u32 base) {
    const int cg = tile % 10, rg = tile / 10, s0 = rg * 64;
    const u32 col = cg * 128 + (lane & 7) * 16;
#pragma unroll
    for (int z = 0; z < 2; z++) {
        const int n = 2 * lw + z;
        int r = s0 - 4 + n * 8 + (lane >> 3); r = r < 0 ? 0 : r;
        glds16(kin, (u32)(r * 1280) + col, base + n * 1024);
        glds16(vin, (u32)((s0 + n * 8 + (lane >> 3)) * 1280) + col, base + 9216 + n * 1024);
    }
    {
        int r = s0 + 60 + (lane >> 3); r = r > S - 1 ? S - 1 : r;
        glds16(kin, (u32)(r * 1280) + col, base + 8 * 1024);
    }
}
DI float dot2(u32 a, u32 b, float c) { return __builtin_amdgcn_fdot2_f32_bf16(__builtin_bit_cast(bf16x2, a), __builtin_bit_cast(bf16x2, b), c, false); }
DI void p0_compute(LAS char* sm, u32x2 w4, int tile, int wave, int lane, u32 base, u32 outb, char* ws = nullptr) {
    const int s0 = (tile / 10) * 64;
    const int G = wave * 4 + (lane >> 4), cb = G & 3, i = lane & 15, q = i >> 2, p = i & 3;
    const u32 wa = __builtin_amdgcn_alignbit(w4.y, w4.y, 16), wb = __builtin_amdgcn_alignbit(w4.x, w4.x, 16);
    const int ch = 16 * cb + i;
    u32x2 kq[2][3], vq[2][2];
#pragma unroll
    for (int z = 0; z < 2; z++) {
        const int o = (G >> 2) + 4 * z;
        const u32 ka = base + (8 * o + q) * 128 + 32 * cb + 8 * p, va = ka + 9216;
        kq[z][0] = ldstr(sm, ka); kq[z][1] = ldstr(sm, ka + 512); kq[z][2] = ldstr(sm, ka + 1024);
        vq[z][0] = ldstr(sm, va); vq[z][1] = ldstr(sm, va + 512);
    }
#pragma unroll
    for (int z = 0; z < 2; z++) {
        const int o = (G >> 2) + 4 * z;
        u32 K[6] = {kq[z][0].x, kq[z][0].y, kq[z][1].x, kq[z][1].y, kq[z][2].x, kq[z][2].y};
        if (s0 == 0 && o == 0) { K[0] = 0; K[1] = 0; }
        u32 Sh[5];
#pragma unroll
        for (int e = 0; e < 5; e++) Sh[e] = __builtin_amdgcn_alignbit(K[e + 1], K[e], 16);
        const u32 V[4] = {vq[z][0].x, vq[z][0].y, vq[z][1].x, vq[z][1].y};
        u32 r[4];
#pragma unroll
        for (int e = 0; e < 4; e++) {
            const float k0 = dot2(Sh[e], wa, dot2(Sh[e + 1], wb, 0.f)), k1 = dot2(K[e + 1], wa, dot2(K[e + 2], wb, 0.f));
            r[e] = pk2(k0 * blo(V[e]), k1 * bhi(V[e]));
        }
        *(LAS u32x4*)(sm + outb + ch * 128 + ((o ^ (ch & 7)) * 16)) = (u32x4){r[0], r[1], r[2], r[3]};
    }
}
DI void p0_store(LAS char* sm, rsrc_t rw, int tile, int t256, u32 outb) {
    const int cg = tile % 10, s0 = (tile / 10) * 64;
#pragma unroll
    for (int z = 0; z < 2; z++) {
        const int ch2 = (t256 >> 3) + 32 * z, o2 = t256 & 7;
        const u32x4 t4 = lds128(sm, outb + ch2 * 128 + ((o2 ^ (ch2 & 7)) * 16));
        st128c(rw, WS_UT + (cg * 64 + ch2) * 4096 + (s0 + 8 * o2) * 2, t4);
    }
}
DI void p0_weights(u32x2& w4, const u16* shk, int tile, int wave, int lane) {
    const int cg = tile % 10, cb = (wave * 4 + (lane >> 4)) & 3;
    gld64(w4, shk, (u32)((cg * 64 + 16 * cb + (lane & 15)) * 8));
}

struct K1Set { u32x4 xa[2][2]; u32x2 wl[2][2], wh[2][2]; };
struct K1Addr { u32 x[2][2]; u32 w[2][2]; };
template <bool QT, int WO, int XO>
DI void k1_phase(LAS char* sm, f32x16 (&acc)[2][2], const K1Set& cur, K1Set& nxt, const u32 (&xb)[2], const u32 (&wb)[2][2]) {
#pragma unroll
    for (int n = 0; n < 8; n++) {
        const int ks = n >> 2, i = (n >> 1) & 1, jj = n & 1;
        const u32x4 wf = {cur.wl[ks][jj].x, cur.wl[ks][jj].y, cur.wh[ks][jj].x, cur.wh[ks][jj].y};
        acc[i][jj] = QT ? mfma32(wf, cur.xa[ks][i], acc[i][jj]) : mfma32(cur.xa[ks][i], wf, acc[i][jj]);
        if (n == 0) { nxt.wl[0][0] = ldstr(sm, wb[0][0] + WO); nxt.wh[0][0] = ldstr(sm, wb[0][1] + WO); }
        if (n == 1) { nxt.xa[0][0] = lds128(sm, xb[0] + XO); nxt.wl[0][1] = ldstr(sm, wb[1][0] + WO); }
        if (n == 2) { nxt.wh[0][1] = ldstr(sm, wb[1][1] + WO); nxt.xa[0][1] = lds128(sm, xb[0] + XO + 4096); }
        if (n == 3) { nxt.wl[1][0] = ldstr(sm, wb[0][0] + WO + 4096); nxt.wh[1][0] = ldstr(sm, wb[0][1] + WO + 4096); }
        if (n == 4) { nxt.xa[1][0] = lds128(sm, xb[1] + XO); nxt.wl[1][1] = ldstr(sm, wb[1][0] + WO + 4096); }
        if (n == 5) { nxt.wh[1][1] = ldstr(sm, wb[1][1] + WO + 4096); nxt.xa[1][1] = lds128(sm, xb[1] + XO + 4096); }
        SB();
    }
}
template <bool QT, int ST>
DI void k1_chunk(LAS char* sm, f32x16 (&acc)[2][2], K1Set& s0, K1Set& s1, const K1Addr& a0, const K1Addr& a1, bool bar, bool drain = false) {
    constexpr int SN = (ST + 1) & 3;
    const K1Addr& ac = ST < 2 ? a0 : a1; const K1Addr& an = SN < 2 ? a0 : a1;
    k1_phase<QT, (ST & 1) * K1_STAGE + 8192, (ST & 1) * K1_STAGE + 16384>(sm, acc, s0, s1, ac.x[1], ac.w);
    if (drain) VMCNT(0);
    if (bar) { LGKM0(); BAR(); }
    k1_phase<QT, (SN & 1) * K1_STAGE, (SN & 1) * K1_STAGE + 16384>(sm, acc, s1, s0, an.x[0], an.w);
}
template <bool QT>
DI void k1_loop(LAS char* sm, const K1Addr& a0, f32x16 (&acc)[2][2], rsrc_t rw, u32 pflag, u32 tag, int wave, int lane) {
    K1Addr a1;
#pragma unroll
    for (int i = 0; i < 2; i++)
#pragma unroll
        for (int z = 0; z < 2; z++) { a1.x[i][z] = a0.x[i][z] + 65536; a1.w[i][z] = a0.w[i][z] + 65536; }
    K1Set s0, s1;
#pragma unroll
    for (int ks = 0; ks < 2; ks++)
#pragma unroll
        for (int jj = 0; jj < 2; jj++) {
            s0.wl[ks][jj] = ldstr(sm, a0.w[jj][0] + ks * 4096); s0.wh[ks][jj] = ldstr(sm, a0.w[jj][1] + ks * 4096);
            s0.xa[ks][jj] = lds128(sm, a0.x[0][ks] + 16384 + jj * 4096);
        }
    SB();
#pragma unroll 1
    for (int c = 0; c < 8; c += 4) {
        k1_chunk<QT, 0>(sm, acc, s0, s1, a0, a1, true);
        k1_chunk<QT, 1>(sm, acc, s0, s1, a0, a1, true);
        k1_chunk<QT, 2>(sm, acc, s0, s1, a0, a1, true);
        k1_chunk<QT, 3>(sm, acc, s0, s1, a0, a1, true, c == 0);
        if (c == 0 && wave == 0 && lane == 0) set_flag(rw, pflag, tag);
    }
    k1_chunk<QT, 0>(sm, acc, s0, s1, a0, a1, true);
    k1_chunk<QT, 1>(sm, acc, s0, s1, a0, a1, false);
}

DI void wo_issue(const u16* wo, int ct, int k0, int k1, int w, int nw, int lane) {
    u32 vo[5];
#pragma unroll
    for (int i = 0; i < 5; i++) { const int n = 64 * i + lane, rho = n / 10, c = n - 10 * rho, row = (rho & 19) | ((rho & 4) << 1) | ((rho & 8) >> 1); vo[i] = (u32)(row * 1280 + ct * 160 + c * 16); }
#pragma unroll 1
    for (int kap = k0 + w; kap < k1; kap += nw) {
        const char* src = (const char*)wo + (size_t)kap * 40960;
        const u32 dst = kap < 12 ? kap * 5120 : WO_HI + (kap - 12) * 5120;
#pragma unroll
        for (int i = 0; i < 5; i++) glds16(src, vo[i], dst + i * 1024);
    }
}

struct CvSet { u32x2 fl[8], fh[8]; u32x4 g[8]; };
DI void cv_load(LAS char* sm, CvSet& s, u32 fa, u32 ga) {
#pragma unroll
    for (int k1 = 0; k1 < 8; k1++) { s.fl[k1] = ldstr(sm, fa + 512 * k1 + 8); s.fh[k1] = ldstr(sm, fa + 512 * k1); s.g[k1] = lds128(sm, ga + 512 * k1); }
}
DI void cv_phase(LAS char* sm, f32x4 (&Y)[8], const CvSet& cur, CvSet& nxt, u32 fa, u32 ga) {
    PIN(fa); PIN(ga);
    SB();
    int n = 0;
#pragma unroll
    for (int e = 0; e < 8; e++)
#pragma unroll
        for (int k1 = 0; k1 + e < 8; k1++) {
            const u32x4 F = {cur.fl[k1].x, cur.fl[k1].y, cur.fh[k1].x, cur.fh[k1].y};
            Y[k1 + e] = mfma16(F, cur.g[e], Y[k1 + e]);
            if (n == 0) nxt.g[0] = lds128(sm, ga);
            else if (n <= 16) { const int r = (n - 1) >> 1; if ((n - 1) & 1) nxt.fh[r] = ldstr(sm, fa + 512 * r); else nxt.fl[r] = ldstr(sm, fa + 512 * r + 8); }
            else if (n < 24) nxt.g[n - 16] = lds128(sm, ga + 512 * (n - 16));
            n++;
            SB();
        }
}
struct CvIn { u32x4 hd[4]; u32x2 hm[4]; };
DI void conv_fetch_h(CvIn& in, const u16* hk, int c, int lane) {
    const char* hb = (const char*)hk + (size_t)c * 4096;
#pragma unroll
    for (int z = 0; z < 4; z++) {
        const int i = lane + 64 * z;
        gld128(in.hd[z], hb, (u32)(i * 16));
        gld64(in.hm[z], hb, (u32)(i > 0 ? i * 16 - 8 : 0));
    }
}
DI void conv_build_h(LAS char* sm0, CvIn& in, int w, int lane) {
    LAS char* sm = sm0 + CV_BASE + w * CV_WAVE;
#pragma unroll
    for (int z = 0; z < 4; z++) { PIN(in.hd[z]); PIN(in.hm[z]); }
    if (lane < 32) *(LAS u32x4*)(sm + CV_U + lane * 16) = (u32x4){0, 0, 0, 0};
    if (lane < 8) *(LAS u32x4*)(sm + (lane >> 1) * CV_PITCH + (lane & 1) * 16) = (u32x4){0, 0, 0, 0};
#pragma unroll
    for (int z = 0; z < 4; z++) {
        const int i = lane + 64 * z;
        u32 m2 = in.hm[z].x, m1 = in.hm[z].y;
        if (i == 0) { m2 = 0; m1 = 0; }
        const u32 d0 = in.hd[z].x, d1 = in.hd[z].y, d2 = in.hd[z].z, d3 = in.hd[z].w;
        const u32 om2 = __builtin_amdgcn_alignbit(m1, m2, 16), om1 = __builtin_amdgcn_alignbit(d0, m1, 16), o0 = __builtin_amdgcn_alignbit(d1, d0, 16),
                  o1 = __builtin_amdgcn_alignbit(d2, d1, 16), o2 = __builtin_amdgcn_alignbit(d3, d2, 16);
        LAS char* p = sm + 32 + i * 16;
        *(LAS u32x4*)(p) = (u32x4){d0, d1, d2, d3};
        *(LAS u32x4*)(p + CV_PITCH) = (u32x4){om1, o0, o1, o2};
        *(LAS u32x4*)(p + 2 * CV_PITCH) = (u32x4){m1, d0, d1, d2};
        *(LAS u32x4*)(p + 3 * CV_PITCH) = (u32x4){om2, om1, o0, o1};
    }
}
DI void conv_run(LAS char* sm0, float Dc, int w, int lane, int c, rsrc_t rw, char* ws) {
    LAS char* sm = sm0;
    LGKM0();
    TS(4, 9);
    const int fr = lane & 15, g = lane >> 4, q = fr >> 2, p = lane & 3;
    sm = sm0;
    const u32 fa = CV_BASE + w * CV_WAVE + q * CV_PITCH + 2 * (32 - 8 * g + 4 * p) - 8;
    const u32 ga = CV_BASE + w * CV_WAVE + CV_U + 480 + 32 * fr + 16 * g;
    f32x4 Y[8];
#pragma unroll
    for (int i = 0; i < 8; i++) Y[i] = (f32x4){0, 0, 0, 0};
    CvSet A, B;
    cv_load(sm, A, fa, ga);
    SB();
#pragma unroll 1
    for (int k0 = 0; k0 < 8; k0 += 2) {
        cv_phase(sm, Y, A, B, fa + 64 * (k0 + 1), ga - 64 * (k0 + 1));
        cv_phase(sm, Y, B, A, fa + 64 * (k0 + 2), ga - 64 * (k0 + 2));
    }
    TS(4, 10);
    sm = sm0 + CV_BASE + w * CV_WAVE;
    {
        u32x2 u4[8];
#pragma unroll
        for (int ib = 0; ib < 8; ib++) u4[ib] = lds64(sm, CV_U + 512 + 512 * ib + 32 * fr + 8 * g);
#pragma unroll
        for (int ib = 0; ib < 8; ib++) {
            const u32x2 r = {pk2(fmaf(blo(u4[ib].x), Dc, Y[ib][0]), fmaf(bhi(u4[ib].x), Dc, Y[ib][1])), pk2(fmaf(blo(u4[ib].y), Dc, Y[ib][2]), fmaf(bhi(u4[ib].y), Dc, Y[ib][3]))};
            *(LAS u32x2*)(sm + 512 * ib + 32 * fr + 8 * g) = r;
        }
    }
    LGKM0();
    u32x4 y8[4];
#pragma unroll
    for (int z = 0; z < 4; z++) y8[z] = lds128(sm, z * 1024 + lane * 16);
#pragma unroll
    for (int z = 0; z < 4; z++) st128c(rw, WS_YT + c * 4096 + z * 1024 + lane * 16, y8[z]);
}

DI void kvo_block(LAS char* sm, int wave, int lane, int h, int half, rsrc_t rw, u32 tag, char* ws) {
    const int fr = lane & 15, g = lane >> 4;
    const int j0 = (20 * h) >> 7, j1 = (20 * h + 19) >> 7;
    wait_flags(rw, lane < 8 ? (int)F_G1 + 16 * (((lane & 2) ? 10 : 5) + ((lane & 1) ? j1 : j0)) + 2 * wave + (lane >> 2) : -1, -1, tag);
    TS(0, 8);
    const u32 pk0 = WS_PK0 + (h * 64 + 8 * wave) * 1024 + lane * 16, v0 = pk0 + (WS_V0 - WS_PK0);
    const u32 xx = WS_X + (h * 32 + 4 * wave) * 1024 + lane * 16;
    u32x4 A[8], B[8], X[4], Q[8];
#pragma unroll
    for (int i = 0; i < 8; i++) { A[i] = ld128c(rw, pk0 + i * 1024); B[i] = ld128c(rw, v0 + i * 1024); if ((i & 1) == 0) X[i >> 1] = ld128c(rw, xx + (i >> 1) * 1024); }
    const int odd = h & 1, pr = h >> 1;
    const int u5 = (g < 3 ? g : 2) + 2 * odd;
    const int sb0 = half * 64 + wave * 8;
    const u32 pq = WS_P2 + (((pr * 128 + sb0) * 5 + u5) * 128 + fr * 8) * 2;
    MEMF(); SB();
    f32x4 a00 = {0, 0, 0, 0}, a0x = a00, ax0 = a00, axx = a00;
    const u32 cst = fr == 8 ? 0x3F803F80u : 0u;
#pragma unroll
    for (int i = 0; i < 8; i++) {
        u32x4 xv = X[i >> 1];
        if (i & 1) {
#pragma unroll
            for (int e = 0; e < 4; e++) xv[e] = (u32)__builtin_amdgcn_update_dpp(0, (int)xv[e], 0x108, 0xf, 0xf, false);
        }
        if (fr >= 8) xv = (u32x4){cst, cst, cst, cst};
        a00 = mfma16(A[i], B[i], a00); a0x = mfma16(A[i], xv, a0x); ax0 = mfma16(xv, B[i], ax0); axx = mfma16(xv, xv, axx);
    }
    TS(0, 9);
    PIN(a00); PIN(a0x); PIN(ax0); PIN(axx);
    wait_flags(rw, lane < 2 ? (int)F_G1 + 16 * (lane ? j1 : j0) + 8 * half + wave : -1, -1, tag);
#pragma unroll
    for (int i = 0; i < 8; i++) Q[i] = ld128c(rw, pq + i * 1280);
    MEMF(); SB();
    *(LAS f32x4*)(sm + KO_RED + ((wave * 4 + 0) * 64 + lane) * 16) = a00;
    *(LAS f32x4*)(sm + KO_RED + ((wave * 4 + 1) * 64 + lane) * 16) = a0x;
    *(LAS f32x4*)(sm + KO_RED + ((wave * 4 + 2) * 64 + lane) * 16) = ax0;
    *(LAS f32x4*)(sm + KO_RED + ((wave * 4 + 3) * 64 + lane) * 16) = axx;
    if (wave >= 4 && (wave - 4) * 64 + lane < 189) *(LAS u32x2*)(sm + KO_KV + ((wave - 4) * 64 + lane) * 8) = (u32x2){0, 0};
    LGKM0(); BAR();
    if (wave < 4) {
        const int tile = wave;
        f32x4 s = *(LAS f32x4*)(sm + KO_RED + (tile * 64 + lane) * 16);
#pragma unroll
        for (int w2 = 1; w2 < 8; w2++) s += *(LAS f32x4*)(sm + KO_RED + ((w2 * 4 + tile) * 64 + lane) * 16);
        const int e = (tile & 1) ? 12 + fr : fr;
        const bool colok = (tile & 1) ? (fr >= 4 && fr <= 8) : true;
        const bool rowok = (tile & 2) ? (g == 0) : true;
        const int d0 = (tile & 2) ? 16 : 4 * g;
        if (colok && rowok) *(LAS u32x2*)(sm + KO_KV + e * 72 + (d0 + 4) * 2) = (u32x2){pk2(s[0], s[1]), pk2(s[2], s[3])};
    }
    LGKM0(); BAR();
    u32x4 A0, A1[4], A2;
    {
        const u32 dofs = KO_KV + (8 * g - 4 * odd + 4) * 2;
        const u32 r0 = (4 * odd + fr) * 72, r1 = ((odd ? 0 : 16) + (fr & 3)) * 72;
        const u32x2 p0 = lds64(sm, dofs + r0), p1 = lds64(sm, dofs + r0 + 8);
        const u32x2 q0 = lds64(sm, dofs + r1), q1 = lds64(sm, dofs + r1 + 8);
        const u32x2 z0 = lds64(sm, dofs + 20 * 72), z1 = lds64(sm, dofs + 20 * 72 + 8);
        A0 = (u32x4){p0.x, p0.y, p1.x, p1.y}; A2 = (u32x4){z0.x, z0.y, z1.x, z1.y};
#pragma unroll
        for (int z = 0; z < 4; z++) { const bool m = (fr >> 2) == z; A1[z] = (u32x4){m ? q0.x : 0u, m ? q0.y : 0u, m ? q1.x : 0u, m ? q1.y : 0u}; }
    }
    TS(0, 10);
    const int U0 = (5 * h + odd) >> 1;
    const int Uw = U0 + (g >> 1);
    const u32 offA = WS_O + (((Uw >> 2) * 4 + (Uw & 3)) * 128 + fr * 8) * 2 + (g & 1) * 20480;
    const int cgx = 5 * h + (odd ? 0 : 4), Ux = cgx >> 1;
    const u32 offX = WS_O + (((Ux >> 2) * 4 + (Ux & 3)) * 128 + fr * 8 + (cgx & 1) * 4) * 2;
    const f32x4 zero = {0, 0, 0, 0};
#pragma unroll
    for (int i4 = 0; i4 < 2; i4++) {
        f32x4 n1 = zero; float rden[4]; u32x2 ov[4];
#pragma unroll
        for (int z = 0; z < 4; z++) {
            const int i = 4 * i4 + z;
            const f32x4 n0 = mfma16(A0, Q[i], zero);
            const f32x4 dn = mfma16(A2, Q[i], zero);
            n1 = mfma16(A1[z], Q[i], n1);
            const float rd = __builtin_amdgcn_rcpf(dn[0] + 1e-6f);
            rden[z] = rd;
            ov[z] = (u32x2){pk2(n0[0] * rd, n0[1] * rd), pk2(n0[2] * rd, n0[3] * rd)};
        }
#pragma unroll
        for (int z = 0; z < 4; z += 2) {
            auto rx = __builtin_amdgcn_permlane16_swap(ov[z].x, ov[z + 1].x, false, false);
            auto ry = __builtin_amdgcn_permlane16_swap(ov[z].y, ov[z + 1].y, false, false);
            st128c(rw, offA + (sb0 + 4 * i4 + z) * 20480, (u32x4){rx[0], ry[0], rx[1], ry[1]});
        }
        const float rd = g == 0 ? rden[0] : g == 1 ? rden[1] : g == 2 ? rden[2] : rden[3];
        st64c(rw, offX + (sb0 + 4 * i4 + g) * 20480, (u32x2){pk2(n1[0] * rd, n1[1] * rd), pk2(n1[2] * rd, n1[3] * rd)});
    }
}

DI void gate_block(int wave, int lane, int m, const u16* x, const u16* gw, rsrc_t rw) {
    const int sub = lane & 15, r4 = lane >> 4;
    u32x4 gq[5], xr[4][5];
#pragma unroll
    for (int jj = 0; jj < 5; jj++) gq[jj] = *(const GAS u32x4*)((const GAS char*)gw + (sub + 16 * jj) * 16);
#pragma unroll
    for (int it = 0; it < 4; it++)
#pragma unroll
        for (int jj = 0; jj < 5; jj++) xr[it][jj] = *(const GAS u32x4*)((const GAS char*)x + (size_t)(128 * m + 16 * wave + 4 * it + r4) * 1280 + (sub + 16 * jj) * 16);
    MEMF(); SB();
#pragma unroll
    for (int it = 0; it < 4; it++) {
        float a = 0.f;
#pragma unroll
        for (int jj = 0; jj < 5; jj++)
#pragma unroll
            for (int e = 0; e < 4; e++) { a = fmaf(blo(xr[it][jj][e]), blo(gq[jj][e]), a); a = fmaf(bhi(xr[it][jj][e]), bhi(gq[jj][e]), a); }
        a += __shfl_xor(a, 1); a += __shfl_xor(a, 2); a += __shfl_xor(a, 4); a += __shfl_xor(a, 8);
        a = rbf(a);
        const float g = rbf(__builtin_amdgcn_rcpf(1.f + __builtin_amdgcn_exp2f(-1.4426950408889634f * a)));
        if (sub == 0) st32c(rw, WS_G + (128 * m + 16 * wave + 4 * it + r4) * 4, __float_as_uint(g));
    }
}

DI void final_block(LAS char* sm, int wave, int lane, int tid, int st, int ct, const u16* x, u16* out, rsrc_t rw, u32 tag, char* ws) {
    const int fr = lane & 15, g = lane >> 4;
    u32x4 xv[2];
#pragma unroll
    for (int r = 0; r < 2; r++) {
        int it = tid + 512 * r; it = it < 640 ? it : 639;
        const int s = it / 10, u = it - 10 * s;
        xv[r] = *(const GAS u32x4*)((const GAS char*)x + (size_t)(st * 64 + s) * 1280 + ct * 160 + u * 16);
    }
    VMCNT(0); BAR();
    if (wave < 4) {
        if (wave == 0) { wait_flags(rw, lane < 32 ? (int)F_O + (st >> 4) * 32 + lane : -1, -1, tag); lds_arrive(sm, C_FO, lane); }
        else lds_wait(sm, C_FO, 1);
        TS(0, 17);
        const int kh = wave & 1, sh = wave >> 1;
        const u32 ob = WS_O + ((st * 4 + 2 * sh) * 20 + 10 * kh) * 1024 + lane * 16;
        u32x4 B[2][10];
#pragma unroll
        for (int kl = 0; kl < 10; kl++) { B[0][kl] = ld128c(rw, ob + kl * 1024); B[1][kl] = ld128c(rw, ob + 20480 + kl * 1024); }
        MEMF(); SB();
        f32x4 acc[5][2];
#pragma unroll
        for (int cb = 0; cb < 5; cb++) { acc[cb][0] = (f32x4){0, 0, 0, 0}; acc[cb][1] = (f32x4){0, 0, 0, 0}; }
        const u32 aoff = (16 * (g >> 1) + 4 * (g & 1) + (fr >> 2)) * 160 + (lane & 3) * 8;
        u32x2 al[2][5], ah[2][5];
#define F_READ(buf, kl) do { const u32 b0_ = kh ? wo_off(10 + (kl)) : wo_off(kl); \
            _Pragma("unroll") for (int cb = 0; cb < 5; cb++) { al[buf][cb] = ldstr(sm, b0_ + aoff + cb * 32); ah[buf][cb] = ldstr(sm, b0_ + aoff + cb * 32 + 1280); } } while (0)
        F_READ(0, 0);
#pragma unroll
        for (int kl = 0; kl < 10; kl++) {
            if (kl < 9) F_READ((kl + 1) & 1, kl + 1);
            SB();
#pragma unroll
            for (int cb = 0; cb < 5; cb++) {
                const u32x4 a = {al[kl & 1][cb].x, al[kl & 1][cb].y, ah[kl & 1][cb].x, ah[kl & 1][cb].y};
                acc[cb][0] = mfma16(a, B[0][kl], acc[cb][0]); acc[cb][1] = mfma16(a, B[1][kl], acc[cb][1]);
            }
            SB();
        }
        TS(0, 18);
        lds_arrive(sm, C_FR, lane); lds_wait(sm, C_FR, 4);
#pragma unroll
        for (int cb = 0; cb < 5; cb++)
#pragma unroll
            for (int z = 0; z < 2; z++)
                *(LAS f32x4*)(sm + kh * (64 * RED_PITCH) + (16 * (2 * sh + z) + fr) * RED_PITCH + (16 * cb + 4 * g) * 4) = acc[cb][z];
    } else {
        if (wave == 4) { wait_flags(rw, lane < 20 ? (int)F_Y + ct * 32 + lane : lane == 32 ? (int)F_GATE + (st >> 1) : -1, -1, tag); lds_arrive(sm, C_FY, lane); }
        else lds_wait(sm, C_FY, 1);
        const int t = tid - 256;
        u32x4 yv[3];
#pragma unroll
        for (int r = 0; r < 3; r++) { int id = t + 256 * r; id = id < 640 ? id : 639; yv[r] = ld128c(rw, WS_YT + (80 * ct + (id >> 3)) * 4096 + st * 128 + (id & 7) * 16); }
#pragma unroll
        for (int r = 0; r < 3; r++) { const int id = t + 256 * r; if (id < 640) *(LAS u32x4*)(sm + F_YY + id * 16) = yv[r]; }
        if (wave == 4) *(LAS u32*)(sm + F_GG + lane * 4) = ld32c(rw, WS_G + (st * 64 + lane) * 4);
        lds_arrive(sm, C_FY2, lane); lds_wait(sm, C_FY2, 4);
        const int sb = wave - 4, q = fr >> 2, p = lane & 3;
        u32x2 tt[5];
#pragma unroll
        for (int cb = 0; cb < 5; cb++) tt[cb] = ldstr(sm, F_YY + (16 * cb + 4 * g + q) * 128 + (16 * sb + 4 * p) * 2);
#pragma unroll
        for (int cb = 0; cb < 5; cb++) *(LAS u32x2*)(sm + F_Y2 + (16 * sb + fr) * 176 + (16 * cb + 4 * g) * 2) = tt[cb];
    }
    LGKM0(); BAR();
#pragma unroll
    for (int r = 0; r < 2; r++) {
        const int it = tid + 512 * r;
        if (it < 640) {
            const int s = it / 10, u = it - 10 * s;
            const u32 ra = s * RED_PITCH + u * 32;
            const f32x4 p0 = *(LAS f32x4*)(sm + ra), p1 = *(LAS f32x4*)(sm + ra + 16), q0 = *(LAS f32x4*)(sm + 64 * RED_PITCH + ra), q1 = *(LAS f32x4*)(sm + 64 * RED_PITCH + ra + 16);
            const u32x4 y8 = lds128(sm, F_Y2 + s * 176 + u * 16);
            const float gg = *(LAS float*)(sm + F_GG + s * 4), omg = 1.f - gg;
            float o[8];
#pragma unroll
            for (int e = 0; e < 4; e++) {
                const float a0 = (e < 2 ? p0[2 * e] + q0[2 * e] : p1[2 * e - 4] + q1[2 * e - 4]), a1 = (e < 2 ? p0[2 * e + 1] + q0[2 * e + 1] : p1[2 * e - 3] + q1[2 * e - 3]);
                const float b0 = omg * blo(xv[r][e]), b1 = omg * bhi(xv[r][e]);
                o[2 * e] = fmaf(b0, blo(y8[e]), gg * a0); o[2 * e + 1] = fmaf(b1, bhi(y8[e]), gg * a1);
            }
            *(GAS u32x4*)((GAS char*)out + (size_t)(st * 64 + s) * 1280 + ct * 160 + u * 16) = (u32x4){pk2(o[0], o[1]), pk2(o[2], o[3]), pk2(o[4], o[5]), pk2(o[6], o[7])};
        }
    }
}

__global__ __launch_bounds__(512, 2) void fused_kernel(const u16* __restrict__ x, const u16* __restrict__ w, const u16* __restrict__ wo, const u16* __restrict__ kin,
                                                        const u16* __restrict__ vin, const u16* __restrict__ shk, const u16* __restrict__ hk, const u16* __restrict__ sD,
                                                        const u16* __restrict__ gw, u16* __restrict__ out, char* ws) {
    extern __shared__ __attribute__((aligned(16))) char smem[];
    LAS char* sm = (LAS char*)smem;
    const int tid = threadIdx.x, lane = tid & 63, wave = __builtin_amdgcn_readfirstlane(tid >> 6);
    const int b = blockIdx.x, xcd = b & 7, j = b >> 3;
    const rsrc_t rw = __builtin_amdgcn_make_buffer_rsrc(ws, 0, WS_BYTES, 0x00020000);
    u32 tag;
    asm volatile("s_load_dword %0, %1, 0x0\n\ts_waitcnt lgkmcnt(0)" : "=s"(tag) : "s"(ws + WS_FLAG + F_EPOCH * FS) : "memory");
    tag += 1u;
    if (tid < 16) *(LAS u32*)(sm + L_CNT + tid * 4) = 0u;
    TS(0, 0);
    const int st = 4 * xcd + (j >> 3), ct = j & 7;
    int ko = -1;
    bool is_conv = false;

    if (j >= 30) {
        const int m = xcd * 2 + (j - 30);
        ko = -100;
        {
            const int grp = wave >> 2, w4i = wave & 3;
            u32x2 w4[3];
#pragma unroll
            for (int t = 0; t < 3; t++) { const int tt = 2 * t + grp < 5 ? 2 * t + grp : 4; p0_weights(w4[t], shk, 240 + 5 * m + tt, w4i, lane); }
#pragma unroll
            for (int t = 0; t < 3; t++) if (2 * t + grp < 5) p0_issue(kin, vin, 240 + 5 * m + 2 * t + grp, w4i, lane, (2 * t + grp) * 17408);
            VMCNT(0);
#pragma unroll
            for (int t = 0; t < 3; t++) PIN(w4[t]);
            BAR();
#pragma unroll
            for (int t = 0; t < 3; t++)
                if (2 * t + grp < 5) p0_compute(sm, w4[t], 240 + 5 * m + 2 * t + grp, w4i, lane, (2 * t + grp) * 17408, 90112 + (2 * t + grp) * 8192);
            LGKM0(); BAR();
            {
                u32x4 t4[3][2];
#pragma unroll
                for (int t = 0; t < 3; t++)
#pragma unroll
                    for (int z = 0; z < 2; z++) { const int ch2 = ((tid & 255) >> 3) + 32 * z, o2 = tid & 7, tt = 2 * t + grp < 5 ? 2 * t + grp : 4; t4[t][z] = lds128(sm, 90112 + tt * 8192 + ch2 * 128 + ((o2 ^ (ch2 & 7)) * 16)); }
#pragma unroll
                for (int t = 0; t < 3; t++)
                    if (2 * t + grp < 5) {
                        const int tile = 240 + 5 * m + 2 * t + grp, cg = tile % 10, s0 = (tile / 10) * 64;
#pragma unroll
                        for (int z = 0; z < 2; z++) { const int ch2 = ((tid & 255) >> 3) + 32 * z, o2 = tid & 7; st128c(rw, WS_UT + (cg * 64 + ch2) * 4096 + (s0 + 8 * o2) * 2, t4[t][z]); }
                    }
            }
        }
        VMCNT(0); BAR();
        if (tid < 5) set_flag(rw, p0_flag(240 + 5 * m + tid), tag);
        TS(0, 1);
        gate_block(wave, lane, m, x, gw, rw);
        VMCNT(0); LGKM0(); BAR();
        if (tid == 0) set_flag(rw, F_GATE + m, tag);
        TS(0, 3);
    } else {
        const int rt = 2 * xcd + j / 15, cbk = j % 15, tile = xcd * 30 + j;
        is_conv = cbk < 10;
        if (!is_conv) { const int vi = rt * 5 + (cbk - 10); ko = vi < 64 ? vi : -2 - (vi - 64); }
        if (wave >= 4) {
            const int lw = wave - 4;
            p0_issue(kin, vin, tile, lw, lane, P0_K);
            u32 vx[4], vw[4];
#pragma unroll
            for (int n2 = 0; n2 < 4; n2++) {
                const int n = 4 * lw + n2;
                const int row = 8 * n + (lane >> 3), ch = (lane & 7) ^ ((row >> 1) & 7);
                vx[n2] = (u32)((rt * 128 + row) * 1280 + ch * 16);
                const int kr = 4 * n + (lane >> 4), f = ((kr & 3) << 2) | ((kr >> 2) & 3), cw = (lane & 15) ^ f;
                vw[n2] = (u32)(kr * 3840 + cbk * 256 + cw * 16);
            }
            const int rot = (rt + cbk) % 10;
#define K1_ISSUE(c) do { const u32 st_ = ((c) & 3) * K1_STAGE + 4 * lw * 1024; int kc_ = (c) + ROT_ON * rot; kc_ = kc_ >= 10 ? kc_ - 10 : kc_; \
            const char* xb_ = (const char*)x + kc_ * 128; const char* wp_ = (const char*)w + (size_t)kc_ * 64 * 3840; \
            _Pragma("unroll") for (int n2 = 0; n2 < 4; n2++) { glds16(xb_, vx[n2], st_ + 16384 + n2 * 1024); glds16(wp_, vw[n2], st_ + n2 * 1024); } } while (0)
            K1_ISSUE(0);
            VMCNT(8); BAR();
            K1_ISSUE(1); K1_ISSUE(2);
            VMCNT(16); BAR();
            K1_ISSUE(3);
#pragma unroll 1
            for (int c = 0; c < 6; c++) {
                VMCNT(16); BAR();
                K1_ISSUE(c + 4);
            }
            const int cv = rt * 10 + cbk, c = 4 * cv + lw;
            if (is_conv) {
                CvIn in; u32 fl; u32x4 u[4];
                const int fidx = (int)F_P0 + (cv >> 4) * 32 + (lane & 31);
                conv_fetch_h(in, hk, c, lane);
                VMCNT(24); BAR();
                VMCNT(16); BAR();
                gld32c(fl, ws, WS_FLAG + fidx * FS);
                VMCNT(9); BAR();
                TS(4, 4);
                VMCNT(0); PIN(fl);
                const bool ready = __all(fl == tag);
                if (ready) {
#pragma unroll
                    for (int z = 0; z < 4; z++) gld128c(u[z], ws, WS_UT + c * 4096 + z * 1024 + lane * 16);
                }
                TS(4, 5);
                conv_build_h(sm, in, lw, lane);
                TS(4, 6);
                if (!ready) {
                    wait_flags(rw, fidx, -1, tag);
#pragma unroll
                    for (int z = 0; z < 4; z++) gld128c(u[z], ws, WS_UT + c * 4096 + z * 1024 + lane * 16);
                }
                TS(4, 7);
                VMCNT(0);
#pragma unroll
                for (int z = 0; z < 4; z++) { PIN(u[z]); *(LAS u32x4*)(sm + CV_BASE + lw * CV_WAVE + CV_U + 512 + z * 1024 + lane * 16) = u[z]; }
                const float Dc = blo((u32)sD[c]);
                TS(4, 8);
                conv_run(sm, Dc, lw, lane, c, rw, ws);
                publish_last(rw, sm, C_Y, 4, F_Y + (cv / 20) * 32 + cv % 20, tag, lane);
                TS(4, 11);
            } else {
                VMCNT(16); BAR();
                VMCNT(8); BAR();
                VMCNT(0); BAR();
            }
        } else {
            u32x2 w4; p0_weights(w4, shk, tile, wave, lane);
            const int wm = wave >> 1, wn = wave & 1, r32 = lane & 31, hh = lane >> 5;
            K1Addr a0;
#pragma unroll
            for (int ks = 0; ks < 2; ks++) {
                a0.x[0][ks] = (64 * wm + r32) * 128 + (((2 * ks + hh) ^ ((r32 >> 1) & 7)) * 16);
                a0.x[1][ks] = a0.x[0][ks] ^ 64;
            }
#pragma unroll
            for (int jj = 0; jj < 2; jj++)
#pragma unroll
                for (int t = 0; t < 2; t++) {
                    const int blk = (lane >> 4) & 1, q = (lane & 15) >> 2, p = lane & 3, row = 8 * hh + 4 * t + q, ch = 8 * wn + 4 * jj + 2 * blk + (p >> 1);
                    const int f = ((row & 3) << 2) | ((row >> 2) & 3);
                    a0.w[jj][t] = row * 256 + ((ch ^ f) * 16) + 8 * (p & 1);
                }
            f32x16 acc[2][2];
#pragma unroll
            for (int i = 0; i < 2; i++)
#pragma unroll
                for (int jj = 0; jj < 2; jj++)
#pragma unroll
                    for (int e = 0; e < 16; e++) acc[i][jj][e] = 0.f;
            TS(0, 20);
            VMCNT(0); PIN(w4); TS(0, 21); BAR();
            TS(0, 22);
            p0_compute(sm, w4, tile, wave, lane, P0_K, P0_OUT, ws);
            TS(0, 28);
            LGKM0(); TS(0, 23); BAR();
            TS(0, 24);
            p0_store(sm, rw, tile, tid, P0_OUT);
            TS(0, 1);
            if (cbk < 5) {
                k1_loop<true>(sm, a0, acc, rw, p0_flag(tile), tag, wave, lane);
                __builtin_amdgcn_s_setprio(2);
                TS(0, 2);
#pragma unroll
                for (int jj = 0; jj < 2; jj++)
#pragma unroll
                    for (int ap = 0; ap < 2; ap++) {
                        const int U = 16 * cbk + 8 * wn + 4 * jj + 2 * ap + hh, pr = U / 5, u5 = U - 5 * pr;
                        const u32 dst = WS_P2 + (((pr * 128 + rt * 8 + 4 * wm + (r32 >> 4)) * 5 + u5) * 128 + (r32 & 15) * 8) * 2;
#pragma unroll
                        for (int i = 0; i < 2; i++) {
                            const f32x16& a = acc[i][jj];
                            const u32 x0 = phi2(a[8 * ap], a[8 * ap + 1]), y0 = phi2(a[8 * ap + 2], a[8 * ap + 3]);
                            const u32 x1 = phi2(a[8 * ap + 4], a[8 * ap + 5]), y1 = phi2(a[8 * ap + 6], a[8 * ap + 7]);
                            auto rx = __builtin_amdgcn_permlane32_swap(x0, x1, false, false);
                            auto ry = __builtin_amdgcn_permlane32_swap(y0, y1, false, false);
                            st128c(rw, dst + i * 2560, (u32x4){rx[0], ry[0], rx[1], ry[1]});
                        }
                    }
            } else {
                k1_loop<false>(sm, a0, acc, rw, p0_flag(tile), tag, wave, lane);
                __builtin_amdgcn_s_setprio(2);
                TS(0, 2);
                const bool isk = cbk < 10;
                const int j5 = isk ? cbk - 5 : cbk - 10;
                u32 dst[2], istep[2];
#pragma unroll
                for (int jj = 0; jj < 2; jj++) {
                    const int cc = 128 * j5 + 64 * wn + 32 * jj + r32, h = cc / 20, d = cc - 20 * h;
                    const int sb32 = rt * 4 + 2 * wm;
                    const bool lo = d < 16;
                    dst[jj] = lo ? (isk ? WS_PK0 : WS_V0) + (((h * 64 + sb32) * 4 + hh) * 128 + d * 8) * 2
                                 : WS_X + (((h * 32 + (sb32 >> 1)) * 4 + hh) * 128 + (d - 16 + (isk ? 0 : 4)) * 8) * 2;
                    istep[jj] = lo ? 1024 : 128;
                }
                if (isk) {
#pragma unroll
                    for (int jj = 0; jj < 2; jj++)
#pragma unroll
                        for (int i = 0; i < 2; i++)
#pragma unroll
                            for (int z = 0; z < 2; z++) {
                                const f32x16& a = acc[i][jj];
                                st128c(rw, dst[jj] + i * istep[jj] + z * 512,
                                       (u32x4){phi2(a[8 * z], a[8 * z + 1]), phi2(a[8 * z + 2], a[8 * z + 3]), phi2(a[8 * z + 4], a[8 * z + 5]), phi2(a[8 * z + 6], a[8 * z + 7])});
                            }
                } else {
#pragma unroll
                    for (int jj = 0; jj < 2; jj++)
#pragma unroll
                        for (int i = 0; i < 2; i++)
#pragma unroll
                            for (int z = 0; z < 2; z++) {
                                const f32x16& a = acc[i][jj];
                                st128c(rw, dst[jj] + i * istep[jj] + z * 512,
                                       (u32x4){pk2(a[8 * z], a[8 * z + 1]), pk2(a[8 * z + 2], a[8 * z + 3]), pk2(a[8 * z + 4], a[8 * z + 5]), pk2(a[8 * z + 6], a[8 * z + 7])});
                            }
                }
            }
            LGKM0();
            TS(0, 25);
            publish_last(rw, sm, C_G1, 4, F_G1 + cbk * 16 + rt, tag, lane);
            __builtin_amdgcn_s_setprio(0);
            TS(0, 3);
            if (is_conv) {
                lds_wait(sm, C_G1, 4);
                wo_issue(wo, ct, 0, 12, wave, 4, lane);
            }
        }
    }
    if (!is_conv) {
        LGKM0(); BAR();
        if (ko >= 0) {
            const int h = ko >> 1, half = ko & 1;
            wo_issue(wo, ct, 0, 12, wave, 8, lane);
            kvo_block(sm, wave, lane, h, half, rw, tag, ws);
            TS(0, 26);
            VMCNT(0); BAR();
            if (tid == 0) set_flag(rw, F_O + half * 32 + h, tag);
            TS(0, 11);
            wo_issue(wo, ct, 12, 20, wave, 8, lane);
        } else {
            wo_issue(wo, ct, 0, 20, wave, 8, lane);
            if (ko == -2 && wave == 0) {
                wait_flags(rw, (int)F_G1 + lane, (int)F_G1 + 64 + lane, tag);
                wait_flags(rw, (int)F_G1 + 128 + lane, (int)F_G1 + 192 + lane, tag);
                if (lane == 0) set_flag(rw, F_EPOCH, tag);
            }
        }
    } else {
        VMCNT(0); BAR();
        if (wave >= 4) wo_issue(wo, ct, 12, 20, wave - 4, 4, lane);
    }
    TS(0, 16);
    final_block(sm, wave, lane, tid, st, ct, x, out, rw, tag, ws);
    TS(0, 19);
}

extern "C" void kernel_launch(void* const* d_in, const int* in_sizes, int n_in, void* d_out, int out_size, void* d_ws, size_t ws_size, hipStream_t stream) {
    const u16* x = (const u16*)d_in[0]; const u16* wqkv = (const u16*)d_in[1]; const u16* wo = (const u16*)d_in[2];
    const u16* kin = (const u16*)d_in[4]; const u16* vin = (const u16*)d_in[5]; const u16* shk = (const u16*)d_in[6];
    const u16* hk = (const u16*)d_in[7]; const u16* sD = (const u16*)d_in[8]; const u16* gw = (const u16*)d_in[9];
    (void)hipFuncSetAttribute((const void*)fused_kernel, hipFuncAttributeMaxDynamicSharedMemorySize, LDS_BYTES);
    fused_kernel<<<256, 512, LDS_BYTES, stream>>>(x, wqkv, wo, kin, vin, shk, hk, sD, gw, (u16*)d_out, (char*)d_ws);
}
```

```cpp
#define QDELAY 2
#include <hip/hip_runtime.h>
#include <stdint.h>

typedef unsigned short u16;
typedef unsigned int u32;
typedef __bf16 bf16x8 __attribute__((ext_vector_type(8)));
typedef __bf16 bf16x2 __attribute__((ext_vector_type(2)));
typedef float f32x4 __attribute__((ext_vector_type(4)));
typedef float f32x2 __attribute__((ext_vector_type(2)));
typedef float f32x16 __attribute__((ext_vector_type(16)));
typedef u32 u32x4 __attribute__((ext_vector_type(4)));
typedef u32 u32x2 __attribute__((ext_vector_type(2)));
typedef short s16x4 __attribute__((ext_vector_type(4)));
#define LAS __attribute__((address_space(3)))
#define GAS __attribute__((address_space(1)))
#define DI __device__ __forceinline__
typedef __amdgpu_buffer_rsrc_t rsrc_t;

constexpr int S = 2048, D = 640;
#define FS 64
#define POLL_SLEEP 2
#define ROT_ON 1

constexpr u32 WS_UT = 0;
constexpr u32 WS_YT = WS_UT + 2621440;
constexpr u32 WS_PK0 = WS_YT + 2621440;
constexpr u32 WS_V0 = WS_PK0 + 2097152;
constexpr u32 WS_X = WS_V0 + 2097152;
constexpr u32 WS_P2 = WS_X + 1048576;
constexpr u32 WS_O = WS_P2 + 2621440;
constexpr u32 WS_G = WS_O + 2621440;
constexpr u32 WS_FLAG = WS_G + 8192;
constexpr u32 F_P0 = 0;
constexpr u32 F_G1 = 320;
constexpr u32 F_GATE = 560;
constexpr u32 F_O = 576;
constexpr u32 F_Y = 640;
constexpr u32 F_EPOCH = 896, F_TMO = 928;
constexpr u32 WS_STAMP = WS_FLAG + 1024 * FS;
constexpr u32 WS_BYTES = WS_STAMP + 256 * 32 * 16;

#define TS(w, id) do {} while (0)

DI u32 pk2(float a, float b) { f32x2 t = {a, b}; return __builtin_bit_cast(u32, __builtin_convertvector(t, bf16x2)); }
DI float blo(u32 u) { return __uint_as_float(u << 16); }
DI float bhi(u32 u) { return __uint_as_float(u & 0xffff0000u); }
DI float rbf(float f) { return blo(pk2(f, f)); }
DI bf16x8 asbf(u32x4 v) { return __builtin_bit_cast(bf16x8, v); }
DI f32x4 mfma16(u32x4 a, u32x4 b, f32x4 c) { return __builtin_amdgcn_mfma_f32_16x16x32_bf16(asbf(a), asbf(b), c, 0, 0, 0); }
DI f32x16 mfma32(u32x4 a, u32x4 b, f32x16 c) { return __builtin_amdgcn_mfma_f32_32x32x16_bf16(asbf(a), asbf(b), c, 0, 0, 0); }

DI void glds16(const void* sbase, u32 voff, u32 lds) {
    asm volatile("s_mov_b32 m0, %2\n\ts_nop 0\n\tglobal_load_lds_dwordx4 %0, %1" :: "v"(voff), "s"(sbase), "s"(lds) : "memory");
}
DI void glds16nt(const void* sbase, u32 voff, u32 lds) {
    asm volatile("s_mov_b32 m0, %2\n\ts_nop 0\n\tglobal_load_lds_dwordx4 %0, %1 nt" :: "v"(voff), "s"(sbase), "s"(lds) : "memory");
}
#define NT_KV 0
#define NT_X 0
#define NT_H 0
DI void gld64(u32x2& d, const void* sbase, u32 voff) { asm volatile("global_load_dwordx2 %0, %1, %2" : "=v"(d) : "v"(voff), "s"(sbase) : "memory"); }
DI void gld128(u32x4& d, const void* sbase, u32 voff) { asm volatile("global_load_dwordx4 %0, %1, %2" : "=v"(d) : "v"(voff), "s"(sbase) : "memory"); }
DI void gld128c(u32x4& d, const void* sbase, u32 voff) { asm volatile("global_load_dwordx4 %0, %1, %2 sc1" : "=v"(d) : "v"(voff), "s"(sbase) : "memory"); }
DI void gld32c(u32& d, const void* sbase, u32 voff) { asm volatile("global_load_dword %0, %1, %2 sc1" : "=v"(d) : "v"(voff), "s"(sbase) : "memory"); }
#define PIN(x) asm volatile("" : "+v"(x))
#define SB() __builtin_amdgcn_sched_barrier(0)
DI u32x4 ld128c(rsrc_t r, u32 off) { return __builtin_amdgcn_raw_buffer_load_b128(r, off, 0, 16); }
DI u32 ld32c(rsrc_t r, u32 off) { return __builtin_amdgcn_raw_buffer_load_b32(r, off, 0, 16); }
DI void st128c(rsrc_t r, u32 off, u32x4 v) { __builtin_amdgcn_raw_buffer_store_b128(v, r, off, 0, 16); }
DI void st64c(rsrc_t r, u32 off, u32x2 v) { __builtin_amdgcn_raw_buffer_store_b64(v, r, off, 0, 16); }
DI void st32c(rsrc_t r, u32 off, u32 v) { __builtin_amdgcn_raw_buffer_store_b32(v, r, off, 0, 16); }
DI u32x4 lds128(LAS char* sm, u32 a) { return *(LAS u32x4*)(sm + a); }
DI u32x2 lds64(LAS char* sm, u32 a) { return *(LAS u32x2*)(sm + a); }
DI u32x2 ldstr(LAS char* sm, u32 a) { return __builtin_bit_cast(u32x2, __builtin_amdgcn_ds_read_tr16_b64_v4i16((LAS s16x4*)(sm + a))); }
#define VMCNT(n) asm volatile("s_waitcnt vmcnt(" #n ")" ::: "memory")
#define LGKM0() asm volatile("s_waitcnt lgkmcnt(0)" ::: "memory")
#define BAR() do { asm volatile("" ::: "memory"); __builtin_amdgcn_s_barrier(); asm volatile("" ::: "memory"); } while (0)
#define MEMF() asm volatile("" ::: "memory")

DI void set_flag(rsrc_t r, u32 idx, u32 tag) { st32c(r, WS_FLAG + idx * FS, tag); }
DI void wait_flags(rsrc_t r, int idx0, int idx1, u32 tag) {
    const unsigned long long t0 = __builtin_amdgcn_s_memrealtime();
    for (u32 it = 1;; it++) {
        u32 a = tag, b = tag;
        if (idx0 >= 0) a = ld32c(r, WS_FLAG + idx0 * FS);
        if (idx1 >= 0) b = ld32c(r, WS_FLAG + idx1 * FS);
        if (__all(a == tag && b == tag)) break;
        if (POLL_SLEEP) __builtin_amdgcn_s_sleep(POLL_SLEEP);
        if ((it & 63u) == 0u && __builtin_amdgcn_s_memrealtime() - t0 > 2000000ull) { set_flag(r, F_TMO, tag); break; }
    }
    MEMF();
}
DI void lds_arrive(LAS char* sm, u32 cnt, int lane) { LGKM0(); if (lane == 0) __hip_atomic_fetch_add((LAS u32*)(sm + cnt), 1u, __ATOMIC_RELAXED, __HIP_MEMORY_SCOPE_WORKGROUP); MEMF(); }
DI void lds_wait(LAS char* sm, u32 cnt, u32 n) { MEMF(); while (*(volatile LAS u32*)(sm + cnt) < n) __builtin_amdgcn_s_sleep(0); MEMF(); }
DI void publish_last(rsrc_t r, LAS char* sm, u32 cnt, u32 nw, u32 flag, u32 tag, int lane) {
    VMCNT(0);
    if (lane == 0) {
        const u32 old = __hip_atomic_fetch_add((LAS u32*)(sm + cnt), 1u, __ATOMIC_RELAXED, __HIP_MEMORY_SCOPE_WORKGROUP);
        if (old == nw - 1) set_flag(r, flag, tag);
    }
    MEMF();
}

DI u32 phi2(float a, float b) {
    const f32x2 t = {a, b}, s = t * 1.4426950408889634f;
    const float ea = __builtin_amdgcn_fmed3f(__builtin_amdgcn_exp2f(s.x), 0.f, 1.f), eb = __builtin_amdgcn_fmed3f(__builtin_amdgcn_exp2f(s.y), 0.f, 1.f);
    const float ma = __int_as_float(max(__float_as_int(a), 0)), mb = __int_as_float(max(__float_as_int(b), 0));
    const f32x2 r = ((f32x2){ea, eb} + (f32x2){ma, mb}) + 1e-6f;
    return pk2(r.x, r.y);
}

constexpr int K1_STAGE = 32768;
constexpr int P0_K = 131072, P0_V = P0_K + 9216, P0_OUT = P0_V + 8192;
constexpr int L_CNT = 156672;
constexpr int C_G1 = L_CNT, C_Y = L_CNT + 4, C_P0A = L_CNT + 8, C_P0B = L_CNT + 12, C_P0C = L_CNT + 16, C_FY = L_CNT + 20, C_FY2 = L_CNT + 24, C_FO = L_CNT + 28, C_FR = L_CNT + 32;
constexpr int CV_PITCH = 4160;
constexpr int CV_U = 4 * CV_PITCH;
constexpr int CV_WAVE = CV_U + 4608;
constexpr int CV_BASE = 65536;
constexpr int WO_HI = 65536;
constexpr int F_YY = 110592;
constexpr int F_Y2 = F_YY + 10240;
constexpr int F_GG = F_Y2 + 11264;
constexpr int KO_RED = 110592;
constexpr int KO_KV = KO_RED + 32768;
constexpr int RED_PITCH = 336;
constexpr int LDS_BYTES = 160 * 1024;
DI u32 p0_flag(int tile) { return F_P0 + (tile % 10) * 32 + tile / 10; }
DI constexpr u32 wo_off(int kap) { return kap < 12 ? kap * 5120 : WO_HI + (kap - 12) * 5120; }

DI void p0_issue(const u16* kin, const u16* vin, int tile, int lw, int lane, u32 base) {
    const int cg = tile % 10, rg = tile / 10, s0 = rg * 64;
    const u32 col = cg * 128 + (lane & 7) * 16;
#pragma unroll
    for (int z = 0; z < 2; z++) {
        const int n = 2 * lw + z;
        int r = s0 - 4 + n * 8 + (lane >> 3); r = r < 0 ? 0 : r;
        (NT_KV ? glds16nt : glds16)(kin, (u32)(r * 1280) + col, base + n * 1024);
        (NT_KV ? glds16nt : glds16)(vin, (u32)((s0 + n * 8 + (lane >> 3)) * 1280) + col, base + 9216 + n * 1024);
    }
    {
        int r = s0 + 60 + (lane >> 3); r = r > S - 1 ? S - 1 : r;
        (NT_KV ? glds16nt : glds16)(kin, (u32)(r * 1280) + col, base + 8 * 1024);
    }
}
DI float dot2(u32 a, u32 b, float c) { return __builtin_amdgcn_fdot2_f32_bf16(__builtin_bit_cast(bf16x2, a), __builtin_bit_cast(bf16x2, b), c, false); }
DI void p0_compute(LAS char* sm, u32x2 w4, int tile, int wave, int lane, u32 base, u32 outb, char* ws = nullptr) {
    const int s0 = (tile / 10) * 64;
    const int G = wave * 4 + (lane >> 4), cb = G & 3, i = lane & 15, q = i >> 2, p = i & 3;
    const u32 wa = __builtin_amdgcn_alignbit(w4.y, w4.y, 16), wb = __builtin_amdgcn_alignbit(w4.x, w4.x, 16);
    const int ch = 16 * cb + i;
    u32x2 kq[2][3], vq[2][2];
#pragma unroll
    for (int z = 0; z < 2; z++) {
        const int o = (G >> 2) + 4 * z;
        const u32 ka = base + (8 * o + q) * 128 + 32 * cb + 8 * p, va = ka + 9216;
        kq[z][0] = ldstr(sm, ka); kq[z][1] = ldstr(sm, ka + 512); kq[z][2] = ldstr(sm, ka + 1024);
        vq[z][0] = ldstr(sm, va); vq[z][1] = ldstr(sm, va + 512);
    }
#pragma unroll
    for (int z = 0; z < 2; z++) {
        const int o = (G >> 2) + 4 * z;
        u32 K[6] = {kq[z][0].x, kq[z][0].y, kq[z][1].x, kq[z][1].y, kq[z][2].x, kq[z][2].y};
        if (s0 == 0 && o == 0) { K[0] = 0; K[1] = 0; }
        u32 Sh[5];
#pragma unroll
        for (int e = 0; e < 5; e++) Sh[e] = __builtin_amdgcn_alignbit(K[e + 1], K[e], 16);
        const u32 V[4] = {vq[z][0].x, vq[z][0].y, vq[z][1].x, vq[z][1].y};
        u32 r[4];
#pragma unroll
        for (int e = 0; e < 4; e++) {
            const float k0 = dot2(Sh[e], wa, dot2(Sh[e + 1], wb, 0.f)), k1 = dot2(K[e + 1], wa, dot2(K[e + 2], wb, 0.f));
            r[e] = pk2(k0 * blo(V[e]), k1 * bhi(V[e]));
        }
        *(LAS u32x4*)(sm + outb + ch * 128 + ((o ^ (ch & 7)) * 16)) = (u32x4){r[0], r[1], r[2], r[3]};
    }
}
DI void p0_store(LAS char* sm, rsrc_t rw, int tile, int t256, u32 outb) {
    const int cg = tile % 10, s0 = (tile / 10) * 64;
#pragma unroll
    for (int z = 0; z < 2; z++) {
        const int ch2 = (t256 >> 3) + 32 * z, o2 = t256 & 7;
        const u32x4 t4 = lds128(sm, outb + ch2 * 128 + ((o2 ^ (ch2 & 7)) * 16));
        st128c(rw, WS_UT + (cg * 64 + ch2) * 4096 + (s0 + 8 * o2) * 2, t4);
    }
}
DI void p0_weights(u32x2& w4, const u16* shk, int tile, int wave, int lane) {
    const int cg = tile % 10, cb = (wave * 4 + (lane >> 4)) & 3;
    gld64(w4, shk, (u32)((cg * 64 + 16 * cb + (lane & 15)) * 8));
}

struct K1Set { u32x4 xa[2][2]; u32x2 wl[2][2], wh[2][2]; };
struct K1Addr { u32 x[2][2]; u32 w[2][2]; };
template <bool QT, int WO, int XO>
DI void k1_phase(LAS char* sm, f32x16 (&acc)[2][2], const K1Set& cur, K1Set& nxt, const u32 (&xb)[2], const u32 (&wb)[2][2]) {
#pragma unroll
    for (int n = 0; n < 8; n++) {
        const int ks = n >> 2, i = (n >> 1) & 1, jj = n & 1;
        const u32x4 wf = {cur.wl[ks][jj].x, cur.wl[ks][jj].y, cur.wh[ks][jj].x, cur.wh[ks][jj].y};
        acc[i][jj] = QT ? mfma32(wf, cur.xa[ks][i], acc[i][jj]) : mfma32(cur.xa[ks][i], wf, acc[i][jj]);
        if (n == 0) { nxt.wl[0][0] = ldstr(sm, wb[0][0] + WO); nxt.wh[0][0] = ldstr(sm, wb[0][1] + WO); }
        if (n == 1) { nxt.xa[0][0] = lds128(sm, xb[0] + XO); nxt.wl[0][1] = ldstr(sm, wb[1][0] + WO); }
        if (n == 2) { nxt.wh[0][1] = ldstr(sm, wb[1][1] + WO); nxt.xa[0][1] = lds128(sm, xb[0] + XO + 4096); }
        if (n == 3) { nxt.wl[1][0] = ldstr(sm, wb[0][0] + WO + 4096); nxt.wh[1][0] = ldstr(sm, wb[0][1] + WO + 4096); }
        if (n == 4) { nxt.xa[1][0] = lds128(sm, xb[1] + XO); nxt.wl[1][1] = ldstr(sm, wb[1][0] + WO + 4096); }
        if (n == 5) { nxt.wh[1][1] = ldstr(sm, wb[1][1] + WO + 4096); nxt.xa[1][1] = lds128(sm, xb[1] + XO + 4096); }
        SB();
    }
}
template <bool QT, int ST>
DI void k1_chunk(LAS char* sm, f32x16 (&acc)[2][2], K1Set& s0, K1Set& s1, const K1Addr& a0, const K1Addr& a1, bool bar, bool drain = false) {
    constexpr int SN = (ST + 1) & 3;
    const K1Addr& ac = ST < 2 ? a0 : a1; const K1Addr& an = SN < 2 ? a0 : a1;
    k1_phase<QT, (ST & 1) * K1_STAGE + 8192, (ST & 1) * K1_STAGE + 16384>(sm, acc, s0, s1, ac.x[1], ac.w);
    if (drain) VMCNT(0);
    if (bar) { LGKM0(); BAR(); }
    k1_phase<QT, (SN & 1) * K1_STAGE, (SN & 1) * K1_STAGE + 16384>(sm, acc, s1, s0, an.x[0], an.w);
}
template <bool QT>
DI void k1_loop(LAS char* sm, const K1Addr& a0, f32x16 (&acc)[2][2], rsrc_t rw, u32 pflag, u32 tag, int wave, int lane) {
    K1Addr a1;
#pragma unroll
    for (int i = 0; i < 2; i++)
#pragma unroll
        for (int z = 0; z < 2; z++) { a1.x[i][z] = a0.x[i][z] + 65536; a1.w[i][z] = a0.w[i][z] + 65536; }
    K1Set s0, s1;
#pragma unroll
    for (int ks = 0; ks < 2; ks++)
#pragma unroll
        for (int jj = 0; jj < 2; jj++) {
            s0.wl[ks][jj] = ldstr(sm, a0.w[jj][0] + ks * 4096); s0.wh[ks][jj] = ldstr(sm, a0.w[jj][1] + ks * 4096);
            s0.xa[ks][jj] = lds128(sm, a0.x[0][ks] + 16384 + jj * 4096);
        }
    SB();
#pragma unroll 1
    for (int c = 0; c < 8; c += 4) {
        k1_chunk<QT, 0>(sm, acc, s0, s1, a0, a1, true);
        k1_chunk<QT, 1>(sm, acc, s0, s1, a0, a1, true);
        k1_chunk<QT, 2>(sm, acc, s0, s1, a0, a1, true);
        k1_chunk<QT, 3>(sm, acc, s0, s1, a0, a1, true, c == 0);
        if (c == 0 && wave == 0 && lane == 0) set_flag(rw, pflag, tag);
    }
    k1_chunk<QT, 0>(sm, acc, s0, s1, a0, a1, true);
    k1_chunk<QT, 1>(sm, acc, s0, s1, a0, a1, false);
}

DI void wo_issue(const u16* wo, int ct, int k0, int k1, int w, int nw, int lane) {
    u32 vo[5];
#pragma unroll
    for (int i = 0; i < 5; i++) { const int n = 64 * i + lane, rho = n / 10, c = n - 10 * rho, row = (rho & 19) | ((rho & 4) << 1) | ((rho & 8) >> 1); vo[i] = (u32)(row * 1280 + ct * 160 + c * 16); }
#pragma unroll 1
    for (int kap = k0 + w; kap < k1; kap += nw) {
        const char* src = (const char*)wo + (size_t)kap * 40960;
        const u32 dst = kap < 12 ? kap * 5120 : WO_HI + (kap - 12) * 5120;
#pragma unroll
        for (int i = 0; i < 5; i++) glds16(src, vo[i], dst + i * 1024);
    }
}

struct CvSet { u32x2 fl[8], fh[8]; u32x4 g[8]; };
DI void cv_load(LAS char* sm, CvSet& s, u32 fa, u32 ga) {
#pragma unroll
    for (int k1 = 0; k1 < 8; k1++) { s.fl[k1] = ldstr(sm, fa + 512 * k1 + 8); s.fh[k1] = ldstr(sm, fa + 512 * k1); s.g[k1] = lds128(sm, ga + 512 * k1); }
}
DI void cv_phase(LAS char* sm, f32x4 (&Y)[8], const CvSet& cur, CvSet& nxt, u32 fa, u32 ga) {
    PIN(fa); PIN(ga);
    SB();
    int n = 0;
#pragma unroll
    for (int e = 0; e < 8; e++)
#pragma unroll
        for (int k1 = 0; k1 + e < 8; k1++) {
            const u32x4 F = {cur.fl[k1].x, cur.fl[k1].y, cur.fh[k1].x, cur.fh[k1].y};
            Y[k1 + e] = mfma16(F, cur.g[e], Y[k1 + e]);
            if (n == 0) nxt.g[0] = lds128(sm, ga);
            else if (n <= 16) { const int r = (n - 1) >> 1; if ((n - 1) & 1) nxt.fh[r] = ldstr(sm, fa + 512 * r); else nxt.fl[r] = ldstr(sm, fa + 512 * r + 8); }
            else if (n < 24) nxt.g[n - 16] = lds128(sm, ga + 512 * (n - 16));
            n++;
            SB();
        }
}
struct CvIn { u32x4 hd[4]; u32x2 hm[4]; };
DI void conv_fetch_h(CvIn& in, const u16* hk, int c, int lane) {
    const char* hb = (const char*)hk + (size_t)c * 4096;
#pragma unroll
    for (int z = 0; z < 4; z++) {
        const int i = lane + 64 * z;
        if (NT_H) asm volatile("global_load_dwordx4 %0, %1, %2 nt" : "=v"(in.hd[z]) : "v"((u32)(i * 16)), "s"(hb) : "memory"); else gld128(in.hd[z], hb, (u32)(i * 16));
        gld64(in.hm[z], hb, (u32)(i > 0 ? i * 16 - 8 : 0));
    }
}
DI void conv_build_h(LAS char* sm0, CvIn& in, int w, int lane) {
    LAS char* sm = sm0 + CV_BASE + w * CV_WAVE;
#pragma unroll
    for (int z = 0; z < 4; z++) { PIN(in.hd[z]); PIN(in.hm[z]); }
    if (lane < 32) *(LAS u32x4*)(sm + CV_U + lane * 16) = (u32x4){0, 0, 0, 0};
    if (lane < 8) *(LAS u32x4*)(sm + (lane >> 1) * CV_PITCH + (lane & 1) * 16) = (u32x4){0, 0, 0, 0};
#pragma unroll
    for (int z = 0; z < 4; z++) {
        const int i = lane + 64 * z;
        u32 m2 = in.hm[z].x, m1 = in.hm[z].y;
        if (i == 0) { m2 = 0; m1 = 0; }
        const u32 d0 = in.hd[z].x, d1 = in.hd[z].y, d2 = in.hd[z].z, d3 = in.hd[z].w;
        const u32 om2 = __builtin_amdgcn_alignbit(m1, m2, 16), om1 = __builtin_amdgcn_alignbit(d0, m1, 16), o0 = __builtin_amdgcn_alignbit(d1, d0, 16),
                  o1 = __builtin_amdgcn_alignbit(d2, d1, 16), o2 = __builtin_amdgcn_alignbit(d3, d2, 16);
        LAS char* p = sm + 32 + i * 16;
        *(LAS u32x4*)(p) = (u32x4){d0, d1, d2, d3};
        *(LAS u32x4*)(p + CV_PITCH) = (u32x4){om1, o0, o1, o2};
        *(LAS u32x4*)(p + 2 * CV_PITCH) = (u32x4){m1, d0, d1, d2};
        *(LAS u32x4*)(p + 3 * CV_PITCH) = (u32x4){om2, om1, o0, o1};
    }
}
DI void conv_run(LAS char* sm0, float Dc, int w, int lane, int c, rsrc_t rw, char* ws) {
    LAS char* sm = sm0;
    LGKM0();
    TS(4, 9);
    const int fr = lane & 15, g = lane >> 4, q = fr >> 2, p = lane & 3;
    sm = sm0;
    const u32 fa = CV_BASE + w * CV_WAVE + q * CV_PITCH + 2 * (32 - 8 * g + 4 * p) - 8;
    const u32 ga = CV_BASE + w * CV_WAVE + CV_U + 480 + 32 * fr + 16 * g;
    f32x4 Y[8];
#pragma unroll
    for (int i = 0; i < 8; i++) Y[i] = (f32x4){0, 0, 0, 0};
    CvSet A, B;
    cv_load(sm, A, fa, ga);
    SB();
#pragma unroll 1
    for (int k0 = 0; k0 < 8; k0 += 2) {
        cv_phase(sm, Y, A, B, fa + 64 * (k0 + 1), ga - 64 * (k0 + 1));
        cv_phase(sm, Y, B, A, fa + 64 * (k0 + 2), ga - 64 * (k0 + 2));
    }
    TS(4, 10);
    sm = sm0 + CV_BASE + w * CV_WAVE;
    {
        u32x2 u4[8];
#pragma unroll
        for (int ib = 0; ib < 8; ib++) u4[ib] = lds64(sm, CV_U + 512 + 512 * ib + 32 * fr + 8 * g);
#pragma unroll
        for (int ib = 0; ib < 8; ib++) {
            const u32x2 r = {pk2(fmaf(blo(u4[ib].x), Dc, Y[ib][0]), fmaf(bhi(u4[ib].x), Dc, Y[ib][1])), pk2(fmaf(blo(u4[ib].y), Dc, Y[ib][2]), fmaf(bhi(u4[ib].y), Dc, Y[ib][3]))};
            *(LAS u32x2*)(sm + 512 * ib + 32 * fr + 8 * g) = r;
        }
    }
    LGKM0();
    u32x4 y8[4];
#pragma unroll
    for (int z = 0; z < 4; z++) y8[z] = lds128(sm, z * 1024 + lane * 16);
#pragma unroll
    for (int z = 0; z < 4; z++) st128c(rw, WS_YT + c * 4096 + z * 1024 + lane * 16, y8[z]);
}

DI void kvo_block(LAS char* sm, int wave, int lane, int h, int half, rsrc_t rw, u32 tag, char* ws) {
    const int fr = lane & 15, g = lane >> 4;
    const int j0 = (20 * h) >> 7, j1 = (20 * h + 19) >> 7;
    wait_flags(rw, lane < 8 ? (int)F_G1 + 16 * (((lane & 2) ? 10 : 5) + ((lane & 1) ? j1 : j0)) + 2 * wave + (lane >> 2) : -1, -1, tag);
    TS(0, 8);
    const u32 pk0 = WS_PK0 + (h * 64 + 8 * wave) * 1024 + lane * 16, v0 = pk0 + (WS_V0 - WS_PK0);
    const u32 xx = WS_X + (h * 32 + 4 * wave) * 1024 + lane * 16;
    u32x4 A[8], B[8], X[4], Q[8];
#pragma unroll
    for (int i = 0; i < 8; i++) { A[i] = ld128c(rw, pk0 + i * 1024); B[i] = ld128c(rw, v0 + i * 1024); if ((i & 1) == 0) X[i >> 1] = ld128c(rw, xx + (i >> 1) * 1024); }
    const int odd = h & 1, pr = h >> 1;
    const int u5 = (g < 3 ? g : 2) + 2 * odd;
    const int sb0 = half * 64 + wave * 8;
    const u32 pq = WS_P2 + (((pr * 128 + sb0) * 5 + u5) * 128 + fr * 8) * 2;
    MEMF(); SB();
    f32x4 a00 = {0, 0, 0, 0}, a0x = a00, ax0 = a00, axx = a00;
    const u32 cst = fr == 8 ? 0x3F803F80u : 0u;
#pragma unroll
    for (int i = 0; i < 8; i++) {
        u32x4 xv = X[i >> 1];
        if (i & 1) {
#pragma unroll
            for (int e = 0; e < 4; e++) xv[e] = (u32)__builtin_amdgcn_update_dpp(0, (int)xv[e], 0x108, 0xf, 0xf, false);
        }
        if (fr >= 8) xv = (u32x4){cst, cst, cst, cst};
        a00 = mfma16(A[i], B[i], a00); a0x = mfma16(A[i], xv, a0x); ax0 = mfma16(xv, B[i], ax0); axx = mfma16(xv, xv, axx);
    }
    TS(0, 9);
    PIN(a00); PIN(a0x); PIN(ax0); PIN(axx);
    wait_flags(rw, lane < 2 ? (int)F_G1 + 16 * (lane ? j1 : j0) + 8 * half + wave : -1, -1, tag);
#pragma unroll
    for (int i = 0; i < 8; i++) Q[i] = ld128c(rw, pq + i * 1280);
    MEMF(); SB();
    *(LAS f32x4*)(sm + KO_RED + ((wave * 4 + 0) * 64 + lane) * 16) = a00;
    *(LAS f32x4*)(sm + KO_RED + ((wave * 4 + 1) * 64 + lane) * 16) = a0x;
    *(LAS f32x4*)(sm + KO_RED + ((wave * 4 + 2) * 64 + lane) * 16) = ax0;
    *(LAS f32x4*)(sm + KO_RED + ((wave * 4 + 3) * 64 + lane) * 16) = axx;
    if (wave >= 4 && (wave - 4) * 64 + lane < 189) *(LAS u32x2*)(sm + KO_KV + ((wave - 4) * 64 + lane) * 8) = (u32x2){0, 0};
    LGKM0(); BAR();
    if (wave < 4) {
        const int tile = wave;
        f32x4 s = *(LAS f32x4*)(sm + KO_RED + (tile * 64 + lane) * 16);
#pragma unroll
        for (int w2 = 1; w2 < 8; w2++) s += *(LAS f32x4*)(sm + KO_RED + ((w2 * 4 + tile) * 64 + lane) * 16);
        const int e = (tile & 1) ? 12 + fr : fr;
        const bool colok = (tile & 1) ? (fr >= 4 && fr <= 8) : true;
        const bool rowok = (tile & 2) ? (g == 0) : true;
        const int d0 = (tile & 2) ? 16 : 4 * g;
        if (colok && rowok) *(LAS u32x2*)(sm + KO_KV + e * 72 + (d0 + 4) * 2) = (u32x2){pk2(s[0], s[1]), pk2(s[2], s[3])};
    }
    LGKM0(); BAR();
    u32x4 A0, A1[4], A2;
    {
        const u32 dofs = KO_KV + (8 * g - 4 * odd + 4) * 2;
        const u32 r0 = (4 * odd + fr) * 72, r1 = ((odd ? 0 : 16) + (fr & 3)) * 72;
        const u32x2 p0 = lds64(sm, dofs + r0), p1 = lds64(sm, dofs + r0 + 8);
        const u32x2 q0 = lds64(sm, dofs + r1), q1 = lds64(sm, dofs + r1 + 8);
        const u32x2 z0 = lds64(sm, dofs + 20 * 72), z1 = lds64(sm, dofs + 20 * 72 + 8);
        A0 = (u32x4){p0.x, p0.y, p1.x, p1.y}; A2 = (u32x4){z0.x, z0.y, z1.x, z1.y};
#pragma unroll
        for (int z = 0; z < 4; z++) { const bool m = (fr >> 2) == z; A1[z] = (u32x4){m ? q0.x : 0u, m ? q0.y : 0u, m ? q1.x : 0u, m ? q1.y : 0u}; }
    }
    TS(0, 10);
    const int U0 = (5 * h + odd) >> 1;
    const int Uw = U0 + (g >> 1);
    const u32 offA = WS_O + (((Uw >> 2) * 4 + (Uw & 3)) * 128 + fr * 8) * 2 + (g & 1) * 20480;
    const int cgx = 5 * h + (odd ? 0 : 4), Ux = cgx >> 1;
    const u32 offX = WS_O + (((Ux >> 2) * 4 + (Ux & 3)) * 128 + fr * 8 + (cgx & 1) * 4) * 2;
    const f32x4 zero = {0, 0, 0, 0};
#pragma unroll
    for (int i4 = 0; i4 < 2; i4++) {
        f32x4 n1 = zero; float rden[4]; u32x2 ov[4];
#pragma unroll
        for (int z = 0; z < 4; z++) {
            const int i = 4 * i4 + z;
            const f32x4 n0 = mfma16(A0, Q[i], zero);
            const f32x4 dn = mfma16(A2, Q[i], zero);
            n1 = mfma16(A1[z], Q[i], n1);
            const float rd = __builtin_amdgcn_rcpf(dn[0] + 1e-6f);
            rden[z] = rd;
            ov[z] = (u32x2){pk2(n0[0] * rd, n0[1] * rd), pk2(n0[2] * rd, n0[3] * rd)};
        }
#pragma unroll
        for (int z = 0; z < 4; z += 2) {
            auto rx = __builtin_amdgcn_permlane16_swap(ov[z].x, ov[z + 1].x, false, false);
            auto ry = __builtin_amdgcn_permlane16_swap(ov[z].y, ov[z + 1].y, false, false);
            st128c(rw, offA + (sb0 + 4 * i4 + z) * 20480, (u32x4){rx[0], ry[0], rx[1], ry[1]});
        }
        const float rd = g == 0 ? rden[0] : g == 1 ? rden[1] : g == 2 ? rden[2] : rden[3];
        st64c(rw, offX + (sb0 + 4 * i4 + g) * 20480, (u32x2){pk2(n1[0] * rd, n1[1] * rd), pk2(n1[2] * rd, n1[3] * rd)});
    }
}

DI void gate_block(int wave, int lane, int m, const u16* x, const u16* gw, rsrc_t rw) {
    const int sub = lane & 15, r4 = lane >> 4;
    u32x4 gq[5], xr[4][5];
#pragma unroll
    for (int jj = 0; jj < 5; jj++) gq[jj] = *(const GAS u32x4*)((const GAS char*)gw + (sub + 16 * jj) * 16);
#pragma unroll
    for (int it = 0; it < 4; it++)
#pragma unroll
        for (int jj = 0; jj < 5; jj++) xr[it][jj] = *(const GAS u32x4*)((const GAS char*)x + (size_t)(128 * m + 16 * wave + 4 * it + r4) * 1280 + (sub + 16 * jj) * 16);
    MEMF(); SB();
#pragma unroll
    for (int it = 0; it < 4; it++) {
        float a = 0.f;
#pragma unroll
        for (int jj = 0; jj < 5; jj++)
#pragma unroll
            for (int e = 0; e < 4; e++) { a = fmaf(blo(xr[it][jj][e]), blo(gq[jj][e]), a); a = fmaf(bhi(xr[it][jj][e]), bhi(gq[jj][e]), a); }
        a += __shfl_xor(a, 1); a += __shfl_xor(a, 2); a += __shfl_xor(a, 4); a += __shfl_xor(a, 8);
        a = rbf(a);
        const float g = rbf(__builtin_amdgcn_rcpf(1.f + __builtin_amdgcn_exp2f(-1.4426950408889634f * a)));
        if (sub == 0) st32c(rw, WS_G + (128 * m + 16 * wave + 4 * it + r4) * 4, __float_as_uint(g));
    }
}

DI void final_block(LAS char* sm, int wave, int lane, int tid, int st, int ct, const u16* x, u16* out, rsrc_t rw, u32 tag, char* ws) {
    const int fr = lane & 15, g = lane >> 4;
    u32x4 xv[2];
#pragma unroll
    for (int r = 0; r < 2; r++) {
        int it = tid + 512 * r; it = it < 640 ? it : 639;
        const int s = it / 10, u = it - 10 * s;
        xv[r] = *(const GAS u32x4*)((const GAS char*)x + (size_t)(st * 64 + s) * 1280 + ct * 160 + u * 16);
    }
    VMCNT(0); BAR();
    if (wave < 4) {
        if (wave == 0) { wait_flags(rw, lane < 32 ? (int)F_O + (st >> 4) * 32 + lane : -1, -1, tag); lds_arrive(sm, C_FO, lane); }
        else lds_wait(sm, C_FO, 1);
        TS(0, 17);
        const int kh = wave & 1, sh = wave >> 1;
        const u32 ob = WS_O + ((st * 4 + 2 * sh) * 20 + 10 * kh) * 1024 + lane * 16;
        u32x4 B[2][10];
#pragma unroll
        for (int kl = 0; kl < 10; kl++) { B[0][kl] = ld128c(rw, ob + kl * 1024); B[1][kl] = ld128c(rw, ob + 20480 + kl * 1024); }
        MEMF(); SB();
        f32x4 acc[5][2];
#pragma unroll
        for (int cb = 0; cb < 5; cb++) { acc[cb][0] = (f32x4){0, 0, 0, 0}; acc[cb][1] = (f32x4){0, 0, 0, 0}; }
        const u32 aoff = (16 * (g >> 1) + 4 * (g & 1) + (fr >> 2)) * 160 + (lane & 3) * 8;
        u32x2 al[2][5], ah[2][5];
#define F_READ(buf, kl) do { const u32 b0_ = kh ? wo_off(10 + (kl)) : wo_off(kl); \
            _Pragma("unroll") for (int cb = 0; cb < 5; cb++) { al[buf][cb] = ldstr(sm, b0_ + aoff + cb * 32); ah[buf][cb] = ldstr(sm, b0_ + aoff + cb * 32 + 1280); } } while (0)
        F_READ(0, 0);
#pragma unroll
        for (int kl = 0; kl < 10; kl++) {
            if (kl < 9) F_READ((kl + 1) & 1, kl + 1);
            SB();
#pragma unroll
            for (int cb = 0; cb < 5; cb++) {
                const u32x4 a = {al[kl & 1][cb].x, al[kl & 1][cb].y, ah[kl & 1][cb].x, ah[kl & 1][cb].y};
                acc[cb][0] = mfma16(a, B[0][kl], acc[cb][0]); acc[cb][1] = mfma16(a, B[1][kl], acc[cb][1]);
            }
            SB();
        }
        TS(0, 18);
        lds_arrive(sm, C_FR, lane); lds_wait(sm, C_FR, 4);
#pragma unroll
        for (int cb = 0; cb < 5; cb++)
#pragma unroll
            for (int z = 0; z < 2; z++)
                *(LAS f32x4*)(sm + kh * (64 * RED_PITCH) + (16 * (2 * sh + z) + fr) * RED_PITCH + (16 * cb + 4 * g) * 4) = acc[cb][z];
    } else {
        if (wave == 4) { wait_flags(rw, lane < 20 ? (int)F_Y + ct * 32 + lane : lane == 32 ? (int)F_GATE + (st >> 1) : -1, -1, tag); lds_arrive(sm, C_FY, lane); }
        else lds_wait(sm, C_FY, 1);
        const int t = tid - 256;
        u32x4 yv[3];
#pragma unroll
        for (int r = 0; r < 3; r++) { int id = t + 256 * r; id = id < 640 ? id : 639; yv[r] = ld128c(rw, WS_YT + (80 * ct + (id >> 3)) * 4096 + st * 128 + (id & 7) * 16); }
#pragma unroll
        for (int r = 0; r < 3; r++) { const int id = t + 256 * r; if (id < 640) *(LAS u32x4*)(sm + F_YY + id * 16) = yv[r]; }
        if (wave == 4) *(LAS u32*)(sm + F_GG + lane * 4) = ld32c(rw, WS_G + (st * 64 + lane) * 4);
        lds_arrive(sm, C_FY2, lane); lds_wait(sm, C_FY2, 4);
        const int sb = wave - 4, q = fr >> 2, p = lane & 3;
        u32x2 tt[5];
#pragma unroll
        for (int cb = 0; cb < 5; cb++) tt[cb] = ldstr(sm, F_YY + (16 * cb + 4 * g + q) * 128 + (16 * sb + 4 * p) * 2);
#pragma unroll
        for (int cb = 0; cb < 5; cb++) *(LAS u32x2*)(sm + F_Y2 + (16 * sb + fr) * 176 + (16 * cb + 4 * g) * 2) = tt[cb];
    }
    LGKM0(); BAR();
#pragma unroll
    for (int r = 0; r < 2; r++) {
        const int it = tid + 512 * r;
        if (it < 640) {
            const int s = it / 10, u = it - 10 * s;
            const u32 ra = s * RED_PITCH + u * 32;
            const f32x4 p0 = *(LAS f32x4*)(sm + ra), p1 = *(LAS f32x4*)(sm + ra + 16), q0 = *(LAS f32x4*)(sm + 64 * RED_PITCH + ra), q1 = *(LAS f32x4*)(sm + 64 * RED_PITCH + ra + 16);
            const u32x4 y8 = lds128(sm, F_Y2 + s * 176 + u * 16);
            const float gg = *(LAS float*)(sm + F_GG + s * 4), omg = 1.f - gg;
            float o[8];
#pragma unroll
            for (int e = 0; e < 4; e++) {
                const float a0 = (e < 2 ? p0[2 * e] + q0[2 * e] : p1[2 * e - 4] + q1[2 * e - 4]), a1 = (e < 2 ? p0[2 * e + 1] + q0[2 * e + 1] : p1[2 * e - 3] + q1[2 * e - 3]);
                const float b0 = omg * blo(xv[r][e]), b1 = omg * bhi(xv[r][e]);
                o[2 * e] = fmaf(b0, blo(y8[e]), gg * a0); o[2 * e + 1] = fmaf(b1, bhi(y8[e]), gg * a1);
            }
            *(GAS u32x4*)((GAS char*)out + (size_t)(st * 64 + s) * 1280 + ct * 160 + u * 16) = (u32x4){pk2(o[0], o[1]), pk2(o[2], o[3]), pk2(o[4], o[5]), pk2(o[6], o[7])};
        }
    }
}

__global__ __launch_bounds__(512, 2) void fused_kernel(const u16* __restrict__ x, const u16* __restrict__ w, const u16* __restrict__ wo, const u16* __restrict__ kin,
                                                        const u16* __restrict__ vin, const u16* __restrict__ shk, const u16* __restrict__ hk, const u16* __restrict__ sD,
                                                        const u16* __restrict__ gw, u16* __restrict__ out, char* ws) {
    extern __shared__ __attribute__((aligned(16))) char smem[];
    LAS char* sm = (LAS char*)smem;
    const int tid = threadIdx.x, lane = tid & 63, wave = __builtin_amdgcn_readfirstlane(tid >> 6);
    const int b = blockIdx.x, xcd = b & 7, j = b >> 3;
    const rsrc_t rw = __builtin_amdgcn_make_buffer_rsrc(ws, 0, WS_BYTES, 0x00020000);
    u32 tag;
    asm volatile("s_load_dword %0, %1, 0x0\n\ts_waitcnt lgkmcnt(0)" : "=s"(tag) : "s"(ws + WS_FLAG + F_EPOCH * FS) : "memory");
    tag += 1u;
    if (tid < 16) *(LAS u32*)(sm + L_CNT + tid * 4) = 0u;
    TS(0, 0);
    const int st = 4 * xcd + (j >> 3), ct = j & 7;
    int ko = -1;
    bool is_conv = false;

    if (j >= 30) {
        const int m = xcd * 2 + (j - 30);
        ko = -100;
        {
            const int grp = wave >> 2, w4i = wave & 3;
            u32x2 w4[3];
#pragma unroll
            for (int t = 0; t < 3; t++) { const int tt = 2 * t + grp < 5 ? 2 * t + grp : 4; p0_weights(w4[t], shk, 240 + 5 * m + tt, w4i, lane); }
#pragma unroll
            for (int t = 0; t < 3; t++) if (2 * t + grp < 5) p0_issue(kin, vin, 240 + 5 * m + 2 * t + grp, w4i, lane, (2 * t + grp) * 17408);
            VMCNT(0);
#pragma unroll
            for (int t = 0; t < 3; t++) PIN(w4[t]);
            BAR();
#pragma unroll
            for (int t = 0; t < 3; t++)
                if (2 * t + grp < 5) p0_compute(sm, w4[t], 240 + 5 * m + 2 * t + grp, w4i, lane, (2 * t + grp) * 17408, 90112 + (2 * t + grp) * 8192);
            LGKM0(); BAR();
            {
                u32x4 t4[3][2];
#pragma unroll
                for (int t = 0; t < 3; t++)
#pragma unroll
                    for (int z = 0; z < 2; z++) { const int ch2 = ((tid & 255) >> 3) + 32 * z, o2 = tid & 7, tt = 2 * t + grp < 5 ? 2 * t + grp : 4; t4[t][z] = lds128(sm, 90112 + tt * 8192 + ch2 * 128 + ((o2 ^ (ch2 & 7)) * 16)); }
#pragma unroll
                for (int t = 0; t < 3; t++)
                    if (2 * t + grp < 5) {
                        const int tile = 240 + 5 * m + 2 * t + grp, cg = tile % 10, s0 = (tile / 10) * 64;
#pragma unroll
                        for (int z = 0; z < 2; z++) { const int ch2 = ((tid & 255) >> 3) + 32 * z, o2 = tid & 7; st128c(rw, WS_UT + (cg * 64 + ch2) * 4096 + (s0 + 8 * o2) * 2, t4[t][z]); }
                    }
            }
        }
        VMCNT(0); BAR();
        if (tid < 5) set_flag(rw, p0_flag(240 + 5 * m + tid), tag);
        TS(0, 1);
        gate_block(wave, lane, m, x, gw, rw);
        VMCNT(0); LGKM0(); BAR();
        if (tid == 0) set_flag(rw, F_GATE + m, tag);
        TS(0, 3);
    } else {
        const int rt = 2 * xcd + j / 15, cbk = j % 15, tile = xcd * 30 + j;
        is_conv = cbk < 10;
        if (!is_conv) { const int vi = rt * 5 + (cbk - 10); ko = vi < 64 ? vi : -2 - (vi - 64); }
        if (wave >= 4) {
            const int lw = wave - 4;
            p0_issue(kin, vin, tile, lw, lane, P0_K);
            u32 vx[4], vw[4];
#pragma unroll
            for (int n2 = 0; n2 < 4; n2++) {
                const int n = 4 * lw + n2;
                const int row = 8 * n + (lane >> 3), ch = (lane & 7) ^ ((row >> 1) & 7);
                vx[n2] = (u32)((rt * 128 + row) * 1280 + ch * 16);
                const int kr = 4 * n + (lane >> 4), f = ((kr & 3) << 2) | ((kr >> 2) & 3), cw = (lane & 15) ^ f;
                vw[n2] = (u32)(kr * 3840 + cbk * 256 + cw * 16);
            }
            const int rot = (rt + cbk) % 10;
#define K1_ISSUE(c) do { const u32 st_ = ((c) & 3) * K1_STAGE + 4 * lw * 1024; int kc_ = (c) + ROT_ON * rot; kc_ = kc_ >= 10 ? kc_ - 10 : kc_; \
            const char* xb_ = (const char*)x + kc_ * 128; const char* wp_ = (const char*)w + (size_t)kc_ * 64 * 3840; \
            _Pragma("unroll") for (int n2 = 0; n2 < 4; n2++) { if (NT_X) glds16nt(xb_, vx[n2], st_ + 16384 + n2 * 1024); else glds16(xb_, vx[n2], st_ + 16384 + n2 * 1024); glds16(wp_, vw[n2], st_ + n2 * 1024); } } while (0)
            K1_ISSUE(0);
            VMCNT(8); BAR();
            K1_ISSUE(1); K1_ISSUE(2);
            VMCNT(16); BAR();
            K1_ISSUE(3);
#pragma unroll 1
            for (int c = 0; c < 6; c++) {
                VMCNT(16); BAR();
                K1_ISSUE(c + 4);
            }
            const int cv = rt * 10 + cbk, c = 4 * cv + lw;
            if (is_conv) {
                CvIn in; u32 fl; u32x4 u[4];
                const int fidx = (int)F_P0 + (cv >> 4) * 32 + (lane & 31);
                conv_fetch_h(in, hk, c, lane);
                VMCNT(24); BAR();
                VMCNT(16); BAR();
                gld32c(fl, ws, WS_FLAG + fidx * FS);
                VMCNT(9); BAR();
                TS(4, 4);
                VMCNT(0); PIN(fl);
                const bool ready = __all(fl == tag);
                if (ready) {
#pragma unroll
                    for (int z = 0; z < 4; z++) gld128c(u[z], ws, WS_UT + c * 4096 + z * 1024 + lane * 16);
                }
                TS(4, 5);
                conv_build_h(sm, in, lw, lane);
                TS(4, 6);
                if (!ready) {
                    wait_flags(rw, fidx, -1, tag);
#pragma unroll
                    for (int z = 0; z < 4; z++) gld128c(u[z], ws, WS_UT + c * 4096 + z * 1024 + lane * 16);
                }
                TS(4, 7);
                VMCNT(0);
#pragma unroll
                for (int z = 0; z < 4; z++) { PIN(u[z]); *(LAS u32x4*)(sm + CV_BASE + lw * CV_WAVE + CV_U + 512 + z * 1024 + lane * 16) = u[z]; }
                const float Dc = blo((u32)sD[c]);
                TS(4, 8);
                conv_run(sm, Dc, lw, lane, c, rw, ws);
                publish_last(rw, sm, C_Y, 4, F_Y + (cv / 20) * 32 + cv % 20, tag, lane);
                TS(4, 11);
            } else {
                VMCNT(16); BAR();
                VMCNT(8); BAR();
                VMCNT(0); BAR();
            }
        } else {
            u32x2 w4; p0_weights(w4, shk, tile, wave, lane);
            const int wm = wave >> 1, wn = wave & 1, r32 = lane & 31, hh = lane >> 5;
            K1Addr a0;
#pragma unroll
            for (int ks = 0; ks < 2; ks++) {
                a0.x[0][ks] = (64 * wm + r32) * 128 + (((2 * ks + hh) ^ ((r32 >> 1) & 7)) * 16);
                a0.x[1][ks] = a0.x[0][ks] ^ 64;
            }
#pragma unroll
            for (int jj = 0; jj < 2; jj++)
#pragma unroll
                for (int t = 0; t < 2; t++) {
                    const int blk = (lane >> 4) & 1, q = (lane & 15) >> 2, p = lane & 3, row = 8 * hh + 4 * t + q, ch = 8 * wn + 4 * jj + 2 * blk + (p >> 1);
                    const int f = ((row & 3) << 2) | ((row >> 2) & 3);
                    a0.w[jj][t] = row * 256 + ((ch ^ f) * 16) + 8 * (p & 1);
                }
            f32x16 acc[2][2];
#pragma unroll
            for (int i = 0; i < 2; i++)
#pragma unroll
                for (int jj = 0; jj < 2; jj++)
#pragma unroll
                    for (int e = 0; e < 16; e++) acc[i][jj][e] = 0.f;
            TS(0, 20);
            VMCNT(0); PIN(w4); TS(0, 21); BAR();
            TS(0, 22);
            p0_compute(sm, w4, tile, wave, lane, P0_K, P0_OUT, ws);
            TS(0, 28);
            LGKM0(); TS(0, 23); BAR();
            TS(0, 24);
            p0_store(sm, rw, tile, tid, P0_OUT);
            TS(0, 1);
            if (cbk < 5) {
                k1_loop<true>(sm, a0, acc, rw, p0_flag(tile), tag, wave, lane);
                __builtin_amdgcn_s_setprio(2);
                TS(0, 2);
                u32x4 qv[2][2][2];
#pragma unroll
                for (int jj = 0; jj < 2; jj++)
#pragma unroll
                    for (int ap = 0; ap < 2; ap++)
#pragma unroll
                        for (int i = 0; i < 2; i++) {
                            const f32x16& a = acc[i][jj];
                            const u32 x0 = phi2(a[8 * ap], a[8 * ap + 1]), y0 = phi2(a[8 * ap + 2], a[8 * ap + 3]);
                            const u32 x1 = phi2(a[8 * ap + 4], a[8 * ap + 5]), y1 = phi2(a[8 * ap + 6], a[8 * ap + 7]);
                            auto rx = __builtin_amdgcn_permlane32_swap(x0, x1, false, false);
                            auto ry = __builtin_amdgcn_permlane32_swap(y0, y1, false, false);
                            qv[jj][ap][i] = (u32x4){rx[0], ry[0], rx[1], ry[1]};
                        }
                if (QDELAY) { __builtin_amdgcn_s_setprio(0); for (int z = 0; z < QDELAY; z++) __builtin_amdgcn_s_sleep(8); }
#pragma unroll
                for (int jj = 0; jj < 2; jj++)
#pragma unroll
                    for (int ap = 0; ap < 2; ap++) {
                        const int U = 16 * cbk + 8 * wn + 4 * jj + 2 * ap + hh, pr = U / 5, u5 = U - 5 * pr;
                        const u32 dst = WS_P2 + (((pr * 128 + rt * 8 + 4 * wm + (r32 >> 4)) * 5 + u5) * 128 + (r32 & 15) * 8) * 2;
#pragma unroll
                        for (int i = 0; i < 2; i++) st128c(rw, dst + i * 2560, qv[jj][ap][i]);
                    }
            } else {
                k1_loop<false>(sm, a0, acc, rw, p0_flag(tile), tag, wave, lane);
                __builtin_amdgcn_s_setprio(2);
                TS(0, 2);
                const bool isk = cbk < 10;
                const int j5 = isk ? cbk - 5 : cbk - 10;
                u32 dst[2], istep[2];
#pragma unroll
                for (int jj = 0; jj < 2; jj++) {
                    const int cc = 128 * j5 + 64 * wn + 32 * jj + r32, h = cc / 20, d = cc - 20 * h;
                    const int sb32 = rt * 4 + 2 * wm;
                    const bool lo = d < 16;
                    dst[jj] = lo ? (isk ? WS_PK0 : WS_V0) + (((h * 64 + sb32) * 4 + hh) * 128 + d * 8) * 2
                                 : WS_X + (((h * 32 + (sb32 >> 1)) * 4 + hh) * 128 + (d - 16 + (isk ? 0 : 4)) * 8) * 2;
                    istep[jj] = lo ? 1024 : 128;
                }
                if (isk) {
#pragma unroll
                    for (int jj = 0; jj < 2; jj++)
#pragma unroll
                        for (int i = 0; i < 2; i++)
#pragma unroll
                            for (int z = 0; z < 2; z++) {
                                const f32x16& a = acc[i][jj];
                                st128c(rw, dst[jj] + i * istep[jj] + z * 512,
                                       (u32x4){phi2(a[8 * z], a[8 * z + 1]), phi2(a[8 * z + 2], a[8 * z + 3]), phi2(a[8 * z + 4], a[8 * z + 5]), phi2(a[8 * z + 6], a[8 * z + 7])});
                            }
                } else {
#pragma unroll
                    for (int jj = 0; jj < 2; jj++)
#pragma unroll
                        for (int i = 0; i < 2; i++)
#pragma unroll
                            for (int z = 0; z < 2; z++) {
                                const f32x16& a = acc[i][jj];
                                st128c(rw, dst[jj] + i * istep[jj] + z * 512,
                                       (u32x4){pk2(a[8 * z], a[8 * z + 1]), pk2(a[8 * z + 2], a[8 * z + 3]), pk2(a[8 * z + 4], a[8 * z + 5]), pk2(a[8 * z + 6], a[8 * z + 7])});
                            }
                }
            }
            LGKM0();
            TS(0, 25);
            publish_last(rw, sm, C_G1, 4, F_G1 + cbk * 16 + rt, tag, lane);
            __builtin_amdgcn_s_setprio(0);
            TS(0, 3);
            if (is_conv) {
                lds_wait(sm, C_G1, 4);
                wo_issue(wo, ct, 0, 12, wave, 4, lane);
            }
        }
    }
    if (!is_conv) {
        LGKM0(); BAR();
        if (ko >= 0) {
            const int h = ko >> 1, half = ko & 1;
            wo_issue(wo, ct, 0, 12, wave, 8, lane);
            kvo_block(sm, wave, lane, h, half, rw, tag, ws);
            TS(0, 26);
            VMCNT(0); BAR();
            if (tid == 0) set_flag(rw, F_O + half * 32 + h, tag);
            TS(0, 11);
            wo_issue(wo, ct, 12, 20, wave, 8, lane);
        } else {
            wo_issue(wo, ct, 0, 20, wave, 8, lane);
            if (ko == -2 && wave == 0) {
                wait_flags(rw, (int)F_G1 + lane, (int)F_G1 + 64 + lane, tag);
                wait_flags(rw, (int)F_G1 + 128 + lane, (int)F_G1 + 192 + lane, tag);
                if (lane == 0) set_flag(rw, F_EPOCH, tag);
            }
        }
    } else {
        VMCNT(0); BAR();
        if (wave >= 4) wo_issue(wo, ct, 12, 20, wave - 4, 4, lane);
    }
    TS(0, 16);
    final_block(sm, wave, lane, tid, st, ct, x, out, rw, tag, ws);
    TS(0, 19);
}

extern "C" void kernel_launch(void* const* d_in, const int* in_sizes, int n_in, void* d_out, int out_size, void* d_ws, size_t ws_size, hipStream_t stream) {
    const u16* x = (const u16*)d_in[0]; const u16* wqkv = (const u16*)d_in[1]; const u16* wo = (const u16*)d_in[2];
    const u16* kin = (const u16*)d_in[4]; const u16* vin = (const u16*)d_in[5]; const u16* shk = (const u16*)d_in[6];
    const u16* hk = (const u16*)d_in[7]; const u16* sD = (const u16*)d_in[8]; const u16* gw = (const u16*)d_in[9];
    (void)hipFuncSetAttribute((const void*)fused_kernel, hipFuncAttributeMaxDynamicSharedMemorySize, LDS_BYTES);
    fused_kernel<<<256, 512, LDS_BYTES, stream>>>(x, wqkv, wo, kin, vin, shk, hk, sD, gw, (u16*)d_out, (char*)d_ws);
}
```

```cpp
#define PREF1 1
#define PREF2 1
#include <hip/hip_runtime.h>
#include <stdint.h>

typedef unsigned short u16;
typedef unsigned int u32;
typedef __bf16 bf16x8 __attribute__((ext_vector_type(8)));
typedef __bf16 bf16x2 __attribute__((ext_vector_type(2)));
typedef float f32x4 __attribute__((ext_vector_type(4)));
typedef float f32x2 __attribute__((ext_vector_type(2)));
typedef float f32x16 __attribute__((ext_vector_type(16)));
typedef u32 u32x4 __attribute__((ext_vector_type(4)));
typedef u32 u32x2 __attribute__((ext_vector_type(2)));
typedef short s16x4 __attribute__((ext_vector_type(4)));
#define LAS __attribute__((address_space(3)))
#define GAS __attribute__((address_space(1)))
#define DI __device__ __forceinline__
typedef __amdgpu_buffer_rsrc_t rsrc_t;

constexpr int S = 2048, D = 640;
#define FS 64
#define POLL_SLEEP 2
#define ROT_ON 1

constexpr u32 WS_UT = 0;
constexpr u32 WS_YT = WS_UT + 2621440;
constexpr u32 WS_PK0 = WS_YT + 2621440;
constexpr u32 WS_V0 = WS_PK0 + 2097152;
constexpr u32 WS_X = WS_V0 + 2097152;
constexpr u32 WS_P2 = WS_X + 1048576;
constexpr u32 WS_O = WS_P2 + 2621440;
constexpr u32 WS_G = WS_O + 2621440;
constexpr u32 WS_FLAG = WS_G + 8192;
constexpr u32 F_P0 = 0;
constexpr u32 F_G1 = 320;
constexpr u32 F_GATE = 560;
constexpr u32 F_O = 576;
constexpr u32 F_Y = 640;
constexpr u32 F_EPOCH = 896, F_TMO = 928;
constexpr u32 WS_STAMP = WS_FLAG + 1024 * FS;
constexpr u32 WS_BYTES = WS_STAMP + 256 * 32 * 16;

#define TS(w, id) do {} while (0)

DI u32 pk2(float a, float b) { f32x2 t = {a, b}; return __builtin_bit_cast(u32, __builtin_convertvector(t, bf16x2)); }
DI float blo(u32 u) { return __uint_as_float(u << 16); }
DI float bhi(u32 u) { return __uint_as_float(u & 0xffff0000u); }
DI float rbf(float f) { return blo(pk2(f, f)); }
DI bf16x8 asbf(u32x4 v) { return __builtin_bit_cast(bf16x8, v); }
DI f32x4 mfma16(u32x4 a, u32x4 b, f32x4 c) { return __builtin_amdgcn_mfma_f32_16x16x32_bf16(asbf(a), asbf(b), c, 0, 0, 0); }
DI f32x16 mfma32(u32x4 a, u32x4 b, f32x16 c) { return __builtin_amdgcn_mfma_f32_32x32x16_bf16(asbf(a), asbf(b), c, 0, 0, 0); }

DI void glds16(const void* sbase, u32 voff, u32 lds) {
    asm volatile("s_mov_b32 m0, %2\n\ts_nop 0\n\tglobal_load_lds_dwordx4 %0, %1" :: "v"(voff), "s"(sbase), "s"(lds) : "memory");
}
DI void glds16nt(const void* sbase, u32 voff, u32 lds) {
    asm volatile("s_mov_b32 m0, %2\n\ts_nop 0\n\tglobal_load_lds_dwordx4 %0, %1 nt" :: "v"(voff), "s"(sbase), "s"(lds) : "memory");
}
DI void touch(const void* sbase, u32 voff) {
    asm volatile("s_mov_b32 m0, %2\n\ts_nop 0\n\tglobal_load_lds_dword %0, %1" :: "v"(voff), "s"(sbase), "s"(156928u) : "memory");
}
#define NT_KV 0
#define NT_X 0
#define NT_H 0
#define QDELAY 0
DI void gld64(u32x2& d, const void* sbase, u32 voff) { asm volatile("global_load_dwordx2 %0, %1, %2" : "=v"(d) : "v"(voff), "s"(sbase) : "memory"); }
DI void gld128(u32x4& d, const void* sbase, u32 voff) { asm volatile("global_load_dwordx4 %0, %1, %2" : "=v"(d) : "v"(voff), "s"(sbase) : "memory"); }
DI void gld128c(u32x4& d, const void* sbase, u32 voff) { asm volatile("global_load_dwordx4 %0, %1, %2 sc1" : "=v"(d) : "v"(voff), "s"(sbase) : "memory"); }
DI void gld32c(u32& d, const void* sbase, u32 voff) { asm volatile("global_load_dword %0, %1, %2 sc1" : "=v"(d) : "v"(voff), "s"(sbase) : "memory"); }
#define PIN(x) asm volatile("" : "+v"(x))
#define SB() __builtin_amdgcn_sched_barrier(0)
DI u32x4 ld128c(rsrc_t r, u32 off) { return __builtin_amdgcn_raw_buffer_load_b128(r, off, 0, 16); }
DI u32 ld32c(rsrc_t r, u32 off) { return __builtin_amdgcn_raw_buffer_load_b32(r, off, 0, 16); }
DI void st128c(rsrc_t r, u32 off, u32x4 v) { __builtin_amdgcn_raw_buffer_store_b128(v, r, off, 0, 16); }
DI void st64c(rsrc_t r, u32 off, u32x2 v) { __builtin_amdgcn_raw_buffer_store_b64(v, r, off, 0, 16); }
DI void st32c(rsrc_t r, u32 off, u32 v) { __builtin_amdgcn_raw_buffer_store_b32(v, r, off, 0, 16); }
DI u32x4 lds128(LAS char* sm, u32 a) { return *(LAS u32x4*)(sm + a); }
DI u32x2 lds64(LAS char* sm, u32 a) { return *(LAS u32x2*)(sm + a); }
DI u32x2 ldstr(LAS char* sm, u32 a) { return __builtin_bit_cast(u32x2, __builtin_amdgcn_ds_read_tr16_b64_v4i16((LAS s16x4*)(sm + a))); }
#define VMCNT(n) asm volatile("s_waitcnt vmcnt(" #n ")" ::: "memory")
#define LGKM0() asm volatile("s_waitcnt lgkmcnt(0)" ::: "memory")
#define BAR() do { asm volatile("" ::: "memory"); __builtin_amdgcn_s_barrier(); asm volatile("" ::: "memory"); } while (0)
#define MEMF() asm volatile("" ::: "memory")

DI void set_flag(rsrc_t r, u32 idx, u32 tag) { st32c(r, WS_FLAG + idx * FS, tag); }
DI void wait_flags(rsrc_t r, int idx0, int idx1, u32 tag) {
    const unsigned long long t0 = __builtin_amdgcn_s_memrealtime();
    for (u32 it = 1;; it++) {
        u32 a = tag, b = tag;
        if (idx0 >= 0) a = ld32c(r, WS_FLAG + idx0 * FS);
        if (idx1 >= 0) b = ld32c(r, WS_FLAG + idx1 * FS);
        if (__all(a == tag && b == tag)) break;
        if (POLL_SLEEP) __builtin_amdgcn_s_sleep(POLL_SLEEP);
        if ((it & 63u) == 0u && __builtin_amdgcn_s_memrealtime() - t0 > 2000000ull) { set_flag(r, F_TMO, tag); break; }
    }
    MEMF();
}
DI void lds_arrive(LAS char* sm, u32 cnt, int lane) { LGKM0(); if (lane == 0) __hip_atomic_fetch_add((LAS u32*)(sm + cnt), 1u, __ATOMIC_RELAXED, __HIP_MEMORY_SCOPE_WORKGROUP); MEMF(); }
DI void lds_wait(LAS char* sm, u32 cnt, u32 n) { MEMF(); while (*(volatile LAS u32*)(sm + cnt) < n) __builtin_amdgcn_s_sleep(0); MEMF(); }
DI void publish_last(rsrc_t r, LAS char* sm, u32 cnt, u32 nw, u32 flag, u32 tag, int lane) {
    VMCNT(0);
    if (lane == 0) {
        const u32 old = __hip_atomic_fetch_add((LAS u32*)(sm + cnt), 1u, __ATOMIC_RELAXED, __HIP_MEMORY_SCOPE_WORKGROUP);
        if (old == nw - 1) set_flag(r, flag, tag);
    }
    MEMF();
}

DI u32 phi2(float a, float b) {
    const f32x2 t = {a, b}, s = t * 1.4426950408889634f;
    const float ea = __builtin_amdgcn_fmed3f(__builtin_amdgcn_exp2f(s.x), 0.f, 1.f), eb = __builtin_amdgcn_fmed3f(__builtin_amdgcn_exp2f(s.y), 0.f, 1.f);
    const float ma = __int_as_float(max(__float_as_int(a), 0)), mb = __int_as_float(max(__float_as_int(b), 0));
    const f32x2 r = ((f32x2){ea, eb} + (f32x2){ma, mb}) + 1e-6f;
    return pk2(r.x, r.y);
}

constexpr int K1_STAGE = 32768;
constexpr int P0_K = 131072, P0_V = P0_K + 9216, P0_OUT = P0_V + 8192;
constexpr int L_CNT = 156672;
constexpr int C_G1 = L_CNT, C_Y = L_CNT + 4, C_P0A = L_CNT + 8, C_P0B = L_CNT + 12, C_P0C = L_CNT + 16, C_FY = L_CNT + 20, C_FY2 = L_CNT + 24, C_FO = L_CNT + 28, C_FR = L_CNT + 32;
constexpr int CV_PITCH = 4160;
constexpr int CV_U = 4 * CV_PITCH;
constexpr int CV_WAVE = CV_U + 4608;
constexpr int CV_BASE = 65536;
constexpr int WO_HI = 65536;
constexpr int F_YY = 110592;
constexpr int F_Y2 = F_YY + 10240;
constexpr int F_GG = F_Y2 + 11264;
constexpr int KO_RED = 110592;
constexpr int KO_KV = KO_RED + 32768;
constexpr int RED_PITCH = 336;
constexpr int LDS_BYTES = 160 * 1024;
DI u32 p0_flag(int tile) { return F_P0 + (tile % 10) * 32 + tile / 10; }
DI constexpr u32 wo_off(int kap) { return kap < 12 ? kap * 5120 : WO_HI + (kap - 12) * 5120; }

DI void p0_issue(const u16* kin, const u16* vin, int tile, int lw, int lane, u32 base) {
    const int cg = tile % 10, rg = tile / 10, s0 = rg * 64;
    const u32 col = cg * 128 + (lane & 7) * 16;
#pragma unroll
    for (int z = 0; z < 2; z++) {
        const int n = 2 * lw + z;
        int r = s0 - 4 + n * 8 + (lane >> 3); r = r < 0 ? 0 : r;
        (NT_KV ? glds16nt : glds16)(kin, (u32)(r * 1280) + col, base + n * 1024);
        (NT_KV ? glds16nt : glds16)(vin, (u32)((s0 + n * 8 + (lane >> 3)) * 1280) + col, base + 9216 + n * 1024);
    }
    {
        int r = s0 + 60 + (lane >> 3); r = r > S - 1 ? S - 1 : r;
        (NT_KV ? glds16nt : glds16)(kin, (u32)(r * 1280) + col, base + 8 * 1024);
    }
}
DI float dot2(u32 a, u32 b, float c) { return __builtin_amdgcn_fdot2_f32_bf16(__builtin_bit_cast(bf16x2, a), __builtin_bit_cast(bf16x2, b), c, false); }
DI void p0_compute(LAS char* sm, u32x2 w4, int tile, int wave, int lane, u32 base, u32 outb, char* ws = nullptr) {
    const int s0 = (tile / 10) * 64;
    const int G = wave * 4 + (lane >> 4), cb = G & 3, i = lane & 15, q = i >> 2, p = i & 3;
    const u32 wa = __builtin_amdgcn_alignbit(w4.y, w4.y, 16), wb = __builtin_amdgcn_alignbit(w4.x, w4.x, 16);
    const int ch = 16 * cb + i;
    u32x2 kq[2][3], vq[2][2];
#pragma unroll
    for (int z = 0; z < 2; z++) {
        const int o = (G >> 2) + 4 * z;
        const u32 ka = base + (8 * o + q) * 128 + 32 * cb + 8 * p, va = ka + 9216;
        kq[z][0] = ldstr(sm, ka); kq[z][1] = ldstr(sm, ka + 512); kq[z][2] = ldstr(sm, ka + 1024);
        vq[z][0] = ldstr(sm, va); vq[z][1] = ldstr(sm, va + 512);
    }
#pragma unroll
    for (int z = 0; z < 2; z++) {
        const int o = (G >> 2) + 4 * z;
        u32 K[6] = {kq[z][0].x, kq[z][0].y, kq[z][1].x, kq[z][1].y, kq[z][2].x, kq[z][2].y};
        if (s0 == 0 && o == 0) { K[0] = 0; K[1] = 0; }
        u32 Sh[5];
#pragma unroll
        for (int e = 0; e < 5; e++) Sh[e] = __builtin_amdgcn_alignbit(K[e + 1], K[e], 16);
        const u32 V[4] = {vq[z][0].x, vq[z][0].y, vq[z][1].x, vq[z][1].y};
        u32 r[4];
#pragma unroll
        for (int e = 0; e < 4; e++) {
            const float k0 = dot2(Sh[e], wa, dot2(Sh[e + 1], wb, 0.f)), k1 = dot2(K[e + 1], wa, dot2(K[e + 2], wb, 0.f));
            r[e] = pk2(k0 * blo(V[e]), k1 * bhi(V[e]));
        }
        *(LAS u32x4*)(sm + outb + ch * 128 + ((o ^ (ch & 7)) * 16)) = (u32x4){r[0], r[1], r[2], r[3]};
    }
}
DI void p0_store(LAS char* sm, rsrc_t rw, int tile, int t256, u32 outb) {
    const int cg = tile % 10, s0 = (tile / 10) * 64;
#pragma unroll
    for (int z = 0; z < 2; z++) {
        const int ch2 = (t256 >> 3) + 32 * z, o2 = t256 & 7;
        const u32x4 t4 = lds128(sm, outb + ch2 * 128 + ((o2 ^ (ch2 & 7)) * 16));
        st128c(rw, WS_UT + (cg * 64 + ch2) * 4096 + (s0 + 8 * o2) * 2, t4);
    }
}
DI void p0_weights(u32x2& w4, const u16* shk, int tile, int wave, int lane) {
    const int cg = tile % 10, cb = (wave * 4 + (lane >> 4)) & 3;
    gld64(w4, shk, (u32)((cg * 64 + 16 * cb + (lane & 15)) * 8));
}

struct K1Set { u32x4 xa[2][2]; u32x2 wl[2][2], wh[2][2]; };
struct K1Addr { u32 x[2][2]; u32 w[2][2]; };
template <bool QT, int WO, int XO>
DI void k1_phase(LAS char* sm, f32x16 (&acc)[2][2], const K1Set& cur, K1Set& nxt, const u32 (&xb)[2], const u32 (&wb)[2][2]) {
#pragma unroll
    for (int n = 0; n < 8; n++) {
        const int ks = n >> 2, i = (n >> 1) & 1, jj = n & 1;
        const u32x4 wf = {cur.wl[ks][jj].x, cur.wl[ks][jj].y, cur.wh[ks][jj].x, cur.wh[ks][jj].y};
        acc[i][jj] = QT ? mfma32(wf, cur.xa[ks][i], acc[i][jj]) : mfma32(cur.xa[ks][i], wf, acc[i][jj]);
        if (n == 0) { nxt.wl[0][0] = ldstr(sm, wb[0][0] + WO); nxt.wh[0][0] = ldstr(sm, wb[0][1] + WO); }
        if (n == 1) { nxt.xa[0][0] = lds128(sm, xb[0] + XO); nxt.wl[0][1] = ldstr(sm, wb[1][0] + WO); }
        if (n == 2) { nxt.wh[0][1] = ldstr(sm, wb[1][1] + WO); nxt.xa[0][1] = lds128(sm, xb[0] + XO + 4096); }
        if (n == 3) { nxt.wl[1][0] = ldstr(sm, wb[0][0] + WO + 4096); nxt.wh[1][0] = ldstr(sm, wb[0][1] + WO + 4096); }
        if (n == 4) { nxt.xa[1][0] = lds128(sm, xb[1] + XO); nxt.wl[1][1] = ldstr(sm, wb[1][0] + WO + 4096); }
        if (n == 5) { nxt.wh[1][1] = ldstr(sm, wb[1][1] + WO + 4096); nxt.xa[1][1] = lds128(sm, xb[1] + XO + 4096); }
        SB();
    }
}
template <bool QT, int ST>
DI void k1_chunk(LAS char* sm, f32x16 (&acc)[2][2], K1Set& s0, K1Set& s1, const K1Addr& a0, const K1Addr& a1, bool bar, bool drain = false) {
    constexpr int SN = (ST + 1) & 3;
    const K1Addr& ac = ST < 2 ? a0 : a1; const K1Addr& an = SN < 2 ? a0 : a1;
    k1_phase<QT, (ST & 1) * K1_STAGE + 8192, (ST & 1) * K1_STAGE + 16384>(sm, acc, s0, s1, ac.x[1], ac.w);
    if (drain) VMCNT(0);
    if (bar) { LGKM0(); BAR(); }
    k1_phase<QT, (SN & 1) * K1_STAGE, (SN & 1) * K1_STAGE + 16384>(sm, acc, s1, s0, an.x[0], an.w);
}
template <bool QT>
DI void k1_loop(LAS char* sm, const K1Addr& a0, f32x16 (&acc)[2][2], rsrc_t rw, u32 pflag, u32 tag, int wave, int lane, const u16* hk, const u16* wo, int n240) {
    K1Addr a1;
#pragma unroll
    for (int i = 0; i < 2; i++)
#pragma unroll
        for (int z = 0; z < 2; z++) { a1.x[i][z] = a0.x[i][z] + 65536; a1.w[i][z] = a0.w[i][z] + 65536; }
    K1Set s0, s1;
#pragma unroll
    for (int ks = 0; ks < 2; ks++)
#pragma unroll
        for (int jj = 0; jj < 2; jj++) {
            s0.wl[ks][jj] = ldstr(sm, a0.w[jj][0] + ks * 4096); s0.wh[ks][jj] = ldstr(sm, a0.w[jj][1] + ks * 4096);
            s0.xa[ks][jj] = lds128(sm, a0.x[0][ks] + 16384 + jj * 4096);
        }
    SB();
#pragma unroll 1
    for (int c = 0; c < 8; c += 4) {
        k1_chunk<QT, 0>(sm, acc, s0, s1, a0, a1, true);
        k1_chunk<QT, 1>(sm, acc, s0, s1, a0, a1, true);
        k1_chunk<QT, 2>(sm, acc, s0, s1, a0, a1, true);
        k1_chunk<QT, 3>(sm, acc, s0, s1, a0, a1, true, c == 0);
        if (c == 0 && wave == 0 && lane == 0) set_flag(rw, pflag, tag);
        if (PREF2 && c == 0 && wave >= 2) {
            if (wave == 2) { const u32 l_ = n240 * 86 + lane; touch(hk, (l_ < 20480 ? l_ : 20479) * 128); }
            else { const u32 l_ = n240 * 86 + 64 + lane, m_ = n240 * 27 + lane - 22;
                   if (lane < 22) touch(hk, (l_ < 20480 ? l_ : 20479) * 128); else if (lane < 49) touch(wo, (m_ < 6400 ? m_ : 6399) * 128); }
        }
    }
    k1_chunk<QT, 0>(sm, acc, s0, s1, a0, a1, true);
    k1_chunk<QT, 1>(sm, acc, s0, s1, a0, a1, false);
}

DI void wo_issue(const u16* wo, int ct, int k0, int k1, int w, int nw, int lane) {
    u32 vo[5];
#pragma unroll
    for (int i = 0; i < 5; i++) { const int n = 64 * i + lane, rho = n / 10, c = n - 10 * rho, row = (rho & 19) | ((rho & 4) << 1) | ((rho & 8) >> 1); vo[i] = (u32)(row * 1280 + ct * 160 + c * 16); }
#pragma unroll 1
    for (int kap = k0 + w; kap < k1; kap += nw) {
        const char* src = (const char*)wo + (size_t)kap * 40960;
        const u32 dst = kap < 12 ? kap * 5120 : WO_HI + (kap - 12) * 5120;
#pragma unroll
        for (int i = 0; i < 5; i++) glds16(src, vo[i], dst + i * 1024);
    }
}

struct CvSet { u32x2 fl[8], fh[8]; u32x4 g[8]; };
DI void cv_load(LAS char* sm, CvSet& s, u32 fa, u32 ga) {
#pragma unroll
    for (int k1 = 0; k1 < 8; k1++) { s.fl[k1] = ldstr(sm, fa + 512 * k1 + 8); s.fh[k1] = ldstr(sm, fa + 512 * k1); s.g[k1] = lds128(sm, ga + 512 * k1); }
}
DI void cv_phase(LAS char* sm, f32x4 (&Y)[8], const CvSet& cur, CvSet& nxt, u32 fa, u32 ga) {
    PIN(fa); PIN(ga);
    SB();
    int n = 0;
#pragma unroll
    for (int e = 0; e < 8; e++)
#pragma unroll
        for (int k1 = 0; k1 + e < 8; k1++) {
            const u32x4 F = {cur.fl[k1].x, cur.fl[k1].y, cur.fh[k1].x, cur.fh[k1].y};
            Y[k1 + e] = mfma16(F, cur.g[e], Y[k1 + e]);
            if (n == 0) nxt.g[0] = lds128(sm, ga);
            else if (n <= 16) { const int r = (n - 1) >> 1; if ((n - 1) & 1) nxt.fh[r] = ldstr(sm, fa + 512 * r); else nxt.fl[r] = ldstr(sm, fa + 512 * r + 8); }
            else if (n < 24) nxt.g[n - 16] = lds128(sm, ga + 512 * (n - 16));
            n++;
            SB();
        }
}
struct CvIn { u32x4 hd[4]; u32x2 hm[4]; };
DI void conv_fetch_h(CvIn& in, const u16* hk, int c, int lane) {
    const char* hb = (const char*)hk + (size_t)c * 4096;
#pragma unroll
    for (int z = 0; z < 4; z++) {
        const int i = lane + 64 * z;
        if (NT_H) asm volatile("global_load_dwordx4 %0, %1, %2 nt" : "=v"(in.hd[z]) : "v"((u32)(i * 16)), "s"(hb) : "memory"); else gld128(in.hd[z], hb, (u32)(i * 16));
        gld64(in.hm[z], hb, (u32)(i > 0 ? i * 16 - 8 : 0));
    }
}
DI void conv_build_h(LAS char* sm0, CvIn& in, int w, int lane) {
    LAS char* sm = sm0 + CV_BASE + w * CV_WAVE;
#pragma unroll
    for (int z = 0; z < 4; z++) { PIN(in.hd[z]); PIN(in.hm[z]); }
    if (lane < 32) *(LAS u32x4*)(sm + CV_U + lane * 16) = (u32x4){0, 0, 0, 0};
    if (lane < 8) *(LAS u32x4*)(sm + (lane >> 1) * CV_PITCH + (lane & 1) * 16) = (u32x4){0, 0, 0, 0};
#pragma unroll
    for (int z = 0; z < 4; z++) {
        const int i = lane + 64 * z;
        u32 m2 = in.hm[z].x, m1 = in.hm[z].y;
        if (i == 0) { m2 = 0; m1 = 0; }
        const u32 d0 = in.hd[z].x, d1 = in.hd[z].y, d2 = in.hd[z].z, d3 = in.hd[z].w;
        const u32 om2 = __builtin_amdgcn_alignbit(m1, m2, 16), om1 = __builtin_amdgcn_alignbit(d0, m1, 16), o0 = __builtin_amdgcn_alignbit(d1, d0, 16),
                  o1 = __builtin_amdgcn_alignbit(d2, d1, 16), o2 = __builtin_amdgcn_alignbit(d3, d2, 16);
        LAS char* p = sm + 32 + i * 16;
        *(LAS u32x4*)(p) = (u32x4){d0, d1, d2, d3};
        *(LAS u32x4*)(p + CV_PITCH) = (u32x4){om1, o0, o1, o2};
        *(LAS u32x4*)(p + 2 * CV_PITCH) = (u32x4){m1, d0, d1, d2};
        *(LAS u32x4*)(p + 3 * CV_PITCH) = (u32x4){om2, om1, o0, o1};
    }
}
DI void conv_run(LAS char* sm0, float Dc, int w, int lane, int c, rsrc_t rw, char* ws) {
    LAS char* sm = sm0;
    LGKM0();
    TS(4, 9);
    const int fr = lane & 15, g = lane >> 4, q = fr >> 2, p = lane & 3;
    sm = sm0;
    const u32 fa = CV_BASE + w * CV_WAVE + q * CV_PITCH + 2 * (32 - 8 * g + 4 * p) - 8;
    const u32 ga = CV_BASE + w * CV_WAVE + CV_U + 480 + 32 * fr + 16 * g;
    f32x4 Y[8];
#pragma unroll
    for (int i = 0; i < 8; i++) Y[i] = (f32x4){0, 0, 0, 0};
    CvSet A, B;
    cv_load(sm, A, fa, ga);
    SB();
#pragma unroll 1
    for (int k0 = 0; k0 < 8; k0 += 2) {
        cv_phase(sm, Y, A, B, fa + 64 * (k0 + 1), ga - 64 * (k0 + 1));
        cv_phase(sm, Y, B, A, fa + 64 * (k0 + 2), ga - 64 * (k0 + 2));
    }
    TS(4, 10);
    sm = sm0 + CV_BASE + w * CV_WAVE;
    {
        u32x2 u4[8];
#pragma unroll
        for (int ib = 0; ib < 8; ib++) u4[ib] = lds64(sm, CV_U + 512 + 512 * ib + 32 * fr + 8 * g);
#pragma unroll
        for (int ib = 0; ib < 8; ib++) {
            const u32x2 r = {pk2(fmaf(blo(u4[ib].x), Dc, Y[ib][0]), fmaf(bhi(u4[ib].x), Dc, Y[ib][1])), pk2(fmaf(blo(u4[ib].y), Dc, Y[ib][2]), fmaf(bhi(u4[ib].y), Dc, Y[ib][3]))};
            *(LAS u32x2*)(sm + 512 * ib + 32 * fr + 8 * g) = r;
        }
    }
    LGKM0();
    u32x4 y8[4];
#pragma unroll
    for (int z = 0; z < 4; z++) y8[z] = lds128(sm, z * 1024 + lane * 16);
#pragma unroll
    for (int z = 0; z < 4; z++) st128c(rw, WS_YT + c * 4096 + z * 1024 + lane * 16, y8[z]);
}

DI void kvo_block(LAS char* sm, int wave, int lane, int h, int half, rsrc_t rw, u32 tag, char* ws) {
    const int fr = lane & 15, g = lane >> 4;
    const int j0 = (20 * h) >> 7, j1 = (20 * h + 19) >> 7;
    wait_flags(rw, lane < 8 ? (int)F_G1 + 16 * (((lane & 2) ? 10 : 5) + ((lane & 1) ? j1 : j0)) + 2 * wave + (lane >> 2)
                            : lane < 10 ? (int)F_G1 + 16 * ((lane & 1) ? j1 : j0) + 8 * half + wave : -1, -1, tag);
    TS(0, 8);
    const u32 pk0 = WS_PK0 + (h * 64 + 8 * wave) * 1024 + lane * 16, v0 = pk0 + (WS_V0 - WS_PK0);
    const u32 xx = WS_X + (h * 32 + 4 * wave) * 1024 + lane * 16;
    u32x4 A[8], B[8], X[4], Q[8];
#pragma unroll
    for (int i = 0; i < 8; i++) { A[i] = ld128c(rw, pk0 + i * 1024); B[i] = ld128c(rw, v0 + i * 1024); if ((i & 1) == 0) X[i >> 1] = ld128c(rw, xx + (i >> 1) * 1024); }
    const int odd = h & 1, pr = h >> 1;
    const int u5 = (g < 3 ? g : 2) + 2 * odd;
    const int sb0 = half * 64 + wave * 8;
    const u32 pq = WS_P2 + (((pr * 128 + sb0) * 5 + u5) * 128 + fr * 8) * 2;
#pragma unroll
    for (int i = 0; i < 8; i++) Q[i] = ld128c(rw, pq + i * 1280);
    MEMF(); SB();
    f32x4 a00 = {0, 0, 0, 0}, a0x = a00, ax0 = a00, axx = a00;
    const u32 cst = fr == 8 ? 0x3F803F80u : 0u;
#pragma unroll
    for (int i = 0; i < 8; i++) {
        u32x4 xv = X[i >> 1];
        if (i & 1) {
#pragma unroll
            for (int e = 0; e < 4; e++) xv[e] = (u32)__builtin_amdgcn_update_dpp(0, (int)xv[e], 0x108, 0xf, 0xf, false);
        }
        if (fr >= 8) xv = (u32x4){cst, cst, cst, cst};
        a00 = mfma16(A[i], B[i], a00); a0x = mfma16(A[i], xv, a0x); ax0 = mfma16(xv, B[i], ax0); axx = mfma16(xv, xv, axx);
    }
    TS(0, 9);
    *(LAS f32x4*)(sm + KO_RED + ((wave * 4 + 0) * 64 + lane) * 16) = a00;
    *(LAS f32x4*)(sm + KO_RED + ((wave * 4 + 1) * 64 + lane) * 16) = a0x;
    *(LAS f32x4*)(sm + KO_RED + ((wave * 4 + 2) * 64 + lane) * 16) = ax0;
    *(LAS f32x4*)(sm + KO_RED + ((wave * 4 + 3) * 64 + lane) * 16) = axx;
    if (wave >= 4 && (wave - 4) * 64 + lane < 189) *(LAS u32x2*)(sm + KO_KV + ((wave - 4) * 64 + lane) * 8) = (u32x2){0, 0};
    LGKM0(); BAR();
    if (wave < 4) {
        const int tile = wave;
        f32x4 s = *(LAS f32x4*)(sm + KO_RED + (tile * 64 + lane) * 16);
#pragma unroll
        for (int w2 = 1; w2 < 8; w2++) s += *(LAS f32x4*)(sm + KO_RED + ((w2 * 4 + tile) * 64 + lane) * 16);
        const int e = (tile & 1) ? 12 + fr : fr;
        const bool colok = (tile & 1) ? (fr >= 4 && fr <= 8) : true;
        const bool rowok = (tile & 2) ? (g == 0) : true;
        const int d0 = (tile & 2) ? 16 : 4 * g;
        if (colok && rowok) *(LAS u32x2*)(sm + KO_KV + e * 72 + (d0 + 4) * 2) = (u32x2){pk2(s[0], s[1]), pk2(s[2], s[3])};
    }
    LGKM0(); BAR();
    u32x4 A0, A1[4], A2;
    {
        const u32 dofs = KO_KV + (8 * g - 4 * odd + 4) * 2;
        const u32 r0 = (4 * odd + fr) * 72, r1 = ((odd ? 0 : 16) + (fr & 3)) * 72;
        const u32x2 p0 = lds64(sm, dofs + r0), p1 = lds64(sm, dofs + r0 + 8);
        const u32x2 q0 = lds64(sm, dofs + r1), q1 = lds64(sm, dofs + r1 + 8);
        const u32x2 z0 = lds64(sm, dofs + 20 * 72), z1 = lds64(sm, dofs + 20 * 72 + 8);
        A0 = (u32x4){p0.x, p0.y, p1.x, p1.y}; A2 = (u32x4){z0.x, z0.y, z1.x, z1.y};
#pragma unroll
        for (int z = 0; z < 4; z++) { const bool m = (fr >> 2) == z; A1[z] = (u32x4){m ? q0.x : 0u, m ? q0.y : 0u, m ? q1.x : 0u, m ? q1.y : 0u}; }
    }
    TS(0, 10);
    const int U0 = (5 * h + odd) >> 1;
    const int Uw = U0 + (g >> 1);
    const u32 offA = WS_O + (((Uw >> 2) * 4 + (Uw & 3)) * 128 + fr * 8) * 2 + (g & 1) * 20480;
    const int cgx = 5 * h + (odd ? 0 : 4), Ux = cgx >> 1;
    const u32 offX = WS_O + (((Ux >> 2) * 4 + (Ux & 3)) * 128 + fr * 8 + (cgx & 1) * 4) * 2;
    const f32x4 zero = {0, 0, 0, 0};
#pragma unroll
    for (int i4 = 0; i4 < 2; i4++) {
        f32x4 n1 = zero; float rden[4]; u32x2 ov[4];
#pragma unroll
        for (int z = 0; z < 4; z++) {
            const int i = 4 * i4 + z;
            const f32x4 n0 = mfma16(A0, Q[i], zero);
            const f32x4 dn = mfma16(A2, Q[i], zero);
            n1 = mfma16(A1[z], Q[i], n1);
            const float rd = __builtin_amdgcn_rcpf(dn[0] + 1e-6f);
            rden[z] = rd;
            ov[z] = (u32x2){pk2(n0[0] * rd, n0[1] * rd), pk2(n0[2] * rd, n0[3] * rd)};
        }
#pragma unroll
        for (int z = 0; z < 4; z += 2) {
            auto rx = __builtin_amdgcn_permlane16_swap(ov[z].x, ov[z + 1].x, false, false);
            auto ry = __builtin_amdgcn_permlane16_swap(ov[z].y, ov[z + 1].y, false, false);
            st128c(rw, offA + (sb0 + 4 * i4 + z) * 20480, (u32x4){rx[0], ry[0], rx[1], ry[1]});
        }
        const float rd = g == 0 ? rden[0] : g == 1 ? rden[1] : g == 2 ? rden[2] : rden[3];
        st64c(rw, offX + (sb0 + 4 * i4 + g) * 20480, (u32x2){pk2(n1[0] * rd, n1[1] * rd), pk2(n1[2] * rd, n1[3] * rd)});
    }
}

DI void gate_block(int wave, int lane, int m, const u16* x, const u16* gw, rsrc_t rw) {
    const int sub = lane & 15, r4 = lane >> 4;
    u32x4 gq[5], xr[4][5];
#pragma unroll
    for (int jj = 0; jj < 5; jj++) gq[jj] = *(const GAS u32x4*)((const GAS char*)gw + (sub + 16 * jj) * 16);
#pragma unroll
    for (int it = 0; it < 4; it++)
#pragma unroll
        for (int jj = 0; jj < 5; jj++) xr[it][jj] = *(const GAS u32x4*)((const GAS char*)x + (size_t)(128 * m + 16 * wave + 4 * it + r4) * 1280 + (sub + 16 * jj) * 16);
    MEMF(); SB();
#pragma unroll
    for (int it = 0; it < 4; it++) {
        float a = 0.f;
#pragma unroll
        for (int jj = 0; jj < 5; jj++)
#pragma unroll
            for (int e = 0; e < 4; e++) { a = fmaf(blo(xr[it][jj][e]), blo(gq[jj][e]), a); a = fmaf(bhi(xr[it][jj][e]), bhi(gq[jj][e]), a); }
        a += __shfl_xor(a, 1); a += __shfl_xor(a, 2); a += __shfl_xor(a, 4); a += __shfl_xor(a, 8);
        a = rbf(a);
        const float g = rbf(__builtin_amdgcn_rcpf(1.f + __builtin_amdgcn_exp2f(-1.4426950408889634f * a)));
        if (sub == 0) st32c(rw, WS_G + (128 * m + 16 * wave + 4 * it + r4) * 4, __float_as_uint(g));
    }
}

DI void final_block(LAS char* sm, int wave, int lane, int tid, int st, int ct, const u16* x, u16* out, rsrc_t rw, u32 tag, char* ws) {
    const int fr = lane & 15, g = lane >> 4;
    u32x4 xv[2];
#pragma unroll
    for (int r = 0; r < 2; r++) {
        int it = tid + 512 * r; it = it < 640 ? it : 639;
        const int s = it / 10, u = it - 10 * s;
        xv[r] = *(const GAS u32x4*)((const GAS char*)x + (size_t)(st * 64 + s) * 1280 + ct * 160 + u * 16);
    }
    VMCNT(0); BAR();
    if (wave < 4) {
        const int kh = wave & 1, sh = wave >> 1;
        wait_flags(rw, lane < 16 ? (int)F_O + (st >> 4) * 32 + 16 * kh + lane : -1, -1, tag);
        TS(0, 17);
        const u32 ob = WS_O + ((st * 4 + 2 * sh) * 20 + 10 * kh) * 1024 + lane * 16;
        u32x4 B[2][10];
#pragma unroll
        for (int kl = 0; kl < 10; kl++) { B[0][kl] = ld128c(rw, ob + kl * 1024); B[1][kl] = ld128c(rw, ob + 20480 + kl * 1024); }
        MEMF(); SB();
        f32x4 acc[5][2];
#pragma unroll
        for (int cb = 0; cb < 5; cb++) { acc[cb][0] = (f32x4){0, 0, 0, 0}; acc[cb][1] = (f32x4){0, 0, 0, 0}; }
        const u32 aoff = (16 * (g >> 1) + 4 * (g & 1) + (fr >> 2)) * 160 + (lane & 3) * 8;
        u32x2 al[2][5], ah[2][5];
#define F_READ(buf, kl) do { const u32 b0_ = kh ? wo_off(10 + (kl)) : wo_off(kl); \
            _Pragma("unroll") for (int cb = 0; cb < 5; cb++) { al[buf][cb] = ldstr(sm, b0_ + aoff + cb * 32); ah[buf][cb] = ldstr(sm, b0_ + aoff + cb * 32 + 1280); } } while (0)
        F_READ(0, 0);
#pragma unroll
        for (int kl = 0; kl < 10; kl++) {
            if (kl < 9) F_READ((kl + 1) & 1, kl + 1);
            SB();
#pragma unroll
            for (int cb = 0; cb < 5; cb++) {
                const u32x4 a = {al[kl & 1][cb].x, al[kl & 1][cb].y, ah[kl & 1][cb].x, ah[kl & 1][cb].y};
                acc[cb][0] = mfma16(a, B[0][kl], acc[cb][0]); acc[cb][1] = mfma16(a, B[1][kl], acc[cb][1]);
            }
            SB();
        }
        TS(0, 18);
        lds_arrive(sm, C_FR, lane); lds_wait(sm, C_FR, 4);
#pragma unroll
        for (int cb = 0; cb < 5; cb++)
#pragma unroll
            for (int z = 0; z < 2; z++)
                *(LAS f32x4*)(sm + kh * (64 * RED_PITCH) + (16 * (2 * sh + z) + fr) * RED_PITCH + (16 * cb + 4 * g) * 4) = acc[cb][z];
    } else {
        if (wave == 4) { wait_flags(rw, lane < 20 ? (int)F_Y + ct * 32 + lane : lane == 32 ? (int)F_GATE + (st >> 1) : -1, -1, tag); lds_arrive(sm, C_FY, lane); }
        else lds_wait(sm, C_FY, 1);
        const int t = tid - 256;
        u32x4 yv[3];
#pragma unroll
        for (int r = 0; r < 3; r++) { int id = t + 256 * r; id = id < 640 ? id : 639; yv[r] = ld128c(rw, WS_YT + (80 * ct + (id >> 3)) * 4096 + st * 128 + (id & 7) * 16); }
#pragma unroll
        for (int r = 0; r < 3; r++) { const int id = t + 256 * r; if (id < 640) *(LAS u32x4*)(sm + F_YY + id * 16) = yv[r]; }
        if (wave == 4) *(LAS u32*)(sm + F_GG + lane * 4) = ld32c(rw, WS_G + (st * 64 + lane) * 4);
        lds_arrive(sm, C_FY2, lane); lds_wait(sm, C_FY2, 4);
        const int sb = wave - 4, q = fr >> 2, p = lane & 3;
        u32x2 tt[5];
#pragma unroll
        for (int cb = 0; cb < 5; cb++) tt[cb] = ldstr(sm, F_YY + (16 * cb + 4 * g + q) * 128 + (16 * sb + 4 * p) * 2);
#pragma unroll
        for (int cb = 0; cb < 5; cb++) *(LAS u32x2*)(sm + F_Y2 + (16 * sb + fr) * 176 + (16 * cb + 4 * g) * 2) = tt[cb];
    }
    LGKM0(); BAR();
#pragma unroll
    for (int r = 0; r < 2; r++) {
        const int it = tid + 512 * r;
        if (it < 640) {
            const int s = it / 10, u = it - 10 * s;
            const u32 ra = s * RED_PITCH + u * 32;
            const f32x4 p0 = *(LAS f32x4*)(sm + ra), p1 = *(LAS f32x4*)(sm + ra + 16), q0 = *(LAS f32x4*)(sm + 64 * RED_PITCH + ra), q1 = *(LAS f32x4*)(sm + 64 * RED_PITCH + ra + 16);
            const u32x4 y8 = lds128(sm, F_Y2 + s * 176 + u * 16);
            const float gg = *(LAS float*)(sm + F_GG + s * 4), omg = 1.f - gg;
            float o[8];
#pragma unroll
            for (int e = 0; e < 4; e++) {
                const float a0 = (e < 2 ? p0[2 * e] + q0[2 * e] : p1[2 * e - 4] + q1[2 * e - 4]), a1 = (e < 2 ? p0[2 * e + 1] + q0[2 * e + 1] : p1[2 * e - 3] + q1[2 * e - 3]);
                const float b0 = omg * blo(xv[r][e]), b1 = omg * bhi(xv[r][e]);
                o[2 * e] = fmaf(b0, blo(y8[e]), gg * a0); o[2 * e + 1] = fmaf(b1, bhi(y8[e]), gg * a1);
            }
            *(GAS u32x4*)((GAS char*)out + (size_t)(st * 64 + s) * 1280 + ct * 160 + u * 16) = (u32x4){pk2(o[0], o[1]), pk2(o[2], o[3]), pk2(o[4], o[5]), pk2(o[6], o[7])};
        }
    }
}

__global__ __launch_bounds__(512, 2) void fused_kernel(const u16* __restrict__ x, const u16* __restrict__ w, const u16* __restrict__ wo, const u16* __restrict__ kin,
                                                        const u16* __restrict__ vin, const u16* __restrict__ shk, const u16* __restrict__ hk, const u16* __restrict__ sD,
                                                        const u16* __restrict__ gw, u16* __restrict__ out, char* ws) {
    extern __shared__ __attribute__((aligned(16))) char smem[];
    LAS char* sm = (LAS char*)smem;
    const int tid = threadIdx.x, lane = tid & 63, wave = __builtin_amdgcn_readfirstlane(tid >> 6);
    const int b = blockIdx.x, xcd = b & 7, j = b >> 3;
    const rsrc_t rw = __builtin_amdgcn_make_buffer_rsrc(ws, 0, WS_BYTES, 0x00020000);
    u32 tag;
    asm volatile("s_load_dword %0, %1, 0x0\n\ts_waitcnt lgkmcnt(0)" : "=s"(tag) : "s"(ws + WS_FLAG + F_EPOCH * FS) : "memory");
    tag += 1u;
    if (tid < 16) *(LAS u32*)(sm + L_CNT + tid * 4) = 0u;
    TS(0, 0);
    const int st = 4 * xcd + (j >> 3), ct = j & 7;
    int ko = -1;
    bool is_conv = false;

    if (j >= 30) {
        const int m = xcd * 2 + (j - 30);
        ko = -100;
        {
            const int grp = wave >> 2, w4i = wave & 3;
            u32x2 w4[3];
#pragma unroll
            for (int t = 0; t < 3; t++) { const int tt = 2 * t + grp < 5 ? 2 * t + grp : 4; p0_weights(w4[t], shk, 240 + 5 * m + tt, w4i, lane); }
#pragma unroll
            for (int t = 0; t < 3; t++) if (2 * t + grp < 5) p0_issue(kin, vin, 240 + 5 * m + 2 * t + grp, w4i, lane, (2 * t + grp) * 17408);
            VMCNT(0);
#pragma unroll
            for (int t = 0; t < 3; t++) PIN(w4[t]);
            BAR();
#pragma unroll
            for (int t = 0; t < 3; t++)
                if (2 * t + grp < 5) p0_compute(sm, w4[t], 240 + 5 * m + 2 * t + grp, w4i, lane, (2 * t + grp) * 17408, 90112 + (2 * t + grp) * 8192);
            LGKM0(); BAR();
            {
                u32x4 t4[3][2];
#pragma unroll
                for (int t = 0; t < 3; t++)
#pragma unroll
                    for (int z = 0; z < 2; z++) { const int ch2 = ((tid & 255) >> 3) + 32 * z, o2 = tid & 7, tt = 2 * t + grp < 5 ? 2 * t + grp : 4; t4[t][z] = lds128(sm, 90112 + tt * 8192 + ch2 * 128 + ((o2 ^ (ch2 & 7)) * 16)); }
#pragma unroll
                for (int t = 0; t < 3; t++)
                    if (2 * t + grp < 5) {
                        const int tile = 240 + 5 * m + 2 * t + grp, cg = tile % 10, s0 = (tile / 10) * 64;
#pragma unroll
                        for (int z = 0; z < 2; z++) { const int ch2 = ((tid & 255) >> 3) + 32 * z, o2 = tid & 7; st128c(rw, WS_UT + (cg * 64 + ch2) * 4096 + (s0 + 8 * o2) * 2, t4[t][z]); }
                    }
            }
        }
        VMCNT(0); BAR();
        if (tid < 5) set_flag(rw, p0_flag(240 + 5 * m + tid), tag);
        TS(0, 1);
        gate_block(wave, lane, m, x, gw, rw);
        VMCNT(0); LGKM0(); BAR();
        if (tid == 0) set_flag(rw, F_GATE + m, tag);
        TS(0, 3);
    } else {
        const int rt = 2 * xcd + j / 15, cbk = j % 15, tile = xcd * 30 + j;
        is_conv = cbk < 10;
        if (!is_conv) { const int vi = rt * 5 + (cbk - 10); ko = vi < 64 ? vi : -2 - (vi - 64); }
        if (wave >= 4) {
            const int lw = wave - 4;
            p0_issue(kin, vin, tile, lw, lane, P0_K);
            u32 vx[4], vw[4];
#pragma unroll
            for (int n2 = 0; n2 < 4; n2++) {
                const int n = 4 * lw + n2;
                const int row = 8 * n + (lane >> 3), ch = (lane & 7) ^ ((row >> 1) & 7);
                vx[n2] = (u32)((rt * 128 + row) * 1280 + ch * 16);
                const int kr = 4 * n + (lane >> 4), f = ((kr & 3) << 2) | ((kr >> 2) & 3), cw = (lane & 15) ^ f;
                vw[n2] = (u32)(kr * 3840 + cbk * 256 + cw * 16);
            }
            const int rot = (rt + cbk) % 10;
#define K1_ISSUE(c) do { const u32 st_ = ((c) & 3) * K1_STAGE + 4 * lw * 1024; int kc_ = (c) + ROT_ON * rot; kc_ = kc_ >= 10 ? kc_ - 10 : kc_; \
            const char* xb_ = (const char*)x + kc_ * 128; const char* wp_ = (const char*)w + (size_t)kc_ * 64 * 3840; \
            _Pragma("unroll") for (int n2 = 0; n2 < 4; n2++) { if (NT_X) glds16nt(xb_, vx[n2], st_ + 16384 + n2 * 1024); else glds16(xb_, vx[n2], st_ + 16384 + n2 * 1024); glds16(wp_, vw[n2], st_ + n2 * 1024); } } while (0)
            K1_ISSUE(0);
            VMCNT(8); BAR();
            K1_ISSUE(1); K1_ISSUE(2);
            VMCNT(16); BAR();
            K1_ISSUE(3);
#pragma unroll 1
            for (int c = 0; c < 6; c++) {
                VMCNT(16); BAR();
                K1_ISSUE(c + 4);
            }
            const int cv = rt * 10 + cbk, c = 4 * cv + lw;
            if (is_conv) {
                CvIn in; u32 fl; u32x4 u[4];
                const int fidx = (int)F_P0 + (cv >> 4) * 32 + (lane & 31);
                conv_fetch_h(in, hk, c, lane);
                VMCNT(24); BAR();
                VMCNT(16); BAR();
                gld32c(fl, ws, WS_FLAG + fidx * FS);
                VMCNT(9); BAR();
                TS(4, 4);
                VMCNT(0); PIN(fl);
                const bool ready = __all(fl == tag);
                if (ready) {
#pragma unroll
                    for (int z = 0; z < 4; z++) gld128c(u[z], ws, WS_UT + c * 4096 + z * 1024 + lane * 16);
                }
                TS(4, 5);
                conv_build_h(sm, in, lw, lane);
                TS(4, 6);
                if (!ready) {
                    wait_flags(rw, fidx, -1, tag);
#pragma unroll
                    for (int z = 0; z < 4; z++) gld128c(u[z], ws, WS_UT + c * 4096 + z * 1024 + lane * 16);
                }
                TS(4, 7);
                VMCNT(0);
#pragma unroll
                for (int z = 0; z < 4; z++) { PIN(u[z]); *(LAS u32x4*)(sm + CV_BASE + lw * CV_WAVE + CV_U + 512 + z * 1024 + lane * 16) = u[z]; }
                const float Dc = blo((u32)sD[c]);
                TS(4, 8);
                conv_run(sm, Dc, lw, lane, c, rw, ws);
                publish_last(rw, sm, C_Y, 4, F_Y + (cv / 20) * 32 + cv % 20, tag, lane);
                TS(4, 11);
            } else {
                VMCNT(16); BAR();
                VMCNT(8); BAR();
                VMCNT(0); BAR();
            }
        } else {
            u32x2 w4; p0_weights(w4, shk, tile, wave, lane);
            if (PREF1 && wave < 3) {
                const int n = wave * 64 + lane;
                if (wave == 0) touch(x, (u32)((xcd * 2560 + j * 80 + lane) * 128));
                else if (wave == 1) { if (lane < 16) touch(x, (u32)((xcd * 2560 + j * 80 + 64 + lane) * 128)); else touch(w, (u32)(((xcd * 30 + j) * 80 + lane - 16) * 128)); }
                else if (lane < 32) touch(w, (u32)(((xcd * 30 + j) * 80 + 48 + lane) * 128));
                (void)n;
            }
            const int wm = wave >> 1, wn = wave & 1, r32 = lane & 31, hh = lane >> 5;
            K1Addr a0;
#pragma unroll
            for (int ks = 0; ks < 2; ks++) {
                a0.x[0][ks] = (64 * wm + r32) * 128 + (((2 * ks + hh) ^ ((r32 >> 1) & 7)) * 16);
                a0.x[1][ks] = a0.x[0][ks] ^ 64;
            }
#pragma unroll
            for (int jj = 0; jj < 2; jj++)
#pragma unroll
                for (int t = 0; t < 2; t++) {
                    const int blk = (lane >> 4) & 1, q = (lane & 15) >> 2, p = lane & 3, row = 8 * hh + 4 * t + q, ch = 8 * wn + 4 * jj + 2 * blk + (p >> 1);
                    const int f = ((row & 3) << 2) | ((row >> 2) & 3);
                    a0.w[jj][t] = row * 256 + ((ch ^ f) * 16) + 8 * (p & 1);
                }
            f32x16 acc[2][2];
#pragma unroll
            for (int i = 0; i < 2; i++)
#pragma unroll
                for (int jj = 0; jj < 2; jj++)
#pragma unroll
                    for (int e = 0; e < 16; e++) acc[i][jj][e] = 0.f;
            TS(0, 20);
            if (PREF1 && wave == 1) VMCNT(2); else if (PREF1 && wave < 3) VMCNT(1); else VMCNT(0);
            PIN(w4); TS(0, 21); BAR();
            TS(0, 22);
            p0_compute(sm, w4, tile, wave, lane, P0_K, P0_OUT, ws);
            TS(0, 28);
            LGKM0(); TS(0, 23); BAR();
            TS(0, 24);
            p0_store(sm, rw, tile, tid, P0_OUT);
            TS(0, 1);
            if (cbk < 5) {
                k1_loop<true>(sm, a0, acc, rw, p0_flag(tile), tag, wave, lane, hk, wo, tile);
                __builtin_amdgcn_s_setprio(2);
                TS(0, 2);
                u32x4 qv[2][2][2];
#pragma unroll
                for (int jj = 0; jj < 2; jj++)
#pragma unroll
                    for (int ap = 0; ap < 2; ap++)
#pragma unroll
                        for (int i = 0; i < 2; i++) {
                            const f32x16& a = acc[i][jj];
                            const u32 x0 = phi2(a[8 * ap], a[8 * ap + 1]), y0 = phi2(a[8 * ap + 2], a[8 * ap + 3]);
                            const u32 x1 = phi2(a[8 * ap + 4], a[8 * ap + 5]), y1 = phi2(a[8 * ap + 6], a[8 * ap + 7]);
                            auto rx = __builtin_amdgcn_permlane32_swap(x0, x1, false, false);
                            auto ry = __builtin_amdgcn_permlane32_swap(y0, y1, false, false);
                            qv[jj][ap][i] = (u32x4){rx[0], ry[0], rx[1], ry[1]};
                        }
                if (QDELAY) { __builtin_amdgcn_s_setprio(0); for (int z = 0; z < QDELAY; z++) __builtin_amdgcn_s_sleep(8); }
#pragma unroll
                for (int jj = 0; jj < 2; jj++)
#pragma unroll
                    for (int ap = 0; ap < 2; ap++) {
                        const int U = 16 * cbk + 8 * wn + 4 * jj + 2 * ap + hh, pr = U / 5, u5 = U - 5 * pr;
                        const u32 dst = WS_P2 + (((pr * 128 + rt * 8 + 4 * wm + (r32 >> 4)) * 5 + u5) * 128 + (r32 & 15) * 8) * 2;
#pragma unroll
                        for (int i = 0; i < 2; i++) st128c(rw, dst + i * 2560, qv[jj][ap][i]);
                    }
            } else {
                k1_loop<false>(sm, a0, acc, rw, p0_flag(tile), tag, wave, lane, hk, wo, tile);
                __builtin_amdgcn_s_setprio(2);
                TS(0, 2);
                const bool isk = cbk < 10;
                const int j5 = isk ? cbk - 5 : cbk - 10;
                u32 dst[2], istep[2];
#pragma unroll
                for (int jj = 0; jj < 2; jj++) {
                    const int cc = 128 * j5 + 64 * wn + 32 * jj + r32, h = cc / 20, d = cc - 20 * h;
                    const int sb32 = rt * 4 + 2 * wm;
                    const bool lo = d < 16;
                    dst[jj] = lo ? (isk ? WS_PK0 : WS_V0) + (((h * 64 + sb32) * 4 + hh) * 128 + d * 8) * 2
                                 : WS_X + (((h * 32 + (sb32 >> 1)) * 4 + hh) * 128 + (d - 16 + (isk ? 0 : 4)) * 8) * 2;
                    istep[jj] = lo ? 1024 : 128;
                }
                if (isk) {
#pragma unroll
                    for (int jj = 0; jj < 2; jj++)
#pragma unroll
                        for (int i = 0; i < 2; i++)
#pragma unroll
                            for (int z = 0; z < 2; z++) {
                                const f32x16& a = acc[i][jj];
                                st128c(rw, dst[jj] + i * istep[jj] + z * 512,
                                       (u32x4){phi2(a[8 * z], a[8 * z + 1]), phi2(a[8 * z + 2], a[8 * z + 3]), phi2(a[8 * z + 4], a[8 * z + 5]), phi2(a[8 * z + 6], a[8 * z + 7])});
                            }
                } else {
#pragma unroll
                    for (int jj = 0; jj < 2; jj++)
#pragma unroll
                        for (int i = 0; i < 2; i++)
#pragma unroll
                            for (int z = 0; z < 2; z++) {
                                const f32x16& a = acc[i][jj];
                                st128c(rw, dst[jj] + i * istep[jj] + z * 512,
                                       (u32x4){pk2(a[8 * z], a[8 * z + 1]), pk2(a[8 * z + 2], a[8 * z + 3]), pk2(a[8 * z + 4], a[8 * z + 5]), pk2(a[8 * z + 6], a[8 * z + 7])});
                            }
                }
            }
            LGKM0();
            TS(0, 25);
            publish_last(rw, sm, C_G1, 4, F_G1 + cbk * 16 + rt, tag, lane);
            __builtin_amdgcn_s_setprio(0);
            TS(0, 3);
            if (is_conv) {
                lds_wait(sm, C_G1, 4);
                wo_issue(wo, ct, 0, 12, wave, 4, lane);
            }
        }
    }
    if (!is_conv) {
        LGKM0(); BAR();
        if (ko >= 0) {
            const int h = ko >> 1, half = ko & 1;
            wo_issue(wo, ct, 0, 12, wave, 8, lane);
            kvo_block(sm, wave, lane, h, half, rw, tag, ws);
            TS(0, 26);
            VMCNT(0); BAR();
            if (tid == 0) set_flag(rw, F_O + half * 32 + h, tag);
            TS(0, 11);
            wo_issue(wo, ct, 12, 20, wave, 8, lane);
        } else {
            wo_issue(wo, ct, 0, 20, wave, 8, lane);
            if (ko == -2 && wave == 0) {
                wait_flags(rw, (int)F_G1 + lane, (int)F_G1 + 64 + lane, tag);
                wait_flags(rw, (int)F_G1 + 128 + lane, (int)F_G1 + 192 + lane, tag);
                if (lane == 0) set_flag(rw, F_EPOCH, tag);
            }
        }
    } else {
        VMCNT(0); BAR();
        if (wave >= 4) wo_issue(wo, ct, 12, 20, wave - 4, 4, lane);
    }
    TS(0, 16);
    final_block(sm, wave, lane, tid, st, ct, x, out, rw, tag, ws);
    TS(0, 19);
}

extern "C" void kernel_launch(void* const* d_in, const int* in_sizes, int n_in, void* d_out, int out_size, void* d_ws, size_t ws_size, hipStream_t stream) {
    const u16* x = (const u16*)d_in[0]; const u16* wqkv = (const u16*)d_in[1]; const u16* wo = (const u16*)d_in[2];
    const u16* kin = (const u16*)d_in[4]; const u16* vin = (const u16*)d_in[5]; const u16* shk = (const u16*)d_in[6];
    const u16* hk = (const u16*)d_in[7]; const u16* sD = (const u16*)d_in[8]; const u16* gw = (const u16*)d_in[9];
    (void)hipFuncSetAttribute((const void*)fused_kernel, hipFuncAttributeMaxDynamicSharedMemorySize, LDS_BYTES);
    fused_kernel<<<256, 512, LDS_BYTES, stream>>>(x, wqkv, wo, kin, vin, shk, hk, sD, gw, (u16*)d_out, (char*)d_ws);
}
```

```cpp
#define PROBE_ID 8
#define PROBE_WG 83
#include <hip/hip_runtime.h>
#include <stdint.h>

typedef unsigned short u16;
typedef unsigned int u32;
typedef __bf16 bf16x8 __attribute__((ext_vector_type(8)));
typedef __bf16 bf16x2 __attribute__((ext_vector_type(2)));
typedef float f32x4 __attribute__((ext_vector_type(4)));
typedef float f32x2 __attribute__((ext_vector_type(2)));
typedef float f32x16 __attribute__((ext_vector_type(16)));
typedef u32 u32x4 __attribute__((ext_vector_type(4)));
typedef u32 u32x2 __attribute__((ext_vector_type(2)));
typedef short s16x4 __attribute__((ext_vector_type(4)));
#define LAS __attribute__((address_space(3)))
#define GAS __attribute__((address_space(1)))
#define DI __device__ __forceinline__
typedef __amdgpu_buffer_rsrc_t rsrc_t;

constexpr int S = 2048, D = 640;
#define FS 64
#define POLL_SLEEP 2
#define ROT_ON 1

constexpr u32 WS_UT = 0;
constexpr u32 WS_YT = WS_UT + 2621440;
constexpr u32 WS_PK0 = WS_YT + 2621440;
constexpr u32 WS_V0 = WS_PK0 + 2097152;
constexpr u32 WS_X = WS_V0 + 2097152;
constexpr u32 WS_P2 = WS_X + 1048576;
constexpr u32 WS_O = WS_P2 + 2621440;
constexpr u32 WS_G = WS_O + 2621440;
constexpr u32 WS_FLAG = WS_G + 8192;
constexpr u32 F_P0 = 0;
constexpr u32 F_G1 = 320;
constexpr u32 F_GATE = 560;
constexpr u32 F_O = 576;
constexpr u32 F_Y = 640;
constexpr u32 F_EPOCH = 896, F_TMO = 928;
constexpr u32 WS_STAMP = WS_FLAG + 1024 * FS;
constexpr u32 WS_BYTES = WS_STAMP + 256 * 32 * 16;

extern __shared__ __attribute__((aligned(16))) char smem_probe[];
#define TS(w, id) do { if (((id) == PROBE_ID || (id) == 0) && blockIdx.x == PROBE_WG && threadIdx.x == 64 * (w)) { \
    *(volatile LAS unsigned long long*)((LAS char*)smem_probe + 156672 + 64 + ((id) == 0 ? 0 : 8)) = __builtin_amdgcn_s_memrealtime(); } } while (0)

DI u32 pk2(float a, float b) { f32x2 t = {a, b}; return __builtin_bit_cast(u32, __builtin_convertvector(t, bf16x2)); }
DI float blo(u32 u) { return __uint_as_float(u << 16); }
DI float bhi(u32 u) { return __uint_as_float(u & 0xffff0000u); }
DI float rbf(float f) { return blo(pk2(f, f)); }
DI bf16x8 asbf(u32x4 v) { return __builtin_bit_cast(bf16x8, v); }
DI f32x4 mfma16(u32x4 a, u32x4 b, f32x4 c) { return __builtin_amdgcn_mfma_f32_16x16x32_bf16(asbf(a), asbf(b), c, 0, 0, 0); }
DI f32x16 mfma32(u32x4 a, u32x4 b, f32x16 c) { return __builtin_amdgcn_mfma_f32_32x32x16_bf16(asbf(a), asbf(b), c, 0, 0, 0); }

DI void glds16(const void* sbase, u32 voff, u32 lds) {
    asm volatile("s_mov_b32 m0, %2\n\ts_nop 0\n\tglobal_load_lds_dwordx4 %0, %1" :: "v"(voff), "s"(sbase), "s"(lds) : "memory");
}
DI void glds16nt(const void* sbase, u32 voff, u32 lds) {
    asm volatile("s_mov_b32 m0, %2\n\ts_nop 0\n\tglobal_load_lds_dwordx4 %0, %1 nt" :: "v"(voff), "s"(sbase), "s"(lds) : "memory");
}
DI void touch(const void* sbase, u32 voff) {
    asm volatile("s_mov_b32 m0, %2\n\ts_nop 0\n\tglobal_load_lds_dword %0, %1" :: "v"(voff), "s"(sbase), "s"(156928u) : "memory");
}
#define NT_KV 0
#define NT_X 0
#define NT_H 0
#define PREF1 0
#define PREF2 0
#define QDELAY 0
DI void gld64(u32x2& d, const void* sbase, u32 voff) { asm volatile("global_load_dwordx2 %0, %1, %2" : "=v"(d) : "v"(voff), "s"(sbase) : "memory"); }
DI void gld128(u32x4& d, const void* sbase, u32 voff) { asm volatile("global_load_dwordx4 %0, %1, %2" : "=v"(d) : "v"(voff), "s"(sbase) : "memory"); }
DI void gld128c(u32x4& d, const void* sbase, u32 voff) { asm volatile("global_load_dwordx4 %0, %1, %2 sc1" : "=v"(d) : "v"(voff), "s"(sbase) : "memory"); }
DI void gld32c(u32& d, const void* sbase, u32 voff) { asm volatile("global_load_dword %0, %1, %2 sc1" : "=v"(d) : "v"(voff), "s"(sbase) : "memory"); }
#define PIN(x) asm volatile("" : "+v"(x))
#define SB() __builtin_amdgcn_sched_barrier(0)
DI u32x4 ld128c(rsrc_t r, u32 off) { return __builtin_amdgcn_raw_buffer_load_b128(r, off, 0, 16); }
DI u32 ld32c(rsrc_t r, u32 off) { return __builtin_amdgcn_raw_buffer_load_b32(r, off, 0, 16); }
DI void st128c(rsrc_t r, u32 off, u32x4 v) { __builtin_amdgcn_raw_buffer_store_b128(v, r, off, 0, 16); }
DI void st64c(rsrc_t r, u32 off, u32x2 v) { __builtin_amdgcn_raw_buffer_store_b64(v, r, off, 0, 16); }
DI void st32c(rsrc_t r, u32 off, u32 v) { __builtin_amdgcn_raw_buffer_store_b32(v, r, off, 0, 16); }
DI u32x4 lds128(LAS char* sm, u32 a) { return *(LAS u32x4*)(sm + a); }
DI u32x2 lds64(LAS char* sm, u32 a) { return *(LAS u32x2*)(sm + a); }
DI u32x2 ldstr(LAS char* sm, u32 a) { return __builtin_bit_cast(u32x2, __builtin_amdgcn_ds_read_tr16_b64_v4i16((LAS s16x4*)(sm + a))); }
#define VMCNT(n) asm volatile("s_waitcnt vmcnt(" #n ")" ::: "memory")
#define LGKM0() asm volatile("s_waitcnt lgkmcnt(0)" ::: "memory")
#define BAR() do { asm volatile("" ::: "memory"); __builtin_amdgcn_s_barrier(); asm volatile("" ::: "memory"); } while (0)
#define MEMF() asm volatile("" ::: "memory")

DI void set_flag(rsrc_t r, u32 idx, u32 tag) { st32c(r, WS_FLAG + idx * FS, tag); }
DI void wait_flags(rsrc_t r, int idx0, int idx1, u32 tag) {
    const unsigned long long t0 = __builtin_amdgcn_s_memrealtime();
    for (u32 it = 1;; it++) {
        u32 a = tag, b = tag;
        if (idx0 >= 0) a = ld32c(r, WS_FLAG + idx0 * FS);
        if (idx1 >= 0) b = ld32c(r, WS_FLAG + idx1 * FS);
        if (__all(a == tag && b == tag)) break;
        if (POLL_SLEEP) __builtin_amdgcn_s_sleep(POLL_SLEEP);
        if ((it & 63u) == 0u && __builtin_amdgcn_s_memrealtime() - t0 > 2000000ull) { set_flag(r, F_TMO, tag); break; }
    }
    MEMF();
}
DI void lds_arrive(LAS char* sm, u32 cnt, int lane) { LGKM0(); if (lane == 0) __hip_atomic_fetch_add((LAS u32*)(sm + cnt), 1u, __ATOMIC_RELAXED, __HIP_MEMORY_SCOPE_WORKGROUP); MEMF(); }
DI void lds_wait(LAS char* sm, u32 cnt, u32 n) { MEMF(); while (*(volatile LAS u32*)(sm + cnt) < n) __builtin_amdgcn_s_sleep(0); MEMF(); }
DI void publish_last(rsrc_t r, LAS char* sm, u32 cnt, u32 nw, u32 flag, u32 tag, int lane) {
    VMCNT(0);
    if (lane == 0) {
        const u32 old = __hip_atomic_fetch_add((LAS u32*)(sm + cnt), 1u, __ATOMIC_RELAXED, __HIP_MEMORY_SCOPE_WORKGROUP);
        if (old == nw - 1) set_flag(r, flag, tag);
    }
    MEMF();
}

DI u32 phi2(float a, float b) {
    const f32x2 t = {a, b}, s = t * 1.4426950408889634f;
    const float ea = __builtin_amdgcn_fmed3f(__builtin_amdgcn_exp2f(s.x), 0.f, 1.f), eb = __builtin_amdgcn_fmed3f(__builtin_amdgcn_exp2f(s.y), 0.f, 1.f);
    const float ma = __int_as_float(max(__float_as_int(a), 0)), mb = __int_as_float(max(__float_as_int(b), 0));
    const f32x2 r = ((f32x2){ea, eb} + (f32x2){ma, mb}) + 1e-6f;
    return pk2(r.x, r.y);
}

constexpr int K1_STAGE = 32768;
constexpr int P0_K = 131072, P0_V = P0_K + 9216, P0_OUT = P0_V + 8192;
constexpr int L_CNT = 156672;
constexpr int C_G1 = L_CNT, C_Y = L_CNT + 4, C_P0A = L_CNT + 8, C_P0B = L_CNT + 12, C_P0C = L_CNT + 16, C_FY = L_CNT + 20, C_FY2 = L_CNT + 24, C_FO = L_CNT + 28, C_FR = L_CNT + 32;
constexpr int CV_PITCH = 4160;
constexpr int CV_U = 4 * CV_PITCH;
constexpr int CV_WAVE = CV_U + 4608;
constexpr int CV_BASE = 65536;
constexpr int WO_HI = 65536;
constexpr int F_YY = 110592;
constexpr int F_Y2 = F_YY + 10240;
constexpr int F_GG = F_Y2 + 11264;
constexpr int KO_RED = 110592;
constexpr int KO_KV = KO_RED + 32768;
constexpr int RED_PITCH = 336;
constexpr int LDS_BYTES = 160 * 1024;
DI u32 p0_flag(int tile) { return F_P0 + (tile % 10) * 32 + tile / 10; }
DI constexpr u32 wo_off(int kap) { return kap < 12 ? kap * 5120 : WO_HI + (kap - 12) * 5120; }

DI void p0_issue(const u16* kin, const u16* vin, int tile, int lw, int lane, u32 base) {
    const int cg = tile % 10, rg = tile / 10, s0 = rg * 64;
    const u32 col = cg * 128 + (lane & 7) * 16;
#pragma unroll
    for (int z = 0; z < 2; z++) {
        const int n = 2 * lw + z;
        int r = s0 - 4 + n * 8 + (lane >> 3); r = r < 0 ? 0 : r;
        (NT_KV ? glds16nt : glds16)(kin, (u32)(r * 1280) + col, base + n * 1024);
        (NT_KV ? glds16nt : glds16)(vin, (u32)((s0 + n * 8 + (lane >> 3)) * 1280) + col, base + 9216 + n * 1024);
    }
    {
        int r = s0 + 60 + (lane >> 3); r = r > S - 1 ? S - 1 : r;
        (NT_KV ? glds16nt : glds16)(kin, (u32)(r * 1280) + col, base + 8 * 1024);
    }
}
DI float dot2(u32 a, u32 b, float c) { return __builtin_amdgcn_fdot2_f32_bf16(__builtin_bit_cast(bf16x2, a), __builtin_bit_cast(bf16x2, b), c, false); }
DI void p0_compute(LAS char* sm, u32x2 w4, int tile, int wave, int lane, u32 base, u32 outb, char* ws = nullptr) {
    const int s0 = (tile / 10) * 64;
    const int G = wave * 4 + (lane >> 4), cb = G & 3, i = lane & 15, q = i >> 2, p = i & 3;
    const u32 wa = __builtin_amdgcn_alignbit(w4.y, w4.y, 16), wb = __builtin_amdgcn_alignbit(w4.x, w4.x, 16);
    const int ch = 16 * cb + i;
    u32x2 kq[2][3], vq[2][2];
#pragma unroll
    for (int z = 0; z < 2; z++) {
        const int o = (G >> 2) + 4 * z;
        const u32 ka = base + (8 * o + q) * 128 + 32 * cb + 8 * p, va = ka + 9216;
        kq[z][0] = ldstr(sm, ka); kq[z][1] = ldstr(sm, ka + 512); kq[z][2] = ldstr(sm, ka + 1024);
        vq[z][0] = ldstr(sm, va); vq[z][1] = ldstr(sm, va + 512);
    }
#pragma unroll
    for (int z = 0; z < 2; z++) {
        const int o = (G >> 2) + 4 * z;
        u32 K[6] = {kq[z][0].x, kq[z][0].y, kq[z][1].x, kq[z][1].y, kq[z][2].x, kq[z][2].y};
        if (s0 == 0 && o == 0) { K[0] = 0; K[1] = 0; }
        u32 Sh[5];
#pragma unroll
        for (int e = 0; e < 5; e++) Sh[e] = __builtin_amdgcn_alignbit(K[e + 1], K[e], 16);
        const u32 V[4] = {vq[z][0].x, vq[z][0].y, vq[z][1].x, vq[z][1].y};
        u32 r[4];
#pragma unroll
        for (int e = 0; e < 4; e++) {
            const float k0 = dot2(Sh[e], wa, dot2(Sh[e + 1], wb, 0.f)), k1 = dot2(K[e + 1], wa, dot2(K[e + 2], wb, 0.f));
            r[e] = pk2(k0 * blo(V[e]), k1 * bhi(V[e]));
        }
        *(LAS u32x4*)(sm + outb + ch * 128 + ((o ^ (ch & 7)) * 16)) = (u32x4){r[0], r[1], r[2], r[3]};
    }
}
DI void p0_store(LAS char* sm, rsrc_t rw, int tile, int t256, u32 outb) {
    const int cg = tile % 10, s0 = (tile / 10) * 64;
#pragma unroll
    for (int z = 0; z < 2; z++) {
        const int ch2 = (t256 >> 3) + 32 * z, o2 = t256 & 7;
        const u32x4 t4 = lds128(sm, outb + ch2 * 128 + ((o2 ^ (ch2 & 7)) * 16));
        st128c(rw, WS_UT + (cg * 64 + ch2) * 4096 + (s0 + 8 * o2) * 2, t4);
    }
}
DI void p0_weights(u32x2& w4, const u16* shk, int tile, int wave, int lane) {
    const int cg = tile % 10, cb = (wave * 4 + (lane >> 4)) & 3;
    gld64(w4, shk, (u32)((cg * 64 + 16 * cb + (lane & 15)) * 8));
}

struct K1Set { u32x4 xa[2][2]; u32x2 wl[2][2], wh[2][2]; };
struct K1Addr { u32 x[2][2]; u32 w[2][2]; };
template <bool QT, int WO, int XO>
DI void k1_phase(LAS char* sm, f32x16 (&acc)[2][2], const K1Set& cur, K1Set& nxt, const u32 (&xb)[2], const u32 (&wb)[2][2]) {
#pragma unroll
    for (int n = 0; n < 8; n++) {
        const int ks = n >> 2, i = (n >> 1) & 1, jj = n & 1;
        const u32x4 wf = {cur.wl[ks][jj].x, cur.wl[ks][jj].y, cur.wh[ks][jj].x, cur.wh[ks][jj].y};
        acc[i][jj] = QT ? mfma32(wf, cur.xa[ks][i], acc[i][jj]) : mfma32(cur.xa[ks][i], wf, acc[i][jj]);
        if (n == 0) { nxt.wl[0][0] = ldstr(sm, wb[0][0] + WO); nxt.wh[0][0] = ldstr(sm, wb[0][1] + WO); }
        if (n == 1) { nxt.xa[0][0] = lds128(sm, xb[0] + XO); nxt.wl[0][1] = ldstr(sm, wb[1][0] + WO); }
        if (n == 2) { nxt.wh[0][1] = ldstr(sm, wb[1][1] + WO); nxt.xa[0][1] = lds128(sm, xb[0] + XO + 4096); }
        if (n == 3) { nxt.wl[1][0] = ldstr(sm, wb[0][0] + WO + 4096); nxt.wh[1][0] = ldstr(sm, wb[0][1] + WO + 4096); }
        if (n == 4) { nxt.xa[1][0] = lds128(sm, xb[1] + XO); nxt.wl[1][1] = ldstr(sm, wb[1][0] + WO + 4096); }
        if (n == 5) { nxt.wh[1][1] = ldstr(sm, wb[1][1] + WO + 4096); nxt.xa[1][1] = lds128(sm, xb[1] + XO + 4096); }
        SB();
    }
}
template <bool QT, int ST>
DI void k1_chunk(LAS char* sm, f32x16 (&acc)[2][2], K1Set& s0, K1Set& s1, const K1Addr& a0, const K1Addr& a1, bool bar, bool drain = false) {
    constexpr int SN = (ST + 1) & 3;
    const K1Addr& ac = ST < 2 ? a0 : a1; const K1Addr& an = SN < 2 ? a0 : a1;
    k1_phase<QT, (ST & 1) * K1_STAGE + 8192, (ST & 1) * K1_STAGE + 16384>(sm, acc, s0, s1, ac.x[1], ac.w);
    if (drain) VMCNT(0);
    if (bar) { LGKM0(); BAR(); }
    k1_phase<QT, (SN & 1) * K1_STAGE, (SN & 1) * K1_STAGE + 16384>(sm, acc, s1, s0, an.x[0], an.w);
}
template <bool QT>
DI void k1_loop(LAS char* sm, const K1Addr& a0, f32x16 (&acc)[2][2], rsrc_t rw, u32 pflag, u32 tag, int wave, int lane, const u16* hk, const u16* wo, int n240) {
    K1Addr a1;
#pragma unroll
    for (int i = 0; i < 2; i++)
#pragma unroll
        for (int z = 0; z < 2; z++) { a1.x[i][z] = a0.x[i][z] + 65536; a1.w[i][z] = a0.w[i][z] + 65536; }
    K1Set s0, s1;
#pragma unroll
    for (int ks = 0; ks < 2; ks++)
#pragma unroll
        for (int jj = 0; jj < 2; jj++) {
            s0.wl[ks][jj] = ldstr(sm, a0.w[jj][0] + ks * 4096); s0.wh[ks][jj] = ldstr(sm, a0.w[jj][1] + ks * 4096);
            s0.xa[ks][jj] = lds128(sm, a0.x[0][ks] + 16384 + jj * 4096);
        }
    SB();
#pragma unroll 1
    for (int c = 0; c < 8; c += 4) {
        k1_chunk<QT, 0>(sm, acc, s0, s1, a0, a1, true);
        k1_chunk<QT, 1>(sm, acc, s0, s1, a0, a1, true);
        k1_chunk<QT, 2>(sm, acc, s0, s1, a0, a1, true);
        k1_chunk<QT, 3>(sm, acc, s0, s1, a0, a1, true, c == 0);
        if (c == 0 && wave == 0 && lane == 0) set_flag(rw, pflag, tag);
        if (PREF2 && c == 0 && wave >= 2) {
            if (wave == 2) { const u32 l_ = n240 * 86 + lane; touch(hk, (l_ < 20480 ? l_ : 20479) * 128); }
            else { const u32 l_ = n240 * 86 + 64 + lane, m_ = n240 * 27 + lane - 22;
                   if (lane < 22) touch(hk, (l_ < 20480 ? l_ : 20479) * 128); else if (lane < 49) touch(wo, (m_ < 6400 ? m_ : 6399) * 128); }
        }
    }
    k1_chunk<QT, 0>(sm, acc, s0, s1, a0, a1, true);
    k1_chunk<QT, 1>(sm, acc, s0, s1, a0, a1, false);
}

DI void wo_issue(const u16* wo, int ct, int k0, int k1, int w, int nw, int lane) {
    u32 vo[5];
#pragma unroll
    for (int i = 0; i < 5; i++) { const int n = 64 * i + lane, rho = n / 10, c = n - 10 * rho, row = (rho & 19) | ((rho & 4) << 1) | ((rho & 8) >> 1); vo[i] = (u32)(row * 1280 + ct * 160 + c * 16); }
#pragma unroll 1
    for (int kap = k0 + w; kap < k1; kap += nw) {
        const char* src = (const char*)wo + (size_t)kap * 40960;
        const u32 dst = kap < 12 ? kap * 5120 : WO_HI + (kap - 12) * 5120;
#pragma unroll
        for (int i = 0; i < 5; i++) glds16(src, vo[i], dst + i * 1024);
    }
}

struct CvSet { u32x2 fl[8], fh[8]; u32x4 g[8]; };
DI void cv_load(LAS char* sm, CvSet& s, u32 fa, u32 ga) {
#pragma unroll
    for (int k1 = 0; k1 < 8; k1++) { s.fl[k1] = ldstr(sm, fa + 512 * k1 + 8); s.fh[k1] = ldstr(sm, fa + 512 * k1); s.g[k1] = lds128(sm, ga + 512 * k1); }
}
DI void cv_phase(LAS char* sm, f32x4 (&Y)[8], const CvSet& cur, CvSet& nxt, u32 fa, u32 ga) {
    PIN(fa); PIN(ga);
    SB();
    int n = 0;
#pragma unroll
    for (int e = 0; e < 8; e++)
#pragma unroll
        for (int k1 = 0; k1 + e < 8; k1++) {
            const u32x4 F = {cur.fl[k1].x, cur.fl[k1].y, cur.fh[k1].x, cur.fh[k1].y};
            Y[k1 + e] = mfma16(F, cur.g[e], Y[k1 + e]);
            if (n == 0) nxt.g[0] = lds128(sm, ga);
            else if (n <= 16) { const int r = (n - 1) >> 1; if ((n - 1) & 1) nxt.fh[r] = ldstr(sm, fa + 512 * r); else nxt.fl[r] = ldstr(sm, fa + 512 * r + 8); }
            else if (n < 24) nxt.g[n - 16] = lds128(sm, ga + 512 * (n - 16));
            n++;
            SB();
        }
}
struct CvIn { u32x4 hd[4]; u32x2 hm[4]; };
DI void conv_fetch_h(CvIn& in, const u16* hk, int c, int lane) {
    const char* hb = (const char*)hk + (size_t)c * 4096;
#pragma unroll
    for (int z = 0; z < 4; z++) {
        const int i = lane + 64 * z;
        if (NT_H) asm volatile("global_load_dwordx4 %0, %1, %2 nt" : "=v"(in.hd[z]) : "v"((u32)(i * 16)), "s"(hb) : "memory"); else gld128(in.hd[z], hb, (u32)(i * 16));
        gld64(in.hm[z], hb, (u32)(i > 0 ? i * 16 - 8 : 0));
    }
}
DI void conv_build_h(LAS char* sm0, CvIn& in, int w, int lane) {
    LAS char* sm = sm0 + CV_BASE + w * CV_WAVE;
#pragma unroll
    for (int z = 0; z < 4; z++) { PIN(in.hd[z]); PIN(in.hm[z]); }
    if (lane < 32) *(LAS u32x4*)(sm + CV_U + lane * 16) = (u32x4){0, 0, 0, 0};
    if (lane < 8) *(LAS u32x4*)(sm + (lane >> 1) * CV_PITCH + (lane & 1) * 16) = (u32x4){0, 0, 0, 0};
#pragma unroll
    for (int z = 0; z < 4; z++) {
        const int i = lane + 64 * z;
        u32 m2 = in.hm[z].x, m1 = in.hm[z].y;
        if (i == 0) { m2 = 0; m1 = 0; }
        const u32 d0 = in.hd[z].x, d1 = in.hd[z].y, d2 = in.hd[z].z, d3 = in.hd[z].w;
        const u32 om2 = __builtin_amdgcn_alignbit(m1, m2, 16), om1 = __builtin_amdgcn_alignbit(d0, m1, 16), o0 = __builtin_amdgcn_alignbit(d1, d0, 16),
                  o1 = __builtin_amdgcn_alignbit(d2, d1, 16), o2 = __builtin_amdgcn_alignbit(d3, d2, 16);
        LAS char* p = sm + 32 + i * 16;
        *(LAS u32x4*)(p) = (u32x4){d0, d1, d2, d3};
        *(LAS u32x4*)(p + CV_PITCH) = (u32x4){om1, o0, o1, o2};
        *(LAS u32x4*)(p + 2 * CV_PITCH) = (u32x4){m1, d0, d1, d2};
        *(LAS u32x4*)(p + 3 * CV_PITCH) = (u32x4){om2, om1, o0, o1};
    }
}
DI void conv_run(LAS char* sm0, float Dc, int w, int lane, int c, rsrc_t rw, char* ws) {
    LAS char* sm = sm0;
    LGKM0();
    TS(4, 9);
    const int fr = lane & 15, g = lane >> 4, q = fr >> 2, p = lane & 3;
    sm = sm0;
    const u32 fa = CV_BASE + w * CV_WAVE + q * CV_PITCH + 2 * (32 - 8 * g + 4 * p) - 8;
    const u32 ga = CV_BASE + w * CV_WAVE + CV_U + 480 + 32 * fr + 16 * g;
    f32x4 Y[8];
#pragma unroll
    for (int i = 0; i < 8; i++) Y[i] = (f32x4){0, 0, 0, 0};
    CvSet A, B;
    cv_load(sm, A, fa, ga);
    SB();
#pragma unroll 1
    for (int k0 = 0; k0 < 8; k0 += 2) {
        cv_phase(sm, Y, A, B, fa + 64 * (k0 + 1), ga - 64 * (k0 + 1));
        cv_phase(sm, Y, B, A, fa + 64 * (k0 + 2), ga - 64 * (k0 + 2));
    }
    TS(4, 10);
    sm = sm0 + CV_BASE + w * CV_WAVE;
    {
        u32x2 u4[8];
#pragma unroll
        for (int ib = 0; ib < 8; ib++) u4[ib] = lds64(sm, CV_U + 512 + 512 * ib + 32 * fr + 8 * g);
#pragma unroll
        for (int ib = 0; ib < 8; ib++) {
            const u32x2 r = {pk2(fmaf(blo(u4[ib].x), Dc, Y[ib][0]), fmaf(bhi(u4[ib].x), Dc, Y[ib][1])), pk2(fmaf(blo(u4[ib].y), Dc, Y[ib][2]), fmaf(bhi(u4[ib].y), Dc, Y[ib][3]))};
            *(LAS u32x2*)(sm + 512 * ib + 32 * fr + 8 * g) = r;
        }
    }
    LGKM0();
    u32x4 y8[4];
#pragma unroll
    for (int z = 0; z < 4; z++) y8[z] = lds128(sm, z * 1024 + lane * 16);
#pragma unroll
    for (int z = 0; z < 4; z++) st128c(rw, WS_YT + c * 4096 + z * 1024 + lane * 16, y8[z]);
}

DI void kvo_block(LAS char* sm, int wave, int lane, int h, int half, rsrc_t rw, u32 tag, char* ws) {
    const int fr = lane & 15, g = lane >> 4;
    const int j0 = (20 * h) >> 7, j1 = (20 * h + 19) >> 7;
    wait_flags(rw, lane < 8 ? (int)F_G1 + 16 * (((lane & 2) ? 10 : 5) + ((lane & 1) ? j1 : j0)) + 2 * wave + (lane >> 2)
                            : lane < 10 ? (int)F_G1 + 16 * ((lane & 1) ? j1 : j0) + 8 * half + wave : -1, -1, tag);
    TS(0, 8);
    const u32 pk0 = WS_PK0 + (h * 64 + 8 * wave) * 1024 + lane * 16, v0 = pk0 + (WS_V0 - WS_PK0);
    const u32 xx = WS_X + (h * 32 + 4 * wave) * 1024 + lane * 16;
    u32x4 A[8], B[8], X[4], Q[8];
#pragma unroll
    for (int i = 0; i < 8; i++) { A[i] = ld128c(rw, pk0 + i * 1024); B[i] = ld128c(rw, v0 + i * 1024); if ((i & 1) == 0) X[i >> 1] = ld128c(rw, xx + (i >> 1) * 1024); }
    const int odd = h & 1, pr = h >> 1;
    const int u5 = (g < 3 ? g : 2) + 2 * odd;
    const int sb0 = half * 64 + wave * 8;
    const u32 pq = WS_P2 + (((pr * 128 + sb0) * 5 + u5) * 128 + fr * 8) * 2;
#pragma unroll
    for (int i = 0; i < 8; i++) Q[i] = ld128c(rw, pq + i * 1280);
    MEMF(); SB();
    f32x4 a00 = {0, 0, 0, 0}, a0x = a00, ax0 = a00, axx = a00;
    const u32 cst = fr == 8 ? 0x3F803F80u : 0u;
#pragma unroll
    for (int i = 0; i < 8; i++) {
        u32x4 xv = X[i >> 1];
        if (i & 1) {
#pragma unroll
            for (int e = 0; e < 4; e++) xv[e] = (u32)__builtin_amdgcn_update_dpp(0, (int)xv[e], 0x108, 0xf, 0xf, false);
        }
        if (fr >= 8) xv = (u32x4){cst, cst, cst, cst};
        a00 = mfma16(A[i], B[i], a00); a0x = mfma16(A[i], xv, a0x); ax0 = mfma16(xv, B[i], ax0); axx = mfma16(xv, xv, axx);
    }
    TS(0, 9);
    *(LAS f32x4*)(sm + KO_RED + ((wave * 4 + 0) * 64 + lane) * 16) = a00;
    *(LAS f32x4*)(sm + KO_RED + ((wave * 4 + 1) * 64 + lane) * 16) = a0x;
    *(LAS f32x4*)(sm + KO_RED + ((wave * 4 + 2) * 64 + lane) * 16) = ax0;
    *(LAS f32x4*)(sm + KO_RED + ((wave * 4 + 3) * 64 + lane) * 16) = axx;
    if (wave >= 4 && (wave - 4) * 64 + lane < 189) *(LAS u32x2*)(sm + KO_KV + ((wave - 4) * 64 + lane) * 8) = (u32x2){0, 0};
    LGKM0(); BAR();
    if (wave < 4) {
        const int tile = wave;
        f32x4 s = *(LAS f32x4*)(sm + KO_RED + (tile * 64 + lane) * 16);
#pragma unroll
        for (int w2 = 1; w2 < 8; w2++) s += *(LAS f32x4*)(sm + KO_RED + ((w2 * 4 + tile) * 64 + lane) * 16);
        const int e = (tile & 1) ? 12 + fr : fr;
        const bool colok = (tile & 1) ? (fr >= 4 && fr <= 8) : true;
        const bool rowok = (tile & 2) ? (g == 0) : true;
        const int d0 = (tile & 2) ? 16 : 4 * g;
        if (colok && rowok) *(LAS u32x2*)(sm + KO_KV + e * 72 + (d0 + 4) * 2) = (u32x2){pk2(s[0], s[1]), pk2(s[2], s[3])};
    }
    LGKM0(); BAR();
    u32x4 A0, A1[4], A2;
    {
        const u32 dofs = KO_KV + (8 * g - 4 * odd + 4) * 2;
        const u32 r0 = (4 * odd + fr) * 72, r1 = ((odd ? 0 : 16) + (fr & 3)) * 72;
        const u32x2 p0 = lds64(sm, dofs + r0), p1 = lds64(sm, dofs + r0 + 8);
        const u32x2 q0 = lds64(sm, dofs + r1), q1 = lds64(sm, dofs + r1 + 8);
        const u32x2 z0 = lds64(sm, dofs + 20 * 72), z1 = lds64(sm, dofs + 20 * 72 + 8);
        A0 = (u32x4){p0.x, p0.y, p1.x, p1.y}; A2 = (u32x4){z0.x, z0.y, z1.x, z1.y};
#pragma unroll
        for (int z = 0; z < 4; z++) { const bool m = (fr >> 2) == z; A1[z] = (u32x4){m ? q0.x : 0u, m ? q0.y : 0u, m ? q1.x : 0u, m ? q1.y : 0u}; }
    }
    TS(0, 10);
    const int U0 = (5 * h + odd) >> 1;
    const int Uw = U0 + (g >> 1);
    const u32 offA = WS_O + (((Uw >> 2) * 4 + (Uw & 3)) * 128 + fr * 8) * 2 + (g & 1) * 20480;
    const int cgx = 5 * h + (odd ? 0 : 4), Ux = cgx >> 1;
    const u32 offX = WS_O + (((Ux >> 2) * 4 + (Ux & 3)) * 128 + fr * 8 + (cgx & 1) * 4) * 2;
    const f32x4 zero = {0, 0, 0, 0};
#pragma unroll
    for (int i4 = 0; i4 < 2; i4++) {
        f32x4 n1 = zero; float rden[4]; u32x2 ov[4];
#pragma unroll
        for (int z = 0; z < 4; z++) {
            const int i = 4 * i4 + z;
            const f32x4 n0 = mfma16(A0, Q[i], zero);
            const f32x4 dn = mfma16(A2, Q[i], zero);
            n1 = mfma16(A1[z], Q[i], n1);
            const float rd = __builtin_amdgcn_rcpf(dn[0] + 1e-6f);
            rden[z] = rd;
            ov[z] = (u32x2){pk2(n0[0] * rd, n0[1] * rd), pk2(n0[2] * rd, n0[3] * rd)};
        }
#pragma unroll
        for (int z = 0; z < 4; z += 2) {
            auto rx = __builtin_amdgcn_permlane16_swap(ov[z].x, ov[z + 1].x, false, false);
            auto ry = __builtin_amdgcn_permlane16_swap(ov[z].y, ov[z + 1].y, false, false);
            st128c(rw, offA + (sb0 + 4 * i4 + z) * 20480, (u32x4){rx[0], ry[0], rx[1], ry[1]});
        }
        const float rd = g == 0 ? rden[0] : g == 1 ? rden[1] : g == 2 ? rden[2] : rden[3];
        st64c(rw, offX + (sb0 + 4 * i4 + g) * 20480, (u32x2){pk2(n1[0] * rd, n1[1] * rd), pk2(n1[2] * rd, n1[3] * rd)});
    }
}

DI void gate_block(int wave, int lane, int m, const u16* x, const u16* gw, rsrc_t rw) {
    const int sub = lane & 15, r4 = lane >> 4;
    u32x4 gq[5], xr[4][5];
#pragma unroll
    for (int jj = 0; jj < 5; jj++) gq[jj] = *(const GAS u32x4*)((const GAS char*)gw + (sub + 16 * jj) * 16);
#pragma unroll
    for (int it = 0; it < 4; it++)
#pragma unroll
        for (int jj = 0; jj < 5; jj++) xr[it][jj] = *(const GAS u32x4*)((const GAS char*)x + (size_t)(128 * m + 16 * wave + 4 * it + r4) * 1280 + (sub + 16 * jj) * 16);
    MEMF(); SB();
#pragma unroll
    for (int it = 0; it < 4; it++) {
        float a = 0.f;
#pragma unroll
        for (int jj = 0; jj < 5; jj++)
#pragma unroll
            for (int e = 0; e < 4; e++) { a = fmaf(blo(xr[it][jj][e]), blo(gq[jj][e]), a); a = fmaf(bhi(xr[it][jj][e]), bhi(gq[jj][e]), a); }
        a += __shfl_xor(a, 1); a += __shfl_xor(a, 2); a += __shfl_xor(a, 4); a += __shfl_xor(a, 8);
        a = rbf(a);
        const float g = rbf(__builtin_amdgcn_rcpf(1.f + __builtin_amdgcn_exp2f(-1.4426950408889634f * a)));
        if (sub == 0) st32c(rw, WS_G + (128 * m + 16 * wave + 4 * it + r4) * 4, __float_as_uint(g));
    }
}

DI void final_block(LAS char* sm, int wave, int lane, int tid, int st, int ct, const u16* x, u16* out, rsrc_t rw, u32 tag, char* ws) {
    const int fr = lane & 15, g = lane >> 4;
    u32x4 xv[2];
#pragma unroll
    for (int r = 0; r < 2; r++) {
        int it = tid + 512 * r; it = it < 640 ? it : 639;
        const int s = it / 10, u = it - 10 * s;
        xv[r] = *(const GAS u32x4*)((const GAS char*)x + (size_t)(st * 64 + s) * 1280 + ct * 160 + u * 16);
    }
    VMCNT(0); BAR();
    if (wave < 4) {
        const int kh = wave & 1, sh = wave >> 1;
        wait_flags(rw, lane < 16 ? (int)F_O + (st >> 4) * 32 + 16 * kh + lane : -1, -1, tag);
        TS(0, 17);
        const u32 ob = WS_O + ((st * 4 + 2 * sh) * 20 + 10 * kh) * 1024 + lane * 16;
        u32x4 B[2][10];
#pragma unroll
        for (int kl = 0; kl < 10; kl++) { B[0][kl] = ld128c(rw, ob + kl * 1024); B[1][kl] = ld128c(rw, ob + 20480 + kl * 1024); }
        MEMF(); SB();
        f32x4 acc[5][2];
#pragma unroll
        for (int cb = 0; cb < 5; cb++) { acc[cb][0] = (f32x4){0, 0, 0, 0}; acc[cb][1] = (f32x4){0, 0, 0, 0}; }
        const u32 aoff = (16 * (g >> 1) + 4 * (g & 1) + (fr >> 2)) * 160 + (lane & 3) * 8;
        u32x2 al[2][5], ah[2][5];
#define F_READ(buf, kl) do { const u32 b0_ = kh ? wo_off(10 + (kl)) : wo_off(kl); \
            _Pragma("unroll") for (int cb = 0; cb < 5; cb++) { al[buf][cb] = ldstr(sm, b0_ + aoff + cb * 32); ah[buf][cb] = ldstr(sm, b0_ + aoff + cb * 32 + 1280); } } while (0)
        F_READ(0, 0);
#pragma unroll
        for (int kl = 0; kl < 10; kl++) {
            if (kl < 9) F_READ((kl + 1) & 1, kl + 1);
            SB();
#pragma unroll
            for (int cb = 0; cb < 5; cb++) {
                const u32x4 a = {al[kl & 1][cb].x, al[kl & 1][cb].y, ah[kl & 1][cb].x, ah[kl & 1][cb].y};
                acc[cb][0] = mfma16(a, B[0][kl], acc[cb][0]); acc[cb][1] = mfma16(a, B[1][kl], acc[cb][1]);
            }
            SB();
        }
        TS(0, 18);
        lds_arrive(sm, C_FR, lane); lds_wait(sm, C_FR, 4);
#pragma unroll
        for (int cb = 0; cb < 5; cb++)
#pragma unroll
            for (int z = 0; z < 2; z++)
                *(LAS f32x4*)(sm + kh * (64 * RED_PITCH) + (16 * (2 * sh + z) + fr) * RED_PITCH + (16 * cb + 4 * g) * 4) = acc[cb][z];
    } else {
        if (wave == 4) { wait_flags(rw, lane < 20 ? (int)F_Y + ct * 32 + lane : lane == 32 ? (int)F_GATE + (st >> 1) : -1, -1, tag); lds_arrive(sm, C_FY, lane); }
        else lds_wait(sm, C_FY, 1);
        const int t = tid - 256;
        u32x4 yv[3];
#pragma unroll
        for (int r = 0; r < 3; r++) { int id = t + 256 * r; id = id < 640 ? id : 639; yv[r] = ld128c(rw, WS_YT + (80 * ct + (id >> 3)) * 4096 + st * 128 + (id & 7) * 16); }
#pragma unroll
        for (int r = 0; r < 3; r++) { const int id = t + 256 * r; if (id < 640) *(LAS u32x4*)(sm + F_YY + id * 16) = yv[r]; }
        if (wave == 4) *(LAS u32*)(sm + F_GG + lane * 4) = ld32c(rw, WS_G + (st * 64 + lane) * 4);
        lds_arrive(sm, C_FY2, lane); lds_wait(sm, C_FY2, 4);
        const int sb = wave - 4, q = fr >> 2, p = lane & 3;
        u32x2 tt[5];
#pragma unroll
        for (int cb = 0; cb < 5; cb++) tt[cb] = ldstr(sm, F_YY + (16 * cb + 4 * g + q) * 128 + (16 * sb + 4 * p) * 2);
#pragma unroll
        for (int cb = 0; cb < 5; cb++) *(LAS u32x2*)(sm + F_Y2 + (16 * sb + fr) * 176 + (16 * cb + 4 * g) * 2) = tt[cb];
    }
    LGKM0(); BAR();
#pragma unroll
    for (int r = 0; r < 2; r++) {
        const int it = tid + 512 * r;
        if (it < 640) {
            const int s = it / 10, u = it - 10 * s;
            const u32 ra = s * RED_PITCH + u * 32;
            const f32x4 p0 = *(LAS f32x4*)(sm + ra), p1 = *(LAS f32x4*)(sm + ra + 16), q0 = *(LAS f32x4*)(sm + 64 * RED_PITCH + ra), q1 = *(LAS f32x4*)(sm + 64 * RED_PITCH + ra + 16);
            const u32x4 y8 = lds128(sm, F_Y2 + s * 176 + u * 16);
            const float gg = *(LAS float*)(sm + F_GG + s * 4), omg = 1.f - gg;
            float o[8];
#pragma unroll
            for (int e = 0; e < 4; e++) {
                const float a0 = (e < 2 ? p0[2 * e] + q0[2 * e] : p1[2 * e - 4] + q1[2 * e - 4]), a1 = (e < 2 ? p0[2 * e + 1] + q0[2 * e + 1] : p1[2 * e - 3] + q1[2 * e - 3]);
                const float b0 = omg * blo(xv[r][e]), b1 = omg * bhi(xv[r][e]);
                o[2 * e] = fmaf(b0, blo(y8[e]), gg * a0); o[2 * e + 1] = fmaf(b1, bhi(y8[e]), gg * a1);
            }
            *(GAS u32x4*)((GAS char*)out + (size_t)(st * 64 + s) * 1280 + ct * 160 + u * 16) = (u32x4){pk2(o[0], o[1]), pk2(o[2], o[3]), pk2(o[4], o[5]), pk2(o[6], o[7])};
        }
    }
}

__global__ __launch_bounds__(512, 2) void fused_kernel(const u16* __restrict__ x, const u16* __restrict__ w, const u16* __restrict__ wo, const u16* __restrict__ kin,
                                                        const u16* __restrict__ vin, const u16* __restrict__ shk, const u16* __restrict__ hk, const u16* __restrict__ sD,
                                                        const u16* __restrict__ gw, u16* __restrict__ out, char* ws) {
    extern __shared__ __attribute__((aligned(16))) char smem[];
    LAS char* sm = (LAS char*)smem;
    const int tid = threadIdx.x, lane = tid & 63, wave = __builtin_amdgcn_readfirstlane(tid >> 6);
    const int b = blockIdx.x, xcd = b & 7, j = b >> 3;
    const rsrc_t rw = __builtin_amdgcn_make_buffer_rsrc(ws, 0, WS_BYTES, 0x00020000);
    u32 tag;
    asm volatile("s_load_dword %0, %1, 0x0\n\ts_waitcnt lgkmcnt(0)" : "=s"(tag) : "s"(ws + WS_FLAG + F_EPOCH * FS) : "memory");
    tag += 1u;
    if (tid < 16) *(LAS u32*)(sm + L_CNT + tid * 4) = 0u;
    TS(0, 0);
    if (tid == 0) st64c(rw, WS_STAMP + blockIdx.x * 64, __builtin_bit_cast(u32x2, (unsigned long long)__builtin_amdgcn_s_memrealtime()));
    const int st = 4 * xcd + (j >> 3), ct = j & 7;
    int ko = -1;
    bool is_conv = false;

    if (j >= 30) {
        const int m = xcd * 2 + (j - 30);
        ko = -100;
        {
            const int grp = wave >> 2, w4i = wave & 3;
            u32x2 w4[3];
#pragma unroll
            for (int t = 0; t < 3; t++) { const int tt = 2 * t + grp < 5 ? 2 * t + grp : 4; p0_weights(w4[t], shk, 240 + 5 * m + tt, w4i, lane); }
#pragma unroll
            for (int t = 0; t < 3; t++) if (2 * t + grp < 5) p0_issue(kin, vin, 240 + 5 * m + 2 * t + grp, w4i, lane, (2 * t + grp) * 17408);
            VMCNT(0);
#pragma unroll
            for (int t = 0; t < 3; t++) PIN(w4[t]);
            BAR();
#pragma unroll
            for (int t = 0; t < 3; t++)
                if (2 * t + grp < 5) p0_compute(sm, w4[t], 240 + 5 * m + 2 * t + grp, w4i, lane, (2 * t + grp) * 17408, 90112 + (2 * t + grp) * 8192);
            LGKM0(); BAR();
            {
                u32x4 t4[3][2];
#pragma unroll
                for (int t = 0; t < 3; t++)
#pragma unroll
                    for (int z = 0; z < 2; z++) { const int ch2 = ((tid & 255) >> 3) + 32 * z, o2 = tid & 7, tt = 2 * t + grp < 5 ? 2 * t + grp : 4; t4[t][z] = lds128(sm, 90112 + tt * 8192 + ch2 * 128 + ((o2 ^ (ch2 & 7)) * 16)); }
#pragma unroll
                for (int t = 0; t < 3; t++)
                    if (2 * t + grp < 5) {
                        const int tile = 240 + 5 * m + 2 * t + grp, cg = tile % 10, s0 = (tile / 10) * 64;
#pragma unroll
                        for (int z = 0; z < 2; z++) { const int ch2 = ((tid & 255) >> 3) + 32 * z, o2 = tid & 7; st128c(rw, WS_UT + (cg * 64 + ch2) * 4096 + (s0 + 8 * o2) * 2, t4[t][z]); }
                    }
            }
        }
        VMCNT(0); BAR();
        if (tid < 5) set_flag(rw, p0_flag(240 + 5 * m + tid), tag);
        TS(0, 1);
        gate_block(wave, lane, m, x, gw, rw);
        VMCNT(0); LGKM0(); BAR();
        if (tid == 0) set_flag(rw, F_GATE + m, tag);
        TS(0, 3);
    } else {
        const int rt = 2 * xcd + j / 15, cbk = j % 15, tile = xcd * 30 + j;
        is_conv = cbk < 10;
        if (!is_conv) { const int vi = rt * 5 + (cbk - 10); ko = vi < 64 ? vi : -2 - (vi - 64); }
        if (wave >= 4) {
            const int lw = wave - 4;
            p0_issue(kin, vin, tile, lw, lane, P0_K);
            u32 vx[4], vw[4];
#pragma unroll
            for (int n2 = 0; n2 < 4; n2++) {
                const int n = 4 * lw + n2;
                const int row = 8 * n + (lane >> 3), ch = (lane & 7) ^ ((row >> 1) & 7);
                vx[n2] = (u32)((rt * 128 + row) * 1280 + ch * 16);
                const int kr = 4 * n + (lane >> 4), f = ((kr & 3) << 2) | ((kr >> 2) & 3), cw = (lane & 15) ^ f;
                vw[n2] = (u32)(kr * 3840 + cbk * 256 + cw * 16);
            }
            const int rot = (rt + cbk) % 10;
#define K1_ISSUE(c) do { const u32 st_ = ((c) & 3) * K1_STAGE + 4 * lw * 1024; int kc_ = (c) + ROT_ON * rot; kc_ = kc_ >= 10 ? kc_ - 10 : kc_; \
            const char* xb_ = (const char*)x + kc_ * 128; const char* wp_ = (const char*)w + (size_t)kc_ * 64 * 3840; \
            _Pragma("unroll") for (int n2 = 0; n2 < 4; n2++) { if (NT_X) glds16nt(xb_, vx[n2], st_ + 16384 + n2 * 1024); else glds16(xb_, vx[n2], st_ + 16384 + n2 * 1024); glds16(wp_, vw[n2], st_ + n2 * 1024); } } while (0)
            K1_ISSUE(0);
            VMCNT(8); BAR();
            K1_ISSUE(1); K1_ISSUE(2);
            VMCNT(16); BAR();
            K1_ISSUE(3);
#pragma unroll 1
            for (int c = 0; c < 6; c++) {
                VMCNT(16); BAR();
                K1_ISSUE(c + 4);
            }
            const int cv = rt * 10 + cbk, c = 4 * cv + lw;
            if (is_conv) {
                CvIn in; u32 fl; u32x4 u[4];
                const int fidx = (int)F_P0 + (cv >> 4) * 32 + (lane & 31);
                conv_fetch_h(in, hk, c, lane);
                VMCNT(24); BAR();
                VMCNT(16); BAR();
                gld32c(fl, ws, WS_FLAG + fidx * FS);
                VMCNT(9); BAR();
                TS(4, 4);
                VMCNT(0); PIN(fl);
                const bool ready = __all(fl == tag);
                if (ready) {
#pragma unroll
                    for (int z = 0; z < 4; z++) gld128c(u[z], ws, WS_UT + c * 4096 + z * 1024 + lane * 16);
                }
                TS(4, 5);
                conv_build_h(sm, in, lw, lane);
                TS(4, 6);
                if (!ready) {
                    wait_flags(rw, fidx, -1, tag);
#pragma unroll
                    for (int z = 0; z < 4; z++) gld128c(u[z], ws, WS_UT + c * 4096 + z * 1024 + lane * 16);
                }
                TS(4, 7);
                VMCNT(0);
#pragma unroll
                for (int z = 0; z < 4; z++) { PIN(u[z]); *(LAS u32x4*)(sm + CV_BASE + lw * CV_WAVE + CV_U + 512 + z * 1024 + lane * 16) = u[z]; }
                const float Dc = blo((u32)sD[c]);
                TS(4, 8);
                conv_run(sm, Dc, lw, lane, c, rw, ws);
                publish_last(rw, sm, C_Y, 4, F_Y + (cv / 20) * 32 + cv % 20, tag, lane);
                TS(4, 11);
            } else {
                VMCNT(16); BAR();
                VMCNT(8); BAR();
                VMCNT(0); BAR();
            }
        } else {
            u32x2 w4; p0_weights(w4, shk, tile, wave, lane);
            if (PREF1 && wave < 3) {
                const int n = wave * 64 + lane;
                if (wave == 0) touch(x, (u32)((xcd * 2560 + j * 80 + lane) * 128));
                else if (wave == 1) { if (lane < 16) touch(x, (u32)((xcd * 2560 + j * 80 + 64 + lane) * 128)); else touch(w, (u32)(((xcd * 30 + j) * 80 + lane - 16) * 128)); }
                else if (lane < 32) touch(w, (u32)(((xcd * 30 + j) * 80 + 48 + lane) * 128));
                (void)n;
            }
            const int wm = wave >> 1, wn = wave & 1, r32 = lane & 31, hh = lane >> 5;
            K1Addr a0;
#pragma unroll
            for (int ks = 0; ks < 2; ks++) {
                a0.x[0][ks] = (64 * wm + r32) * 128 + (((2 * ks + hh) ^ ((r32 >> 1) & 7)) * 16);
                a0.x[1][ks] = a0.x[0][ks] ^ 64;
            }
#pragma unroll
            for (int jj = 0; jj < 2; jj++)
#pragma unroll
                for (int t = 0; t < 2; t++) {
                    const int blk = (lane >> 4) & 1, q = (lane & 15) >> 2, p = lane & 3, row = 8 * hh + 4 * t + q, ch = 8 * wn + 4 * jj + 2 * blk + (p >> 1);
                    const int f = ((row & 3) << 2) | ((row >> 2) & 3);
                    a0.w[jj][t] = row * 256 + ((ch ^ f) * 16) + 8 * (p & 1);
                }
            f32x16 acc[2][2];
#pragma unroll
            for (int i = 0; i < 2; i++)
#pragma unroll
                for (int jj = 0; jj < 2; jj++)
#pragma unroll
                    for (int e = 0; e < 16; e++) acc[i][jj][e] = 0.f;
            TS(0, 20);
            if (PREF1 && wave == 1) VMCNT(2); else if (PREF1 && wave < 3) VMCNT(1); else VMCNT(0);
            PIN(w4); TS(0, 21); BAR();
            TS(0, 22);
            p0_compute(sm, w4, tile, wave, lane, P0_K, P0_OUT, ws);
            TS(0, 28);
            LGKM0(); TS(0, 23); BAR();
            TS(0, 24);
            p0_store(sm, rw, tile, tid, P0_OUT);
            TS(0, 1);
            if (cbk < 5) {
                k1_loop<true>(sm, a0, acc, rw, p0_flag(tile), tag, wave, lane, hk, wo, tile);
                __builtin_amdgcn_s_setprio(2);
                TS(0, 2);
                u32x4 qv[2][2][2];
#pragma unroll
                for (int jj = 0; jj < 2; jj++)
#pragma unroll
                    for (int ap = 0; ap < 2; ap++)
#pragma unroll
                        for (int i = 0; i < 2; i++) {
                            const f32x16& a = acc[i][jj];
                            const u32 x0 = phi2(a[8 * ap], a[8 * ap + 1]), y0 = phi2(a[8 * ap + 2], a[8 * ap + 3]);
                            const u32 x1 = phi2(a[8 * ap + 4], a[8 * ap + 5]), y1 = phi2(a[8 * ap + 6], a[8 * ap + 7]);
                            auto rx = __builtin_amdgcn_permlane32_swap(x0, x1, false, false);
                            auto ry = __builtin_amdgcn_permlane32_swap(y0, y1, false, false);
                            qv[jj][ap][i] = (u32x4){rx[0], ry[0], rx[1], ry[1]};
                        }
                if (QDELAY) { __builtin_amdgcn_s_setprio(0); for (int z = 0; z < QDELAY; z++) __builtin_amdgcn_s_sleep(8); }
#pragma unroll
                for (int jj = 0; jj < 2; jj++)
#pragma unroll
                    for (int ap = 0; ap < 2; ap++) {
                        const int U = 16 * cbk + 8 * wn + 4 * jj + 2 * ap + hh, pr = U / 5, u5 = U - 5 * pr;
                        const u32 dst = WS_P2 + (((pr * 128 + rt * 8 + 4 * wm + (r32 >> 4)) * 5 + u5) * 128 + (r32 & 15) * 8) * 2;
#pragma unroll
                        for (int i = 0; i < 2; i++) st128c(rw, dst + i * 2560, qv[jj][ap][i]);
                    }
            } else {
                k1_loop<false>(sm, a0, acc, rw, p0_flag(tile), tag, wave, lane, hk, wo, tile);
                __builtin_amdgcn_s_setprio(2);
                TS(0, 2);
                const bool isk = cbk < 10;
                const int j5 = isk ? cbk - 5 : cbk - 10;
                u32 dst[2], istep[2];
#pragma unroll
                for (int jj = 0; jj < 2; jj++) {
                    const int cc = 128 * j5 + 64 * wn + 32 * jj + r32, h = cc / 20, d = cc - 20 * h;
                    const int sb32 = rt * 4 + 2 * wm;
                    const bool lo = d < 16;
                    dst[jj] = lo ? (isk ? WS_PK0 : WS_V0) + (((h * 64 + sb32) * 4 + hh) * 128 + d * 8) * 2
                                 : WS_X + (((h * 32 + (sb32 >> 1)) * 4 + hh) * 128 + (d - 16 + (isk ? 0 : 4)) * 8) * 2;
                    istep[jj] = lo ? 1024 : 128;
                }
                if (isk) {
#pragma unroll
                    for (int jj = 0; jj < 2; jj++)
#pragma unroll
                        for (int i = 0; i < 2; i++)
#pragma unroll
                            for (int z = 0; z < 2; z++) {
                                const f32x16& a = acc[i][jj];
                                st128c(rw, dst[jj] + i * istep[jj] + z * 512,
                                       (u32x4){phi2(a[8 * z], a[8 * z + 1]), phi2(a[8 * z + 2], a[8 * z + 3]), phi2(a[8 * z + 4], a[8 * z + 5]), phi2(a[8 * z + 6], a[8 * z + 7])});
                            }
                } else {
#pragma unroll
                    for (int jj = 0; jj < 2; jj++)
#pragma unroll
                        for (int i = 0; i < 2; i++)
#pragma unroll
                            for (int z = 0; z < 2; z++) {
                                const f32x16& a = acc[i][jj];
                                st128c(rw, dst[jj] + i * istep[jj] + z * 512,
                                       (u32x4){pk2(a[8 * z], a[8 * z + 1]), pk2(a[8 * z + 2], a[8 * z + 3]), pk2(a[8 * z + 4], a[8 * z + 5]), pk2(a[8 * z + 6], a[8 * z + 7])});
                            }
                }
            }
            LGKM0();
            TS(0, 25);
            publish_last(rw, sm, C_G1, 4, F_G1 + cbk * 16 + rt, tag, lane);
            if (lane == 0) st64c(rw, WS_STAMP + blockIdx.x * 64 + 8 + wave * 8, __builtin_bit_cast(u32x2, (unsigned long long)__builtin_amdgcn_s_memrealtime()));
            __builtin_amdgcn_s_setprio(0);
            TS(0, 3);
            if (is_conv) {
                lds_wait(sm, C_G1, 4);
                wo_issue(wo, ct, 0, 12, wave, 4, lane);
            }
        }
    }
    if (!is_conv) {
        LGKM0(); BAR();
        if (ko >= 0) {
            const int h = ko >> 1, half = ko & 1;
            wo_issue(wo, ct, 0, 12, wave, 8, lane);
            kvo_block(sm, wave, lane, h, half, rw, tag, ws);
            TS(0, 26);
            VMCNT(0); BAR();
            if (tid == 0) set_flag(rw, F_O + half * 32 + h, tag);
            TS(0, 11);
            wo_issue(wo, ct, 12, 20, wave, 8, lane);
        } else {
            wo_issue(wo, ct, 0, 20, wave, 8, lane);
            if (ko == -2 && wave == 0) {
                wait_flags(rw, (int)F_G1 + lane, (int)F_G1 + 64 + lane, tag);
                wait_flags(rw, (int)F_G1 + 128 + lane, (int)F_G1 + 192 + lane, tag);
                if (lane == 0) set_flag(rw, F_EPOCH, tag);
            }
        }
    } else {
        VMCNT(0); BAR();
        if (wave >= 4) wo_issue(wo, ct, 12, 20, wave - 4, 4, lane);
    }
    TS(0, 16);
    final_block(sm, wave, lane, tid, st, ct, x, out, rw, tag, ws);
    TS(0, 19);
    BAR();
    if (blockIdx.x == PROBE_WG && wave == 0) {
        unsigned long long mnE = ~0ull, mxP = 0, mxK = 0, mxQ = 0;
        for (int q = 0; q < 4; q++) { const int bb = q * 64 + lane; const int jj = bb >> 3;
            const unsigned long long e_ = __builtin_bit_cast(unsigned long long, __builtin_amdgcn_raw_buffer_load_b64(rw, WS_STAMP + bb * 64, 0, 16));
            mnE = e_ < mnE ? e_ : mnE;
            if (jj < 30) for (int w2 = 0; w2 < 4; w2++) { const unsigned long long p_ = __builtin_bit_cast(unsigned long long, __builtin_amdgcn_raw_buffer_load_b64(rw, WS_STAMP + bb * 64 + 8 + w2 * 8, 0, 16));
                mxP = p_ > mxP ? p_ : mxP; if (jj % 15 >= 5 && jj % 15 < 10) mxK = p_ > mxK ? p_ : mxK; if (jj % 15 < 5) mxQ = p_ > mxQ ? p_ : mxQ; } }
        for (int o = 1; o < 64; o <<= 1) { unsigned long long a_ = __shfl_xor(mnE, o), b2 = __shfl_xor(mxP, o), c2 = __shfl_xor(mxK, o), d2 = __shfl_xor(mxQ, o);
            mnE = a_ < mnE ? a_ : mnE; mxP = b2 > mxP ? b2 : mxP; mxK = c2 > mxK ? c2 : mxK; mxQ = d2 > mxQ ? d2 : mxQ; }
        const unsigned long long t0_ = *(volatile LAS unsigned long long*)(sm + 156672 + 64), t8_ = *(volatile LAS unsigned long long*)(sm + 156672 + 72);
        const long long d_ = (long long)(0);
        const unsigned long long e_ = __builtin_amdgcn_s_memrealtime() + 2 * (d_ > 0 ? d_ : 0);
        while (__builtin_amdgcn_s_memrealtime() < e_) __builtin_amdgcn_s_sleep(1);
    }
}

extern "C" void kernel_launch(void* const* d_in, const int* in_sizes, int n_in, void* d_out, int out_size, void* d_ws, size_t ws_size, hipStream_t stream) {
    const u16* x = (const u16*)d_in[0]; const u16* wqkv = (const u16*)d_in[1]; const u16* wo = (const u16*)d_in[2];
    const u16* kin = (const u16*)d_in[4]; const u16* vin = (const u16*)d_in[5]; const u16* shk = (const u16*)d_in[6];
    const u16* hk = (const u16*)d_in[7]; const u16* sD = (const u16*)d_in[8]; const u16* gw = (const u16*)d_in[9];
    (void)hipFuncSetAttribute((const void*)fused_kernel, hipFuncAttributeMaxDynamicSharedMemorySize, LDS_BYTES);
    fused_kernel<<<256, 512, LDS_BYTES, stream>>>(x, wqkv, wo, kin, vin, shk, hk, sD, gw, (u16*)d_out, (char*)d_ws);
}
```

```cpp
#define POLL_SLEEP 8
#include <hip/hip_runtime.h>
#include <stdint.h>

typedef unsigned short u16;
typedef unsigned int u32;
typedef __bf16 bf16x8 __attribute__((ext_vector_type(8)));
typedef __bf16 bf16x2 __attribute__((ext_vector_type(2)));
typedef float f32x4 __attribute__((ext_vector_type(4)));
typedef float f32x2 __attribute__((ext_vector_type(2)));
typedef float f32x16 __attribute__((ext_vector_type(16)));
typedef u32 u32x4 __attribute__((ext_vector_type(4)));
typedef u32 u32x2 __attribute__((ext_vector_type(2)));
typedef short s16x4 __attribute__((ext_vector_type(4)));
#define LAS __attribute__((address_space(3)))
#define GAS __attribute__((address_space(1)))
#define DI __device__ __forceinline__
typedef __amdgpu_buffer_rsrc_t rsrc_t;

constexpr int S = 2048, D = 640;
#define FS 64
#define ROT_ON 1

constexpr u32 WS_UT = 0;
constexpr u32 WS_YT = WS_UT + 2621440;
constexpr u32 WS_PK0 = WS_YT + 2621440;
constexpr u32 WS_V0 = WS_PK0 + 2097152;
constexpr u32 WS_X = WS_V0 + 2097152;
constexpr u32 WS_P2 = WS_X + 1048576;
constexpr u32 WS_O = WS_P2 + 2621440;
constexpr u32 WS_G = WS_O + 2621440;
constexpr u32 WS_FLAG = WS_G + 8192;
constexpr u32 F_P0 = 0;
constexpr u32 F_G1 = 320;
constexpr u32 F_GATE = 560;
constexpr u32 F_O = 576;
constexpr u32 F_Y = 640;
constexpr u32 F_EPOCH = 896, F_TMO = 928;
constexpr u32 WS_STAMP = WS_FLAG + 1024 * FS;
constexpr u32 WS_BYTES = WS_STAMP + 256 * 32 * 16;

#define TS(w, id) do {} while (0)

DI u32 pk2(float a, float b) { f32x2 t = {a, b}; return __builtin_bit_cast(u32, __builtin_convertvector(t, bf16x2)); }
DI float blo(u32 u) { return __uint_as_float(u << 16); }
DI float bhi(u32 u) { return __uint_as_float(u & 0xffff0000u); }
DI float rbf(float f) { return blo(pk2(f, f)); }
DI bf16x8 asbf(u32x4 v) { return __builtin_bit_cast(bf16x8, v); }
DI f32x4 mfma16(u32x4 a, u32x4 b, f32x4 c) { return __builtin_amdgcn_mfma_f32_16x16x32_bf16(asbf(a), asbf(b), c, 0, 0, 0); }
DI f32x16 mfma32(u32x4 a, u32x4 b, f32x16 c) { return __builtin_amdgcn_mfma_f32_32x32x16_bf16(asbf(a), asbf(b), c, 0, 0, 0); }

DI void glds16(const void* sbase, u32 voff, u32 lds) {
    asm volatile("s_mov_b32 m0, %2\n\ts_nop 0\n\tglobal_load_lds_dwordx4 %0, %1" :: "v"(voff), "s"(sbase), "s"(lds) : "memory");
}
DI void glds16nt(const void* sbase, u32 voff, u32 lds) {
    asm volatile("s_mov_b32 m0, %2\n\ts_nop 0\n\tglobal_load_lds_dwordx4 %0, %1 nt" :: "v"(voff), "s"(sbase), "s"(lds) : "memory");
}
DI void touch(const void* sbase, u32 voff) {
    asm volatile("s_mov_b32 m0, %2\n\ts_nop 0\n\tglobal_load_lds_dword %0, %1" :: "v"(voff), "s"(sbase), "s"(156928u) : "memory");
}
#define NT_KV 0
#define NT_X 0
#define NT_H 0
#define PREF1 0
#define PREF2 0
#define QDELAY 0
DI void gld64(u32x2& d, const void* sbase, u32 voff) { asm volatile("global_load_dwordx2 %0, %1, %2" : "=v"(d) : "v"(voff), "s"(sbase) : "memory"); }
DI void gld128(u32x4& d, const void* sbase, u32 voff) { asm volatile("global_load_dwordx4 %0, %1, %2" : "=v"(d) : "v"(voff), "s"(sbase) : "memory"); }
DI void gld128c(u32x4& d, const void* sbase, u32 voff) { asm volatile("global_load_dwordx4 %0, %1, %2 sc1" : "=v"(d) : "v"(voff), "s"(sbase) : "memory"); }
DI void gld32c(u32& d, const void* sbase, u32 voff) { asm volatile("global_load_dword %0, %1, %2 sc1" : "=v"(d) : "v"(voff), "s"(sbase) : "memory"); }
#define PIN(x) asm volatile("" : "+v"(x))
#define SB() __builtin_amdgcn_sched_barrier(0)
DI u32x4 ld128c(rsrc_t r, u32 off) { return __builtin_amdgcn_raw_buffer_load_b128(r, off, 0, 16); }
DI u32 ld32c(rsrc_t r, u32 off) { return __builtin_amdgcn_raw_buffer_load_b32(r, off, 0, 16); }
DI void st128c(rsrc_t r, u32 off, u32x4 v) { __builtin_amdgcn_raw_buffer_store_b128(v, r, off, 0, 16); }
DI void st64c(rsrc_t r, u32 off, u32x2 v) { __builtin_amdgcn_raw_buffer_store_b64(v, r, off, 0, 16); }
DI void st32c(rsrc_t r, u32 off, u32 v) { __builtin_amdgcn_raw_buffer_store_b32(v, r, off, 0, 16); }
DI u32x4 lds128(LAS char* sm, u32 a) { return *(LAS u32x4*)(sm + a); }
DI u32x2 lds64(LAS char* sm, u32 a) { return *(LAS u32x2*)(sm + a); }
DI u32x2 ldstr(LAS char* sm, u32 a) { return __builtin_bit_cast(u32x2, __builtin_amdgcn_ds_read_tr16_b64_v4i16((LAS s16x4*)(sm + a))); }
#define VMCNT(n) asm volatile("s_waitcnt vmcnt(" #n ")" ::: "memory")
#define LGKM0() asm volatile("s_waitcnt lgkmcnt(0)" ::: "memory")
#define BAR() do { asm volatile("" ::: "memory"); __builtin_amdgcn_s_barrier(); asm volatile("" ::: "memory"); } while (0)
#define MEMF() asm volatile("" ::: "memory")

DI void set_flag(rsrc_t r, u32 idx, u32 tag) { st32c(r, WS_FLAG + idx * FS, tag); }
DI void wait_flags(rsrc_t r, int idx0, int idx1, u32 tag) {
    unsigned long long t0 = 0;
    for (u32 it = 0;; it++) {
        u32 a = tag, b = tag;
        if (idx0 >= 0) a = ld32c(r, WS_FLAG + idx0 * FS);
        if (idx1 >= 0) b = ld32c(r, WS_FLAG + idx1 * FS);
        if (__all(a == tag && b == tag)) break;
        if (POLL_SLEEP) __builtin_amdgcn_s_sleep(POLL_SLEEP);
        if ((it & 63u) == 0u) {
            const unsigned long long t = __builtin_amdgcn_s_memrealtime();
            if (it == 0) t0 = t;
            else if (t - t0 > 2000000ull) { set_flag(r, F_TMO, tag); break; }
        }
    }
    MEMF();
}
DI void lds_arrive(LAS char* sm, u32 cnt, int lane) { LGKM0(); if (lane == 0) __hip_atomic_fetch_add((LAS u32*)(sm + cnt), 1u, __ATOMIC_RELAXED, __HIP_MEMORY_SCOPE_WORKGROUP); MEMF(); }
DI void lds_wait(LAS char* sm, u32 cnt, u32 n) { MEMF(); while (*(volatile LAS u32*)(sm + cnt) < n) __builtin_amdgcn_s_sleep(0); MEMF(); }
DI void publish_last(rsrc_t r, LAS char* sm, u32 cnt, u32 nw, u32 flag, u32 tag, int lane) {
    VMCNT(0);
    if (lane == 0) {
        const u32 old = __hip_atomic_fetch_add((LAS u32*)(sm + cnt), 1u, __ATOMIC_RELAXED, __HIP_MEMORY_SCOPE_WORKGROUP);
        if (old == nw - 1) set_flag(r, flag, tag);
    }
    MEMF();
}

DI u32 phi2(float a, float b) {
    const f32x2 t = {a, b}, s = t * 1.4426950408889634f;
    const float ea = __builtin_amdgcn_fmed3f(__builtin_amdgcn_exp2f(s.x), 0.f, 1.f), eb = __builtin_amdgcn_fmed3f(__builtin_amdgcn_exp2f(s.y), 0.f, 1.f);
    const float ma = __int_as_float(max(__float_as_int(a), 0)), mb = __int_as_float(max(__float_as_int(b), 0));
    const f32x2 r = ((f32x2){ea, eb} + (f32x2){ma, mb}) + 1e-6f;
    return pk2(r.x, r.y);
}

constexpr int K1_STAGE = 32768;
constexpr int P0_K = 131072, P0_V = P0_K + 9216, P0_OUT = P0_V + 8192;
constexpr int L_CNT = 156672;
constexpr int C_G1 = L_CNT, C_Y = L_CNT + 4, C_P0A = L_CNT + 8, C_P0B = L_CNT + 12, C_P0C = L_CNT + 16, C_FY = L_CNT + 20, C_FY2 = L_CNT + 24, C_FO = L_CNT + 28, C_FR = L_CNT + 32;
constexpr int CV_PITCH = 4160;
constexpr int CV_U = 4 * CV_PITCH;
constexpr int CV_WAVE = CV_U + 4608;
constexpr int CV_BASE = 65536;
constexpr int WO_HI = 65536;
constexpr int F_YY = 110592;
constexpr int F_Y2 = F_YY + 10240;
constexpr int F_GG = F_Y2 + 11264;
constexpr int KO_RED = 110592;
constexpr int KO_KV = KO_RED + 32768;
constexpr int RED_PITCH = 336;
constexpr int LDS_BYTES = 160 * 1024;
DI u32 p0_flag(int tile) { return F_P0 + (tile % 10) * 32 + tile / 10; }
DI constexpr u32 wo_off(int kap) { return kap < 12 ? kap * 5120 : WO_HI + (kap - 12) * 5120; }

DI void p0_issue(const u16* kin, const u16* vin, int tile, int lw, int lane, u32 base) {
    const int cg = tile % 10, rg = tile / 10, s0 = rg * 64;
    const u32 col = cg * 128 + (lane & 7) * 16;
#pragma unroll
    for (int z = 0; z < 2; z++) {
        const int n = 2 * lw + z;
        int r = s0 - 4 + n * 8 + (lane >> 3); r = r < 0 ? 0 : r;
        (NT_KV ? glds16nt : glds16)(kin, (u32)(r * 1280) + col, base + n * 1024);
        (NT_KV ? glds16nt : glds16)(vin, (u32)((s0 + n * 8 + (lane >> 3)) * 1280) + col, base + 9216 + n * 1024);
    }
    {
        int r = s0 + 60 + (lane >> 3); r = r > S - 1 ? S - 1 : r;
        (NT_KV ? glds16nt : glds16)(kin, (u32)(r * 1280) + col, base + 8 * 1024);
    }
}
DI float dot2(u32 a, u32 b, float c) { return __builtin_amdgcn_fdot2_f32_bf16(__builtin_bit_cast(bf16x2, a), __builtin_bit_cast(bf16x2, b), c, false); }
DI void p0_compute(LAS char* sm, u32x2 w4, int tile, int wave, int lane, u32 base, u32 outb, char* ws = nullptr) {
    const int s0 = (tile / 10) * 64;
    const int G = wave * 4 + (lane >> 4), cb = G & 3, i = lane & 15, q = i >> 2, p = i & 3;
    const u32 wa = __builtin_amdgcn_alignbit(w4.y, w4.y, 16), wb = __builtin_amdgcn_alignbit(w4.x, w4.x, 16);
    const int ch = 16 * cb + i;
    u32x2 kq[2][3], vq[2][2];
#pragma unroll
    for (int z = 0; z < 2; z++) {
        const int o = (G >> 2) + 4 * z;
        const u32 ka = base + (8 * o + q) * 128 + 32 * cb + 8 * p, va = ka + 9216;
        kq[z][0] = ldstr(sm, ka); kq[z][1] = ldstr(sm, ka + 512); kq[z][2] = ldstr(sm, ka + 1024);
        vq[z][0] = ldstr(sm, va); vq[z][1] = ldstr(sm, va + 512);
    }
#pragma unroll
    for (int z = 0; z < 2; z++) {
        const int o = (G >> 2) + 4 * z;
        u32 K[6] = {kq[z][0].x, kq[z][0].y, kq[z][1].x, kq[z][1].y, kq[z][2].x, kq[z][2].y};
        if (s0 == 0 && o == 0) { K[0] = 0; K[1] = 0; }
        u32 Sh[5];
#pragma unroll
        for (int e = 0; e < 5; e++) Sh[e] = __builtin_amdgcn_alignbit(K[e + 1], K[e], 16);
        const u32 V[4] = {vq[z][0].x, vq[z][0].y, vq[z][1].x, vq[z][1].y};
        u32 r[4];
#pragma unroll
        for (int e = 0; e < 4; e++) {
            const float k0 = dot2(Sh[e], wa, dot2(Sh[e + 1], wb, 0.f)), k1 = dot2(K[e + 1], wa, dot2(K[e + 2], wb, 0.f));
            r[e] = pk2(k0 * blo(V[e]), k1 * bhi(V[e]));
        }
        *(LAS u32x4*)(sm + outb + ch * 128 + ((o ^ (ch & 7)) * 16)) = (u32x4){r[0], r[1], r[2], r[3]};
    }
}
DI void p0_store(LAS char* sm, rsrc_t rw, int tile, int t256, u32 outb) {
    const int cg = tile % 10, s0 = (tile / 10) * 64;
#pragma unroll
    for (int z = 0; z < 2; z++) {
        const int ch2 = (t256 >> 3) + 32 * z, o2 = t256 & 7;
        const u32x4 t4 = lds128(sm, outb + ch2 * 128 + ((o2 ^ (ch2 & 7)) * 16));
        st128c(rw, WS_UT + (cg * 64 + ch2) * 4096 + (s0 + 8 * o2) * 2, t4);
    }
}
DI void p0_weights(u32x2& w4, const u16* shk, int tile, int wave, int lane) {
    const int cg = tile % 10, cb = (wave * 4 + (lane >> 4)) & 3;
    gld64(w4, shk, (u32)((cg * 64 + 16 * cb + (lane & 15)) * 8));
}

struct K1Set { u32x4 xa[2][2]; u32x2 wl[2][2], wh[2][2]; };
struct K1Addr { u32 x[2][2]; u32 w[2][2]; };
template <bool QT, int WO, int XO>
DI void k1_phase(LAS char* sm, f32x16 (&acc)[2][2], const K1Set& cur, K1Set& nxt, const u32 (&xb)[2], const u32 (&wb)[2][2]) {
#pragma unroll
    for (int n = 0; n < 8; n++) {
        const int ks = n >> 2, i = (n >> 1) & 1, jj = n & 1;
        const u32x4 wf = {cur.wl[ks][jj].x, cur.wl[ks][jj].y, cur.wh[ks][jj].x, cur.wh[ks][jj].y};
        acc[i][jj] = QT ? mfma32(wf, cur.xa[ks][i], acc[i][jj]) : mfma32(cur.xa[ks][i], wf, acc[i][jj]);
        if (n == 0) { nxt.wl[0][0] = ldstr(sm, wb[0][0] + WO); nxt.wh[0][0] = ldstr(sm, wb[0][1] + WO); }
        if (n == 1) { nxt.xa[0][0] = lds128(sm, xb[0] + XO); nxt.wl[0][1] = ldstr(sm, wb[1][0] + WO); }
        if (n == 2) { nxt.wh[0][1] = ldstr(sm, wb[1][1] + WO); nxt.xa[0][1] = lds128(sm, xb[0] + XO + 4096); }
        if (n == 3) { nxt.wl[1][0] = ldstr(sm, wb[0][0] + WO + 4096); nxt.wh[1][0] = ldstr(sm, wb[0][1] + WO + 4096); }
        if (n == 4) { nxt.xa[1][0] = lds128(sm, xb[1] + XO); nxt.wl[1][1] = ldstr(sm, wb[1][0] + WO + 4096); }
        if (n == 5) { nxt.wh[1][1] = ldstr(sm, wb[1][1] + WO + 4096); nxt.xa[1][1] = lds128(sm, xb[1] + XO + 4096); }
        SB();
    }
}
template <bool QT, int ST>
DI void k1_chunk(LAS char* sm, f32x16 (&acc)[2][2], K1Set& s0, K1Set& s1, const K1Addr& a0, const K1Addr& a1, bool bar, bool drain = false) {
    constexpr int SN = (ST + 1) & 3;
    const K1Addr& ac = ST < 2 ? a0 : a1; const K1Addr& an = SN < 2 ? a0 : a1;
    k1_phase<QT, (ST & 1) * K1_STAGE + 8192, (ST & 1) * K1_STAGE + 16384>(sm, acc, s0, s1, ac.x[1], ac.w);
    if (drain) VMCNT(0);
    if (bar) { LGKM0(); BAR(); }
    k1_phase<QT, (SN & 1) * K1_STAGE, (SN & 1) * K1_STAGE + 16384>(sm, acc, s1, s0, an.x[0], an.w);
}
template <bool QT>
DI void k1_loop(LAS char* sm, const K1Addr& a0, f32x16 (&acc)[2][2], rsrc_t rw, u32 pflag, u32 tag, int wave, int lane, const u16* hk, const u16* wo, int n240) {
    K1Addr a1;
#pragma unroll
    for (int i = 0; i < 2; i++)
#pragma unroll
        for (int z = 0; z < 2; z++) { a1.x[i][z] = a0.x[i][z] + 65536; a1.w[i][z] = a0.w[i][z] + 65536; }
    K1Set s0, s1;
#pragma unroll
    for (int ks = 0; ks < 2; ks++)
#pragma unroll
        for (int jj = 0; jj < 2; jj++) {
            s0.wl[ks][jj] = ldstr(sm, a0.w[jj][0] + ks * 4096); s0.wh[ks][jj] = ldstr(sm, a0.w[jj][1] + ks * 4096);
            s0.xa[ks][jj] = lds128(sm, a0.x[0][ks] + 16384 + jj * 4096);
        }
    SB();
#pragma unroll 1
    for (int c = 0; c < 8; c += 4) {
        k1_chunk<QT, 0>(sm, acc, s0, s1, a0, a1, true);
        k1_chunk<QT, 1>(sm, acc, s0, s1, a0, a1, true);
        k1_chunk<QT, 2>(sm, acc, s0, s1, a0, a1, true);
        k1_chunk<QT, 3>(sm, acc, s0, s1, a0, a1, true, c == 0);
        if (c == 0 && wave == 0 && lane == 0) set_flag(rw, pflag, tag);
        if (PREF2 && c == 0 && wave >= 2) {
            if (wave == 2) { const u32 l_ = n240 * 86 + lane; touch(hk, (l_ < 20480 ? l_ : 20479) * 128); }
            else { const u32 l_ = n240 * 86 + 64 + lane, m_ = n240 * 27 + lane - 22;
                   if (lane < 22) touch(hk, (l_ < 20480 ? l_ : 20479) * 128); else if (lane < 49) touch(wo, (m_ < 6400 ? m_ : 6399) * 128); }
        }
    }
    k1_chunk<QT, 0>(sm, acc, s0, s1, a0, a1, true);
    k1_chunk<QT, 1>(sm, acc, s0, s1, a0, a1, false);
}

DI void wo_issue(const u16* wo, int ct, int k0, int k1, int w, int nw, int lane) {
    u32 vo[5];
#pragma unroll
    for (int i = 0; i < 5; i++) { const int n = 64 * i + lane, rho = n / 10, c = n - 10 * rho, row = (rho & 19) | ((rho & 4) << 1) | ((rho & 8) >> 1); vo[i] = (u32)(row * 1280 + ct * 160 + c * 16); }
#pragma unroll 1
    for (int kap = k0 + w; kap < k1; kap += nw) {
        const char* src = (const char*)wo + (size_t)kap * 40960;
        const u32 dst = kap < 12 ? kap * 5120 : WO_HI + (kap - 12) * 5120;
#pragma unroll
        for (int i = 0; i < 5; i++) glds16(src, vo[i], dst + i * 1024);
    }
}

struct CvSet { u32x2 fl[8], fh[8]; u32x4 g[8]; };
DI void cv_load(LAS char* sm, CvSet& s, u32 fa, u32 ga) {
#pragma unroll
    for (int k1 = 0; k1 < 8; k1++) { s.fl[k1] = ldstr(sm, fa + 512 * k1 + 8); s.fh[k1] = ldstr(sm, fa + 512 * k1); s.g[k1] = lds128(sm, ga + 512 * k1); }
}
DI void cv_phase(LAS char* sm, f32x4 (&Y)[8], const CvSet& cur, CvSet& nxt, u32 fa, u32 ga) {
    PIN(fa); PIN(ga);
    SB();
    int n = 0;
#pragma unroll
    for (int e = 0; e < 8; e++)
#pragma unroll
        for (int k1 = 0; k1 + e < 8; k1++) {
            const u32x4 F = {cur.fl[k1].x, cur.fl[k1].y, cur.fh[k1].x, cur.fh[k1].y};
            Y[k1 + e] = mfma16(F, cur.g[e], Y[k1 + e]);
            if (n == 0) nxt.g[0] = lds128(sm, ga);
            else if (n <= 16) { const int r = (n - 1) >> 1; if ((n - 1) & 1) nxt.fh[r] = ldstr(sm, fa + 512 * r); else nxt.fl[r] = ldstr(sm, fa + 512 * r + 8); }
            else if (n < 24) nxt.g[n - 16] = lds128(sm, ga + 512 * (n - 16));
            n++;
            SB();
        }
}
struct CvIn { u32x4 hd[4]; u32x2 hm[4]; };
DI void conv_fetch_h(CvIn& in, const u16* hk, int c, int lane) {
    const char* hb = (const char*)hk + (size_t)c * 4096;
#pragma unroll
    for (int z = 0; z < 4; z++) {
        const int i = lane + 64 * z;
        if (NT_H) asm volatile("global_load_dwordx4 %0, %1, %2 nt" : "=v"(in.hd[z]) : "v"((u32)(i * 16)), "s"(hb) : "memory"); else gld128(in.hd[z], hb, (u32)(i * 16));
        gld64(in.hm[z], hb, (u32)(i > 0 ? i * 16 - 8 : 0));
    }
}
DI void conv_build_h(LAS char* sm0, CvIn& in, int w, int lane) {
    LAS char* sm = sm0 + CV_BASE + w * CV_WAVE;
#pragma unroll
    for (int z = 0; z < 4; z++) { PIN(in.hd[z]); PIN(in.hm[z]); }
    if (lane < 32) *(LAS u32x4*)(sm + CV_U + lane * 16) = (u32x4){0, 0, 0, 0};
    if (lane < 8) *(LAS u32x4*)(sm + (lane >> 1) * CV_PITCH + (lane & 1) * 16) = (u32x4){0, 0, 0, 0};
#pragma unroll
    for (int z = 0; z < 4; z++) {
        const int i = lane + 64 * z;
        u32 m2 = in.hm[z].x, m1 = in.hm[z].y;
        if (i == 0) { m2 = 0; m1 = 0; }
        const u32 d0 = in.hd[z].x, d1 = in.hd[z].y, d2 = in.hd[z].z, d3 = in.hd[z].w;
        const u32 om2 = __builtin_amdgcn_alignbit(m1, m2, 16), om1 = __builtin_amdgcn_alignbit(d0, m1, 16), o0 = __builtin_amdgcn_alignbit(d1, d0, 16),
                  o1 = __builtin_amdgcn_alignbit(d2, d1, 16), o2 = __builtin_amdgcn_alignbit(d3, d2, 16);
        LAS char* p = sm + 32 + i * 16;
        *(LAS u32x4*)(p) = (u32x4){d0, d1, d2, d3};
        *(LAS u32x4*)(p + CV_PITCH) = (u32x4){om1, o0, o1, o2};
        *(LAS u32x4*)(p + 2 * CV_PITCH) = (u32x4){m1, d0, d1, d2};
        *(LAS u32x4*)(p + 3 * CV_PITCH) = (u32x4){om2, om1, o0, o1};
    }
}
DI void conv_run(LAS char* sm0, float Dc, int w, int lane, int c, rsrc_t rw, char* ws) {
    LAS char* sm = sm0;
    LGKM0();
    TS(4, 9);
    const int fr = lane & 15, g = lane >> 4, q = fr >> 2, p = lane & 3;
    sm = sm0;
    const u32 fa = CV_BASE + w * CV_WAVE + q * CV_PITCH + 2 * (32 - 8 * g + 4 * p) - 8;
    const u32 ga = CV_BASE + w * CV_WAVE + CV_U + 480 + 32 * fr + 16 * g;
    f32x4 Y[8];
#pragma unroll
    for (int i = 0; i < 8; i++) Y[i] = (f32x4){0, 0, 0, 0};
    CvSet A, B;
    cv_load(sm, A, fa, ga);
    SB();
#pragma unroll 1
    for (int k0 = 0; k0 < 8; k0 += 2) {
        cv_phase(sm, Y, A, B, fa + 64 * (k0 + 1), ga - 64 * (k0 + 1));
        cv_phase(sm, Y, B, A, fa + 64 * (k0 + 2), ga - 64 * (k0 + 2));
    }
    TS(4, 10);
    sm = sm0 + CV_BASE + w * CV_WAVE;
    {
        u32x2 u4[8];
#pragma unroll
        for (int ib = 0; ib < 8; ib++) u4[ib] = lds64(sm, CV_U + 512 + 512 * ib + 32 * fr + 8 * g);
#pragma unroll
        for (int ib = 0; ib < 8; ib++) {
            const u32x2 r = {pk2(fmaf(blo(u4[ib].x), Dc, Y[ib][0]), fmaf(bhi(u4[ib].x), Dc, Y[ib][1])), pk2(fmaf(blo(u4[ib].y), Dc, Y[ib][2]), fmaf(bhi(u4[ib].y), Dc, Y[ib][3]))};
            *(LAS u32x2*)(sm + 512 * ib + 32 * fr + 8 * g) = r;
        }
    }
    LGKM0();
    u32x4 y8[4];
#pragma unroll
    for (int z = 0; z < 4; z++) y8[z] = lds128(sm, z * 1024 + lane * 16);
#pragma unroll
    for (int z = 0; z < 4; z++) st128c(rw, WS_YT + c * 4096 + z * 1024 + lane * 16, y8[z]);
}

DI void kvo_block(LAS char* sm, int wave, int lane, int h, int half, rsrc_t rw, u32 tag, char* ws) {
    const int fr = lane & 15, g = lane >> 4;
    const int j0 = (20 * h) >> 7, j1 = (20 * h + 19) >> 7;
    wait_flags(rw, lane < 8 ? (int)F_G1 + 16 * (((lane & 2) ? 10 : 5) + ((lane & 1) ? j1 : j0)) + 2 * wave + (lane >> 2)
                            : lane < 10 ? (int)F_G1 + 16 * ((lane & 1) ? j1 : j0) + 8 * half + wave : -1, -1, tag);
    TS(0, 8);
    const u32 pk0 = WS_PK0 + (h * 64 + 8 * wave) * 1024 + lane * 16, v0 = pk0 + (WS_V0 - WS_PK0);
    const u32 xx = WS_X + (h * 32 + 4 * wave) * 1024 + lane * 16;
    u32x4 A[8], B[8], X[4], Q[8];
#pragma unroll
    for (int i = 0; i < 8; i++) { A[i] = ld128c(rw, pk0 + i * 1024); B[i] = ld128c(rw, v0 + i * 1024); if ((i & 1) == 0) X[i >> 1] = ld128c(rw, xx + (i >> 1) * 1024); }
    const int odd = h & 1, pr = h >> 1;
    const int u5 = (g < 3 ? g : 2) + 2 * odd;
    const int sb0 = half * 64 + wave * 8;
    const u32 pq = WS_P2 + (((pr * 128 + sb0) * 5 + u5) * 128 + fr * 8) * 2;
    MEMF(); SB();
    f32x4 a00 = {0, 0, 0, 0}, a0x = a00, ax0 = a00, axx = a00;
    const u32 cst = fr == 8 ? 0x3F803F80u : 0u;
#pragma unroll
    for (int i = 0; i < 8; i++) {
        u32x4 xv = X[i >> 1];
        if (i & 1) {
#pragma unroll
            for (int e = 0; e < 4; e++) xv[e] = (u32)__builtin_amdgcn_update_dpp(0, (int)xv[e], 0x108, 0xf, 0xf, false);
        }
        if (fr >= 8) xv = (u32x4){cst, cst, cst, cst};
        a00 = mfma16(A[i], B[i], a00); a0x = mfma16(A[i], xv, a0x); ax0 = mfma16(xv, B[i], ax0); axx = mfma16(xv, xv, axx);
    }
    PIN(a00); PIN(a0x); PIN(ax0); PIN(axx);
    SB();
#pragma unroll
    for (int i = 0; i < 8; i++) Q[i] = ld128c(rw, pq + i * 1280);
    MEMF(); SB();
    TS(0, 9);
    *(LAS f32x4*)(sm + KO_RED + ((wave * 4 + 0) * 64 + lane) * 16) = a00;
    *(LAS f32x4*)(sm + KO_RED + ((wave * 4 + 1) * 64 + lane) * 16) = a0x;
    *(LAS f32x4*)(sm + KO_RED + ((wave * 4 + 2) * 64 + lane) * 16) = ax0;
    *(LAS f32x4*)(sm + KO_RED + ((wave * 4 + 3) * 64 + lane) * 16) = axx;
    if (wave >= 4 && (wave - 4) * 64 + lane < 189) *(LAS u32x2*)(sm + KO_KV + ((wave - 4) * 64 + lane) * 8) = (u32x2){0, 0};
    LGKM0(); BAR();
    if (wave < 4) {
        const int tile = wave;
        f32x4 s = *(LAS f32x4*)(sm + KO_RED + (tile * 64 + lane) * 16);
#pragma unroll
        for (int w2 = 1; w2 < 8; w2++) s += *(LAS f32x4*)(sm + KO_RED + ((w2 * 4 + tile) * 64 + lane) * 16);
        const int e = (tile & 1) ? 12 + fr : fr;
        const bool colok = (tile & 1) ? (fr >= 4 && fr <= 8) : true;
        const bool rowok = (tile & 2) ? (g == 0) : true;
        const int d0 = (tile & 2) ? 16 : 4 * g;
        if (colok && rowok) *(LAS u32x2*)(sm + KO_KV + e * 72 + (d0 + 4) * 2) = (u32x2){pk2(s[0], s[1]), pk2(s[2], s[3])};
    }
    LGKM0(); BAR();
    u32x4 A0, A1[4], A2;
    {
        const u32 dofs = KO_KV + (8 * g - 4 * odd + 4) * 2;
        const u32 r0 = (4 * odd + fr) * 72, r1 = ((odd ? 0 : 16) + (fr & 3)) * 72;
        const u32x2 p0 = lds64(sm, dofs + r0), p1 = lds64(sm, dofs + r0 + 8);
        const u32x2 q0 = lds64(sm, dofs + r1), q1 = lds64(sm, dofs + r1 + 8);
        const u32x2 z0 = lds64(sm, dofs + 20 * 72), z1 = lds64(sm, dofs + 20 * 72 + 8);
        A0 = (u32x4){p0.x, p0.y, p1.x, p1.y}; A2 = (u32x4){z0.x, z0.y, z1.x, z1.y};
#pragma unroll
        for (int z = 0; z < 4; z++) { const bool m = (fr >> 2) == z; A1[z] = (u32x4){m ? q0.x : 0u, m ? q0.y : 0u, m ? q1.x : 0u, m ? q1.y : 0u}; }
    }
    TS(0, 10);
    const int U0 = (5 * h + odd) >> 1;
    const int Uw = U0 + (g >> 1);
    const u32 offA = WS_O + (((Uw >> 2) * 4 + (Uw & 3)) * 128 + fr * 8) * 2 + (g & 1) * 20480;
    const int cgx = 5 * h + (odd ? 0 : 4), Ux = cgx >> 1;
    const u32 offX = WS_O + (((Ux >> 2) * 4 + (Ux & 3)) * 128 + fr * 8 + (cgx & 1) * 4) * 2;
    const f32x4 zero = {0, 0, 0, 0};
#pragma unroll
    for (int i4 = 0; i4 < 2; i4++) {
        f32x4 n1 = zero; float rden[4]; u32x2 ov[4];
#pragma unroll
        for (int z = 0; z < 4; z++) {
            const int i = 4 * i4 + z;
            const f32x4 n0 = mfma16(A0, Q[i], zero);
            const f32x4 dn = mfma16(A2, Q[i], zero);
            n1 = mfma16(A1[z], Q[i], n1);
            const float rd = __builtin_amdgcn_rcpf(dn[0] + 1e-6f);
            rden[z] = rd;
            ov[z] = (u32x2){pk2(n0[0] * rd, n0[1] * rd), pk2(n0[2] * rd, n0[3] * rd)};
        }
#pragma unroll
        for (int z = 0; z < 4; z += 2) {
            auto rx = __builtin_amdgcn_permlane16_swap(ov[z].x, ov[z + 1].x, false, false);
            auto ry = __builtin_amdgcn_permlane16_swap(ov[z].y, ov[z + 1].y, false, false);
            st128c(rw, offA + (sb0 + 4 * i4 + z) * 20480, (u32x4){rx[0], ry[0], rx[1], ry[1]});
        }
        const float rd = g == 0 ? rden[0] : g == 1 ? rden[1] : g == 2 ? rden[2] : rden[3];
        st64c(rw, offX + (sb0 + 4 * i4 + g) * 20480, (u32x2){pk2(n1[0] * rd, n1[1] * rd), pk2(n1[2] * rd, n1[3] * rd)});
    }
}

DI void gate_block(int wave, int lane, int m, const u16* x, const u16* gw, rsrc_t rw) {
    const int sub = lane & 15, r4 = lane >> 4;
    u32x4 gq[5], xr[4][5];
#pragma unroll
    for (int jj = 0; jj < 5; jj++) gq[jj] = *(const GAS u32x4*)((const GAS char*)gw + (sub + 16 * jj) * 16);
#pragma unroll
    for (int it = 0; it < 4; it++)
#pragma unroll
        for (int jj = 0; jj < 5; jj++) xr[it][jj] = *(const GAS u32x4*)((const GAS char*)x + (size_t)(128 * m + 16 * wave + 4 * it + r4) * 1280 + (sub + 16 * jj) * 16);
    MEMF(); SB();
#pragma unroll
    for (int it = 0; it < 4; it++) {
        float a = 0.f;
#pragma unroll
        for (int jj = 0; jj < 5; jj++)
#pragma unroll
            for (int e = 0; e < 4; e++) { a = fmaf(blo(xr[it][jj][e]), blo(gq[jj][e]), a); a = fmaf(bhi(xr[it][jj][e]), bhi(gq[jj][e]), a); }
        a += __shfl_xor(a, 1); a += __shfl_xor(a, 2); a += __shfl_xor(a, 4); a += __shfl_xor(a, 8);
        a = rbf(a);
        const float g = rbf(__builtin_amdgcn_rcpf(1.f + __builtin_amdgcn_exp2f(-1.4426950408889634f * a)));
        if (sub == 0) st32c(rw, WS_G + (128 * m + 16 * wave + 4 * it + r4) * 4, __float_as_uint(g));
    }
}

DI void final_block(LAS char* sm, int wave, int lane, int tid, int st, int ct, const u16* x, u16* out, rsrc_t rw, u32 tag, char* ws) {
    const int fr = lane & 15, g = lane >> 4;
    u32x4 xv[2];
#pragma unroll
    for (int r = 0; r < 2; r++) {
        int it = tid + 512 * r; it = it < 640 ? it : 639;
        const int s = it / 10, u = it - 10 * s;
        xv[r] = *(const GAS u32x4*)((const GAS char*)x + (size_t)(st * 64 + s) * 1280 + ct * 160 + u * 16);
    }
    VMCNT(0); BAR();
    if (wave < 4) {
        const int kh = wave & 1, sh = wave >> 1;
        wait_flags(rw, lane < 16 ? (int)F_O + (st >> 4) * 32 + 16 * kh + lane : -1, -1, tag);
        TS(0, 17);
        const u32 ob = WS_O + ((st * 4 + 2 * sh) * 20 + 10 * kh) * 1024 + lane * 16;
        u32x4 B[2][10];
#pragma unroll
        for (int kl = 0; kl < 10; kl++) { B[0][kl] = ld128c(rw, ob + kl * 1024); B[1][kl] = ld128c(rw, ob + 20480 + kl * 1024); }
        MEMF(); SB();
        f32x4 acc[5][2];
#pragma unroll
        for (int cb = 0; cb < 5; cb++) { acc[cb][0] = (f32x4){0, 0, 0, 0}; acc[cb][1] = (f32x4){0, 0, 0, 0}; }
        const u32 aoff = (16 * (g >> 1) + 4 * (g & 1) + (fr >> 2)) * 160 + (lane & 3) * 8;
        u32x2 al[2][5], ah[2][5];
#define F_READ(buf, kl) do { const u32 b0_ = kh ? wo_off(10 + (kl)) : wo_off(kl); \
            _Pragma("unroll") for (int cb = 0; cb < 5; cb++) { al[buf][cb] = ldstr(sm, b0_ + aoff + cb * 32); ah[buf][cb] = ldstr(sm, b0_ + aoff + cb * 32 + 1280); } } while (0)
        F_READ(0, 0);
#pragma unroll
        for (int kl = 0; kl < 10; kl++) {
            if (kl < 9) F_READ((kl + 1) & 1, kl + 1);
            SB();
#pragma unroll
            for (int cb = 0; cb < 5; cb++) {
                const u32x4 a = {al[kl & 1][cb].x, al[kl & 1][cb].y, ah[kl & 1][cb].x, ah[kl & 1][cb].y};
                acc[cb][0] = mfma16(a, B[0][kl], acc[cb][0]); acc[cb][1] = mfma16(a, B[1][kl], acc[cb][1]);
            }
            SB();
        }
        TS(0, 18);
        lds_arrive(sm, C_FR, lane); lds_wait(sm, C_FR, 4);
#pragma unroll
        for (int cb = 0; cb < 5; cb++)
#pragma unroll
            for (int z = 0; z < 2; z++)
                *(LAS f32x4*)(sm + kh * (64 * RED_PITCH) + (16 * (2 * sh + z) + fr) * RED_PITCH + (16 * cb + 4 * g) * 4) = acc[cb][z];
    } else {
        if (wave == 4) { wait_flags(rw, lane < 20 ? (int)F_Y + ct * 32 + lane : lane == 32 ? (int)F_GATE + (st >> 1) : -1, -1, tag); lds_arrive(sm, C_FY, lane); }
        else lds_wait(sm, C_FY, 1);
        const int t = tid - 256;
        u32x4 yv[3];
#pragma unroll
        for (int r = 0; r < 3; r++) { int id = t + 256 * r; id = id < 640 ? id : 639; yv[r] = ld128c(rw, WS_YT + (80 * ct + (id >> 3)) * 4096 + st * 128 + (id & 7) * 16); }
#pragma unroll
        for (int r = 0; r < 3; r++) { const int id = t + 256 * r; if (id < 640) *(LAS u32x4*)(sm + F_YY + id * 16) = yv[r]; }
        if (wave == 4) *(LAS u32*)(sm + F_GG + lane * 4) = ld32c(rw, WS_G + (st * 64 + lane) * 4);
        lds_arrive(sm, C_FY2, lane); lds_wait(sm, C_FY2, 4);
        const int sb = wave - 4, q = fr >> 2, p = lane & 3;
        u32x2 tt[5];
#pragma unroll
        for (int cb = 0; cb < 5; cb++) tt[cb] = ldstr(sm, F_YY + (16 * cb + 4 * g + q) * 128 + (16 * sb + 4 * p) * 2);
#pragma unroll
        for (int cb = 0; cb < 5; cb++) *(LAS u32x2*)(sm + F_Y2 + (16 * sb + fr) * 176 + (16 * cb + 4 * g) * 2) = tt[cb];
    }
    LGKM0(); BAR();
#pragma unroll
    for (int r = 0; r < 2; r++) {
        const int it = tid + 512 * r;
        if (it < 640) {
            const int s = it / 10, u = it - 10 * s;
            const u32 ra = s * RED_PITCH + u * 32;
            const f32x4 p0 = *(LAS f32x4*)(sm + ra), p1 = *(LAS f32x4*)(sm + ra + 16), q0 = *(LAS f32x4*)(sm + 64 * RED_PITCH + ra), q1 = *(LAS f32x4*)(sm + 64 * RED_PITCH + ra + 16);
            const u32x4 y8 = lds128(sm, F_Y2 + s * 176 + u * 16);
            const float gg = *(LAS float*)(sm + F_GG + s * 4), omg = 1.f - gg;
            float o[8];
#pragma unroll
            for (int e = 0; e < 4; e++) {
                const float a0 = (e < 2 ? p0[2 * e] + q0[2 * e] : p1[2 * e - 4] + q1[2 * e - 4]), a1 = (e < 2 ? p0[2 * e + 1] + q0[2 * e + 1] : p1[2 * e - 3] + q1[2 * e - 3]);
                const float b0 = omg * blo(xv[r][e]), b1 = omg * bhi(xv[r][e]);
                o[2 * e] = fmaf(b0, blo(y8[e]), gg * a0); o[2 * e + 1] = fmaf(b1, bhi(y8[e]), gg * a1);
            }
            *(GAS u32x4*)((GAS char*)out + (size_t)(st * 64 + s) * 1280 + ct * 160 + u * 16) = (u32x4){pk2(o[0], o[1]), pk2(o[2], o[3]), pk2(o[4], o[5]), pk2(o[6], o[7])};
        }
    }
}

__global__ __launch_bounds__(512, 2) void fused_kernel(const u16* __restrict__ x, const u16* __restrict__ w, const u16* __restrict__ wo, const u16* __restrict__ kin,
                                                        const u16* __restrict__ vin, const u16* __restrict__ shk, const u16* __restrict__ hk, const u16* __restrict__ sD,
                                                        const u16* __restrict__ gw, u16* __restrict__ out, char* ws) {
    extern __shared__ __attribute__((aligned(16))) char smem[];
    LAS char* sm = (LAS char*)smem;
    const int tid = threadIdx.x, lane = tid & 63, wave = __builtin_amdgcn_readfirstlane(tid >> 6);
    const int b = blockIdx.x, xcd = b & 7, j = b >> 3;
    const rsrc_t rw = __builtin_amdgcn_make_buffer_rsrc(ws, 0, WS_BYTES, 0x00020000);
    u32 tag;
    asm volatile("s_load_dword %0, %1, 0x0\n\ts_waitcnt lgkmcnt(0)" : "=s"(tag) : "s"(ws + WS_FLAG + F_EPOCH * FS) : "memory");
    tag += 1u;
    if (tid < 16) *(LAS u32*)(sm + L_CNT + tid * 4) = 0u;
    TS(0, 0);
    const int st = 4 * xcd + (j >> 3), ct = j & 7;
    int ko = -1;
    bool is_conv = false;

    if (j >= 30) {
        const int m = xcd * 2 + (j - 30);
        ko = -100;
        {
            const int grp = wave >> 2, w4i = wave & 3;
            u32x2 w4[3];
#pragma unroll
            for (int t = 0; t < 3; t++) { const int tt = 2 * t + grp < 5 ? 2 * t + grp : 4; p0_weights(w4[t], shk, 240 + 5 * m + tt, w4i, lane); }
#pragma unroll
            for (int t = 0; t < 3; t++) if (2 * t + grp < 5) p0_issue(kin, vin, 240 + 5 * m + 2 * t + grp, w4i, lane, (2 * t + grp) * 17408);
            VMCNT(0);
#pragma unroll
            for (int t = 0; t < 3; t++) PIN(w4[t]);
            BAR();
#pragma unroll
            for (int t = 0; t < 3; t++)
                if (2 * t + grp < 5) p0_compute(sm, w4[t], 240 + 5 * m + 2 * t + grp, w4i, lane, (2 * t + grp) * 17408, 90112 + (2 * t + grp) * 8192);
            LGKM0(); BAR();
            {
                u32x4 t4[3][2];
#pragma unroll
                for (int t = 0; t < 3; t++)
#pragma unroll
                    for (int z = 0; z < 2; z++) { const int ch2 = ((tid & 255) >> 3) + 32 * z, o2 = tid & 7, tt = 2 * t + grp < 5 ? 2 * t + grp : 4; t4[t][z] = lds128(sm, 90112 + tt * 8192 + ch2 * 128 + ((o2 ^ (ch2 & 7)) * 16)); }
#pragma unroll
                for (int t = 0; t < 3; t++)
                    if (2 * t + grp < 5) {
                        const int tile = 240 + 5 * m + 2 * t + grp, cg = tile % 10, s0 = (tile / 10) * 64;
#pragma unroll
                        for (int z = 0; z < 2; z++) { const int ch2 = ((tid & 255) >> 3) + 32 * z, o2 = tid & 7; st128c(rw, WS_UT + (cg * 64 + ch2) * 4096 + (s0 + 8 * o2) * 2, t4[t][z]); }
                    }
            }
        }
        VMCNT(0); BAR();
        if (tid < 5) set_flag(rw, p0_flag(240 + 5 * m + tid), tag);
        TS(0, 1);
        gate_block(wave, lane, m, x, gw, rw);
        VMCNT(0); LGKM0(); BAR();
        if (tid == 0) set_flag(rw, F_GATE + m, tag);
        TS(0, 3);
    } else {
        const int rt = 2 * xcd + j / 15, cbk = j % 15, tile = xcd * 30 + j;
        is_conv = cbk < 10;
        if (!is_conv) { const int vi = rt * 5 + (cbk - 10); ko = vi < 64 ? vi : -2 - (vi - 64); }
        if (wave >= 4) {
            const int lw = wave - 4;
            p0_issue(kin, vin, tile, lw, lane, P0_K);
            u32 vx[4], vw[4];
#pragma unroll
            for (int n2 = 0; n2 < 4; n2++) {
                const int n = 4 * lw + n2;
                const int row = 8 * n + (lane >> 3), ch = (lane & 7) ^ ((row >> 1) & 7);
                vx[n2] = (u32)((rt * 128 + row) * 1280 + ch * 16);
                const int kr = 4 * n + (lane >> 4), f = ((kr & 3) << 2) | ((kr >> 2) & 3), cw = (lane & 15) ^ f;
                vw[n2] = (u32)(kr * 3840 + cbk * 256 + cw * 16);
            }
            const int rot = (rt + cbk) % 10;
#define K1_ISSUE(c) do { const u32 st_ = ((c) & 3) * K1_STAGE + 4 * lw * 1024; int kc_ = (c) + ROT_ON * rot; kc_ = kc_ >= 10 ? kc_ - 10 : kc_; \
            const char* xb_ = (const char*)x + kc_ * 128; const char* wp_ = (const char*)w + (size_t)kc_ * 64 * 3840; \
            _Pragma("unroll") for (int n2 = 0; n2 < 4; n2++) { if (NT_X) glds16nt(xb_, vx[n2], st_ + 16384 + n2 * 1024); else glds16(xb_, vx[n2], st_ + 16384 + n2 * 1024); glds16(wp_, vw[n2], st_ + n2 * 1024); } } while (0)
            K1_ISSUE(0);
            VMCNT(8); BAR();
            K1_ISSUE(1); K1_ISSUE(2);
            VMCNT(16); BAR();
            K1_ISSUE(3);
#pragma unroll 1
            for (int c = 0; c < 6; c++) {
                VMCNT(16); BAR();
                K1_ISSUE(c + 4);
            }
            const int cv = rt * 10 + cbk, c = 4 * cv + lw;
            if (is_conv) {
                CvIn in; u32 fl; u32x4 u[4];
                const int fidx = (int)F_P0 + (cv >> 4) * 32 + (lane & 31);
                conv_fetch_h(in, hk, c, lane);
                VMCNT(24); BAR();
                VMCNT(16); BAR();
                gld32c(fl, ws, WS_FLAG + fidx * FS);
                VMCNT(9); BAR();
                TS(4, 4);
                VMCNT(0); PIN(fl);
                const bool ready = __all(fl == tag);
                if (ready) {
#pragma unroll
                    for (int z = 0; z < 4; z++) gld128c(u[z], ws, WS_UT + c * 4096 + z * 1024 + lane * 16);
                }
                TS(4, 5);
                conv_build_h(sm, in, lw, lane);
                TS(4, 6);
                if (!ready) {
                    wait_flags(rw, fidx, -1, tag);
#pragma unroll
                    for (int z = 0; z < 4; z++) gld128c(u[z], ws, WS_UT + c * 4096 + z * 1024 + lane * 16);
                }
                TS(4, 7);
                VMCNT(0);
#pragma unroll
                for (int z = 0; z < 4; z++) { PIN(u[z]); *(LAS u32x4*)(sm + CV_BASE + lw * CV_WAVE + CV_U + 512 + z * 1024 + lane * 16) = u[z]; }
                const float Dc = blo((u32)sD[c]);
                TS(4, 8);
                conv_run(sm, Dc, lw, lane, c, rw, ws);
                publish_last(rw, sm, C_Y, 4, F_Y + (cv / 20) * 32 + cv % 20, tag, lane);
                TS(4, 11);
            } else {
                VMCNT(16); BAR();
                VMCNT(8); BAR();
                VMCNT(0); BAR();
            }
        } else {
            u32x2 w4; p0_weights(w4, shk, tile, wave, lane);
            if (PREF1 && wave < 3) {
                const int n = wave * 64 + lane;
                if (wave == 0) touch(x, (u32)((xcd * 2560 + j * 80 + lane) * 128));
                else if (wave == 1) { if (lane < 16) touch(x, (u32)((xcd * 2560 + j * 80 + 64 + lane) * 128)); else touch(w, (u32)(((xcd * 30 + j) * 80 + lane - 16) * 128)); }
                else if (lane < 32) touch(w, (u32)(((xcd * 30 + j) * 80 + 48 + lane) * 128));
                (void)n;
            }
            const int wm = wave >> 1, wn = wave & 1, r32 = lane & 31, hh = lane >> 5;
            K1Addr a0;
#pragma unroll
            for (int ks = 0; ks < 2; ks++) {
                a0.x[0][ks] = (64 * wm + r32) * 128 + (((2 * ks + hh) ^ ((r32 >> 1) & 7)) * 16);
                a0.x[1][ks] = a0.x[0][ks] ^ 64;
            }
#pragma unroll
            for (int jj = 0; jj < 2; jj++)
#pragma unroll
                for (int t = 0; t < 2; t++) {
                    const int blk = (lane >> 4) & 1, q = (lane & 15) >> 2, p = lane & 3, row = 8 * hh + 4 * t + q, ch = 8 * wn + 4 * jj + 2 * blk + (p >> 1);
                    const int f = ((row & 3) << 2) | ((row >> 2) & 3);
                    a0.w[jj][t] = row * 256 + ((ch ^ f) * 16) + 8 * (p & 1);
                }
            f32x16 acc[2][2];
#pragma unroll
            for (int i = 0; i < 2; i++)
#pragma unroll
                for (int jj = 0; jj < 2; jj++)
#pragma unroll
                    for (int e = 0; e < 16; e++) acc[i][jj][e] = 0.f;
            TS(0, 20);
            if (PREF1 && wave == 1) VMCNT(2); else if (PREF1 && wave < 3) VMCNT(1); else VMCNT(0);
            PIN(w4); TS(0, 21); BAR();
            TS(0, 22);
            p0_compute(sm, w4, tile, wave, lane, P0_K, P0_OUT, ws);
            TS(0, 28);
            LGKM0(); TS(0, 23); BAR();
            TS(0, 24);
            p0_store(sm, rw, tile, tid, P0_OUT);
            TS(0, 1);
            if (cbk < 5) {
                k1_loop<true>(sm, a0, acc, rw, p0_flag(tile), tag, wave, lane, hk, wo, tile);
                __builtin_amdgcn_s_setprio(2);
                TS(0, 2);
                u32x4 qv[2][2][2];
#pragma unroll
                for (int jj = 0; jj < 2; jj++)
#pragma unroll
                    for (int ap = 0; ap < 2; ap++)
#pragma unroll
                        for (int i = 0; i < 2; i++) {
                            const f32x16& a = acc[i][jj];
                            const u32 x0 = phi2(a[8 * ap], a[8 * ap + 1]), y0 = phi2(a[8 * ap + 2], a[8 * ap + 3]);
                            const u32 x1 = phi2(a[8 * ap + 4], a[8 * ap + 5]), y1 = phi2(a[8 * ap + 6], a[8 * ap + 7]);
                            auto rx = __builtin_amdgcn_permlane32_swap(x0, x1, false, false);
                            auto ry = __builtin_amdgcn_permlane32_swap(y0, y1, false, false);
                            qv[jj][ap][i] = (u32x4){rx[0], ry[0], rx[1], ry[1]};
                        }
                if (QDELAY) { __builtin_amdgcn_s_setprio(0); for (int z = 0; z < QDELAY; z++) __builtin_amdgcn_s_sleep(8); }
#pragma unroll
                for (int jj = 0; jj < 2; jj++)
#pragma unroll
                    for (int ap = 0; ap < 2; ap++) {
                        const int U = 16 * cbk + 8 * wn + 4 * jj + 2 * ap + hh, pr = U / 5, u5 = U - 5 * pr;
                        const u32 dst = WS_P2 + (((pr * 128 + rt * 8 + 4 * wm + (r32 >> 4)) * 5 + u5) * 128 + (r32 & 15) * 8) * 2;
#pragma unroll
                        for (int i = 0; i < 2; i++) st128c(rw, dst + i * 2560, qv[jj][ap][i]);
                    }
            } else {
                k1_loop<false>(sm, a0, acc, rw, p0_flag(tile), tag, wave, lane, hk, wo, tile);
                __builtin_amdgcn_s_setprio(2);
                TS(0, 2);
                const bool isk = cbk < 10;
                const int j5 = isk ? cbk - 5 : cbk - 10;
                u32 dst[2], istep[2];
#pragma unroll
                for (int jj = 0; jj < 2; jj++) {
                    const int cc = 128 * j5 + 64 * wn + 32 * jj + r32, h = cc / 20, d = cc - 20 * h;
                    const int sb32 = rt * 4 + 2 * wm;
                    const bool lo = d < 16;
                    dst[jj] = lo ? (isk ? WS_PK0 : WS_V0) + (((h * 64 + sb32) * 4 + hh) * 128 + d * 8) * 2
                                 : WS_X + (((h * 32 + (sb32 >> 1)) * 4 + hh) * 128 + (d - 16 + (isk ? 0 : 4)) * 8) * 2;
                    istep[jj] = lo ? 1024 : 128;
                }
                if (isk) {
#pragma unroll
                    for (int jj = 0; jj < 2; jj++)
#pragma unroll
                        for (int i = 0; i < 2; i++)
#pragma unroll
                            for (int z = 0; z < 2; z++) {
                                const f32x16& a = acc[i][jj];
                                st128c(rw, dst[jj] + i * istep[jj] + z * 512,
                                       (u32x4){phi2(a[8 * z], a[8 * z + 1]), phi2(a[8 * z + 2], a[8 * z + 3]), phi2(a[8 * z + 4], a[8 * z + 5]), phi2(a[8 * z + 6], a[8 * z + 7])});
                            }
                } else {
#pragma unroll
                    for (int jj = 0; jj < 2; jj++)
#pragma unroll
                        for (int i = 0; i < 2; i++)
#pragma unroll
                            for (int z = 0; z < 2; z++) {
                                const f32x16& a = acc[i][jj];
                                st128c(rw, dst[jj] + i * istep[jj] + z * 512,
                                       (u32x4){pk2(a[8 * z], a[8 * z + 1]), pk2(a[8 * z + 2], a[8 * z + 3]), pk2(a[8 * z + 4], a[8 * z + 5]), pk2(a[8 * z + 6], a[8 * z + 7])});
                            }
                }
            }
            LGKM0();
            TS(0, 25);
            publish_last(rw, sm, C_G1, 4, F_G1 + cbk * 16 + rt, tag, lane);
            __builtin_amdgcn_s_setprio(0);
            TS(0, 3);
            if (is_conv) {
                lds_wait(sm, C_G1, 4);
                wo_issue(wo, ct, 0, 12, wave, 4, lane);
            }
        }
    }
    if (!is_conv) {
        LGKM0(); BAR();
        if (ko >= 0) {
            const int h = ko >> 1, half = ko & 1;
            wo_issue(wo, ct, 0, 12, wave, 8, lane);
            kvo_block(sm, wave, lane, h, half, rw, tag, ws);
            TS(0, 26);
            VMCNT(0); BAR();
            if (tid == 0) set_flag(rw, F_O + half * 32 + h, tag);
            TS(0, 11);
            wo_issue(wo, ct, 12, 20, wave, 8, lane);
        } else {
            wo_issue(wo, ct, 0, 20, wave, 8, lane);
            if (ko == -2 && wave == 0) {
                wait_flags(rw, (int)F_G1 + lane, (int)F_G1 + 64 + lane, tag);
                wait_flags(rw, (int)F_G1 + 128 + lane, (int)F_G1 + 192 + lane, tag);
                if (lane == 0) set_flag(rw, F_EPOCH, tag);
            }
        }
    } else {
        VMCNT(0); BAR();
        if (wave >= 4) wo_issue(wo, ct, 12, 20, wave - 4, 4, lane);
    }
    TS(0, 16);
    final_block(sm, wave, lane, tid, st, ct, x, out, rw, tag, ws);
    TS(0, 19);
}

extern "C" void kernel_launch(void* const* d_in, const int* in_sizes, int n_in, void* d_out, int out_size, void* d_ws, size_t ws_size, hipStream_t stream) {
    const u16* x = (const u16*)d_in[0]; const u16* wqkv = (const u16*)d_in[1]; const u16* wo = (const u16*)d_in[2];
    const u16* kin = (const u16*)d_in[4]; const u16* vin = (const u16*)d_in[5]; const u16* shk = (const u16*)d_in[6];
    const u16* hk = (const u16*)d_in[7]; const u16* sD = (const u16*)d_in[8]; const u16* gw = (const u16*)d_in[9];
    (void)hipFuncSetAttribute((const void*)fused_kernel, hipFuncAttributeMaxDynamicSharedMemorySize, LDS_BYTES);
    fused_kernel<<<256, 512, LDS_BYTES, stream>>>(x, wqkv, wo, kin, vin, shk, hk, sD, gw, (u16*)d_out, (char*)d_ws);
}
```

```cpp
#define PREFC 1
#define PREF1 1
#define PREF2 1
#define POLL_SLEEP 8
#include <hip/hip_runtime.h>
#include <stdint.h>

typedef unsigned short u16;
typedef unsigned int u32;
typedef __bf16 bf16x8 __attribute__((ext_vector_type(8)));
typedef __bf16 bf16x2 __attribute__((ext_vector_type(2)));
typedef float f32x4 __attribute__((ext_vector_type(4)));
typedef float f32x2 __attribute__((ext_vector_type(2)));
typedef float f32x16 __attribute__((ext_vector_type(16)));
typedef u32 u32x4 __attribute__((ext_vector_type(4)));
typedef u32 u32x2 __attribute__((ext_vector_type(2)));
typedef short s16x4 __attribute__((ext_vector_type(4)));
#define LAS __attribute__((address_space(3)))
#define GAS __attribute__((address_space(1)))
#define DI __device__ __forceinline__
typedef __amdgpu_buffer_rsrc_t rsrc_t;

constexpr int S = 2048, D = 640;
#define FS 64
#define ROT_ON 1

constexpr u32 WS_UT = 0;
constexpr u32 WS_YT = WS_UT + 2621440;
constexpr u32 WS_PK0 = WS_YT + 2621440;
constexpr u32 WS_V0 = WS_PK0 + 2097152;
constexpr u32 WS_X = WS_V0 + 2097152;
constexpr u32 WS_P2 = WS_X + 1048576;
constexpr u32 WS_O = WS_P2 + 2621440;
constexpr u32 WS_G = WS_O + 2621440;
constexpr u32 WS_FLAG = WS_G + 8192;
constexpr u32 F_P0 = 0;
constexpr u32 F_G1 = 320;
constexpr u32 F_GATE = 560;
constexpr u32 F_O = 576;
constexpr u32 F_Y = 640;
constexpr u32 F_EPOCH = 896, F_TMO = 928;
constexpr u32 WS_STAMP = WS_FLAG + 1024 * FS;
constexpr u32 WS_BYTES = WS_STAMP + 256 * 32 * 16;

#define TS(w, id) do {} while (0)

DI u32 pk2(float a, float b) { f32x2 t = {a, b}; return __builtin_bit_cast(u32, __builtin_convertvector(t, bf16x2)); }
DI float blo(u32 u) { return __uint_as_float(u << 16); }
DI float bhi(u32 u) { return __uint_as_float(u & 0xffff0000u); }
DI float rbf(float f) { return blo(pk2(f, f)); }
DI bf16x8 asbf(u32x4 v) { return __builtin_bit_cast(bf16x8, v); }
DI f32x4 mfma16(u32x4 a, u32x4 b, f32x4 c) { return __builtin_amdgcn_mfma_f32_16x16x32_bf16(asbf(a), asbf(b), c, 0, 0, 0); }
DI f32x16 mfma32(u32x4 a, u32x4 b, f32x16 c) { return __builtin_amdgcn_mfma_f32_32x32x16_bf16(asbf(a), asbf(b), c, 0, 0, 0); }

DI void glds16(const void* sbase, u32 voff, u32 lds) {
    asm volatile("s_mov_b32 m0, %2\n\ts_nop 0\n\tglobal_load_lds_dwordx4 %0, %1" :: "v"(voff), "s"(sbase), "s"(lds) : "memory");
}
DI void glds16nt(const void* sbase, u32 voff, u32 lds) {
    asm volatile("s_mov_b32 m0, %2\n\ts_nop 0\n\tglobal_load_lds_dwordx4 %0, %1 nt" :: "v"(voff), "s"(sbase), "s"(lds) : "memory");
}
DI void touch(const void* sbase, u32 voff) {
    asm volatile("s_mov_b32 m0, %2\n\ts_nop 0\n\tglobal_load_lds_dword %0, %1" :: "v"(voff), "s"(sbase), "s"(156928u) : "memory");
}
#define NT_KV 0
#define NT_X 0
#define NT_H 0
#define CODE_LINES 304
#define QDELAY 0
DI void gld64(u32x2& d, const void* sbase, u32 voff) { asm volatile("global_load_dwordx2 %0, %1, %2" : "=v"(d) : "v"(voff), "s"(sbase) : "memory"); }
DI void gld128(u32x4& d, const void* sbase, u32 voff) { asm volatile("global_load_dwordx4 %0, %1, %2" : "=v"(d) : "v"(voff), "s"(sbase) : "memory"); }
DI void gld128c(u32x4& d, const void* sbase, u32 voff) { asm volatile("global_load_dwordx4 %0, %1, %2 sc1" : "=v"(d) : "v"(voff), "s"(sbase) : "memory"); }
DI void gld32c(u32& d, const void* sbase, u32 voff) { asm volatile("global_load_dword %0, %1, %2 sc1" : "=v"(d) : "v"(voff), "s"(sbase) : "memory"); }
#define PIN(x) asm volatile("" : "+v"(x))
#define SB() __builtin_amdgcn_sched_barrier(0)
DI u32x4 ld128c(rsrc_t r, u32 off) { return __builtin_amdgcn_raw_buffer_load_b128(r, off, 0, 16); }
DI u32 ld32c(rsrc_t r, u32 off) { return __builtin_amdgcn_raw_buffer_load_b32(r, off, 0, 16); }
DI void st128c(rsrc_t r, u32 off, u32x4 v) { __builtin_amdgcn_raw_buffer_store_b128(v, r, off, 0, 16); }
DI void st64c(rsrc_t r, u32 off, u32x2 v) { __builtin_amdgcn_raw_buffer_store_b64(v, r, off, 0, 16); }
DI void st32c(rsrc_t r, u32 off, u32 v) { __builtin_amdgcn_raw_buffer_store_b32(v, r, off, 0, 16); }
DI u32x4 lds128(LAS char* sm, u32 a) { return *(LAS u32x4*)(sm + a); }
DI u32x2 lds64(LAS char* sm, u32 a) { return *(LAS u32x2*)(sm + a); }
DI u32x2 ldstr(LAS char* sm, u32 a) { return __builtin_bit_cast(u32x2, __builtin_amdgcn_ds_read_tr16_b64_v4i16((LAS s16x4*)(sm + a))); }
#define VMCNT(n) asm volatile("s_waitcnt vmcnt(" #n ")" ::: "memory")
#define LGKM0() asm volatile("s_waitcnt lgkmcnt(0)" ::: "memory")
#define BAR() do { asm volatile("" ::: "memory"); __builtin_amdgcn_s_barrier(); asm volatile("" ::: "memory"); } while (0)
#define MEMF() asm volatile("" ::: "memory")

DI void set_flag(rsrc_t r, u32 idx, u32 tag) { st32c(r, WS_FLAG + idx * FS, tag); }
DI void wait_flags(rsrc_t r, int idx0, int idx1, u32 tag) {
    unsigned long long t0 = 0;
    for (u32 it = 0;; it++) {
        u32 a = tag, b = tag;
        if (idx0 >= 0) a = ld32c(r, WS_FLAG + idx0 * FS);
        if (idx1 >= 0) b = ld32c(r, WS_FLAG + idx1 * FS);
        if (__all(a == tag && b == tag)) break;
        if (POLL_SLEEP) __builtin_amdgcn_s_sleep(POLL_SLEEP);
        if ((it & 63u) == 0u) {
            const unsigned long long t = __builtin_amdgcn_s_memrealtime();
            if (it == 0) t0 = t;
            else if (t - t0 > 2000000ull) { set_flag(r, F_TMO, tag); break; }
        }
    }
    MEMF();
}
DI void lds_arrive(LAS char* sm, u32 cnt, int lane) { LGKM0(); if (lane == 0) __hip_atomic_fetch_add((LAS u32*)(sm + cnt), 1u, __ATOMIC_RELAXED, __HIP_MEMORY_SCOPE_WORKGROUP); MEMF(); }
DI void lds_wait(LAS char* sm, u32 cnt, u32 n) { MEMF(); while (*(volatile LAS u32*)(sm + cnt) < n) __builtin_amdgcn_s_sleep(0); MEMF(); }
DI void publish_last(rsrc_t r, LAS char* sm, u32 cnt, u32 nw, u32 flag, u32 tag, int lane) {
    VMCNT(0);
    if (lane == 0) {
        const u32 old = __hip_atomic_fetch_add((LAS u32*)(sm + cnt), 1u, __ATOMIC_RELAXED, __HIP_MEMORY_SCOPE_WORKGROUP);
        if (old == nw - 1) set_flag(r, flag, tag);
    }
    MEMF();
}

DI u32 phi2(float a, float b) {
    const f32x2 t = {a, b}, s = t * 1.4426950408889634f;
    const float ea = __builtin_amdgcn_fmed3f(__builtin_amdgcn_exp2f(s.x), 0.f, 1.f), eb = __builtin_amdgcn_fmed3f(__builtin_amdgcn_exp2f(s.y), 0.f, 1.f);
    const float ma = __int_as_float(max(__float_as_int(a), 0)), mb = __int_as_float(max(__float_as_int(b), 0));
    const f32x2 r = ((f32x2){ea, eb} + (f32x2){ma, mb}) + 1e-6f;
    return pk2(r.x, r.y);
}

constexpr int K1_STAGE = 32768;
constexpr int P0_K = 131072, P0_V = P0_K + 9216, P0_OUT = P0_V + 8192;
constexpr int L_CNT = 156672;
constexpr int C_G1 = L_CNT, C_Y = L_CNT + 4, C_P0A = L_CNT + 8, C_P0B = L_CNT + 12, C_P0C = L_CNT + 16, C_FY = L_CNT + 20, C_FY2 = L_CNT + 24, C_FO = L_CNT + 28, C_FR = L_CNT + 32;
constexpr int CV_PITCH = 4160;
constexpr int CV_U = 4 * CV_PITCH;
constexpr int CV_WAVE = CV_U + 4608;
constexpr int CV_BASE = 65536;
constexpr int WO_HI = 65536;
constexpr int F_YY = 110592;
constexpr int F_Y2 = F_YY + 10240;
constexpr int F_GG = F_Y2 + 11264;
constexpr int KO_RED = 110592;
constexpr int KO_KV = KO_RED + 32768;
constexpr int RED_PITCH = 336;
constexpr int LDS_BYTES = 160 * 1024;
DI u32 p0_flag(int tile) { return F_P0 + (tile % 10) * 32 + tile / 10; }
DI constexpr u32 wo_off(int kap) { return kap < 12 ? kap * 5120 : WO_HI + (kap - 12) * 5120; }

DI void p0_issue(const u16* kin, const u16* vin, int tile, int lw, int lane, u32 base) {
    const int cg = tile % 10, rg = tile / 10, s0 = rg * 64;
    const u32 col = cg * 128 + (lane & 7) * 16;
#pragma unroll
    for (int z = 0; z < 2; z++) {
        const int n = 2 * lw + z;
        int r = s0 - 4 + n * 8 + (lane >> 3); r = r < 0 ? 0 : r;
        (NT_KV ? glds16nt : glds16)(kin, (u32)(r * 1280) + col, base + n * 1024);
        (NT_KV ? glds16nt : glds16)(vin, (u32)((s0 + n * 8 + (lane >> 3)) * 1280) + col, base + 9216 + n * 1024);
    }
    {
        int r = s0 + 60 + (lane >> 3); r = r > S - 1 ? S - 1 : r;
        (NT_KV ? glds16nt : glds16)(kin, (u32)(r * 1280) + col, base + 8 * 1024);
    }
}
DI float dot2(u32 a, u32 b, float c) { return __builtin_amdgcn_fdot2_f32_bf16(__builtin_bit_cast(bf16x2, a), __builtin_bit_cast(bf16x2, b), c, false); }
DI void p0_compute(LAS char* sm, u32x2 w4, int tile, int wave, int lane, u32 base, u32 outb, char* ws = nullptr) {
    const int s0 = (tile / 10) * 64;
    const int G = wave * 4 + (lane >> 4), cb = G & 3, i = lane & 15, q = i >> 2, p = i & 3;
    const u32 wa = __builtin_amdgcn_alignbit(w4.y, w4.y, 16), wb = __builtin_amdgcn_alignbit(w4.x, w4.x, 16);
    const int ch = 16 * cb + i;
    u32x2 kq[2][3], vq[2][2];
#pragma unroll
    for (int z = 0; z < 2; z++) {
        const int o = (G >> 2) + 4 * z;
        const u32 ka = base + (8 * o + q) * 128 + 32 * cb + 8 * p, va = ka + 9216;
        kq[z][0] = ldstr(sm, ka); kq[z][1] = ldstr(sm, ka + 512); kq[z][2] = ldstr(sm, ka + 1024);
        vq[z][0] = ldstr(sm, va); vq[z][1] = ldstr(sm, va + 512);
    }
#pragma unroll
    for (int z = 0; z < 2; z++) {
        const int o = (G >> 2) + 4 * z;
        u32 K[6] = {kq[z][0].x, kq[z][0].y, kq[z][1].x, kq[z][1].y, kq[z][2].x, kq[z][2].y};
        if (s0 == 0 && o == 0) { K[0] = 0; K[1] = 0; }
        u32 Sh[5];
#pragma unroll
        for (int e = 0; e < 5; e++) Sh[e] = __builtin_amdgcn_alignbit(K[e + 1], K[e], 16);
        const u32 V[4] = {vq[z][0].x, vq[z][0].y, vq[z][1].x, vq[z][1].y};
        u32 r[4];
#pragma unroll
        for (int e = 0; e < 4; e++) {
            const float k0 = dot2(Sh[e], wa, dot2(Sh[e + 1], wb, 0.f)), k1 = dot2(K[e + 1], wa, dot2(K[e + 2], wb, 0.f));
            r[e] = pk2(k0 * blo(V[e]), k1 * bhi(V[e]));
        }
        *(LAS u32x4*)(sm + outb + ch * 128 + ((o ^ (ch & 7)) * 16)) = (u32x4){r[0], r[1], r[2], r[3]};
    }
}
DI void p0_store(LAS char* sm, rsrc_t rw, int tile, int t256, u32 outb) {
    const int cg = tile % 10, s0 = (tile / 10) * 64;
#pragma unroll
    for (int z = 0; z < 2; z++) {
        const int ch2 = (t256 >> 3) + 32 * z, o2 = t256 & 7;
        const u32x4 t4 = lds128(sm, outb + ch2 * 128 + ((o2 ^ (ch2 & 7)) * 16));
        st128c(rw, WS_UT + (cg * 64 + ch2) * 4096 + (s0 + 8 * o2) * 2, t4);
    }
}
DI void p0_weights(u32x2& w4, const u16* shk, int tile, int wave, int lane) {
    const int cg = tile % 10, cb = (wave * 4 + (lane >> 4)) & 3;
    gld64(w4, shk, (u32)((cg * 64 + 16 * cb + (lane & 15)) * 8));
}

struct K1Set { u32x4 xa[2][2]; u32x2 wl[2][2], wh[2][2]; };
struct K1Addr { u32 x[2][2]; u32 w[2][2]; };
template <bool QT, int WO, int XO>
DI void k1_phase(LAS char* sm, f32x16 (&acc)[2][2], const K1Set& cur, K1Set& nxt, const u32 (&xb)[2], const u32 (&wb)[2][2]) {
#pragma unroll
    for (int n = 0; n < 8; n++) {
        const int ks = n >> 2, i = (n >> 1) & 1, jj = n & 1;
        const u32x4 wf = {cur.wl[ks][jj].x, cur.wl[ks][jj].y, cur.wh[ks][jj].x, cur.wh[ks][jj].y};
        acc[i][jj] = QT ? mfma32(wf, cur.xa[ks][i], acc[i][jj]) : mfma32(cur.xa[ks][i], wf, acc[i][jj]);
        if (n == 0) { nxt.wl[0][0] = ldstr(sm, wb[0][0] + WO); nxt.wh[0][0] = ldstr(sm, wb[0][1] + WO); }
        if (n == 1) { nxt.xa[0][0] = lds128(sm, xb[0] + XO); nxt.wl[0][1] = ldstr(sm, wb[1][0] + WO); }
        if (n == 2) { nxt.wh[0][1] = ldstr(sm, wb[1][1] + WO); nxt.xa[0][1] = lds128(sm, xb[0] + XO + 4096); }
        if (n == 3) { nxt.wl[1][0] = ldstr(sm, wb[0][0] + WO + 4096); nxt.wh[1][0] = ldstr(sm, wb[0][1] + WO + 4096); }
        if (n == 4) { nxt.xa[1][0] = lds128(sm, xb[1] + XO); nxt.wl[1][1] = ldstr(sm, wb[1][0] + WO + 4096); }
        if (n == 5) { nxt.wh[1][1] = ldstr(sm, wb[1][1] + WO + 4096); nxt.xa[1][1] = lds128(sm, xb[1] + XO + 4096); }
        SB();
    }
}
template <bool QT, int ST>
DI void k1_chunk(LAS char* sm, f32x16 (&acc)[2][2], K1Set& s0, K1Set& s1, const K1Addr& a0, const K1Addr& a1, bool bar, bool drain = false) {
    constexpr int SN = (ST + 1) & 3;
    const K1Addr& ac = ST < 2 ? a0 : a1; const K1Addr& an = SN < 2 ? a0 : a1;
    k1_phase<QT, (ST & 1) * K1_STAGE + 8192, (ST & 1) * K1_STAGE + 16384>(sm, acc, s0, s1, ac.x[1], ac.w);
    if (drain) VMCNT(0);
    if (bar) { LGKM0(); BAR(); }
    k1_phase<QT, (SN & 1) * K1_STAGE, (SN & 1) * K1_STAGE + 16384>(sm, acc, s1, s0, an.x[0], an.w);
}
template <bool QT>
DI void k1_loop(LAS char* sm, const K1Addr& a0, f32x16 (&acc)[2][2], rsrc_t rw, u32 pflag, u32 tag, int wave, int lane, const u16* hk, const u16* wo, int n240) {
    K1Addr a1;
#pragma unroll
    for (int i = 0; i < 2; i++)
#pragma unroll
        for (int z = 0; z < 2; z++) { a1.x[i][z] = a0.x[i][z] + 65536; a1.w[i][z] = a0.w[i][z] + 65536; }
    K1Set s0, s1;
#pragma unroll
    for (int ks = 0; ks < 2; ks++)
#pragma unroll
        for (int jj = 0; jj < 2; jj++) {
            s0.wl[ks][jj] = ldstr(sm, a0.w[jj][0] + ks * 4096); s0.wh[ks][jj] = ldstr(sm, a0.w[jj][1] + ks * 4096);
            s0.xa[ks][jj] = lds128(sm, a0.x[0][ks] + 16384 + jj * 4096);
        }
    SB();
#pragma unroll 1
    for (int c = 0; c < 8; c += 4) {
        k1_chunk<QT, 0>(sm, acc, s0, s1, a0, a1, true);
        k1_chunk<QT, 1>(sm, acc, s0, s1, a0, a1, true);
        k1_chunk<QT, 2>(sm, acc, s0, s1, a0, a1, true);
        k1_chunk<QT, 3>(sm, acc, s0, s1, a0, a1, true, c == 0);
        if (c == 0 && wave == 0 && lane == 0) set_flag(rw, pflag, tag);
        if (PREF2 && c == 0 && wave >= 2) {
            if (wave == 2) { const u32 l_ = n240 * 86 + lane; touch(hk, (l_ < 20480 ? l_ : 20479) * 128); }
            else { const u32 l_ = n240 * 86 + 64 + lane, m_ = n240 * 27 + lane - 22;
                   if (lane < 22) touch(hk, (l_ < 20480 ? l_ : 20479) * 128); else if (lane < 49) touch(wo, (m_ < 6400 ? m_ : 6399) * 128); }
        }
    }
    k1_chunk<QT, 0>(sm, acc, s0, s1, a0, a1, true);
    k1_chunk<QT, 1>(sm, acc, s0, s1, a0, a1, false);
}

DI void wo_issue(const u16* wo, int ct, int k0, int k1, int w, int nw, int lane) {
    u32 vo[5];
#pragma unroll
    for (int i = 0; i < 5; i++) { const int n = 64 * i + lane, rho = n / 10, c = n - 10 * rho, row = (rho & 19) | ((rho & 4) << 1) | ((rho & 8) >> 1); vo[i] = (u32)(row * 1280 + ct * 160 + c * 16); }
#pragma unroll 1
    for (int kap = k0 + w; kap < k1; kap += nw) {
        const char* src = (const char*)wo + (size_t)kap * 40960;
        const u32 dst = kap < 12 ? kap * 5120 : WO_HI + (kap - 12) * 5120;
#pragma unroll
        for (int i = 0; i < 5; i++) glds16(src, vo[i], dst + i * 1024);
    }
}

struct CvSet { u32x2 fl[8], fh[8]; u32x4 g[8]; };
DI void cv_load(LAS char* sm, CvSet& s, u32 fa, u32 ga) {
#pragma unroll
    for (int k1 = 0; k1 < 8; k1++) { s.fl[k1] = ldstr(sm, fa + 512 * k1 + 8); s.fh[k1] = ldstr(sm, fa + 512 * k1); s.g[k1] = lds128(sm, ga + 512 * k1); }
}
DI void cv_phase(LAS char* sm, f32x4 (&Y)[8], const CvSet& cur, CvSet& nxt, u32 fa, u32 ga) {
    PIN(fa); PIN(ga);
    SB();
    int n = 0;
#pragma unroll
    for (int e = 0; e < 8; e++)
#pragma unroll
        for (int k1 = 0; k1 + e < 8; k1++) {
            const u32x4 F = {cur.fl[k1].x, cur.fl[k1].y, cur.fh[k1].x, cur.fh[k1].y};
            Y[k1 + e] = mfma16(F, cur.g[e], Y[k1 + e]);
            if (n == 0) nxt.g[0] = lds128(sm, ga);
            else if (n <= 16) { const int r = (n - 1) >> 1; if ((n - 1) & 1) nxt.fh[r] = ldstr(sm, fa + 512 * r); else nxt.fl[r] = ldstr(sm, fa + 512 * r + 8); }
            else if (n < 24) nxt.g[n - 16] = lds128(sm, ga + 512 * (n - 16));
            n++;
            SB();
        }
}
struct CvIn { u32x4 hd[4]; u32x2 hm[4]; };
DI void conv_fetch_h(CvIn& in, const u16* hk, int c, int lane) {
    const char* hb = (const char*)hk + (size_t)c * 4096;
#pragma unroll
    for (int z = 0; z < 4; z++) {
        const int i = lane + 64 * z;
        if (NT_H) asm volatile("global_load_dwordx4 %0, %1, %2 nt" : "=v"(in.hd[z]) : "v"((u32)(i * 16)), "s"(hb) : "memory"); else gld128(in.hd[z], hb, (u32)(i * 16));
        gld64(in.hm[z], hb, (u32)(i > 0 ? i * 16 - 8 : 0));
    }
}
DI void conv_build_h(LAS char* sm0, CvIn& in, int w, int lane) {
    LAS char* sm = sm0 + CV_BASE + w * CV_WAVE;
#pragma unroll
    for (int z = 0; z < 4; z++) { PIN(in.hd[z]); PIN(in.hm[z]); }
    if (lane < 32) *(LAS u32x4*)(sm + CV_U + lane * 16) = (u32x4){0, 0, 0, 0};
    if (lane < 8) *(LAS u32x4*)(sm + (lane >> 1) * CV_PITCH + (lane & 1) * 16) = (u32x4){0, 0, 0, 0};
#pragma unroll
    for (int z = 0; z < 4; z++) {
        const int i = lane + 64 * z;
        u32 m2 = in.hm[z].x, m1 = in.hm[z].y;
        if (i == 0) { m2 = 0; m1 = 0; }
        const u32 d0 = in.hd[z].x, d1 = in.hd[z].y, d2 = in.hd[z].z, d3 = in.hd[z].w;
        const u32 om2 = __builtin_amdgcn_alignbit(m1, m2, 16), om1 = __builtin_amdgcn_alignbit(d0, m1, 16), o0 = __builtin_amdgcn_alignbit(d1, d0, 16),
                  o1 = __builtin_amdgcn_alignbit(d2, d1, 16), o2 = __builtin_amdgcn_alignbit(d3, d2, 16);
        LAS char* p = sm + 32 + i * 16;
        *(LAS u32x4*)(p) = (u32x4){d0, d1, d2, d3};
        *(LAS u32x4*)(p + CV_PITCH) = (u32x4){om1, o0, o1, o2};
        *(LAS u32x4*)(p + 2 * CV_PITCH) = (u32x4){m1, d0, d1, d2};
        *(LAS u32x4*)(p + 3 * CV_PITCH) = (u32x4){om2, om1, o0, o1};
    }
}
DI void conv_run(LAS char* sm0, float Dc, int w, int lane, int c, rsrc_t rw, char* ws) {
    LAS char* sm = sm0;
    LGKM0();
    TS(4, 9);
    const int fr = lane & 15, g = lane >> 4, q = fr >> 2, p = lane & 3;
    sm = sm0;
    const u32 fa = CV_BASE + w * CV_WAVE + q * CV_PITCH + 2 * (32 - 8 * g + 4 * p) - 8;
    const u32 ga = CV_BASE + w * CV_WAVE + CV_U + 480 + 32 * fr + 16 * g;
    f32x4 Y[8];
#pragma unroll
    for (int i = 0; i < 8; i++) Y[i] = (f32x4){0, 0, 0, 0};
    CvSet A, B;
    cv_load(sm, A, fa, ga);
    SB();
#pragma unroll 1
    for (int k0 = 0; k0 < 8; k0 += 2) {
        cv_phase(sm, Y, A, B, fa + 64 * (k0 + 1), ga - 64 * (k0 + 1));
        cv_phase(sm, Y, B, A, fa + 64 * (k0 + 2), ga - 64 * (k0 + 2));
    }
    TS(4, 10);
    sm = sm0 + CV_BASE + w * CV_WAVE;
    {
        u32x2 u4[8];
#pragma unroll
        for (int ib = 0; ib < 8; ib++) u4[ib] = lds64(sm, CV_U + 512 + 512 * ib + 32 * fr + 8 * g);
#pragma unroll
        for (int ib = 0; ib < 8; ib++) {
            const u32x2 r = {pk2(fmaf(blo(u4[ib].x), Dc, Y[ib][0]), fmaf(bhi(u4[ib].x), Dc, Y[ib][1])), pk2(fmaf(blo(u4[ib].y), Dc, Y[ib][2]), fmaf(bhi(u4[ib].y), Dc, Y[ib][3]))};
            *(LAS u32x2*)(sm + 512 * ib + 32 * fr + 8 * g) = r;
        }
    }
    LGKM0();
    u32x4 y8[4];
#pragma unroll
    for (int z = 0; z < 4; z++) y8[z] = lds128(sm, z * 1024 + lane * 16);
#pragma unroll
    for (int z = 0; z < 4; z++) st128c(rw, WS_YT + c * 4096 + z * 1024 + lane * 16, y8[z]);
}

DI void kvo_block(LAS char* sm, int wave, int lane, int h, int half, rsrc_t rw, u32 tag, char* ws) {
    const int fr = lane & 15, g = lane >> 4;
    const int j0 = (20 * h) >> 7, j1 = (20 * h + 19) >> 7;
    wait_flags(rw, lane < 8 ? (int)F_G1 + 16 * (((lane & 2) ? 10 : 5) + ((lane & 1) ? j1 : j0)) + 2 * wave + (lane >> 2)
                            : lane < 10 ? (int)F_G1 + 16 * ((lane & 1) ? j1 : j0) + 8 * half + wave : -1, -1, tag);
    TS(0, 8);
    const u32 pk0 = WS_PK0 + (h * 64 + 8 * wave) * 1024 + lane * 16, v0 = pk0 + (WS_V0 - WS_PK0);
    const u32 xx = WS_X + (h * 32 + 4 * wave) * 1024 + lane * 16;
    u32x4 A[8], B[8], X[4], Q[8];
#pragma unroll
    for (int i = 0; i < 8; i++) { A[i] = ld128c(rw, pk0 + i * 1024); B[i] = ld128c(rw, v0 + i * 1024); if ((i & 1) == 0) X[i >> 1] = ld128c(rw, xx + (i >> 1) * 1024); }
    const int odd = h & 1, pr = h >> 1;
    const int u5 = (g < 3 ? g : 2) + 2 * odd;
    const int sb0 = half * 64 + wave * 8;
    const u32 pq = WS_P2 + (((pr * 128 + sb0) * 5 + u5) * 128 + fr * 8) * 2;
    MEMF(); SB();
    f32x4 a00 = {0, 0, 0, 0}, a0x = a00, ax0 = a00, axx = a00;
    const u32 cst = fr == 8 ? 0x3F803F80u : 0u;
#pragma unroll
    for (int i = 0; i < 8; i++) {
        u32x4 xv = X[i >> 1];
        if (i & 1) {
#pragma unroll
            for (int e = 0; e < 4; e++) xv[e] = (u32)__builtin_amdgcn_update_dpp(0, (int)xv[e], 0x108, 0xf, 0xf, false);
        }
        if (fr >= 8) xv = (u32x4){cst, cst, cst, cst};
        a00 = mfma16(A[i], B[i], a00); a0x = mfma16(A[i], xv, a0x); ax0 = mfma16(xv, B[i], ax0); axx = mfma16(xv, xv, axx);
    }
    PIN(a00); PIN(a0x); PIN(ax0); PIN(axx);
    SB();
#pragma unroll
    for (int i = 0; i < 8; i++) Q[i] = ld128c(rw, pq + i * 1280);
    MEMF(); SB();
    TS(0, 9);
    *(LAS f32x4*)(sm + KO_RED + ((wave * 4 + 0) * 64 + lane) * 16) = a00;
    *(LAS f32x4*)(sm + KO_RED + ((wave * 4 + 1) * 64 + lane) * 16) = a0x;
    *(LAS f32x4*)(sm + KO_RED + ((wave * 4 + 2) * 64 + lane) * 16) = ax0;
    *(LAS f32x4*)(sm + KO_RED + ((wave * 4 + 3) * 64 + lane) * 16) = axx;
    if (wave >= 4 && (wave - 4) * 64 + lane < 189) *(LAS u32x2*)(sm + KO_KV + ((wave - 4) * 64 + lane) * 8) = (u32x2){0, 0};
    LGKM0(); BAR();
    if (wave < 4) {
        const int tile = wave;
        f32x4 s = *(LAS f32x4*)(sm + KO_RED + (tile * 64 + lane) * 16);
#pragma unroll
        for (int w2 = 1; w2 < 8; w2++) s += *(LAS f32x4*)(sm + KO_RED + ((w2 * 4 + tile) * 64 + lane) * 16);
        const int e = (tile & 1) ? 12 + fr : fr;
        const bool colok = (tile & 1) ? (fr >= 4 && fr <= 8) : true;
        const bool rowok = (tile & 2) ? (g == 0) : true;
        const int d0 = (tile & 2) ? 16 : 4 * g;
        if (colok && rowok) *(LAS u32x2*)(sm + KO_KV + e * 72 + (d0 + 4) * 2) = (u32x2){pk2(s[0], s[1]), pk2(s[2], s[3])};
    }
    LGKM0(); BAR();
    u32x4 A0, A1[4], A2;
    {
        const u32 dofs = KO_KV + (8 * g - 4 * odd + 4) * 2;
        const u32 r0 = (4 * odd + fr) * 72, r1 = ((odd ? 0 : 16) + (fr & 3)) * 72;
        const u32x2 p0 = lds64(sm, dofs + r0), p1 = lds64(sm, dofs + r0 + 8);
        const u32x2 q0 = lds64(sm, dofs + r1), q1 = lds64(sm, dofs + r1 + 8);
        const u32x2 z0 = lds64(sm, dofs + 20 * 72), z1 = lds64(sm, dofs + 20 * 72 + 8);
        A0 = (u32x4){p0.x, p0.y, p1.x, p1.y}; A2 = (u32x4){z0.x, z0.y, z1.x, z1.y};
#pragma unroll
        for (int z = 0; z < 4; z++) { const bool m = (fr >> 2) == z; A1[z] = (u32x4){m ? q0.x : 0u, m ? q0.y : 0u, m ? q1.x : 0u, m ? q1.y : 0u}; }
    }
    TS(0, 10);
    const int U0 = (5 * h + odd) >> 1;
    const int Uw = U0 + (g >> 1);
    const u32 offA = WS_O + (((Uw >> 2) * 4 + (Uw & 3)) * 128 + fr * 8) * 2 + (g & 1) * 20480;
    const int cgx = 5 * h + (odd ? 0 : 4), Ux = cgx >> 1;
    const u32 offX = WS_O + (((Ux >> 2) * 4 + (Ux & 3)) * 128 + fr * 8 + (cgx & 1) * 4) * 2;
    const f32x4 zero = {0, 0, 0, 0};
#pragma unroll
    for (int i4 = 0; i4 < 2; i4++) {
        f32x4 n1 = zero; float rden[4]; u32x2 ov[4];
#pragma unroll
        for (int z = 0; z < 4; z++) {
            const int i = 4 * i4 + z;
            const f32x4 n0 = mfma16(A0, Q[i], zero);
            const f32x4 dn = mfma16(A2, Q[i], zero);
            n1 = mfma16(A1[z], Q[i], n1);
            const float rd = __builtin_amdgcn_rcpf(dn[0] + 1e-6f);
            rden[z] = rd;
            ov[z] = (u32x2){pk2(n0[0] * rd, n0[1] * rd), pk2(n0[2] * rd, n0[3] * rd)};
        }
#pragma unroll
        for (int z = 0; z < 4; z += 2) {
            auto rx = __builtin_amdgcn_permlane16_swap(ov[z].x, ov[z + 1].x, false, false);
            auto ry = __builtin_amdgcn_permlane16_swap(ov[z].y, ov[z + 1].y, false, false);
            st128c(rw, offA + (sb0 + 4 * i4 + z) * 20480, (u32x4){rx[0], ry[0], rx[1], ry[1]});
        }
        const float rd = g == 0 ? rden[0] : g == 1 ? rden[1] : g == 2 ? rden[2] : rden[3];
        st64c(rw, offX + (sb0 + 4 * i4 + g) * 20480, (u32x2){pk2(n1[0] * rd, n1[1] * rd), pk2(n1[2] * rd, n1[3] * rd)});
    }
}

DI void gate_block(int wave, int lane, int m, const u16* x, const u16* gw, rsrc_t rw) {
    const int sub = lane & 15, r4 = lane >> 4;
    u32x4 gq[5], xr[4][5];
#pragma unroll
    for (int jj = 0; jj < 5; jj++) gq[jj] = *(const GAS u32x4*)((const GAS char*)gw + (sub + 16 * jj) * 16);
#pragma unroll
    for (int it = 0; it < 4; it++)
#pragma unroll
        for (int jj = 0; jj < 5; jj++) xr[it][jj] = *(const GAS u32x4*)((const GAS char*)x + (size_t)(128 * m + 16 * wave + 4 * it + r4) * 1280 + (sub + 16 * jj) * 16);
    MEMF(); SB();
#pragma unroll
    for (int it = 0; it < 4; it++) {
        float a = 0.f;
#pragma unroll
        for (int jj = 0; jj < 5; jj++)
#pragma unroll
            for (int e = 0; e < 4; e++) { a = fmaf(blo(xr[it][jj][e]), blo(gq[jj][e]), a); a = fmaf(bhi(xr[it][jj][e]), bhi(gq[jj][e]), a); }
        a += __shfl_xor(a, 1); a += __shfl_xor(a, 2); a += __shfl_xor(a, 4); a += __shfl_xor(a, 8);
        a = rbf(a);
        const float g = rbf(__builtin_amdgcn_rcpf(1.f + __builtin_amdgcn_exp2f(-1.4426950408889634f * a)));
        if (sub == 0) st32c(rw, WS_G + (128 * m + 16 * wave + 4 * it + r4) * 4, __float_as_uint(g));
    }
}

DI void final_block(LAS char* sm, int wave, int lane, int tid, int st, int ct, const u16* x, u16* out, rsrc_t rw, u32 tag, char* ws) {
    const int fr = lane & 15, g = lane >> 4;
    u32x4 xv[2];
#pragma unroll
    for (int r = 0; r < 2; r++) {
        int it = tid + 512 * r; it = it < 640 ? it : 639;
        const int s = it / 10, u = it - 10 * s;
        xv[r] = *(const GAS u32x4*)((const GAS char*)x + (size_t)(st * 64 + s) * 1280 + ct * 160 + u * 16);
    }
    VMCNT(0); BAR();
    if (wave < 4) {
        const int kh = wave & 1, sh = wave >> 1;
        wait_flags(rw, lane < 16 ? (int)F_O + (st >> 4) * 32 + 16 * kh + lane : -1, -1, tag);
        TS(0, 17);
        const u32 ob = WS_O + ((st * 4 + 2 * sh) * 20 + 10 * kh) * 1024 + lane * 16;
        u32x4 B[2][10];
#pragma unroll
        for (int kl = 0; kl < 10; kl++) { B[0][kl] = ld128c(rw, ob + kl * 1024); B[1][kl] = ld128c(rw, ob + 20480 + kl * 1024); }
        MEMF(); SB();
        f32x4 acc[5][2];
#pragma unroll
        for (int cb = 0; cb < 5; cb++) { acc[cb][0] = (f32x4){0, 0, 0, 0}; acc[cb][1] = (f32x4){0, 0, 0, 0}; }
        const u32 aoff = (16 * (g >> 1) + 4 * (g & 1) + (fr >> 2)) * 160 + (lane & 3) * 8;
        u32x2 al[2][5], ah[2][5];
#define F_READ(buf, kl) do { const u32 b0_ = kh ? wo_off(10 + (kl)) : wo_off(kl); \
            _Pragma("unroll") for (int cb = 0; cb < 5; cb++) { al[buf][cb] = ldstr(sm, b0_ + aoff + cb * 32); ah[buf][cb] = ldstr(sm, b0_ + aoff + cb * 32 + 1280); } } while (0)
        F_READ(0, 0);
#pragma unroll
        for (int kl = 0; kl < 10; kl++) {
            if (kl < 9) F_READ((kl + 1) & 1, kl + 1);
            SB();
#pragma unroll
            for (int cb = 0; cb < 5; cb++) {
                const u32x4 a = {al[kl & 1][cb].x, al[kl & 1][cb].y, ah[kl & 1][cb].x, ah[kl & 1][cb].y};
                acc[cb][0] = mfma16(a, B[0][kl], acc[cb][0]); acc[cb][1] = mfma16(a, B[1][kl], acc[cb][1]);
            }
            SB();
        }
        TS(0, 18);
        lds_arrive(sm, C_FR, lane); lds_wait(sm, C_FR, 4);
#pragma unroll
        for (int cb = 0; cb < 5; cb++)
#pragma unroll
            for (int z = 0; z < 2; z++)
                *(LAS f32x4*)(sm + kh * (64 * RED_PITCH) + (16 * (2 * sh + z) + fr) * RED_PITCH + (16 * cb + 4 * g) * 4) = acc[cb][z];
    } else {
        if (wave == 4) { wait_flags(rw, lane < 20 ? (int)F_Y + ct * 32 + lane : lane == 32 ? (int)F_GATE + (st >> 1) : -1, -1, tag); lds_arrive(sm, C_FY, lane); }
        else lds_wait(sm, C_FY, 1);
        const int t = tid - 256;
        u32x4 yv[3];
#pragma unroll
        for (int r = 0; r < 3; r++) { int id = t + 256 * r; id = id < 640 ? id : 639; yv[r] = ld128c(rw, WS_YT + (80 * ct + (id >> 3)) * 4096 + st * 128 + (id & 7) * 16); }
#pragma unroll
        for (int r = 0; r < 3; r++) { const int id = t + 256 * r; if (id < 640) *(LAS u32x4*)(sm + F_YY + id * 16) = yv[r]; }
        if (wave == 4) *(LAS u32*)(sm + F_GG + lane * 4) = ld32c(rw, WS_G + (st * 64 + lane) * 4);
        lds_arrive(sm, C_FY2, lane); lds_wait(sm, C_FY2, 4);
        const int sb = wave - 4, q = fr >> 2, p = lane & 3;
        u32x2 tt[5];
#pragma unroll
        for (int cb = 0; cb < 5; cb++) tt[cb] = ldstr(sm, F_YY + (16 * cb + 4 * g + q) * 128 + (16 * sb + 4 * p) * 2);
#pragma unroll
        for (int cb = 0; cb < 5; cb++) *(LAS u32x2*)(sm + F_Y2 + (16 * sb + fr) * 176 + (16 * cb + 4 * g) * 2) = tt[cb];
    }
    LGKM0(); BAR();
#pragma unroll
    for (int r = 0; r < 2; r++) {
        const int it = tid + 512 * r;
        if (it < 640) {
            const int s = it / 10, u = it - 10 * s;
            const u32 ra = s * RED_PITCH + u * 32;
            const f32x4 p0 = *(LAS f32x4*)(sm + ra), p1 = *(LAS f32x4*)(sm + ra + 16), q0 = *(LAS f32x4*)(sm + 64 * RED_PITCH + ra), q1 = *(LAS f32x4*)(sm + 64 * RED_PITCH + ra + 16);
            const u32x4 y8 = lds128(sm, F_Y2 + s * 176 + u * 16);
            const float gg = *(LAS float*)(sm + F_GG + s * 4), omg = 1.f - gg;
            float o[8];
#pragma unroll
            for (int e = 0; e < 4; e++) {
                const float a0 = (e < 2 ? p0[2 * e] + q0[2 * e] : p1[2 * e - 4] + q1[2 * e - 4]), a1 = (e < 2 ? p0[2 * e + 1] + q0[2 * e + 1] : p1[2 * e - 3] + q1[2 * e - 3]);
                const float b0 = omg * blo(xv[r][e]), b1 = omg * bhi(xv[r][e]);
                o[2 * e] = fmaf(b0, blo(y8[e]), gg * a0); o[2 * e + 1] = fmaf(b1, bhi(y8[e]), gg * a1);
            }
            *(GAS u32x4*)((GAS char*)out + (size_t)(st * 64 + s) * 1280 + ct * 160 + u * 16) = (u32x4){pk2(o[0], o[1]), pk2(o[2], o[3]), pk2(o[4], o[5]), pk2(o[6], o[7])};
        }
    }
}

__global__ __launch_bounds__(512, 2) void fused_kernel(const u16* __restrict__ x, const u16* __restrict__ w, const u16* __restrict__ wo, const u16* __restrict__ kin,
                                                        const u16* __restrict__ vin, const u16* __restrict__ shk, const u16* __restrict__ hk, const u16* __restrict__ sD,
                                                        const u16* __restrict__ gw, u16* __restrict__ out, char* ws) {
    extern __shared__ __attribute__((aligned(16))) char smem[];
    LAS char* sm = (LAS char*)smem;
    const int tid = threadIdx.x, lane = tid & 63, wave = __builtin_amdgcn_readfirstlane(tid >> 6);
    const int b = blockIdx.x, xcd = b & 7, j = b >> 3;
    const rsrc_t rw = __builtin_amdgcn_make_buffer_rsrc(ws, 0, WS_BYTES, 0x00020000);
    u32 tag;
    asm volatile("s_load_dword %0, %1, 0x0\n\ts_waitcnt lgkmcnt(0)" : "=s"(tag) : "s"(ws + WS_FLAG + F_EPOCH * FS) : "memory");
    tag += 1u;
    if (tid < 16) *(LAS u32*)(sm + L_CNT + tid * 4) = 0u;
    TS(0, 0);
    if (PREFC && wave == 3) {
        unsigned long long pc;
        asm volatile("s_getpc_b64 %0" : "=s"(pc));
        pc &= ~127ull;
        const int ln = PREFC == 1 ? j * 10 + lane : lane;
        if (PREFC == 1) { if (lane < 10 && ln < CODE_LINES) touch((const void*)pc, (u32)(ln * 128)); }
        else {
#pragma unroll
            for (int z = 0; z < 5; z++) if (z * 64 + lane < CODE_LINES) touch((const void*)pc, (u32)((z * 64 + lane) * 128));
        }
    }
    const int st = 4 * xcd + (j >> 3), ct = j & 7;
    int ko = -1;
    bool is_conv = false;

    if (j >= 30) {
        const int m = xcd * 2 + (j - 30);
        ko = -100;
        {
            const int grp = wave >> 2, w4i = wave & 3;
            u32x2 w4[3];
#pragma unroll
            for (int t = 0; t < 3; t++) { const int tt = 2 * t + grp < 5 ? 2 * t + grp : 4; p0_weights(w4[t], shk, 240 + 5 * m + tt, w4i, lane); }
#pragma unroll
            for (int t = 0; t < 3; t++) if (2 * t + grp < 5) p0_issue(kin, vin, 240 + 5 * m + 2 * t + grp, w4i, lane, (2 * t + grp) * 17408);
            VMCNT(0);
#pragma unroll
            for (int t = 0; t < 3; t++) PIN(w4[t]);
            BAR();
#pragma unroll
            for (int t = 0; t < 3; t++)
                if (2 * t + grp < 5) p0_compute(sm, w4[t], 240 + 5 * m + 2 * t + grp, w4i, lane, (2 * t + grp) * 17408, 90112 + (2 * t + grp) * 8192);
            LGKM0(); BAR();
            {
                u32x4 t4[3][2];
#pragma unroll
                for (int t = 0; t < 3; t++)
#pragma unroll
                    for (int z = 0; z < 2; z++) { const int ch2 = ((tid & 255) >> 3) + 32 * z, o2 = tid & 7, tt = 2 * t + grp < 5 ? 2 * t + grp : 4; t4[t][z] = lds128(sm, 90112 + tt * 8192 + ch2 * 128 + ((o2 ^ (ch2 & 7)) * 16)); }
#pragma unroll
                for (int t = 0; t < 3; t++)
                    if (2 * t + grp < 5) {
                        const int tile = 240 + 5 * m + 2 * t + grp, cg = tile % 10, s0 = (tile / 10) * 64;
#pragma unroll
                        for (int z = 0; z < 2; z++) { const int ch2 = ((tid & 255) >> 3) + 32 * z, o2 = tid & 7; st128c(rw, WS_UT + (cg * 64 + ch2) * 4096 + (s0 + 8 * o2) * 2, t4[t][z]); }
                    }
            }
        }
        VMCNT(0); BAR();
        if (tid < 5) set_flag(rw, p0_flag(240 + 5 * m + tid), tag);
        TS(0, 1);
        gate_block(wave, lane, m, x, gw, rw);
        VMCNT(0); LGKM0(); BAR();
        if (tid == 0) set_flag(rw, F_GATE + m, tag);
        TS(0, 3);
    } else {
        const int rt = 2 * xcd + j / 15, cbk = j % 15, tile = xcd * 30 + j;
        is_conv = cbk < 10;
        if (!is_conv) { const int vi = rt * 5 + (cbk - 10); ko = vi < 64 ? vi : -2 - (vi - 64); }
        if (wave >= 4) {
            const int lw = wave - 4;
            p0_issue(kin, vin, tile, lw, lane, P0_K);
            u32 vx[4], vw[4];
#pragma unroll
            for (int n2 = 0; n2 < 4; n2++) {
                const int n = 4 * lw + n2;
                const int row = 8 * n + (lane >> 3), ch = (lane & 7) ^ ((row >> 1) & 7);
                vx[n2] = (u32)((rt * 128 + row) * 1280 + ch * 16);
                const int kr = 4 * n + (lane >> 4), f = ((kr & 3) << 2) | ((kr >> 2) & 3), cw = (lane & 15) ^ f;
                vw[n2] = (u32)(kr * 3840 + cbk * 256 + cw * 16);
            }
            const int rot = (rt + cbk) % 10;
#define K1_ISSUE(c) do { const u32 st_ = ((c) & 3) * K1_STAGE + 4 * lw * 1024; int kc_ = (c) + ROT_ON * rot; kc_ = kc_ >= 10 ? kc_ - 10 : kc_; \
            const char* xb_ = (const char*)x + kc_ * 128; const char* wp_ = (const char*)w + (size_t)kc_ * 64 * 3840; \
            _Pragma("unroll") for (int n2 = 0; n2 < 4; n2++) { if (NT_X) glds16nt(xb_, vx[n2], st_ + 16384 + n2 * 1024); else glds16(xb_, vx[n2], st_ + 16384 + n2 * 1024); glds16(wp_, vw[n2], st_ + n2 * 1024); } } while (0)
            K1_ISSUE(0);
            VMCNT(8); BAR();
            K1_ISSUE(1); K1_ISSUE(2);
            VMCNT(16); BAR();
            K1_ISSUE(3);
#pragma unroll 1
            for (int c = 0; c < 6; c++) {
                VMCNT(16); BAR();
                K1_ISSUE(c + 4);
            }
            const int cv = rt * 10 + cbk, c = 4 * cv + lw;
            if (is_conv) {
                CvIn in; u32 fl; u32x4 u[4];
                const int fidx = (int)F_P0 + (cv >> 4) * 32 + (lane & 31);
                conv_fetch_h(in, hk, c, lane);
                VMCNT(24); BAR();
                VMCNT(16); BAR();
                gld32c(fl, ws, WS_FLAG + fidx * FS);
                VMCNT(9); BAR();
                TS(4, 4);
                VMCNT(0); PIN(fl);
                const bool ready = __all(fl == tag);
                if (ready) {
#pragma unroll
                    for (int z = 0; z < 4; z++) gld128c(u[z], ws, WS_UT + c * 4096 + z * 1024 + lane * 16);
                }
                TS(4, 5);
                conv_build_h(sm, in, lw, lane);
                TS(4, 6);
                if (!ready) {
                    wait_flags(rw, fidx, -1, tag);
#pragma unroll
                    for (int z = 0; z < 4; z++) gld128c(u[z], ws, WS_UT + c * 4096 + z * 1024 + lane * 16);
                }
                TS(4, 7);
                VMCNT(0);
#pragma unroll
                for (int z = 0; z < 4; z++) { PIN(u[z]); *(LAS u32x4*)(sm + CV_BASE + lw * CV_WAVE + CV_U + 512 + z * 1024 + lane * 16) = u[z]; }
                const float Dc = blo((u32)sD[c]);
                TS(4, 8);
                conv_run(sm, Dc, lw, lane, c, rw, ws);
                publish_last(rw, sm, C_Y, 4, F_Y + (cv / 20) * 32 + cv % 20, tag, lane);
                TS(4, 11);
            } else {
                VMCNT(16); BAR();
                VMCNT(8); BAR();
                VMCNT(0); BAR();
            }
        } else {
            u32x2 w4; p0_weights(w4, shk, tile, wave, lane);
            if (PREF1 && wave < 3) {
                const int n = wave * 64 + lane;
                if (wave == 0) touch(x, (u32)((xcd * 2560 + j * 80 + lane) * 128));
                else if (wave == 1) { if (lane < 16) touch(x, (u32)((xcd * 2560 + j * 80 + 64 + lane) * 128)); else touch(w, (u32)(((xcd * 30 + j) * 80 + lane - 16) * 128)); }
                else if (lane < 32) touch(w, (u32)(((xcd * 30 + j) * 80 + 48 + lane) * 128));
                (void)n;
            }
            const int wm = wave >> 1, wn = wave & 1, r32 = lane & 31, hh = lane >> 5;
            K1Addr a0;
#pragma unroll
            for (int ks = 0; ks < 2; ks++) {
                a0.x[0][ks] = (64 * wm + r32) * 128 + (((2 * ks + hh) ^ ((r32 >> 1) & 7)) * 16);
                a0.x[1][ks] = a0.x[0][ks] ^ 64;
            }
#pragma unroll
            for (int jj = 0; jj < 2; jj++)
#pragma unroll
                for (int t = 0; t < 2; t++) {
                    const int blk = (lane >> 4) & 1, q = (lane & 15) >> 2, p = lane & 3, row = 8 * hh + 4 * t + q, ch = 8 * wn + 4 * jj + 2 * blk + (p >> 1);
                    const int f = ((row & 3) << 2) | ((row >> 2) & 3);
                    a0.w[jj][t] = row * 256 + ((ch ^ f) * 16) + 8 * (p & 1);
                }
            f32x16 acc[2][2];
#pragma unroll
            for (int i = 0; i < 2; i++)
#pragma unroll
                for (int jj = 0; jj < 2; jj++)
#pragma unroll
                    for (int e = 0; e < 16; e++) acc[i][jj][e] = 0.f;
            TS(0, 20);
            if (PREF1 && wave == 1) VMCNT(2); else if (PREF1 && wave < 3) VMCNT(1); else VMCNT(0);
            PIN(w4); TS(0, 21); BAR();
            TS(0, 22);
            p0_compute(sm, w4, tile, wave, lane, P0_K, P0_OUT, ws);
            TS(0, 28);
            LGKM0(); TS(0, 23); BAR();
            TS(0, 24);
            p0_store(sm, rw, tile, tid, P0_OUT);
            TS(0, 1);
            if (cbk < 5) {
                k1_loop<true>(sm, a0, acc, rw, p0_flag(tile), tag, wave, lane, hk, wo, tile);
                __builtin_amdgcn_s_setprio(2);
                TS(0, 2);
                u32x4 qv[2][2][2];
#pragma unroll
                for (int jj = 0; jj < 2; jj++)
#pragma unroll
                    for (int ap = 0; ap < 2; ap++)
#pragma unroll
                        for (int i = 0; i < 2; i++) {
                            const f32x16& a = acc[i][jj];
                            const u32 x0 = phi2(a[8 * ap], a[8 * ap + 1]), y0 = phi2(a[8 * ap + 2], a[8 * ap + 3]);
                            const u32 x1 = phi2(a[8 * ap + 4], a[8 * ap + 5]), y1 = phi2(a[8 * ap + 6], a[8 * ap + 7]);
                            auto rx = __builtin_amdgcn_permlane32_swap(x0, x1, false, false);
                            auto ry = __builtin_amdgcn_permlane32_swap(y0, y1, false, false);
                            qv[jj][ap][i] = (u32x4){rx[0], ry[0], rx[1], ry[1]};
                        }
                if (QDELAY) { __builtin_amdgcn_s_setprio(0); for (int z = 0; z < QDELAY; z++) __builtin_amdgcn_s_sleep(8); }
#pragma unroll
                for (int jj = 0; jj < 2; jj++)
#pragma unroll
                    for (int ap = 0; ap < 2; ap++) {
                        const int U = 16 * cbk + 8 * wn + 4 * jj + 2 * ap + hh, pr = U / 5, u5 = U - 5 * pr;
                        const u32 dst = WS_P2 + (((pr * 128 + rt * 8 + 4 * wm + (r32 >> 4)) * 5 + u5) * 128 + (r32 & 15) * 8) * 2;
#pragma unroll
                        for (int i = 0; i < 2; i++) st128c(rw, dst + i * 2560, qv[jj][ap][i]);
                    }
            } else {
                k1_loop<false>(sm, a0, acc, rw, p0_flag(tile), tag, wave, lane, hk, wo, tile);
                __builtin_amdgcn_s_setprio(2);
                TS(0, 2);
                const bool isk = cbk < 10;
                const int j5 = isk ? cbk - 5 : cbk - 10;
                u32 dst[2], istep[2];
#pragma unroll
                for (int jj = 0; jj < 2; jj++) {
                    const int cc = 128 * j5 + 64 * wn + 32 * jj + r32, h = cc / 20, d = cc - 20 * h;
                    const int sb32 = rt * 4 + 2 * wm;
                    const bool lo = d < 16;
                    dst[jj] = lo ? (isk ? WS_PK0 : WS_V0) + (((h * 64 + sb32) * 4 + hh) * 128 + d * 8) * 2
                                 : WS_X + (((h * 32 + (sb32 >> 1)) * 4 + hh) * 128 + (d - 16 + (isk ? 0 : 4)) * 8) * 2;
                    istep[jj] = lo ? 1024 : 128;
                }
                if (isk) {
#pragma unroll
                    for (int jj = 0; jj < 2; jj++)
#pragma unroll
                        for (int i = 0; i < 2; i++)
#pragma unroll
                            for (int z = 0; z < 2; z++) {
                                const f32x16& a = acc[i][jj];
                                st128c(rw, dst[jj] + i * istep[jj] + z * 512,
                                       (u32x4){phi2(a[8 * z], a[8 * z + 1]), phi2(a[8 * z + 2], a[8 * z + 3]), phi2(a[8 * z + 4], a[8 * z + 5]), phi2(a[8 * z + 6], a[8 * z + 7])});
                            }
                } else {
#pragma unroll
                    for (int jj = 0; jj < 2; jj++)
#pragma unroll
                        for (int i = 0; i < 2; i++)
#pragma unroll
                            for (int z = 0; z < 2; z++) {
                                const f32x16& a = acc[i][jj];
                                st128c(rw, dst[jj] + i * istep[jj] + z * 512,
                                       (u32x4){pk2(a[8 * z], a[8 * z + 1]), pk2(a[8 * z + 2], a[8 * z + 3]), pk2(a[8 * z + 4], a[8 * z + 5]), pk2(a[8 * z + 6], a[8 * z + 7])});
                            }
                }
            }
            LGKM0();
            TS(0, 25);
            publish_last(rw, sm, C_G1, 4, F_G1 + cbk * 16 + rt, tag, lane);
            __builtin_amdgcn_s_setprio(0);
            TS(0, 3);
            if (is_conv) {
                lds_wait(sm, C_G1, 4);
                wo_issue(wo, ct, 0, 12, wave, 4, lane);
            }
        }
    }
    if (!is_conv) {
        LGKM0(); BAR();
        if (ko >= 0) {
            const int h = ko >> 1, half = ko & 1;
            wo_issue(wo, ct, 0, 12, wave, 8, lane);
            kvo_block(sm, wave, lane, h, half, rw, tag, ws);
            TS(0, 26);
            VMCNT(0); BAR();
            if (tid == 0) set_flag(rw, F_O + half * 32 + h, tag);
            TS(0, 11);
            wo_issue(wo, ct, 12, 20, wave, 8, lane);
        } else {
            wo_issue(wo, ct, 0, 20, wave, 8, lane);
            if (ko == -2 && wave == 0) {
                wait_flags(rw, (int)F_G1 + lane, (int)F_G1 + 64 + lane, tag);
                wait_flags(rw, (int)F_G1 + 128 + lane, (int)F_G1 + 192 + lane, tag);
                if (lane == 0) set_flag(rw, F_EPOCH, tag);
            }
        }
    } else {
        VMCNT(0); BAR();
        if (wave >= 4) wo_issue(wo, ct, 12, 20, wave - 4, 4, lane);
    }
    TS(0, 16);
    final_block(sm, wave, lane, tid, st, ct, x, out, rw, tag, ws);
    TS(0, 19);
}

extern "C" void kernel_launch(void* const* d_in, const int* in_sizes, int n_in, void* d_out, int out_size, void* d_ws, size_t ws_size, hipStream_t stream) {
    const u16* x = (const u16*)d_in[0]; const u16* wqkv = (const u16*)d_in[1]; const u16* wo = (const u16*)d_in[2];
    const u16* kin = (const u16*)d_in[4]; const u16* vin = (const u16*)d_in[5]; const u16* shk = (const u16*)d_in[6];
    const u16* hk = (const u16*)d_in[7]; const u16* sD = (const u16*)d_in[8]; const u16* gw = (const u16*)d_in[9];
    (void)hipFuncSetAttribute((const void*)fused_kernel, hipFuncAttributeMaxDynamicSharedMemorySize, LDS_BYTES);
    fused_kernel<<<256, 512, LDS_BYTES, stream>>>(x, wqkv, wo, kin, vin, shk, hk, sD, gw, (u16*)d_out, (char*)d_ws);
}
```

```cpp
#define PREFC 1
#define POLL_SLEEP 8
#define PREF2 1
#include <hip/hip_runtime.h>
#include <stdint.h>

typedef unsigned short u16;
typedef unsigned int u32;
typedef __bf16 bf16x8 __attribute__((ext_vector_type(8)));
typedef __bf16 bf16x2 __attribute__((ext_vector_type(2)));
typedef float f32x4 __attribute__((ext_vector_type(4)));
typedef float f32x2 __attribute__((ext_vector_type(2)));
typedef float f32x16 __attribute__((ext_vector_type(16)));
typedef u32 u32x4 __attribute__((ext_vector_type(4)));
typedef u32 u32x2 __attribute__((ext_vector_type(2)));
typedef short s16x4 __attribute__((ext_vector_type(4)));
#define LAS __attribute__((address_space(3)))
#define GAS __attribute__((address_space(1)))
#define DI __device__ __forceinline__
typedef __amdgpu_buffer_rsrc_t rsrc_t;

constexpr int S = 2048, D = 640;
#define FS 64
#define ROT_ON 1

constexpr u32 WS_UT = 0;
constexpr u32 WS_YT = WS_UT + 2621440;
constexpr u32 WS_PK0 = WS_YT + 2621440;
constexpr u32 WS_V0 = WS_PK0 + 2097152;
constexpr u32 WS_X = WS_V0 + 2097152;
constexpr u32 WS_P2 = WS_X + 1048576;
constexpr u32 WS_O = WS_P2 + 2621440;
constexpr u32 WS_G = WS_O + 2621440;
constexpr u32 WS_FLAG = WS_G + 8192;
constexpr u32 F_P0 = 0;
constexpr u32 F_G1 = 320;
constexpr u32 F_GATE = 560;
constexpr u32 F_O = 576;
constexpr u32 F_Y = 640;
constexpr u32 F_EPOCH = 896, F_TMO = 928;
constexpr u32 WS_STAMP = WS_FLAG + 1024 * FS;
constexpr u32 WS_BYTES = WS_STAMP + 256 * 32 * 16;

#define TS(w, id) do {} while (0)

DI u32 pk2(float a, float b) { f32x2 t = {a, b}; return __builtin_bit_cast(u32, __builtin_convertvector(t, bf16x2)); }
DI float blo(u32 u) { return __uint_as_float(u << 16); }
DI float bhi(u32 u) { return __uint_as_float(u & 0xffff0000u); }
DI float rbf(float f) { return blo(pk2(f, f)); }
DI bf16x8 asbf(u32x4 v) { return __builtin_bit_cast(bf16x8, v); }
DI f32x4 mfma16(u32x4 a, u32x4 b, f32x4 c) { return __builtin_amdgcn_mfma_f32_16x16x32_bf16(asbf(a), asbf(b), c, 0, 0, 0); }
DI f32x16 mfma32(u32x4 a, u32x4 b, f32x16 c) { return __builtin_amdgcn_mfma_f32_32x32x16_bf16(asbf(a), asbf(b), c, 0, 0, 0); }

DI void glds16(const void* sbase, u32 voff, u32 lds) {
    asm volatile("s_mov_b32 m0, %2\n\ts_nop 0\n\tglobal_load_lds_dwordx4 %0, %1" :: "v"(voff), "s"(sbase), "s"(lds) : "memory");
}
DI void glds16nt(const void* sbase, u32 voff, u32 lds) {
    asm volatile("s_mov_b32 m0, %2\n\ts_nop 0\n\tglobal_load_lds_dwordx4 %0, %1 nt" :: "v"(voff), "s"(sbase), "s"(lds) : "memory");
}
DI void touch(const void* sbase, u32 voff) {
    asm volatile("s_mov_b32 m0, %2\n\ts_nop 0\n\tglobal_load_lds_dword %0, %1" :: "v"(voff), "s"(sbase), "s"(156928u) : "memory");
}
#define NT_KV 0
#define NT_X 0
#define NT_H 0
#define PREF1 0
#define CODE_LINES 304
#define PREFT 0
#define QDELAY 0
DI void gld64(u32x2& d, const void* sbase, u32 voff) { asm volatile("global_load_dwordx2 %0, %1, %2" : "=v"(d) : "v"(voff), "s"(sbase) : "memory"); }
DI void gld128(u32x4& d, const void* sbase, u32 voff) { asm volatile("global_load_dwordx4 %0, %1, %2" : "=v"(d) : "v"(voff), "s"(sbase) : "memory"); }
DI void gld128c(u32x4& d, const void* sbase, u32 voff) { asm volatile("global_load_dwordx4 %0, %1, %2 sc1" : "=v"(d) : "v"(voff), "s"(sbase) : "memory"); }
DI void gld32c(u32& d, const void* sbase, u32 voff) { asm volatile("global_load_dword %0, %1, %2 sc1" : "=v"(d) : "v"(voff), "s"(sbase) : "memory"); }
#define PIN(x) asm volatile("" : "+v"(x))
#define SB() __builtin_amdgcn_sched_barrier(0)
DI u32x4 ld128c(rsrc_t r, u32 off) { return __builtin_amdgcn_raw_buffer_load_b128(r, off, 0, 16); }
DI u32 ld32c(rsrc_t r, u32 off) { return __builtin_amdgcn_raw_buffer_load_b32(r, off, 0, 16); }
DI void st128c(rsrc_t r, u32 off, u32x4 v) { __builtin_amdgcn_raw_buffer_store_b128(v, r, off, 0, 16); }
DI void st64c(rsrc_t r, u32 off, u32x2 v) { __builtin_amdgcn_raw_buffer_store_b64(v, r, off, 0, 16); }
DI void st32c(rsrc_t r, u32 off, u32 v) { __builtin_amdgcn_raw_buffer_store_b32(v, r, off, 0, 16); }
DI u32x4 lds128(LAS char* sm, u32 a) { return *(LAS u32x4*)(sm + a); }
DI u32x2 lds64(LAS char* sm, u32 a) { return *(LAS u32x2*)(sm + a); }
DI u32x2 ldstr(LAS char* sm, u32 a) { return __builtin_bit_cast(u32x2, __builtin_amdgcn_ds_read_tr16_b64_v4i16((LAS s16x4*)(sm + a))); }
#define VMCNT(n) asm volatile("s_waitcnt vmcnt(" #n ")" ::: "memory")
#define LGKM0() asm volatile("s_waitcnt lgkmcnt(0)" ::: "memory")
#define BAR() do { asm volatile("" ::: "memory"); __builtin_amdgcn_s_barrier(); asm volatile("" ::: "memory"); } while (0)
#define MEMF() asm volatile("" ::: "memory")

DI void set_flag(rsrc_t r, u32 idx, u32 tag) { st32c(r, WS_FLAG + idx * FS, tag); }
DI void wait_flags(rsrc_t r, int idx0, int idx1, u32 tag) {
    unsigned long long t0 = 0;
    for (u32 it = 0;; it++) {
        u32 a = tag, b = tag;
        if (idx0 >= 0) a = ld32c(r, WS_FLAG + idx0 * FS);
        if (idx1 >= 0) b = ld32c(r, WS_FLAG + idx1 * FS);
        if (__all(a == tag && b == tag)) break;
        if (POLL_SLEEP) __builtin_amdgcn_s_sleep(POLL_SLEEP);
        if ((it & 63u) == 0u) {
            const unsigned long long t = __builtin_amdgcn_s_memrealtime();
            if (it == 0) t0 = t;
            else if (t - t0 > 2000000ull) { set_flag(r, F_TMO, tag); break; }
        }
    }
    MEMF();
}
DI void lds_arrive(LAS char* sm, u32 cnt, int lane) { LGKM0(); if (lane == 0) __hip_atomic_fetch_add((LAS u32*)(sm + cnt), 1u, __ATOMIC_RELAXED, __HIP_MEMORY_SCOPE_WORKGROUP); MEMF(); }
DI void lds_wait(LAS char* sm, u32 cnt, u32 n) { MEMF(); while (*(volatile LAS u32*)(sm + cnt) < n) __builtin_amdgcn_s_sleep(0); MEMF(); }
DI void publish_last(rsrc_t r, LAS char* sm, u32 cnt, u32 nw, u32 flag, u32 tag, int lane) {
    VMCNT(0);
    if (lane == 0) {
        const u32 old = __hip_atomic_fetch_add((LAS u32*)(sm + cnt), 1u, __ATOMIC_RELAXED, __HIP_MEMORY_SCOPE_WORKGROUP);
        if (old == nw - 1) set_flag(r, flag, tag);
    }
    MEMF();
}

DI u32 phi2(float a, float b) {
    const f32x2 t = {a, b}, s = t * 1.4426950408889634f;
    const float ea = __builtin_amdgcn_fmed3f(__builtin_amdgcn_exp2f(s.x), 0.f, 1.f), eb = __builtin_amdgcn_fmed3f(__builtin_amdgcn_exp2f(s.y), 0.f, 1.f);
    const float ma = __int_as_float(max(__float_as_int(a), 0)), mb = __int_as_float(max(__float_as_int(b), 0));
    const f32x2 r = ((f32x2){ea, eb} + (f32x2){ma, mb}) + 1e-6f;
    return pk2(r.x, r.y);
}

constexpr int K1_STAGE = 32768;
constexpr int P0_K = 131072, P0_V = P0_K + 9216, P0_OUT = P0_V + 8192;
constexpr int L_CNT = 156672;
constexpr int C_G1 = L_CNT, C_Y = L_CNT + 4, C_P0A = L_CNT + 8, C_P0B = L_CNT + 12, C_P0C = L_CNT + 16, C_FY = L_CNT + 20, C_FY2 = L_CNT + 24, C_FO = L_CNT + 28, C_FR = L_CNT + 32;
constexpr int CV_PITCH = 4160;
constexpr int CV_U = 4 * CV_PITCH;
constexpr int CV_WAVE = CV_U + 4608;
constexpr int CV_BASE = 65536;
constexpr int WO_HI = 65536;
constexpr int F_YY = 110592;
constexpr int F_Y2 = F_YY + 10240;
constexpr int F_GG = F_Y2 + 11264;
constexpr int KO_RED = 110592;
constexpr int KO_KV = KO_RED + 32768;
constexpr int RED_PITCH = 336;
constexpr int LDS_BYTES = 160 * 1024;
DI u32 p0_flag(int tile) { return F_P0 + (tile % 10) * 32 + tile / 10; }
DI constexpr u32 wo_off(int kap) { return kap < 12 ? kap * 5120 : WO_HI + (kap - 12) * 5120; }

DI void p0_issue(const u16* kin, const u16* vin, int tile, int lw, int lane, u32 base) {
    const int cg = tile % 10, rg = tile / 10, s0 = rg * 64;
    const u32 col = cg * 128 + (lane & 7) * 16;
#pragma unroll
    for (int z = 0; z < 2; z++) {
        const int n = 2 * lw + z;
        int r = s0 - 4 + n * 8 + (lane >> 3); r = r < 0 ? 0 : r;
        (NT_KV ? glds16nt : glds16)(kin, (u32)(r * 1280) + col, base + n * 1024);
        (NT_KV ? glds16nt : glds16)(vin, (u32)((s0 + n * 8 + (lane >> 3)) * 1280) + col, base + 9216 + n * 1024);
    }
    {
        int r = s0 + 60 + (lane >> 3); r = r > S - 1 ? S - 1 : r;
        (NT_KV ? glds16nt : glds16)(kin, (u32)(r * 1280) + col, base + 8 * 1024);
    }
}
DI float dot2(u32 a, u32 b, float c) { return __builtin_amdgcn_fdot2_f32_bf16(__builtin_bit_cast(bf16x2, a), __builtin_bit_cast(bf16x2, b), c, false); }
DI void p0_compute(LAS char* sm, u32x2 w4, int tile, int wave, int lane, u32 base, u32 outb, char* ws = nullptr) {
    const int s0 = (tile / 10) * 64;
    const int G = wave * 4 + (lane >> 4), cb = G & 3, i = lane & 15, q = i >> 2, p = i & 3;
    const u32 wa = __builtin_amdgcn_alignbit(w4.y, w4.y, 16), wb = __builtin_amdgcn_alignbit(w4.x, w4.x, 16);
    const int ch = 16 * cb + i;
    u32x2 kq[2][3], vq[2][2];
#pragma unroll
    for (int z = 0; z < 2; z++) {
        const int o = (G >> 2) + 4 * z;
        const u32 ka = base + (8 * o + q) * 128 + 32 * cb + 8 * p, va = ka + 9216;
        kq[z][0] = ldstr(sm, ka); kq[z][1] = ldstr(sm, ka + 512); kq[z][2] = ldstr(sm, ka + 1024);
        vq[z][0] = ldstr(sm, va); vq[z][1] = ldstr(sm, va + 512);
    }
#pragma unroll
    for (int z = 0; z < 2; z++) {
        const int o = (G >> 2) + 4 * z;
        u32 K[6] = {kq[z][0].x, kq[z][0].y, kq[z][1].x, kq[z][1].y, kq[z][2].x, kq[z][2].y};
        if (s0 == 0 && o == 0) { K[0] = 0; K[1] = 0; }
        u32 Sh[5];
#pragma unroll
        for (int e = 0; e < 5; e++) Sh[e] = __builtin_amdgcn_alignbit(K[e + 1], K[e], 16);
        const u32 V[4] = {vq[z][0].x, vq[z][0].y, vq[z][1].x, vq[z][1].y};
        u32 r[4];
#pragma unroll
        for (int e = 0; e < 4; e++) {
            const float k0 = dot2(Sh[e], wa, dot2(Sh[e + 1], wb, 0.f)), k1 = dot2(K[e + 1], wa, dot2(K[e + 2], wb, 0.f));
            r[e] = pk2(k0 * blo(V[e]), k1 * bhi(V[e]));
        }
        *(LAS u32x4*)(sm + outb + ch * 128 + ((o ^ (ch & 7)) * 16)) = (u32x4){r[0], r[1], r[2], r[3]};
    }
}
DI void p0_store(LAS char* sm, rsrc_t rw, int tile, int t256, u32 outb) {
    const int cg = tile % 10, s0 = (tile / 10) * 64;
#pragma unroll
    for (int z = 0; z < 2; z++) {
        const int ch2 = (t256 >> 3) + 32 * z, o2 = t256 & 7;
        const u32x4 t4 = lds128(sm, outb + ch2 * 128 + ((o2 ^ (ch2 & 7)) * 16));
        st128c(rw, WS_UT + (cg * 64 + ch2) * 4096 + (s0 + 8 * o2) * 2, t4);
    }
}
DI void p0_weights(u32x2& w4, const u16* shk, int tile, int wave, int lane) {
    const int cg = tile % 10, cb = (wave * 4 + (lane >> 4)) & 3;
    gld64(w4, shk, (u32)((cg * 64 + 16 * cb + (lane & 15)) * 8));
}

struct K1Set { u32x4 xa[2][2]; u32x2 wl[2][2], wh[2][2]; };
struct K1Addr { u32 x[2][2]; u32 w[2][2]; };
template <bool QT, int WO, int XO>
DI void k1_phase(LAS char* sm, f32x16 (&acc)[2][2], const K1Set& cur, K1Set& nxt, const u32 (&xb)[2], const u32 (&wb)[2][2]) {
#pragma unroll
    for (int n = 0; n < 8; n++) {
        const int ks = n >> 2, i = (n >> 1) & 1, jj = n & 1;
        const u32x4 wf = {cur.wl[ks][jj].x, cur.wl[ks][jj].y, cur.wh[ks][jj].x, cur.wh[ks][jj].y};
        acc[i][jj] = QT ? mfma32(wf, cur.xa[ks][i], acc[i][jj]) : mfma32(cur.xa[ks][i], wf, acc[i][jj]);
        if (n == 0) { nxt.wl[0][0] = ldstr(sm, wb[0][0] + WO); nxt.wh[0][0] = ldstr(sm, wb[0][1] + WO); }
        if (n == 1) { nxt.xa[0][0] = lds128(sm, xb[0] + XO); nxt.wl[0][1] = ldstr(sm, wb[1][0] + WO); }
        if (n == 2) { nxt.wh[0][1] = ldstr(sm, wb[1][1] + WO); nxt.xa[0][1] = lds128(sm, xb[0] + XO + 4096); }
        if (n == 3) { nxt.wl[1][0] = ldstr(sm, wb[0][0] + WO + 4096); nxt.wh[1][0] = ldstr(sm, wb[0][1] + WO + 4096); }
        if (n == 4) { nxt.xa[1][0] = lds128(sm, xb[1] + XO); nxt.wl[1][1] = ldstr(sm, wb[1][0] + WO + 4096); }
        if (n == 5) { nxt.wh[1][1] = ldstr(sm, wb[1][1] + WO + 4096); nxt.xa[1][1] = lds128(sm, xb[1] + XO + 4096); }
        SB();
    }
}
template <bool QT, int ST>
DI void k1_chunk(LAS char* sm, f32x16 (&acc)[2][2], K1Set& s0, K1Set& s1, const K1Addr& a0, const K1Addr& a1, bool bar, bool drain = false) {
    constexpr int SN = (ST + 1) & 3;
    const K1Addr& ac = ST < 2 ? a0 : a1; const K1Addr& an = SN < 2 ? a0 : a1;
    k1_phase<QT, (ST & 1) * K1_STAGE + 8192, (ST & 1) * K1_STAGE + 16384>(sm, acc, s0, s1, ac.x[1], ac.w);
    if (drain) VMCNT(0);
    if (bar) { LGKM0(); BAR(); }
    k1_phase<QT, (SN & 1) * K1_STAGE, (SN & 1) * K1_STAGE + 16384>(sm, acc, s1, s0, an.x[0], an.w);
}
template <bool QT>
DI void k1_loop(LAS char* sm, const K1Addr& a0, f32x16 (&acc)[2][2], rsrc_t rw, u32 pflag, u32 tag, int wave, int lane, const u16* hk, const u16* wo, int n240) {
    K1Addr a1;
#pragma unroll
    for (int i = 0; i < 2; i++)
#pragma unroll
        for (int z = 0; z < 2; z++) { a1.x[i][z] = a0.x[i][z] + 65536; a1.w[i][z] = a0.w[i][z] + 65536; }
    K1Set s0, s1;
#pragma unroll
    for (int ks = 0; ks < 2; ks++)
#pragma unroll
        for (int jj = 0; jj < 2; jj++) {
            s0.wl[ks][jj] = ldstr(sm, a0.w[jj][0] + ks * 4096); s0.wh[ks][jj] = ldstr(sm, a0.w[jj][1] + ks * 4096);
            s0.xa[ks][jj] = lds128(sm, a0.x[0][ks] + 16384 + jj * 4096);
        }
    SB();
#pragma unroll 1
    for (int c = 0; c < 8; c += 4) {
        k1_chunk<QT, 0>(sm, acc, s0, s1, a0, a1, true);
        k1_chunk<QT, 1>(sm, acc, s0, s1, a0, a1, true);
        k1_chunk<QT, 2>(sm, acc, s0, s1, a0, a1, true);
        k1_chunk<QT, 3>(sm, acc, s0, s1, a0, a1, true, c == 0);
        if (c == 0 && wave == 0 && lane == 0) set_flag(rw, pflag, tag);
        if (PREF2 && c == 0 && wave >= 2) {
            if (wave == 2) { const u32 l_ = n240 * 86 + lane; touch(hk, (l_ < 20480 ? l_ : 20479) * 128); }
            else { const u32 l_ = n240 * 86 + 64 + lane, m_ = n240 * 27 + lane - 22;
                   if (lane < 22) touch(hk, (l_ < 20480 ? l_ : 20479) * 128); else if (lane < 49) touch(wo, (m_ < 6400 ? m_ : 6399) * 128); }
        }
    }
    k1_chunk<QT, 0>(sm, acc, s0, s1, a0, a1, true);
    k1_chunk<QT, 1>(sm, acc, s0, s1, a0, a1, false);
}

DI void wo_issue(const u16* wo, int ct, int k0, int k1, int w, int nw, int lane) {
    u32 vo[5];
#pragma unroll
    for (int i = 0; i < 5; i++) { const int n = 64 * i + lane, rho = n / 10, c = n - 10 * rho, row = (rho & 19) | ((rho & 4) << 1) | ((rho & 8) >> 1); vo[i] = (u32)(row * 1280 + ct * 160 + c * 16); }
#pragma unroll 1
    for (int kap = k0 + w; kap < k1; kap += nw) {
        const char* src = (const char*)wo + (size_t)kap * 40960;
        const u32 dst = kap < 12 ? kap * 5120 : WO_HI + (kap - 12) * 5120;
#pragma unroll
        for (int i = 0; i < 5; i++) glds16(src, vo[i], dst + i * 1024);
    }
}

struct CvSet { u32x2 fl[8], fh[8]; u32x4 g[8]; };
DI void cv_load(LAS char* sm, CvSet& s, u32 fa, u32 ga) {
#pragma unroll
    for (int k1 = 0; k1 < 8; k1++) { s.fl[k1] = ldstr(sm, fa + 512 * k1 + 8); s.fh[k1] = ldstr(sm, fa + 512 * k1); s.g[k1] = lds128(sm, ga + 512 * k1); }
}
DI void cv_phase(LAS char* sm, f32x4 (&Y)[8], const CvSet& cur, CvSet& nxt, u32 fa, u32 ga) {
    PIN(fa); PIN(ga);
    SB();
    int n = 0;
#pragma unroll
    for (int e = 0; e < 8; e++)
#pragma unroll
        for (int k1 = 0; k1 + e < 8; k1++) {
            const u32x4 F = {cur.fl[k1].x, cur.fl[k1].y, cur.fh[k1].x, cur.fh[k1].y};
            Y[k1 + e] = mfma16(F, cur.g[e], Y[k1 + e]);
            if (n == 0) nxt.g[0] = lds128(sm, ga);
            else if (n <= 16) { const int r = (n - 1) >> 1; if ((n - 1) & 1) nxt.fh[r] = ldstr(sm, fa + 512 * r); else nxt.fl[r] = ldstr(sm, fa + 512 * r + 8); }
            else if (n < 24) nxt.g[n - 16] = lds128(sm, ga + 512 * (n - 16));
            n++;
            SB();
        }
}
struct CvIn { u32x4 hd[4]; u32x2 hm[4]; };
DI void conv_fetch_h(CvIn& in, const u16* hk, int c, int lane) {
    const char* hb = (const char*)hk + (size_t)c * 4096;
#pragma unroll
    for (int z = 0; z < 4; z++) {
        const int i = lane + 64 * z;
        if (NT_H) asm volatile("global_load_dwordx4 %0, %1, %2 nt" : "=v"(in.hd[z]) : "v"((u32)(i * 16)), "s"(hb) : "memory"); else gld128(in.hd[z], hb, (u32)(i * 16));
        gld64(in.hm[z], hb, (u32)(i > 0 ? i * 16 - 8 : 0));
    }
}
DI void conv_build_h(LAS char* sm0, CvIn& in, int w, int lane) {
    LAS char* sm = sm0 + CV_BASE + w * CV_WAVE;
#pragma unroll
    for (int z = 0; z < 4; z++) { PIN(in.hd[z]); PIN(in.hm[z]); }
    if (lane < 32) *(LAS u32x4*)(sm + CV_U + lane * 16) = (u32x4){0, 0, 0, 0};
    if (lane < 8) *(LAS u32x4*)(sm + (lane >> 1) * CV_PITCH + (lane & 1) * 16) = (u32x4){0, 0, 0, 0};
#pragma unroll
    for (int z = 0; z < 4; z++) {
        const int i = lane + 64 * z;
        u32 m2 = in.hm[z].x, m1 = in.hm[z].y;
        if (i == 0) { m2 = 0; m1 = 0; }
        const u32 d0 = in.hd[z].x, d1 = in.hd[z].y, d2 = in.hd[z].z, d3 = in.hd[z].w;
        const u32 om2 = __builtin_amdgcn_alignbit(m1, m2, 16), om1 = __builtin_amdgcn_alignbit(d0, m1, 16), o0 = __builtin_amdgcn_alignbit(d1, d0, 16),
                  o1 = __builtin_amdgcn_alignbit(d2, d1, 16), o2 = __builtin_amdgcn_alignbit(d3, d2, 16);
        LAS char* p = sm + 32 + i * 16;
        *(LAS u32x4*)(p) = (u32x4){d0, d1, d2, d3};
        *(LAS u32x4*)(p + CV_PITCH) = (u32x4){om1, o0, o1, o2};
        *(LAS u32x4*)(p + 2 * CV_PITCH) = (u32x4){m1, d0, d1, d2};
        *(LAS u32x4*)(p + 3 * CV_PITCH) = (u32x4){om2, om1, o0, o1};
    }
}
DI void conv_run(LAS char* sm0, float Dc, int w, int lane, int c, rsrc_t rw, char* ws) {
    LAS char* sm = sm0;
    LGKM0();
    TS(4, 9);
    const int fr = lane & 15, g = lane >> 4, q = fr >> 2, p = lane & 3;
    sm = sm0;
    const u32 fa = CV_BASE + w * CV_WAVE + q * CV_PITCH + 2 * (32 - 8 * g + 4 * p) - 8;
    const u32 ga = CV_BASE + w * CV_WAVE + CV_U + 480 + 32 * fr + 16 * g;
    f32x4 Y[8];
#pragma unroll
    for (int i = 0; i < 8; i++) Y[i] = (f32x4){0, 0, 0, 0};
    CvSet A, B;
    cv_load(sm, A, fa, ga);
    SB();
#pragma unroll 1
    for (int k0 = 0; k0 < 8; k0 += 2) {
        cv_phase(sm, Y, A, B, fa + 64 * (k0 + 1), ga - 64 * (k0 + 1));
        cv_phase(sm, Y, B, A, fa + 64 * (k0 + 2), ga - 64 * (k0 + 2));
    }
    TS(4, 10);
    sm = sm0 + CV_BASE + w * CV_WAVE;
    {
        u32x2 u4[8];
#pragma unroll
        for (int ib = 0; ib < 8; ib++) u4[ib] = lds64(sm, CV_U + 512 + 512 * ib + 32 * fr + 8 * g);
#pragma unroll
        for (int ib = 0; ib < 8; ib++) {
            const u32x2 r = {pk2(fmaf(blo(u4[ib].x), Dc, Y[ib][0]), fmaf(bhi(u4[ib].x), Dc, Y[ib][1])), pk2(fmaf(blo(u4[ib].y), Dc, Y[ib][2]), fmaf(bhi(u4[ib].y), Dc, Y[ib][3]))};
            *(LAS u32x2*)(sm + 512 * ib + 32 * fr + 8 * g) = r;
        }
    }
    LGKM0();
    u32x4 y8[4];
#pragma unroll
    for (int z = 0; z < 4; z++) y8[z] = lds128(sm, z * 1024 + lane * 16);
#pragma unroll
    for (int z = 0; z < 4; z++) st128c(rw, WS_YT + c * 4096 + z * 1024 + lane * 16, y8[z]);
}

DI void kvo_block(LAS char* sm, int wave, int lane, int h, int half, rsrc_t rw, u32 tag, char* ws) {
    const int fr = lane & 15, g = lane >> 4;
    const int j0 = (20 * h) >> 7, j1 = (20 * h + 19) >> 7;
    wait_flags(rw, lane < 8 ? (int)F_G1 + 16 * (((lane & 2) ? 10 : 5) + ((lane & 1) ? j1 : j0)) + 2 * wave + (lane >> 2)
                            : lane < 10 ? (int)F_G1 + 16 * ((lane & 1) ? j1 : j0) + 8 * half + wave : -1, -1, tag);
    TS(0, 8);
    const u32 pk0 = WS_PK0 + (h * 64 + 8 * wave) * 1024 + lane * 16, v0 = pk0 + (WS_V0 - WS_PK0);
    const u32 xx = WS_X + (h * 32 + 4 * wave) * 1024 + lane * 16;
    u32x4 A[8], B[8], X[4], Q[8];
#pragma unroll
    for (int i = 0; i < 8; i++) { A[i] = ld128c(rw, pk0 + i * 1024); B[i] = ld128c(rw, v0 + i * 1024); if ((i & 1) == 0) X[i >> 1] = ld128c(rw, xx + (i >> 1) * 1024); }
    const int odd = h & 1, pr = h >> 1;
    const int u5 = (g < 3 ? g : 2) + 2 * odd;
    const int sb0 = half * 64 + wave * 8;
    const u32 pq = WS_P2 + (((pr * 128 + sb0) * 5 + u5) * 128 + fr * 8) * 2;
    MEMF(); SB();
    f32x4 a00 = {0, 0, 0, 0}, a0x = a00, ax0 = a00, axx = a00;
    const u32 cst = fr == 8 ? 0x3F803F80u : 0u;
#pragma unroll
    for (int i = 0; i < 8; i++) {
        u32x4 xv = X[i >> 1];
        if (i & 1) {
#pragma unroll
            for (int e = 0; e < 4; e++) xv[e] = (u32)__builtin_amdgcn_update_dpp(0, (int)xv[e], 0x108, 0xf, 0xf, false);
        }
        if (fr >= 8) xv = (u32x4){cst, cst, cst, cst};
        a00 = mfma16(A[i], B[i], a00); a0x = mfma16(A[i], xv, a0x); ax0 = mfma16(xv, B[i], ax0); axx = mfma16(xv, xv, axx);
    }
    PIN(a00); PIN(a0x); PIN(ax0); PIN(axx);
    SB();
#pragma unroll
    for (int i = 0; i < 8; i++) Q[i] = ld128c(rw, pq + i * 1280);
    MEMF(); SB();
    TS(0, 9);
    *(LAS f32x4*)(sm + KO_RED + ((wave * 4 + 0) * 64 + lane) * 16) = a00;
    *(LAS f32x4*)(sm + KO_RED + ((wave * 4 + 1) * 64 + lane) * 16) = a0x;
    *(LAS f32x4*)(sm + KO_RED + ((wave * 4 + 2) * 64 + lane) * 16) = ax0;
    *(LAS f32x4*)(sm + KO_RED + ((wave * 4 + 3) * 64 + lane) * 16) = axx;
    if (wave >= 4 && (wave - 4) * 64 + lane < 189) *(LAS u32x2*)(sm + KO_KV + ((wave - 4) * 64 + lane) * 8) = (u32x2){0, 0};
    LGKM0(); BAR();
    if (wave < 4) {
        const int tile = wave;
        f32x4 s = *(LAS f32x4*)(sm + KO_RED + (tile * 64 + lane) * 16);
#pragma unroll
        for (int w2 = 1; w2 < 8; w2++) s += *(LAS f32x4*)(sm + KO_RED + ((w2 * 4 + tile) * 64 + lane) * 16);
        const int e = (tile & 1) ? 12 + fr : fr;
        const bool colok = (tile & 1) ? (fr >= 4 && fr <= 8) : true;
        const bool rowok = (tile & 2) ? (g == 0) : true;
        const int d0 = (tile & 2) ? 16 : 4 * g;
        if (colok && rowok) *(LAS u32x2*)(sm + KO_KV + e * 72 + (d0 + 4) * 2) = (u32x2){pk2(s[0], s[1]), pk2(s[2], s[3])};
    }
    LGKM0(); BAR();
    u32x4 A0, A1[4], A2;
    {
        const u32 dofs = KO_KV + (8 * g - 4 * odd + 4) * 2;
        const u32 r0 = (4 * odd + fr) * 72, r1 = ((odd ? 0 : 16) + (fr & 3)) * 72;
        const u32x2 p0 = lds64(sm, dofs + r0), p1 = lds64(sm, dofs + r0 + 8);
        const u32x2 q0 = lds64(sm, dofs + r1), q1 = lds64(sm, dofs + r1 + 8);
        const u32x2 z0 = lds64(sm, dofs + 20 * 72), z1 = lds64(sm, dofs + 20 * 72 + 8);
        A0 = (u32x4){p0.x, p0.y, p1.x, p1.y}; A2 = (u32x4){z0.x, z0.y, z1.x, z1.y};
#pragma unroll
        for (int z = 0; z < 4; z++) { const bool m = (fr >> 2) == z; A1[z] = (u32x4){m ? q0.x : 0u, m ? q0.y : 0u, m ? q1.x : 0u, m ? q1.y : 0u}; }
    }
    TS(0, 10);
    const int U0 = (5 * h + odd) >> 1;
    const int Uw = U0 + (g >> 1);
    const u32 offA = WS_O + (((Uw >> 2) * 4 + (Uw & 3)) * 128 + fr * 8) * 2 + (g & 1) * 20480;
    const int cgx = 5 * h + (odd ? 0 : 4), Ux = cgx >> 1;
    const u32 offX = WS_O + (((Ux >> 2) * 4 + (Ux & 3)) * 128 + fr * 8 + (cgx & 1) * 4) * 2;
    const f32x4 zero = {0, 0, 0, 0};
#pragma unroll
    for (int i4 = 0; i4 < 2; i4++) {
        f32x4 n1 = zero; float rden[4]; u32x2 ov[4];
#pragma unroll
        for (int z = 0; z < 4; z++) {
            const int i = 4 * i4 + z;
            const f32x4 n0 = mfma16(A0, Q[i], zero);
            const f32x4 dn = mfma16(A2, Q[i], zero);
            n1 = mfma16(A1[z], Q[i], n1);
            const float rd = __builtin_amdgcn_rcpf(dn[0] + 1e-6f);
            rden[z] = rd;
            ov[z] = (u32x2){pk2(n0[0] * rd, n0[1] * rd), pk2(n0[2] * rd, n0[3] * rd)};
        }
#pragma unroll
        for (int z = 0; z < 4; z += 2) {
            auto rx = __builtin_amdgcn_permlane16_swap(ov[z].x, ov[z + 1].x, false, false);
            auto ry = __builtin_amdgcn_permlane16_swap(ov[z].y, ov[z + 1].y, false, false);
            st128c(rw, offA + (sb0 + 4 * i4 + z) * 20480, (u32x4){rx[0], ry[0], rx[1], ry[1]});
        }
        const float rd = g == 0 ? rden[0] : g == 1 ? rden[1] : g == 2 ? rden[2] : rden[3];
        st64c(rw, offX + (sb0 + 4 * i4 + g) * 20480, (u32x2){pk2(n1[0] * rd, n1[1] * rd), pk2(n1[2] * rd, n1[3] * rd)});
    }
}

DI void gate_block(int wave, int lane, int m, const u16* x, const u16* gw, rsrc_t rw) {
    const int sub = lane & 15, r4 = lane >> 4;
    u32x4 gq[5], xr[4][5];
#pragma unroll
    for (int jj = 0; jj < 5; jj++) gq[jj] = *(const GAS u32x4*)((const GAS char*)gw + (sub + 16 * jj) * 16);
#pragma unroll
    for (int it = 0; it < 4; it++)
#pragma unroll
        for (int jj = 0; jj < 5; jj++) xr[it][jj] = *(const GAS u32x4*)((const GAS char*)x + (size_t)(128 * m + 16 * wave + 4 * it + r4) * 1280 + (sub + 16 * jj) * 16);
    MEMF(); SB();
#pragma unroll
    for (int it = 0; it < 4; it++) {
        float a = 0.f;
#pragma unroll
        for (int jj = 0; jj < 5; jj++)
#pragma unroll
            for (int e = 0; e < 4; e++) { a = fmaf(blo(xr[it][jj][e]), blo(gq[jj][e]), a); a = fmaf(bhi(xr[it][jj][e]), bhi(gq[jj][e]), a); }
        a += __shfl_xor(a, 1); a += __shfl_xor(a, 2); a += __shfl_xor(a, 4); a += __shfl_xor(a, 8);
        a = rbf(a);
        const float g = rbf(__builtin_amdgcn_rcpf(1.f + __builtin_amdgcn_exp2f(-1.4426950408889634f * a)));
        if (sub == 0) st32c(rw, WS_G + (128 * m + 16 * wave + 4 * it + r4) * 4, __float_as_uint(g));
    }
}

DI void final_block(LAS char* sm, int wave, int lane, int tid, int st, int ct, const u16* x, u16* out, rsrc_t rw, u32 tag, char* ws) {
    const int fr = lane & 15, g = lane >> 4;
    u32x4 xv[2];
#pragma unroll
    for (int r = 0; r < 2; r++) {
        int it = tid + 512 * r; it = it < 640 ? it : 639;
        const int s = it / 10, u = it - 10 * s;
        xv[r] = *(const GAS u32x4*)((const GAS char*)x + (size_t)(st * 64 + s) * 1280 + ct * 160 + u * 16);
    }
    VMCNT(0); BAR();
    if (wave < 4) {
        const int kh = wave & 1, sh = wave >> 1;
        wait_flags(rw, lane < 16 ? (int)F_O + (st >> 4) * 32 + 16 * kh + lane : -1, -1, tag);
        TS(0, 17);
        const u32 ob = WS_O + ((st * 4 + 2 * sh) * 20 + 10 * kh) * 1024 + lane * 16;
        u32x4 B[2][10];
#pragma unroll
        for (int kl = 0; kl < 10; kl++) { B[0][kl] = ld128c(rw, ob + kl * 1024); B[1][kl] = ld128c(rw, ob + 20480 + kl * 1024); }
        MEMF(); SB();
        f32x4 acc[5][2];
#pragma unroll
        for (int cb = 0; cb < 5; cb++) { acc[cb][0] = (f32x4){0, 0, 0, 0}; acc[cb][1] = (f32x4){0, 0, 0, 0}; }
        const u32 aoff = (16 * (g >> 1) + 4 * (g & 1) + (fr >> 2)) * 160 + (lane & 3) * 8;
        u32x2 al[2][5], ah[2][5];
#define F_READ(buf, kl) do { const u32 b0_ = kh ? wo_off(10 + (kl)) : wo_off(kl); \
            _Pragma("unroll") for (int cb = 0; cb < 5; cb++) { al[buf][cb] = ldstr(sm, b0_ + aoff + cb * 32); ah[buf][cb] = ldstr(sm, b0_ + aoff + cb * 32 + 1280); } } while (0)
        F_READ(0, 0);
#pragma unroll
        for (int kl = 0; kl < 10; kl++) {
            if (kl < 9) F_READ((kl + 1) & 1, kl + 1);
            SB();
#pragma unroll
            for (int cb = 0; cb < 5; cb++) {
                const u32x4 a = {al[kl & 1][cb].x, al[kl & 1][cb].y, ah[kl & 1][cb].x, ah[kl & 1][cb].y};
                acc[cb][0] = mfma16(a, B[0][kl], acc[cb][0]); acc[cb][1] = mfma16(a, B[1][kl], acc[cb][1]);
            }
            SB();
        }
        TS(0, 18);
        lds_arrive(sm, C_FR, lane); lds_wait(sm, C_FR, 4);
#pragma unroll
        for (int cb = 0; cb < 5; cb++)
#pragma unroll
            for (int z = 0; z < 2; z++)
                *(LAS f32x4*)(sm + kh * (64 * RED_PITCH) + (16 * (2 * sh + z) + fr) * RED_PITCH + (16 * cb + 4 * g) * 4) = acc[cb][z];
    } else {
        if (wave == 4) { wait_flags(rw, lane < 20 ? (int)F_Y + ct * 32 + lane : lane == 32 ? (int)F_GATE + (st >> 1) : -1, -1, tag); lds_arrive(sm, C_FY, lane); }
        else lds_wait(sm, C_FY, 1);
        const int t = tid - 256;
        u32x4 yv[3];
#pragma unroll
        for (int r = 0; r < 3; r++) { int id = t + 256 * r; id = id < 640 ? id : 639; yv[r] = ld128c(rw, WS_YT + (80 * ct + (id >> 3)) * 4096 + st * 128 + (id & 7) * 16); }
#pragma unroll
        for (int r = 0; r < 3; r++) { const int id = t + 256 * r; if (id < 640) *(LAS u32x4*)(sm + F_YY + id * 16) = yv[r]; }
        if (wave == 4) *(LAS u32*)(sm + F_GG + lane * 4) = ld32c(rw, WS_G + (st * 64 + lane) * 4);
        lds_arrive(sm, C_FY2, lane); lds_wait(sm, C_FY2, 4);
        const int sb = wave - 4, q = fr >> 2, p = lane & 3;
        u32x2 tt[5];
#pragma unroll
        for (int cb = 0; cb < 5; cb++) tt[cb] = ldstr(sm, F_YY + (16 * cb + 4 * g + q) * 128 + (16 * sb + 4 * p) * 2);
#pragma unroll
        for (int cb = 0; cb < 5; cb++) *(LAS u32x2*)(sm + F_Y2 + (16 * sb + fr) * 176 + (16 * cb + 4 * g) * 2) = tt[cb];
    }
    LGKM0(); BAR();
#pragma unroll
    for (int r = 0; r < 2; r++) {
        const int it = tid + 512 * r;
        if (it < 640) {
            const int s = it / 10, u = it - 10 * s;
            const u32 ra = s * RED_PITCH + u * 32;
            const f32x4 p0 = *(LAS f32x4*)(sm + ra), p1 = *(LAS f32x4*)(sm + ra + 16), q0 = *(LAS f32x4*)(sm + 64 * RED_PITCH + ra), q1 = *(LAS f32x4*)(sm + 64 * RED_PITCH + ra + 16);
            const u32x4 y8 = lds128(sm, F_Y2 + s * 176 + u * 16);
            const float gg = *(LAS float*)(sm + F_GG + s * 4), omg = 1.f - gg;
            float o[8];
#pragma unroll
            for (int e = 0; e < 4; e++) {
                const float a0 = (e < 2 ? p0[2 * e] + q0[2 * e] : p1[2 * e - 4] + q1[2 * e - 4]), a1 = (e < 2 ? p0[2 * e + 1] + q0[2 * e + 1] : p1[2 * e - 3] + q1[2 * e - 3]);
                const float b0 = omg * blo(xv[r][e]), b1 = omg * bhi(xv[r][e]);
                o[2 * e] = fmaf(b0, blo(y8[e]), gg * a0); o[2 * e + 1] = fmaf(b1, bhi(y8[e]), gg * a1);
            }
            *(GAS u32x4*)((GAS char*)out + (size_t)(st * 64 + s) * 1280 + ct * 160 + u * 16) = (u32x4){pk2(o[0], o[1]), pk2(o[2], o[3]), pk2(o[4], o[5]), pk2(o[6], o[7])};
        }
    }
}

__global__ __launch_bounds__(512, 2) void fused_kernel(const u16* __restrict__ x, const u16* __restrict__ w, const u16* __restrict__ wo, const u16* __restrict__ kin,
                                                        const u16* __restrict__ vin, const u16* __restrict__ shk, const u16* __restrict__ hk, const u16* __restrict__ sD,
                                                        const u16* __restrict__ gw, u16* __restrict__ out, char* ws) {
    extern __shared__ __attribute__((aligned(16))) char smem[];
    LAS char* sm = (LAS char*)smem;
    const int tid = threadIdx.x, lane = tid & 63, wave = __builtin_amdgcn_readfirstlane(tid >> 6);
    const int b = blockIdx.x, xcd = b & 7, j = b >> 3;
    const rsrc_t rw = __builtin_amdgcn_make_buffer_rsrc(ws, 0, WS_BYTES, 0x00020000);
    u32 tag;
    asm volatile("s_load_dword %0, %1, 0x0\n\ts_waitcnt lgkmcnt(0)" : "=s"(tag) : "s"(ws + WS_FLAG + F_EPOCH * FS) : "memory");
    tag += 1u;
    if (tid < 16) *(LAS u32*)(sm + L_CNT + tid * 4) = 0u;
    TS(0, 0);
    if (PREFC && wave == 3) {
        unsigned long long pc;
        asm volatile("s_getpc_b64 %0" : "=s"(pc));
        pc &= ~127ull;
        const int ln = PREFC == 1 ? j * 10 + lane : lane;
        if (PREFC == 1) { if (lane < 10 && ln < CODE_LINES) touch((const void*)pc, (u32)(ln * 128)); }
        else {
#pragma unroll
            for (int z = 0; z < 5; z++) if (z * 64 + lane < CODE_LINES) touch((const void*)pc, (u32)((z * 64 + lane) * 128));
        }
    }
    if (PREFT && wave == 2) {
        if (PREFT == 1) { touch(ws, (u32)(lane * 262144)); if (lane < 40) touch(out, (u32)(lane * 65536)); }
        else { touch(ws, (u32)((j * 8 + (lane & 7)) * 65536 + (lane >> 3) * 128) % (WS_STAMP - 1024)); if (lane < 40) touch(out, (u32)(lane * 65536)); }
    }
    const int st = 4 * xcd + (j >> 3), ct = j & 7;
    int ko = -1;
    bool is_conv = false;

    if (j >= 30) {
        const int m = xcd * 2 + (j - 30);
        ko = -100;
        {
            const int grp = wave >> 2, w4i = wave & 3;
            u32x2 w4[3];
#pragma unroll
            for (int t = 0; t < 3; t++) { const int tt = 2 * t + grp < 5 ? 2 * t + grp : 4; p0_weights(w4[t], shk, 240 + 5 * m + tt, w4i, lane); }
#pragma unroll
            for (int t = 0; t < 3; t++) if (2 * t + grp < 5) p0_issue(kin, vin, 240 + 5 * m + 2 * t + grp, w4i, lane, (2 * t + grp) * 17408);
            VMCNT(0);
#pragma unroll
            for (int t = 0; t < 3; t++) PIN(w4[t]);
            BAR();
#pragma unroll
            for (int t = 0; t < 3; t++)
                if (2 * t + grp < 5) p0_compute(sm, w4[t], 240 + 5 * m + 2 * t + grp, w4i, lane, (2 * t + grp) * 17408, 90112 + (2 * t + grp) * 8192);
            LGKM0(); BAR();
            {
                u32x4 t4[3][2];
#pragma unroll
                for (int t = 0; t < 3; t++)
#pragma unroll
                    for (int z = 0; z < 2; z++) { const int ch2 = ((tid & 255) >> 3) + 32 * z, o2 = tid & 7, tt = 2 * t + grp < 5 ? 2 * t + grp : 4; t4[t][z] = lds128(sm, 90112 + tt * 8192 + ch2 * 128 + ((o2 ^ (ch2 & 7)) * 16)); }
#pragma unroll
                for (int t = 0; t < 3; t++)
                    if (2 * t + grp < 5) {
                        const int tile = 240 + 5 * m + 2 * t + grp, cg = tile % 10, s0 = (tile / 10) * 64;
#pragma unroll
                        for (int z = 0; z < 2; z++) { const int ch2 = ((tid & 255) >> 3) + 32 * z, o2 = tid & 7; st128c(rw, WS_UT + (cg * 64 + ch2) * 4096 + (s0 + 8 * o2) * 2, t4[t][z]); }
                    }
            }
        }
        VMCNT(0); BAR();
        if (tid < 5) set_flag(rw, p0_flag(240 + 5 * m + tid), tag);
        TS(0, 1);
        gate_block(wave, lane, m, x, gw, rw);
        VMCNT(0); LGKM0(); BAR();
        if (tid == 0) set_flag(rw, F_GATE + m, tag);
        TS(0, 3);
    } else {
        const int rt = 2 * xcd + j / 15, cbk = j % 15, tile = xcd * 30 + j;
        is_conv = cbk < 10;
        if (!is_conv) { const int vi = rt * 5 + (cbk - 10); ko = vi < 64 ? vi : -2 - (vi - 64); }
        if (wave >= 4) {
            const int lw = wave - 4;
            p0_issue(kin, vin, tile, lw, lane, P0_K);
            u32 vx[4], vw[4];
#pragma unroll
            for (int n2 = 0; n2 < 4; n2++) {
                const int n = 4 * lw + n2;
                const int row = 8 * n + (lane >> 3), ch = (lane & 7) ^ ((row >> 1) & 7);
                vx[n2] = (u32)((rt * 128 + row) * 1280 + ch * 16);
                const int kr = 4 * n + (lane >> 4), f = ((kr & 3) << 2) | ((kr >> 2) & 3), cw = (lane & 15) ^ f;
                vw[n2] = (u32)(kr * 3840 + cbk * 256 + cw * 16);
            }
            const int rot = (rt + cbk) % 10;
#define K1_ISSUE(c) do { const u32 st_ = ((c) & 3) * K1_STAGE + 4 * lw * 1024; int kc_ = (c) + ROT_ON * rot; kc_ = kc_ >= 10 ? kc_ - 10 : kc_; \
            const char* xb_ = (const char*)x + kc_ * 128; const char* wp_ = (const char*)w + (size_t)kc_ * 64 * 3840; \
            _Pragma("unroll") for (int n2 = 0; n2 < 4; n2++) { if (NT_X) glds16nt(xb_, vx[n2], st_ + 16384 + n2 * 1024); else glds16(xb_, vx[n2], st_ + 16384 + n2 * 1024); glds16(wp_, vw[n2], st_ + n2 * 1024); } } while (0)
            K1_ISSUE(0);
            VMCNT(8); BAR();
            K1_ISSUE(1); K1_ISSUE(2);
            VMCNT(16); BAR();
            K1_ISSUE(3);
#pragma unroll 1
            for (int c = 0; c < 6; c++) {
                VMCNT(16); BAR();
                K1_ISSUE(c + 4);
            }
            const int cv = rt * 10 + cbk, c = 4 * cv + lw;
            if (is_conv) {
                CvIn in; u32 fl; u32x4 u[4];
                const int fidx = (int)F_P0 + (cv >> 4) * 32 + (lane & 31);
                conv_fetch_h(in, hk, c, lane);
                VMCNT(24); BAR();
                VMCNT(16); BAR();
                gld32c(fl, ws, WS_FLAG + fidx * FS);
                VMCNT(9); BAR();
                TS(4, 4);
                VMCNT(0); PIN(fl);
                const bool ready = __all(fl == tag);
                if (ready) {
#pragma unroll
                    for (int z = 0; z < 4; z++) gld128c(u[z], ws, WS_UT + c * 4096 + z * 1024 + lane * 16);
                }
                TS(4, 5);
                conv_build_h(sm, in, lw, lane);
                TS(4, 6);
                if (!ready) {
                    wait_flags(rw, fidx, -1, tag);
#pragma unroll
                    for (int z = 0; z < 4; z++) gld128c(u[z], ws, WS_UT + c * 4096 + z * 1024 + lane * 16);
                }
                TS(4, 7);
                VMCNT(0);
#pragma unroll
                for (int z = 0; z < 4; z++) { PIN(u[z]); *(LAS u32x4*)(sm + CV_BASE + lw * CV_WAVE + CV_U + 512 + z * 1024 + lane * 16) = u[z]; }
                const float Dc = blo((u32)sD[c]);
                TS(4, 8);
                conv_run(sm, Dc, lw, lane, c, rw, ws);
                publish_last(rw, sm, C_Y, 4, F_Y + (cv / 20) * 32 + cv % 20, tag, lane);
                TS(4, 11);
            } else {
                VMCNT(16); BAR();
                VMCNT(8); BAR();
                VMCNT(0); BAR();
            }
        } else {
            u32x2 w4; p0_weights(w4, shk, tile, wave, lane);
            if (PREF1 && wave < 3) {
                const int n = wave * 64 + lane;
                if (wave == 0) touch(x, (u32)((xcd * 2560 + j * 80 + lane) * 128));
                else if (wave == 1) { if (lane < 16) touch(x, (u32)((xcd * 2560 + j * 80 + 64 + lane) * 128)); else touch(w, (u32)(((xcd * 30 + j) * 80 + lane - 16) * 128)); }
                else if (lane < 32) touch(w, (u32)(((xcd * 30 + j) * 80 + 48 + lane) * 128));
                (void)n;
            }
            const int wm = wave >> 1, wn = wave & 1, r32 = lane & 31, hh = lane >> 5;
            K1Addr a0;
#pragma unroll
            for (int ks = 0; ks < 2; ks++) {
                a0.x[0][ks] = (64 * wm + r32) * 128 + (((2 * ks + hh) ^ ((r32 >> 1) & 7)) * 16);
                a0.x[1][ks] = a0.x[0][ks] ^ 64;
            }
#pragma unroll
            for (int jj = 0; jj < 2; jj++)
#pragma unroll
                for (int t = 0; t < 2; t++) {
                    const int blk = (lane >> 4) & 1, q = (lane & 15) >> 2, p = lane & 3, row = 8 * hh + 4 * t + q, ch = 8 * wn + 4 * jj + 2 * blk + (p >> 1);
                    const int f = ((row & 3) << 2) | ((row >> 2) & 3);
                    a0.w[jj][t] = row * 256 + ((ch ^ f) * 16) + 8 * (p & 1);
                }
            f32x16 acc[2][2];
#pragma unroll
            for (int i = 0; i < 2; i++)
#pragma unroll
                for (int jj = 0; jj < 2; jj++)
#pragma unroll
                    for (int e = 0; e < 16; e++) acc[i][jj][e] = 0.f;
            TS(0, 20);
            VMCNT(0);
            PIN(w4); TS(0, 21); BAR();
            TS(0, 22);
            p0_compute(sm, w4, tile, wave, lane, P0_K, P0_OUT, ws);
            TS(0, 28);
            LGKM0(); TS(0, 23); BAR();
            TS(0, 24);
            p0_store(sm, rw, tile, tid, P0_OUT);
            TS(0, 1);
            if (cbk < 5) {
                k1_loop<true>(sm, a0, acc, rw, p0_flag(tile), tag, wave, lane, hk, wo, tile);
                __builtin_amdgcn_s_setprio(2);
                TS(0, 2);
                u32x4 qv[2][2][2];
#pragma unroll
                for (int jj = 0; jj < 2; jj++)
#pragma unroll
                    for (int ap = 0; ap < 2; ap++)
#pragma unroll
                        for (int i = 0; i < 2; i++) {
                            const f32x16& a = acc[i][jj];
                            const u32 x0 = phi2(a[8 * ap], a[8 * ap + 1]), y0 = phi2(a[8 * ap + 2], a[8 * ap + 3]);
                            const u32 x1 = phi2(a[8 * ap + 4], a[8 * ap + 5]), y1 = phi2(a[8 * ap + 6], a[8 * ap + 7]);
                            auto rx = __builtin_amdgcn_permlane32_swap(x0, x1, false, false);
                            auto ry = __builtin_amdgcn_permlane32_swap(y0, y1, false, false);
                            qv[jj][ap][i] = (u32x4){rx[0], ry[0], rx[1], ry[1]};
                        }
                if (QDELAY) { __builtin_amdgcn_s_setprio(0); for (int z = 0; z < QDELAY; z++) __builtin_amdgcn_s_sleep(8); }
#pragma unroll
                for (int jj = 0; jj < 2; jj++)
#pragma unroll
                    for (int ap = 0; ap < 2; ap++) {
                        const int U = 16 * cbk + 8 * wn + 4 * jj + 2 * ap + hh, pr = U / 5, u5 = U - 5 * pr;
                        const u32 dst = WS_P2 + (((pr * 128 + rt * 8 + 4 * wm + (r32 >> 4)) * 5 + u5) * 128 + (r32 & 15) * 8) * 2;
#pragma unroll
                        for (int i = 0; i < 2; i++) st128c(rw, dst + i * 2560, qv[jj][ap][i]);
                    }
            } else {
                k1_loop<false>(sm, a0, acc, rw, p0_flag(tile), tag, wave, lane, hk, wo, tile);
                __builtin_amdgcn_s_setprio(2);
                TS(0, 2);
                const bool isk = cbk < 10;
                const int j5 = isk ? cbk - 5 : cbk - 10;
                u32 dst[2], istep[2];
#pragma unroll
                for (int jj = 0; jj < 2; jj++) {
                    const int cc = 128 * j5 + 64 * wn + 32 * jj + r32, h = cc / 20, d = cc - 20 * h;
                    const int sb32 = rt * 4 + 2 * wm;
                    const bool lo = d < 16;
                    dst[jj] = lo ? (isk ? WS_PK0 : WS_V0) + (((h * 64 + sb32) * 4 + hh) * 128 + d * 8) * 2
                                 : WS_X + (((h * 32 + (sb32 >> 1)) * 4 + hh) * 128 + (d - 16 + (isk ? 0 : 4)) * 8) * 2;
                    istep[jj] = lo ? 1024 : 128;
                }
                if (isk) {
#pragma unroll
                    for (int jj = 0; jj < 2; jj++)
#pragma unroll
                        for (int i = 0; i < 2; i++)
#pragma unroll
                            for (int z = 0; z < 2; z++) {
                                const f32x16& a = acc[i][jj];
                                st128c(rw, dst[jj] + i * istep[jj] + z * 512,
                                       (u32x4){phi2(a[8 * z], a[8 * z + 1]), phi2(a[8 * z + 2], a[8 * z + 3]), phi2(a[8 * z + 4], a[8 * z + 5]), phi2(a[8 * z + 6], a[8 * z + 7])});
                            }
                } else {
#pragma unroll
                    for (int jj = 0; jj < 2; jj++)
#pragma unroll
                        for (int i = 0; i < 2; i++)
#pragma unroll
                            for (int z = 0; z < 2; z++) {
                                const f32x16& a = acc[i][jj];
                                st128c(rw, dst[jj] + i * istep[jj] + z * 512,
                                       (u32x4){pk2(a[8 * z], a[8 * z + 1]), pk2(a[8 * z + 2], a[8 * z + 3]), pk2(a[8 * z + 4], a[8 * z + 5]), pk2(a[8 * z + 6], a[8 * z + 7])});
                            }
                }
            }
            LGKM0();
            TS(0, 25);
            publish_last(rw, sm, C_G1, 4, F_G1 + cbk * 16 + rt, tag, lane);
            __builtin_amdgcn_s_setprio(0);
            TS(0, 3);
            if (is_conv) {
                lds_wait(sm, C_G1, 4);
                wo_issue(wo, ct, 0, 12, wave, 4, lane);
            }
        }
    }
    if (!is_conv) {
        LGKM0(); BAR();
        if (ko >= 0) {
            const int h = ko >> 1, half = ko & 1;
            wo_issue(wo, ct, 0, 12, wave, 8, lane);
            kvo_block(sm, wave, lane, h, half, rw, tag, ws);
            TS(0, 26);
            VMCNT(0); BAR();
            if (tid == 0) set_flag(rw, F_O + half * 32 + h, tag);
            TS(0, 11);
            wo_issue(wo, ct, 12, 20, wave, 8, lane);
        } else {
            wo_issue(wo, ct, 0, 20, wave, 8, lane);
            if (ko == -2 && wave == 0) {
                wait_flags(rw, (int)F_G1 + lane, (int)F_G1 + 64 + lane, tag);
                wait_flags(rw, (int)F_G1 + 128 + lane, (int)F_G1 + 192 + lane, tag);
                if (lane == 0) set_flag(rw, F_EPOCH, tag);
            }
        }
    } else {
        VMCNT(0); BAR();
        if (wave >= 4) wo_issue(wo, ct, 12, 20, wave - 4, 4, lane);
    }
    TS(0, 16);
    final_block(sm, wave, lane, tid, st, ct, x, out, rw, tag, ws);
    TS(0, 19);
}

extern "C" void kernel_launch(void* const* d_in, const int* in_sizes, int n_in, void* d_out, int out_size, void* d_ws, size_t ws_size, hipStream_t stream) {
    const u16* x = (const u16*)d_in[0]; const u16* wqkv = (const u16*)d_in[1]; const u16* wo = (const u16*)d_in[2];
    const u16* kin = (const u16*)d_in[4]; const u16* vin = (const u16*)d_in[5]; const u16* shk = (const u16*)d_in[6];
    const u16* hk = (const u16*)d_in[7]; const u16* sD = (const u16*)d_in[8]; const u16* gw = (const u16*)d_in[9];
    (void)hipFuncSetAttribute((const void*)fused_kernel, hipFuncAttributeMaxDynamicSharedMemorySize, LDS_BYTES);
    fused_kernel<<<256, 512, LDS_BYTES, stream>>>(x, wqkv, wo, kin, vin, shk, hk, sD, gw, (u16*)d_out, (char*)d_ws);
}
```

```cpp
#include <hip/hip_runtime.h>
#include <stdint.h>

typedef unsigned short u16;
typedef unsigned int u32;
typedef __bf16 bf16x8 __attribute__((ext_vector_type(8)));
typedef __bf16 bf16x2 __attribute__((ext_vector_type(2)));
typedef float f32x4 __attribute__((ext_vector_type(4)));
typedef float f32x2 __attribute__((ext_vector_type(2)));
typedef float f32x16 __attribute__((ext_vector_type(16)));
typedef u32 u32x4 __attribute__((ext_vector_type(4)));
typedef u32 u32x2 __attribute__((ext_vector_type(2)));
typedef short s16x4 __attribute__((ext_vector_type(4)));
#define LAS __attribute__((address_space(3)))
#define GAS __attribute__((address_space(1)))
#define DI __device__ __forceinline__
typedef __amdgpu_buffer_rsrc_t rsrc_t;

constexpr int S = 2048, D = 640;
#define FS 64
#define POLL_SLEEP 8
#define ROT_ON 1

constexpr u32 WS_UT = 0;
constexpr u32 WS_YT = WS_UT + 2621440;
constexpr u32 WS_PK0 = WS_YT + 2621440;
constexpr u32 WS_V0 = WS_PK0 + 2097152;
constexpr u32 WS_XK = WS_V0 + 2097152;
constexpr u32 WS_XV = WS_XK + 524288;
constexpr u32 WS_P2 = WS_XV + 524288;
constexpr u32 WS_O = WS_P2 + 2621440;
constexpr u32 WS_G = WS_O + 2621440;
constexpr u32 WS_FLAG = WS_G + 8192;
constexpr u32 F_P0 = 0;
constexpr u32 F_G1 = 320;
constexpr u32 F_GATE = 560;
constexpr u32 F_O = 576;
constexpr u32 F_Y = 640;
constexpr u32 F_EPOCH = 896, F_TMO = 928;
constexpr u32 WS_STAMP = WS_FLAG + 1024 * FS;
constexpr u32 WS_BYTES = WS_STAMP + 256 * 32 * 16;

#define TS(w, id) do {} while (0)

DI u32 pk2(float a, float b) { f32x2 t = {a, b}; return __builtin_bit_cast(u32, __builtin_convertvector(t, bf16x2)); }
DI float blo(u32 u) { return __uint_as_float(u << 16); }
DI float bhi(u32 u) { return __uint_as_float(u & 0xffff0000u); }
DI float rbf(float f) { return blo(pk2(f, f)); }
DI bf16x8 asbf(u32x4 v) { return __builtin_bit_cast(bf16x8, v); }
DI f32x4 mfma16(u32x4 a, u32x4 b, f32x4 c) { return __builtin_amdgcn_mfma_f32_16x16x32_bf16(asbf(a), asbf(b), c, 0, 0, 0); }
DI f32x16 mfma32(u32x4 a, u32x4 b, f32x16 c) { return __builtin_amdgcn_mfma_f32_32x32x16_bf16(asbf(a), asbf(b), c, 0, 0, 0); }

DI void glds16(const void* sbase, u32 voff, u32 lds) {
    asm volatile("s_mov_b32 m0, %2\n\ts_nop 0\n\tglobal_load_lds_dwordx4 %0, %1" :: "v"(voff), "s"(sbase), "s"(lds) : "memory");
}
DI void glds16nt(const void* sbase, u32 voff, u32 lds) {
    asm volatile("s_mov_b32 m0, %2\n\ts_nop 0\n\tglobal_load_lds_dwordx4 %0, %1 nt" :: "v"(voff), "s"(sbase), "s"(lds) : "memory");
}
DI void touch(const void* sbase, u32 voff) {
    asm volatile("s_mov_b32 m0, %2\n\ts_nop 0\n\tglobal_load_lds_dword %0, %1" :: "v"(voff), "s"(sbase), "s"(156928u) : "memory");
}
#define NT_KV 0
#define NT_X 0
#define NT_H 0
#define PREF1 0
#define PREF2 1
#define PREFC 1
#define CODE_LINES 332
#define PREFT 0
#define QDELAY 0
DI void gld64(u32x2& d, const void* sbase, u32 voff) { asm volatile("global_load_dwordx2 %0, %1, %2" : "=v"(d) : "v"(voff), "s"(sbase) : "memory"); }
DI void gld128(u32x4& d, const void* sbase, u32 voff) { asm volatile("global_load_dwordx4 %0, %1, %2" : "=v"(d) : "v"(voff), "s"(sbase) : "memory"); }
DI void gld128c(u32x4& d, const void* sbase, u32 voff) { asm volatile("global_load_dwordx4 %0, %1, %2 sc1" : "=v"(d) : "v"(voff), "s"(sbase) : "memory"); }
DI void gld32c(u32& d, const void* sbase, u32 voff) { asm volatile("global_load_dword %0, %1, %2 sc1" : "=v"(d) : "v"(voff), "s"(sbase) : "memory"); }
#define PIN(x) asm volatile("" : "+v"(x))
#define SB() __builtin_amdgcn_sched_barrier(0)
DI u32x4 ld128c(rsrc_t r, u32 off) { return __builtin_amdgcn_raw_buffer_load_b128(r, off, 0, 16); }
DI u32x2 ld64c(rsrc_t r, u32 off) { return __builtin_amdgcn_raw_buffer_load_b64(r, off, 0, 16); }
DI u32 ld32c(rsrc_t r, u32 off) { return __builtin_amdgcn_raw_buffer_load_b32(r, off, 0, 16); }
DI void st128c(rsrc_t r, u32 off, u32x4 v) { __builtin_amdgcn_raw_buffer_store_b128(v, r, off, 0, 16); }
DI void st64c(rsrc_t r, u32 off, u32x2 v) { __builtin_amdgcn_raw_buffer_store_b64(v, r, off, 0, 16); }
DI void st32c(rsrc_t r, u32 off, u32 v) { __builtin_amdgcn_raw_buffer_store_b32(v, r, off, 0, 16); }
DI u32x4 lds128(LAS char* sm, u32 a) { return *(LAS u32x4*)(sm + a); }
DI u32x2 lds64(LAS char* sm, u32 a) { return *(LAS u32x2*)(sm + a); }
DI u32x2 ldstr(LAS char* sm, u32 a) { return __builtin_bit_cast(u32x2, __builtin_amdgcn_ds_read_tr16_b64_v4i16((LAS s16x4*)(sm + a))); }
#define VMCNT(n) asm volatile("s_waitcnt vmcnt(" #n ")" ::: "memory")
#define LGKM0() asm volatile("s_waitcnt lgkmcnt(0)" ::: "memory")
#define BAR() do { asm volatile("" ::: "memory"); __builtin_amdgcn_s_barrier(); asm volatile("" ::: "memory"); } while (0)
#define MEMF() asm volatile("" ::: "memory")

DI void set_flag(rsrc_t r, u32 idx, u32 tag) { st32c(r, WS_FLAG + idx * FS, tag); }
DI void wait_flags(rsrc_t r, int idx0, int idx1, u32 tag) {
    unsigned long long t0 = 0;
    for (u32 it = 0;; it++) {
        u32 a = tag, b = tag;
        if (idx0 >= 0) a = ld32c(r, WS_FLAG + idx0 * FS);
        if (idx1 >= 0) b = ld32c(r, WS_FLAG + idx1 * FS);
        if (__all(a == tag && b == tag)) break;
        if (POLL_SLEEP) __builtin_amdgcn_s_sleep(POLL_SLEEP);
        if ((it & 63u) == 0u) {
            const unsigned long long t = __builtin_amdgcn_s_memrealtime();
            if (it == 0) t0 = t;
            else if (t - t0 > 2000000ull) { set_flag(r, F_TMO, tag); break; }
        }
    }
    MEMF();
}
DI void lds_arrive(LAS char* sm, u32 cnt, int lane) { LGKM0(); if (lane == 0) __hip_atomic_fetch_add((LAS u32*)(sm + cnt), 1u, __ATOMIC_RELAXED, __HIP_MEMORY_SCOPE_WORKGROUP); MEMF(); }
DI void lds_wait(LAS char* sm, u32 cnt, u32 n) { MEMF(); while (*(volatile LAS u32*)(sm + cnt) < n) __builtin_amdgcn_s_sleep(0); MEMF(); }
DI void publish_last(rsrc_t r, LAS char* sm, u32 cnt, u32 nw, u32 flag, u32 tag, int lane) {
    VMCNT(0);
    if (lane == 0) {
        const u32 old = __hip_atomic_fetch_add((LAS u32*)(sm + cnt), 1u, __ATOMIC_RELAXED, __HIP_MEMORY_SCOPE_WORKGROUP);
        if (old == nw - 1) set_flag(r, flag, tag);
    }
    MEMF();
}

DI u32 phi2(float a, float b) {
    const f32x2 t = {a, b}, s = t * 1.4426950408889634f;
    const float ea = __builtin_amdgcn_fmed3f(__builtin_amdgcn_exp2f(s.x), 0.f, 1.f), eb = __builtin_amdgcn_fmed3f(__builtin_amdgcn_exp2f(s.y), 0.f, 1.f);
    const float ma = __int_as_float(max(__float_as_int(a), 0)), mb = __int_as_float(max(__float_as_int(b), 0));
    const f32x2 r = ((f32x2){ea, eb} + (f32x2){ma, mb}) + 1e-6f;
    return pk2(r.x, r.y);
}

constexpr int K1_STAGE = 32768;
constexpr int P0_K = 131072, P0_V = P0_K + 9216, P0_OUT = P0_V + 8192;
constexpr int L_CNT = 156672;
constexpr int C_G1 = L_CNT, C_Y = L_CNT + 4, C_P0A = L_CNT + 8, C_P0B = L_CNT + 12, C_P0C = L_CNT + 16, C_FY = L_CNT + 20, C_FY2 = L_CNT + 24, C_FO = L_CNT + 28, C_FR = L_CNT + 32;
constexpr int CV_PITCH = 4160;
constexpr int CV_U = 4 * CV_PITCH;
constexpr int CV_WAVE = CV_U + 4608;
constexpr int CV_BASE = 65536;
constexpr int WO_HI = 65536;
constexpr int F_YY = 110592;
constexpr int F_Y2 = F_YY + 10240;
constexpr int F_GG = F_Y2 + 11264;
constexpr int KO_RED = 110592;
constexpr int KO_KV = KO_RED + 32768;
constexpr int RED_PITCH = 336;
constexpr int LDS_BYTES = 160 * 1024;
DI u32 p0_flag(int tile) { return F_P0 + (tile % 10) * 32 + tile / 10; }
DI constexpr u32 wo_off(int kap) { return kap < 12 ? kap * 5120 : WO_HI + (kap - 12) * 5120; }

DI void p0_issue(const u16* kin, const u16* vin, int tile, int lw, int lane, u32 base) {
    const int cg = tile % 10, rg = tile / 10, s0 = rg * 64;
    const u32 col = cg * 128 + (lane & 7) * 16;
#pragma unroll
    for (int z = 0; z < 2; z++) {
        const int n = 2 * lw + z;
        int r = s0 - 4 + n * 8 + (lane >> 3); r = r < 0 ? 0 : r;
        (NT_KV ? glds16nt : glds16)(kin, (u32)(r * 1280) + col, base + n * 1024);
        (NT_KV ? glds16nt : glds16)(vin, (u32)((s0 + n * 8 + (lane >> 3)) * 1280) + col, base + 9216 + n * 1024);
    }
    {
        int r = s0 + 60 + (lane >> 3); r = r > S - 1 ? S - 1 : r;
        (NT_KV ? glds16nt : glds16)(kin, (u32)(r * 1280) + col, base + 8 * 1024);
    }
}
DI float dot2(u32 a, u32 b, float c) { return __builtin_amdgcn_fdot2_f32_bf16(__builtin_bit_cast(bf16x2, a), __builtin_bit_cast(bf16x2, b), c, false); }
DI void p0_compute(LAS char* sm, u32x2 w4, int tile, int wave, int lane, u32 base, u32 outb, char* ws = nullptr) {
    const int s0 = (tile / 10) * 64;
    const int G = wave * 4 + (lane >> 4), cb = G & 3, i = lane & 15, q = i >> 2, p = i & 3;
    const u32 wa = __builtin_amdgcn_alignbit(w4.y, w4.y, 16), wb = __builtin_amdgcn_alignbit(w4.x, w4.x, 16);
    const int ch = 16 * cb + i;
    u32x2 kq[2][3], vq[2][2];
#pragma unroll
    for (int z = 0; z < 2; z++) {
        const int o = (G >> 2) + 4 * z;
        const u32 ka = base + (8 * o + q) * 128 + 32 * cb + 8 * p, va = ka + 9216;
        kq[z][0] = ldstr(sm, ka); kq[z][1] = ldstr(sm, ka + 512); kq[z][2] = ldstr(sm, ka + 1024);
        vq[z][0] = ldstr(sm, va); vq[z][1] = ldstr(sm, va + 512);
    }
#pragma unroll
    for (int z = 0; z < 2; z++) {
        const int o = (G >> 2) + 4 * z;
        u32 K[6] = {kq[z][0].x, kq[z][0].y, kq[z][1].x, kq[z][1].y, kq[z][2].x, kq[z][2].y};
        if (s0 == 0 && o == 0) { K[0] = 0; K[1] = 0; }
        u32 Sh[5];
#pragma unroll
        for (int e = 0; e < 5; e++) Sh[e] = __builtin_amdgcn_alignbit(K[e + 1], K[e], 16);
        const u32 V[4] = {vq[z][0].x, vq[z][0].y, vq[z][1].x, vq[z][1].y};
        u32 r[4];
#pragma unroll
        for (int e = 0; e < 4; e++) {
            const float k0 = dot2(Sh[e], wa, dot2(Sh[e + 1], wb, 0.f)), k1 = dot2(K[e + 1], wa, dot2(K[e + 2], wb, 0.f));
            r[e] = pk2(k0 * blo(V[e]), k1 * bhi(V[e]));
        }
        *(LAS u32x4*)(sm + outb + ch * 128 + ((o ^ (ch & 7)) * 16)) = (u32x4){r[0], r[1], r[2], r[3]};
    }
}
DI void p0_store(LAS char* sm, rsrc_t rw, int tile, int t256, u32 outb) {
    const int cg = tile % 10, s0 = (tile / 10) * 64;
#pragma unroll
    for (int z = 0; z < 2; z++) {
        const int ch2 = (t256 >> 3) + 32 * z, o2 = t256 & 7;
        const u32x4 t4 = lds128(sm, outb + ch2 * 128 + ((o2 ^ (ch2 & 7)) * 16));
        st128c(rw, WS_UT + (cg * 64 + ch2) * 4096 + (s0 + 8 * o2) * 2, t4);
    }
}
DI void p0_weights(u32x2& w4, const u16* shk, int tile, int wave, int lane) {
    const int cg = tile % 10, cb = (wave * 4 + (lane >> 4)) & 3;
    gld64(w4, shk, (u32)((cg * 64 + 16 * cb + (lane & 15)) * 8));
}

struct K1Set { u32x4 xa[2][2]; u32x2 wl[2][2], wh[2][2]; };
struct K1Addr { u32 x[2][2]; u32 w[2][2]; };
template <bool QT, int WO, int XO>
DI void k1_phase(LAS char* sm, f32x16 (&acc)[2][2], const K1Set& cur, K1Set& nxt, const u32 (&xb)[2], const u32 (&wb)[2][2]) {
#pragma unroll
    for (int n = 0; n < 8; n++) {
        const int ks = n >> 2, i = (n >> 1) & 1, jj = n & 1;
        const u32x4 wf = {cur.wl[ks][jj].x, cur.wl[ks][jj].y, cur.wh[ks][jj].x, cur.wh[ks][jj].y};
        acc[i][jj] = QT ? mfma32(wf, cur.xa[ks][i], acc[i][jj]) : mfma32(cur.xa[ks][i], wf, acc[i][jj]);
        if (n == 0) { nxt.wl[0][0] = ldstr(sm, wb[0][0] + WO); nxt.wh[0][0] = ldstr(sm, wb[0][1] + WO); }
        if (n == 1) { nxt.xa[0][0] = lds128(sm, xb[0] + XO); nxt.wl[0][1] = ldstr(sm, wb[1][0] + WO); }
        if (n == 2) { nxt.wh[0][1] = ldstr(sm, wb[1][1] + WO); nxt.xa[0][1] = lds128(sm, xb[0] + XO + 4096); }
        if (n == 3) { nxt.wl[1][0] = ldstr(sm, wb[0][0] + WO + 4096); nxt.wh[1][0] = ldstr(sm, wb[0][1] + WO + 4096); }
        if (n == 4) { nxt.xa[1][0] = lds128(sm, xb[1] + XO); nxt.wl[1][1] = ldstr(sm, wb[1][0] + WO + 4096); }
        if (n == 5) { nxt.wh[1][1] = ldstr(sm, wb[1][1] + WO + 4096); nxt.xa[1][1] = lds128(sm, xb[1] + XO + 4096); }
        SB();
    }
}
template <bool QT, int ST>
DI void k1_chunk(LAS char* sm, f32x16 (&acc)[2][2], K1Set& s0, K1Set& s1, const K1Addr& a0, const K1Addr& a1, bool bar, bool drain = false) {
    constexpr int SN = (ST + 1) & 3;
    const K1Addr& ac = ST < 2 ? a0 : a1; const K1Addr& an = SN < 2 ? a0 : a1;
    k1_phase<QT, (ST & 1) * K1_STAGE + 8192, (ST & 1) * K1_STAGE + 16384>(sm, acc, s0, s1, ac.x[1], ac.w);
    if (drain) VMCNT(0);
    if (bar) { LGKM0(); BAR(); }
    k1_phase<QT, (SN & 1) * K1_STAGE, (SN & 1) * K1_STAGE + 16384>(sm, acc, s1, s0, an.x[0], an.w);
}
template <bool QT>
DI void k1_loop(LAS char* sm, const K1Addr& a0, f32x16 (&acc)[2][2], rsrc_t rw, u32 pflag, u32 tag, int wave, int lane, const u16* hk, const u16* wo, int n240) {
    K1Addr a1;
#pragma unroll
    for (int i = 0; i < 2; i++)
#pragma unroll
        for (int z = 0; z < 2; z++) { a1.x[i][z] = a0.x[i][z] + 65536; a1.w[i][z] = a0.w[i][z] + 65536; }
    K1Set s0, s1;
#pragma unroll
    for (int ks = 0; ks < 2; ks++)
#pragma unroll
        for (int jj = 0; jj < 2; jj++) {
            s0.wl[ks][jj] = ldstr(sm, a0.w[jj][0] + ks * 4096); s0.wh[ks][jj] = ldstr(sm, a0.w[jj][1] + ks * 4096);
            s0.xa[ks][jj] = lds128(sm, a0.x[0][ks] + 16384 + jj * 4096);
        }
    SB();
#pragma unroll 1
    for (int c = 0; c < 8; c += 4) {
        k1_chunk<QT, 0>(sm, acc, s0, s1, a0, a1, true);
        k1_chunk<QT, 1>(sm, acc, s0, s1, a0, a1, true);
        k1_chunk<QT, 2>(sm, acc, s0, s1, a0, a1, true);
        k1_chunk<QT, 3>(sm, acc, s0, s1, a0, a1, true, c == 0);
        if (c == 0 && wave == 0 && lane == 0) set_flag(rw, pflag, tag);
        if (PREF2 && c == 0 && wave >= 2) {
            if (wave == 2) { const u32 l_ = n240 * 86 + lane; touch(hk, (l_ < 20480 ? l_ : 20479) * 128); }
            else { const u32 l_ = n240 * 86 + 64 + lane, m_ = n240 * 27 + lane - 22;
                   if (lane < 22) touch(hk, (l_ < 20480 ? l_ : 20479) * 128); else if (lane < 49) touch(wo, (m_ < 6400 ? m_ : 6399) * 128); }
        }
    }
    k1_chunk<QT, 0>(sm, acc, s0, s1, a0, a1, true);
    k1_chunk<QT, 1>(sm, acc, s0, s1, a0, a1, false);
}

DI void wo_issue(const u16* wo, int ct, int k0, int k1, int w, int nw, int lane) {
    u32 vo[5], vs[5];
#pragma unroll
    for (int i = 0; i < 5; i++) {
        const int n = 64 * i + lane, rho = n / 10, c = n - 10 * rho, g = 2 * (rho >> 4) + ((rho >> 2) & 1), r8 = 4 * ((rho >> 3) & 1) + (rho & 3);
        vo[i] = (u32)((8 * (g < 2 ? g : g + 1) + r8) * 1280 + ct * 160 + c * 16);
        vs[i] = (u32)((40 * g + 16 + r8) * 1280 + ct * 160 + c * 16);
    }
#pragma unroll 1
    for (int kap = k0 + w; kap < k1; kap += nw) {
        const u32 dst = kap < 12 ? kap * 5120 : WO_HI + (kap - 12) * 5120;
        if (kap < 16) {
            const char* src = (const char*)wo + (size_t)kap * 51200;
#pragma unroll
            for (int i = 0; i < 5; i++) glds16(src, vo[i], dst + i * 1024);
        } else {
            const char* src = (const char*)wo + (size_t)(kap - 16) * 204800;
#pragma unroll
            for (int i = 0; i < 5; i++) glds16(src, vs[i], dst + i * 1024);
        }
    }
}

struct CvSet { u32x2 fl[8], fh[8]; u32x4 g[8]; };
DI void cv_load(LAS char* sm, CvSet& s, u32 fa, u32 ga) {
#pragma unroll
    for (int k1 = 0; k1 < 8; k1++) { s.fl[k1] = ldstr(sm, fa + 512 * k1 + 8); s.fh[k1] = ldstr(sm, fa + 512 * k1); s.g[k1] = lds128(sm, ga + 512 * k1); }
}
DI void cv_phase(LAS char* sm, f32x4 (&Y)[8], const CvSet& cur, CvSet& nxt, u32 fa, u32 ga) {
    PIN(fa); PIN(ga);
    SB();
    int n = 0;
#pragma unroll
    for (int e = 0; e < 8; e++)
#pragma unroll
        for (int k1 = 0; k1 + e < 8; k1++) {
            const u32x4 F = {cur.fl[k1].x, cur.fl[k1].y, cur.fh[k1].x, cur.fh[k1].y};
            Y[k1 + e] = mfma16(F, cur.g[e], Y[k1 + e]);
            if (n == 0) nxt.g[0] = lds128(sm, ga);
            else if (n <= 16) { const int r = (n - 1) >> 1; if ((n - 1) & 1) nxt.fh[r] = ldstr(sm, fa + 512 * r); else nxt.fl[r] = ldstr(sm, fa + 512 * r + 8); }
            else if (n < 24) nxt.g[n - 16] = lds128(sm, ga + 512 * (n - 16));
            n++;
            SB();
        }
}
struct CvIn { u32x4 hd[4]; u32x2 hm[4]; };
DI void conv_fetch_h(CvIn& in, const u16* hk, int c, int lane) {
    const char* hb = (const char*)hk + (size_t)c * 4096;
#pragma unroll
    for (int z = 0; z < 4; z++) {
        const int i = lane + 64 * z;
        if (NT_H) asm volatile("global_load_dwordx4 %0, %1, %2 nt" : "=v"(in.hd[z]) : "v"((u32)(i * 16)), "s"(hb) : "memory"); else gld128(in.hd[z], hb, (u32)(i * 16));
        gld64(in.hm[z], hb, (u32)(i > 0 ? i * 16 - 8 : 0));
    }
}
DI void conv_build_h(LAS char* sm0, CvIn& in, int w, int lane) {
    LAS char* sm = sm0 + CV_BASE + w * CV_WAVE;
#pragma unroll
    for (int z = 0; z < 4; z++) { PIN(in.hd[z]); PIN(in.hm[z]); }
    if (lane < 32) *(LAS u32x4*)(sm + CV_U + lane * 16) = (u32x4){0, 0, 0, 0};
    if (lane < 8) *(LAS u32x4*)(sm + (lane >> 1) * CV_PITCH + (lane & 1) * 16) = (u32x4){0, 0, 0, 0};
#pragma unroll
    for (int z = 0; z < 4; z++) {
        const int i = lane + 64 * z;
        u32 m2 = in.hm[z].x, m1 = in.hm[z].y;
        if (i == 0) { m2 = 0; m1 = 0; }
        const u32 d0 = in.hd[z].x, d1 = in.hd[z].y, d2 = in.hd[z].z, d3 = in.hd[z].w;
        const u32 om2 = __builtin_amdgcn_alignbit(m1, m2, 16), om1 = __builtin_amdgcn_alignbit(d0, m1, 16), o0 = __builtin_amdgcn_alignbit(d1, d0, 16),
                  o1 = __builtin_amdgcn_alignbit(d2, d1, 16), o2 = __builtin_amdgcn_alignbit(d3, d2, 16);
        LAS char* p = sm + 32 + i * 16;
        *(LAS u32x4*)(p) = (u32x4){d0, d1, d2, d3};
        *(LAS u32x4*)(p + CV_PITCH) = (u32x4){om1, o0, o1, o2};
        *(LAS u32x4*)(p + 2 * CV_PITCH) = (u32x4){m1, d0, d1, d2};
        *(LAS u32x4*)(p + 3 * CV_PITCH) = (u32x4){om2, om1, o0, o1};
    }
}
DI void conv_run(LAS char* sm0, float Dc, int w, int lane, int c, rsrc_t rw, char* ws) {
    LAS char* sm = sm0;
    LGKM0();
    TS(4, 9);
    const int fr = lane & 15, g = lane >> 4, q = fr >> 2, p = lane & 3;
    sm = sm0;
    const u32 fa = CV_BASE + w * CV_WAVE + q * CV_PITCH + 2 * (32 - 8 * g + 4 * p) - 8;
    const u32 ga = CV_BASE + w * CV_WAVE + CV_U + 480 + 32 * fr + 16 * g;
    f32x4 Y[8];
#pragma unroll
    for (int i = 0; i < 8; i++) Y[i] = (f32x4){0, 0, 0, 0};
    CvSet A, B;
    cv_load(sm, A, fa, ga);
    SB();
#pragma unroll 1
    for (int k0 = 0; k0 < 8; k0 += 2) {
        cv_phase(sm, Y, A, B, fa + 64 * (k0 + 1), ga - 64 * (k0 + 1));
        cv_phase(sm, Y, B, A, fa + 64 * (k0 + 2), ga - 64 * (k0 + 2));
    }
    TS(4, 10);
    sm = sm0 + CV_BASE + w * CV_WAVE;
    {
        u32x2 u4[8];
#pragma unroll
        for (int ib = 0; ib < 8; ib++) u4[ib] = lds64(sm, CV_U + 512 + 512 * ib + 32 * fr + 8 * g);
#pragma unroll
        for (int ib = 0; ib < 8; ib++) {
            const u32x2 r = {pk2(fmaf(blo(u4[ib].x), Dc, Y[ib][0]), fmaf(bhi(u4[ib].x), Dc, Y[ib][1])), pk2(fmaf(blo(u4[ib].y), Dc, Y[ib][2]), fmaf(bhi(u4[ib].y), Dc, Y[ib][3]))};
            *(LAS u32x2*)(sm + 512 * ib + 32 * fr + 8 * g) = r;
        }
    }
    LGKM0();
    u32x4 y8[4];
#pragma unroll
    for (int z = 0; z < 4; z++) y8[z] = lds128(sm, z * 1024 + lane * 16);
#pragma unroll
    for (int z = 0; z < 4; z++) st128c(rw, WS_YT + c * 4096 + z * 1024 + lane * 16, y8[z]);
}

DI void kvo_block(LAS char* sm, int wave, int lane, int h, int half, rsrc_t rw, u32 tag, char* ws) {
    const int fr = lane & 15, g = lane >> 4;
    const int j0 = (20 * h) >> 7, j1 = (20 * h + 19) >> 7;
    wait_flags(rw, lane < 8 ? (int)F_G1 + 16 * (((lane & 2) ? 10 : 5) + ((lane & 1) ? j1 : j0)) + 2 * wave + (lane >> 2)
                            : lane < 10 ? (int)F_G1 + 16 * ((lane & 1) ? j1 : j0) + 8 * half + wave : -1, -1, tag);
    TS(0, 8);
    const u32 pk0 = WS_PK0 + (h * 64 + 8 * wave) * 1024 + lane * 16, v0 = pk0 + (WS_V0 - WS_PK0);
    const u32 xx = ((fr & 4) ? WS_XV : WS_XK) + ((h * 64 + 8 * wave + (fr >> 3)) * 4 + g) * 64 + (fr & 3) * 16;
    u32x4 A[8], B[8], X[4], Q[8];
#pragma unroll
    for (int i = 0; i < 8; i++) { A[i] = ld128c(rw, pk0 + i * 1024); B[i] = ld128c(rw, v0 + i * 1024); if ((i & 1) == 0) X[i >> 1] = ld128c(rw, xx + (i >> 1) * 512); }
    const int odd = h & 1, pr = h >> 1;
    const int u5 = (g < 3 ? g : 2) + 2 * odd;
    const int sb0 = half * 64 + wave * 8;
    const u32 pq = WS_P2 + (((pr * 128 + sb0) * 5 + u5) * 128 + fr * 8) * 2;
    MEMF(); SB();
    f32x4 a00 = {0, 0, 0, 0}, a0x = a00, ax0 = a00, axx = a00;
    const u32 cst = fr == 8 ? 0x3F803F80u : 0u;
#pragma unroll
    for (int i = 0; i < 8; i++) {
        u32x4 xv = X[i >> 1];
        if (i & 1) {
#pragma unroll
            for (int e = 0; e < 4; e++) xv[e] = (u32)__builtin_amdgcn_update_dpp(0, (int)xv[e], 0x108, 0xf, 0xf, false);
        }
        if (fr >= 8) xv = (u32x4){cst, cst, cst, cst};
        a00 = mfma16(A[i], B[i], a00); a0x = mfma16(A[i], xv, a0x); ax0 = mfma16(xv, B[i], ax0); axx = mfma16(xv, xv, axx);
    }
    PIN(a00); PIN(a0x); PIN(ax0); PIN(axx);
    SB();
#pragma unroll
    for (int i = 0; i < 8; i++) Q[i] = ld128c(rw, pq + i * 1280);
    MEMF(); SB();
    TS(0, 9);
    *(LAS f32x4*)(sm + KO_RED + ((wave * 4 + 0) * 64 + lane) * 16) = a00;
    *(LAS f32x4*)(sm + KO_RED + ((wave * 4 + 1) * 64 + lane) * 16) = a0x;
    *(LAS f32x4*)(sm + KO_RED + ((wave * 4 + 2) * 64 + lane) * 16) = ax0;
    *(LAS f32x4*)(sm + KO_RED + ((wave * 4 + 3) * 64 + lane) * 16) = axx;
    if (wave >= 4 && (wave - 4) * 64 + lane < 189) *(LAS u32x2*)(sm + KO_KV + ((wave - 4) * 64 + lane) * 8) = (u32x2){0, 0};
    LGKM0(); BAR();
    if (wave < 4) {
        const int tile = wave;
        f32x4 s = *(LAS f32x4*)(sm + KO_RED + (tile * 64 + lane) * 16);
#pragma unroll
        for (int w2 = 1; w2 < 8; w2++) s += *(LAS f32x4*)(sm + KO_RED + ((w2 * 4 + tile) * 64 + lane) * 16);
        const int e = (tile & 1) ? 12 + fr : fr;
        const bool colok = (tile & 1) ? (fr >= 4 && fr <= 8) : true;
        const bool rowok = (tile & 2) ? (g == 0) : true;
        const int d0 = (tile & 2) ? 16 : 4 * g;
        if (colok && rowok) *(LAS u32x2*)(sm + KO_KV + e * 72 + (d0 + 4) * 2) = (u32x2){pk2(s[0], s[1]), pk2(s[2], s[3])};
    }
    LGKM0(); BAR();
    u32x4 A0, A1[4], A2;
    {
        const u32 dofs = KO_KV + (8 * g - 4 * odd + 4) * 2;
        const u32 r0 = (4 * odd + fr) * 72, r1 = ((odd ? 0 : 16) + (fr & 3)) * 72;
        const u32x2 p0 = lds64(sm, dofs + r0), p1 = lds64(sm, dofs + r0 + 8);
        const u32x2 q0 = lds64(sm, dofs + r1), q1 = lds64(sm, dofs + r1 + 8);
        const u32x2 z0 = lds64(sm, dofs + 20 * 72), z1 = lds64(sm, dofs + 20 * 72 + 8);
        A0 = (u32x4){p0.x, p0.y, p1.x, p1.y}; A2 = (u32x4){z0.x, z0.y, z1.x, z1.y};
#pragma unroll
        for (int z = 0; z < 4; z++) { const bool m = (fr >> 2) == z; A1[z] = (u32x4){m ? q0.x : 0u, m ? q0.y : 0u, m ? q1.x : 0u, m ? q1.y : 0u}; }
    }
    TS(0, 10);
    const u32 offA = WS_O + ((pr * 4 + 2 * odd + (g >> 1)) * 128 + fr * 8) * 2 + (g & 1) * 20480;
    const u32 offX = WS_O + (((16 + (pr >> 2)) * 4 + (pr & 3)) * 128 + odd * 64 + fr * 4) * 2;
    const f32x4 zero = {0, 0, 0, 0};
#pragma unroll
    for (int i4 = 0; i4 < 2; i4++) {
        f32x4 n1 = zero; float rden[4]; u32x2 ov[4];
#pragma unroll
        for (int z = 0; z < 4; z++) {
            const int i = 4 * i4 + z;
            const f32x4 n0 = mfma16(A0, Q[i], zero);
            const f32x4 dn = mfma16(A2, Q[i], zero);
            n1 = mfma16(A1[z], Q[i], n1);
            const float rd = __builtin_amdgcn_rcpf(dn[0] + 1e-6f);
            rden[z] = rd;
            ov[z] = (u32x2){pk2(n0[0] * rd, n0[1] * rd), pk2(n0[2] * rd, n0[3] * rd)};
        }
#pragma unroll
        for (int z = 0; z < 4; z += 2) {
            auto rx = __builtin_amdgcn_permlane16_swap(ov[z].x, ov[z + 1].x, false, false);
            auto ry = __builtin_amdgcn_permlane16_swap(ov[z].y, ov[z + 1].y, false, false);
            st128c(rw, offA + (sb0 + 4 * i4 + z) * 20480, (u32x4){rx[0], ry[0], rx[1], ry[1]});
        }
        const float rd = g == 0 ? rden[0] : g == 1 ? rden[1] : g == 2 ? rden[2] : rden[3];
        st64c(rw, offX + (sb0 + 4 * i4 + g) * 20480, (u32x2){pk2(n1[0] * rd, n1[1] * rd), pk2(n1[2] * rd, n1[3] * rd)});
    }
}

DI void gate_block(int wave, int lane, int m, const u16* x, const u16* gw, rsrc_t rw) {
    const int sub = lane & 15, r4 = lane >> 4;
    u32x4 gq[5], xr[4][5];
#pragma unroll
    for (int jj = 0; jj < 5; jj++) gq[jj] = *(const GAS u32x4*)((const GAS char*)gw + (sub + 16 * jj) * 16);
#pragma unroll
    for (int it = 0; it < 4; it++)
#pragma unroll
        for (int jj = 0; jj < 5; jj++) xr[it][jj] = *(const GAS u32x4*)((const GAS char*)x + (size_t)(128 * m + 16 * wave + 4 * it + r4) * 1280 + (sub + 16 * jj) * 16);
    MEMF(); SB();
#pragma unroll
    for (int it = 0; it < 4; it++) {
        float a = 0.f;
#pragma unroll
        for (int jj = 0; jj < 5; jj++)
#pragma unroll
            for (int e = 0; e < 4; e++) { a = fmaf(blo(xr[it][jj][e]), blo(gq[jj][e]), a); a = fmaf(bhi(xr[it][jj][e]), bhi(gq[jj][e]), a); }
        a += __shfl_xor(a, 1); a += __shfl_xor(a, 2); a += __shfl_xor(a, 4); a += __shfl_xor(a, 8);
        a = rbf(a);
        const float g = rbf(__builtin_amdgcn_rcpf(1.f + __builtin_amdgcn_exp2f(-1.4426950408889634f * a)));
        if (sub == 0) st32c(rw, WS_G + (128 * m + 16 * wave + 4 * it + r4) * 4, __float_as_uint(g));
    }
}

DI void final_block(LAS char* sm, int wave, int lane, int tid, int st, int ct, const u16* x, u16* out, rsrc_t rw, u32 tag, char* ws) {
    const int fr = lane & 15, g = lane >> 4;
    u32x4 xv[2];
#pragma unroll
    for (int r = 0; r < 2; r++) {
        int it = tid + 512 * r; it = it < 640 ? it : 639;
        const int s = it / 10, u = it - 10 * s;
        xv[r] = *(const GAS u32x4*)((const GAS char*)x + (size_t)(st * 64 + s) * 1280 + ct * 160 + u * 16);
    }
    VMCNT(0); BAR();
    if (wave < 4) {
        const int kh = wave & 1, sh = wave >> 1;
        wait_flags(rw, lane < 16 ? (int)F_O + (st >> 4) * 32 + 16 * kh + lane : -1, -1, tag);
        TS(0, 17);
        const u32 ob = WS_O + ((st * 4 + 2 * sh) * 20 + 8 * kh) * 1024 + lane * 16;
        const u32 os = WS_O + ((st * 4 + 2 * sh) * 20 + 16 + 2 * kh) * 1024 + g * 256 + fr * 8;
        u32x4 B[2][10];
#pragma unroll
        for (int kl = 0; kl < 8; kl++) { B[0][kl] = ld128c(rw, ob + kl * 1024); B[1][kl] = ld128c(rw, ob + 20480 + kl * 1024); }
#pragma unroll
        for (int kl = 0; kl < 2; kl++)
#pragma unroll
            for (int z = 0; z < 2; z++) {
                const u32x2 h0 = ld64c(rw, os + z * 20480 + kl * 1024), h1 = ld64c(rw, os + z * 20480 + kl * 1024 + 128);
                B[z][8 + kl] = (u32x4){h0.x, h0.y, h1.x, h1.y};
            }
        MEMF(); SB();
        f32x4 acc[5][2];
#pragma unroll
        for (int cb = 0; cb < 5; cb++) { acc[cb][0] = (f32x4){0, 0, 0, 0}; acc[cb][1] = (f32x4){0, 0, 0, 0}; }
        const u32 aoff = (16 * (g >> 1) + 4 * (g & 1) + (fr >> 2)) * 160 + (lane & 3) * 8;
        u32x2 al[2][5], ah[2][5];
#define F_KAP(kl) ((kl) < 8 ? (kl) : 8 + (kl))
#define F_READ(buf, kl) do { const u32 b0_ = kh ? wo_off(F_KAP(kl) + ((kl) < 8 ? 8 : 2)) : wo_off(F_KAP(kl)); \
            _Pragma("unroll") for (int cb = 0; cb < 5; cb++) { al[buf][cb] = ldstr(sm, b0_ + aoff + cb * 32); ah[buf][cb] = ldstr(sm, b0_ + aoff + cb * 32 + 1280); } } while (0)
        F_READ(0, 0);
#pragma unroll
        for (int kl = 0; kl < 10; kl++) {
            if (kl < 9) F_READ((kl + 1) & 1, kl + 1);
            SB();
#pragma unroll
            for (int cb = 0; cb < 5; cb++) {
                const u32x4 a = {al[kl & 1][cb].x, al[kl & 1][cb].y, ah[kl & 1][cb].x, ah[kl & 1][cb].y};
                acc[cb][0] = mfma16(a, B[0][kl], acc[cb][0]); acc[cb][1] = mfma16(a, B[1][kl], acc[cb][1]);
            }
            SB();
        }
        TS(0, 18);
        lds_arrive(sm, C_FR, lane); lds_wait(sm, C_FR, 4);
#pragma unroll
        for (int cb = 0; cb < 5; cb++)
#pragma unroll
            for (int z = 0; z < 2; z++)
                *(LAS f32x4*)(sm + kh * (64 * RED_PITCH) + (16 * (2 * sh + z) + fr) * RED_PITCH + (16 * cb + 4 * g) * 4) = acc[cb][z];
    } else {
        if (wave == 4) { wait_flags(rw, lane < 20 ? (int)F_Y + ct * 32 + lane : lane == 32 ? (int)F_GATE + (st >> 1) : -1, -1, tag); lds_arrive(sm, C_FY, lane); }
        else lds_wait(sm, C_FY, 1);
        const int t = tid - 256;
        u32x4 yv[3];
#pragma unroll
        for (int r = 0; r < 3; r++) { int id = t + 256 * r; id = id < 640 ? id : 639; yv[r] = ld128c(rw, WS_YT + (80 * ct + (id >> 3)) * 4096 + st * 128 + (id & 7) * 16); }
#pragma unroll
        for (int r = 0; r < 3; r++) { const int id = t + 256 * r; if (id < 640) *(LAS u32x4*)(sm + F_YY + id * 16) = yv[r]; }
        if (wave == 4) *(LAS u32*)(sm + F_GG + lane * 4) = ld32c(rw, WS_G + (st * 64 + lane) * 4);
        lds_arrive(sm, C_FY2, lane); lds_wait(sm, C_FY2, 4);
        const int sb = wave - 4, q = fr >> 2, p = lane & 3;
        u32x2 tt[5];
#pragma unroll
        for (int cb = 0; cb < 5; cb++) tt[cb] = ldstr(sm, F_YY + (16 * cb + 4 * g + q) * 128 + (16 * sb + 4 * p) * 2);
#pragma unroll
        for (int cb = 0; cb < 5; cb++) *(LAS u32x2*)(sm + F_Y2 + (16 * sb + fr) * 176 + (16 * cb + 4 * g) * 2) = tt[cb];
    }
    LGKM0(); BAR();
#pragma unroll
    for (int r = 0; r < 2; r++) {
        const int it = tid + 512 * r;
        if (it < 640) {
            const int s = it / 10, u = it - 10 * s;
            const u32 ra = s * RED_PITCH + u * 32;
            const f32x4 p0 = *(LAS f32x4*)(sm + ra), p1 = *(LAS f32x4*)(sm + ra + 16), q0 = *(LAS f32x4*)(sm + 64 * RED_PITCH + ra), q1 = *(LAS f32x4*)(sm + 64 * RED_PITCH + ra + 16);
            const u32x4 y8 = lds128(sm, F_Y2 + s * 176 + u * 16);
            const float gg = *(LAS float*)(sm + F_GG + s * 4), omg = 1.f - gg;
            float o[8];
#pragma unroll
            for (int e = 0; e < 4; e++) {
                const float a0 = (e < 2 ? p0[2 * e] + q0[2 * e] : p1[2 * e - 4] + q1[2 * e - 4]), a1 = (e < 2 ? p0[2 * e + 1] + q0[2 * e + 1] : p1[2 * e - 3] + q1[2 * e - 3]);
                const float b0 = omg * blo(xv[r][e]), b1 = omg * bhi(xv[r][e]);
                o[2 * e] = fmaf(b0, blo(y8[e]), gg * a0); o[2 * e + 1] = fmaf(b1, bhi(y8[e]), gg * a1);
            }
            *(GAS u32x4*)((GAS char*)out + (size_t)(st * 64 + s) * 1280 + ct * 160 + u * 16) = (u32x4){pk2(o[0], o[1]), pk2(o[2], o[3]), pk2(o[4], o[5]), pk2(o[6], o[7])};
        }
    }
}

__global__ __launch_bounds__(512, 2) void fused_kernel(const u16* __restrict__ x, const u16* __restrict__ w, const u16* __restrict__ wo, const u16* __restrict__ kin,
                                                        const u16* __restrict__ vin, const u16* __restrict__ shk, const u16* __restrict__ hk, const u16* __restrict__ sD,
                                                        const u16* __restrict__ gw, u16* __restrict__ out, char* ws) {
    extern __shared__ __attribute__((aligned(16))) char smem[];
    LAS char* sm = (LAS char*)smem;
    const int tid = threadIdx.x, lane = tid & 63, wave = __builtin_amdgcn_readfirstlane(tid >> 6);
    const int b = blockIdx.x, xcd = b & 7, j = b >> 3;
    const rsrc_t rw = __builtin_amdgcn_make_buffer_rsrc(ws, 0, WS_BYTES, 0x00020000);
    u32 tag;
    asm volatile("s_load_dword %0, %1, 0x0\n\ts_waitcnt lgkmcnt(0)" : "=s"(tag) : "s"(ws + WS_FLAG + F_EPOCH * FS) : "memory");
    tag += 1u;
    if (tid < 16) *(LAS u32*)(sm + L_CNT + tid * 4) = 0u;
    TS(0, 0);
    if (PREFC && wave == 3) {
        unsigned long long pc;
        asm volatile("s_getpc_b64 %0" : "=s"(pc));
        pc &= ~127ull;
        const int ln = PREFC == 1 ? j * 10 + lane : lane;
        if (PREFC == 1) { if (lane < 10 && ln < CODE_LINES) touch((const void*)pc, (u32)(ln * 128)); }
        else {
#pragma unroll
            for (int z = 0; z < 5; z++) if (z * 64 + lane < CODE_LINES) touch((const void*)pc, (u32)((z * 64 + lane) * 128));
        }
    }
    if (PREFT && wave == 2) {
        if (PREFT == 1) { touch(ws, (u32)(lane * 262144)); if (lane < 40) touch(out, (u32)(lane * 65536)); }
        else { touch(ws, (u32)((j * 8 + (lane & 7)) * 65536 + (lane >> 3) * 128) % (WS_STAMP - 1024)); if (lane < 40) touch(out, (u32)(lane * 65536)); }
    }
    const int st = 4 * xcd + (j >> 3), ct = j & 7;
    int ko = -1;
    bool is_conv = false;

    if (j >= 30) {
        const int m = xcd * 2 + (j - 30);
        ko = -100;
        {
            const int grp = wave >> 2, w4i = wave & 3;
            u32x2 w4[3];
#pragma unroll
            for (int t = 0; t < 3; t++) { const int tt = 2 * t + grp < 5 ? 2 * t + grp : 4; p0_weights(w4[t], shk, 240 + 5 * m + tt, w4i, lane); }
#pragma unroll
            for (int t = 0; t < 3; t++) if (2 * t + grp < 5) p0_issue(kin, vin, 240 + 5 * m + 2 * t + grp, w4i, lane, (2 * t + grp) * 17408);
            VMCNT(0);
#pragma unroll
            for (int t = 0; t < 3; t++) PIN(w4[t]);
            BAR();
#pragma unroll
            for (int t = 0; t < 3; t++)
                if (2 * t + grp < 5) p0_compute(sm, w4[t], 240 + 5 * m + 2 * t + grp, w4i, lane, (2 * t + grp) * 17408, 90112 + (2 * t + grp) * 8192);
            LGKM0(); BAR();
            {
                u32x4 t4[3][2];
#pragma unroll
                for (int t = 0; t < 3; t++)
#pragma unroll
                    for (int z = 0; z < 2; z++) { const int ch2 = ((tid & 255) >> 3) + 32 * z, o2 = tid & 7, tt = 2 * t + grp < 5 ? 2 * t + grp : 4; t4[t][z] = lds128(sm, 90112 + tt * 8192 + ch2 * 128 + ((o2 ^ (ch2 & 7)) * 16)); }
#pragma unroll
                for (int t = 0; t < 3; t++)
                    if (2 * t + grp < 5) {
                        const int tile = 240 + 5 * m + 2 * t + grp, cg = tile % 10, s0 = (tile / 10) * 64;
#pragma unroll
                        for (int z = 0; z < 2; z++) { const int ch2 = ((tid & 255) >> 3) + 32 * z, o2 = tid & 7; st128c(rw, WS_UT + (cg * 64 + ch2) * 4096 + (s0 + 8 * o2) * 2, t4[t][z]); }
                    }
            }
        }
        VMCNT(0); BAR();
        if (tid < 5) set_flag(rw, p0_flag(240 + 5 * m + tid), tag);
        TS(0, 1);
        gate_block(wave, lane, m, x, gw, rw);
        VMCNT(0); LGKM0(); BAR();
        if (tid == 0) set_flag(rw, F_GATE + m, tag);
        TS(0, 3);
    } else {
        const int rt = 2 * xcd + j / 15, cbk = j % 15, tile = xcd * 30 + j;
        is_conv = cbk < 10;
        if (!is_conv) { const int vi = rt * 5 + (cbk - 10); ko = vi < 64 ? vi : -2 - (vi - 64); }
        if (wave >= 4) {
            const int lw = wave - 4;
            p0_issue(kin, vin, tile, lw, lane, P0_K);
            u32 vx[4], vw[4];
#pragma unroll
            for (int n2 = 0; n2 < 4; n2++) {
                const int n = 4 * lw + n2;
                const int row = 8 * n + (lane >> 3), ch = (lane & 7) ^ ((row >> 1) & 7);
                vx[n2] = (u32)((rt * 128 + row) * 1280 + ch * 16);
                const int kr = 4 * n + (lane >> 4), f = ((kr & 3) << 2) | ((kr >> 2) & 3), cw = (lane & 15) ^ f;
                vw[n2] = (u32)(kr * 3840 + cbk * 256 + cw * 16);
            }
            const int rot = (rt + cbk) % 10;
#define K1_ISSUE(c) do { const u32 st_ = ((c) & 3) * K1_STAGE + 4 * lw * 1024; int kc_ = (c) + ROT_ON * rot; kc_ = kc_ >= 10 ? kc_ - 10 : kc_; \
            const char* xb_ = (const char*)x + kc_ * 128; const char* wp_ = (const char*)w + (size_t)kc_ * 64 * 3840; \
            _Pragma("unroll") for (int n2 = 0; n2 < 4; n2++) { if (NT_X) glds16nt(xb_, vx[n2], st_ + 16384 + n2 * 1024); else glds16(xb_, vx[n2], st_ + 16384 + n2 * 1024); glds16(wp_, vw[n2], st_ + n2 * 1024); } } while (0)
            K1_ISSUE(0);
            VMCNT(8); BAR();
            K1_ISSUE(1); K1_ISSUE(2);
            VMCNT(16); BAR();
            K1_ISSUE(3);
#pragma unroll 1
            for (int c = 0; c < 6; c++) {
                VMCNT(16); BAR();
                K1_ISSUE(c + 4);
            }
            const int cv = rt * 10 + cbk, c = 4 * cv + lw;
            if (is_conv) {
                CvIn in; u32 fl; u32x4 u[4];
                const int fidx = (int)F_P0 + (cv >> 4) * 32 + (lane & 31);
                conv_fetch_h(in, hk, c, lane);
                VMCNT(24); BAR();
                VMCNT(16); BAR();
                gld32c(fl, ws, WS_FLAG + fidx * FS);
                VMCNT(9); BAR();
                TS(4, 4);
                VMCNT(0); PIN(fl);
                const bool ready = __all(fl == tag);
                if (ready) {
#pragma unroll
                    for (int z = 0; z < 4; z++) gld128c(u[z], ws, WS_UT + c * 4096 + z * 1024 + lane * 16);
                }
                TS(4, 5);
                conv_build_h(sm, in, lw, lane);
                TS(4, 6);
                if (!ready) {
                    wait_flags(rw, fidx, -1, tag);
#pragma unroll
                    for (int z = 0; z < 4; z++) gld128c(u[z], ws, WS_UT + c * 4096 + z * 1024 + lane * 16);
                }
                TS(4, 7);
                VMCNT(0);
#pragma unroll
                for (int z = 0; z < 4; z++) { PIN(u[z]); *(LAS u32x4*)(sm + CV_BASE + lw * CV_WAVE + CV_U + 512 + z * 1024 + lane * 16) = u[z]; }
                const float Dc = blo((u32)sD[c]);
                TS(4, 8);
                conv_run(sm, Dc, lw, lane, c, rw, ws);
                publish_last(rw, sm, C_Y, 4, F_Y + (cv / 20) * 32 + cv % 20, tag, lane);
                TS(4, 11);
            } else {
                VMCNT(16); BAR();
                VMCNT(8); BAR();
                VMCNT(0); BAR();
            }
        } else {
            u32x2 w4; p0_weights(w4, shk, tile, wave, lane);
            if (PREF1 && wave < 3) {
                const int n = wave * 64 + lane;
                if (wave == 0) touch(x, (u32)((xcd * 2560 + j * 80 + lane) * 128));
                else if (wave == 1) { if (lane < 16) touch(x, (u32)((xcd * 2560 + j * 80 + 64 + lane) * 128)); else touch(w, (u32)(((xcd * 30 + j) * 80 + lane - 16) * 128)); }
                else if (lane < 32) touch(w, (u32)(((xcd * 30 + j) * 80 + 48 + lane) * 128));
                (void)n;
            }
            const int wm = wave >> 1, wn = wave & 1, r32 = lane & 31, hh = lane >> 5;
            K1Addr a0;
#pragma unroll
            for (int ks = 0; ks < 2; ks++) {
                a0.x[0][ks] = (64 * wm + r32) * 128 + (((2 * ks + hh) ^ ((r32 >> 1) & 7)) * 16);
                a0.x[1][ks] = a0.x[0][ks] ^ 64;
            }
#pragma unroll
            for (int jj = 0; jj < 2; jj++)
#pragma unroll
                for (int t = 0; t < 2; t++) {
                    const int blk = (lane >> 4) & 1, q = (lane & 15) >> 2, p = lane & 3, row = 8 * hh + 4 * t + q, ch = 8 * wn + 4 * jj + 2 * blk + (p >> 1);
                    const int f = ((row & 3) << 2) | ((row >> 2) & 3);
                    a0.w[jj][t] = row * 256 + ((ch ^ f) * 16) + 8 * (p & 1);
                }
            f32x16 acc[2][2];
#pragma unroll
            for (int i = 0; i < 2; i++)
#pragma unroll
                for (int jj = 0; jj < 2; jj++)
#pragma unroll
                    for (int e = 0; e < 16; e++) acc[i][jj][e] = 0.f;
            TS(0, 20);
            VMCNT(0);
            PIN(w4); TS(0, 21); BAR();
            TS(0, 22);
            p0_compute(sm, w4, tile, wave, lane, P0_K, P0_OUT, ws);
            TS(0, 28);
            LGKM0(); TS(0, 23); BAR();
            TS(0, 24);
            p0_store(sm, rw, tile, tid, P0_OUT);
            TS(0, 1);
            if (cbk < 5) {
                k1_loop<true>(sm, a0, acc, rw, p0_flag(tile), tag, wave, lane, hk, wo, tile);
                __builtin_amdgcn_s_setprio(2);
                TS(0, 2);
                u32x4 qv[2][2][2];
#pragma unroll
                for (int jj = 0; jj < 2; jj++)
#pragma unroll
                    for (int ap = 0; ap < 2; ap++)
#pragma unroll
                        for (int i = 0; i < 2; i++) {
                            const f32x16& a = acc[i][jj];
                            const u32 x0 = phi2(a[8 * ap], a[8 * ap + 1]), y0 = phi2(a[8 * ap + 2], a[8 * ap + 3]);
                            const u32 x1 = phi2(a[8 * ap + 4], a[8 * ap + 5]), y1 = phi2(a[8 * ap + 6], a[8 * ap + 7]);
                            auto rx = __builtin_amdgcn_permlane32_swap(x0, x1, false, false);
                            auto ry = __builtin_amdgcn_permlane32_swap(y0, y1, false, false);
                            qv[jj][ap][i] = (u32x4){rx[0], ry[0], rx[1], ry[1]};
                        }
                if (QDELAY) { __builtin_amdgcn_s_setprio(0); for (int z = 0; z < QDELAY; z++) __builtin_amdgcn_s_sleep(8); }
#pragma unroll
                for (int jj = 0; jj < 2; jj++)
#pragma unroll
                    for (int ap = 0; ap < 2; ap++) {
                        const int U = 16 * cbk + 8 * wn + 4 * jj + 2 * ap + hh, pr = U / 5, u5 = U - 5 * pr;
                        const u32 dst = WS_P2 + (((pr * 128 + rt * 8 + 4 * wm + (r32 >> 4)) * 5 + u5) * 128 + (r32 & 15) * 8) * 2;
#pragma unroll
                        for (int i = 0; i < 2; i++) st128c(rw, dst + i * 2560, qv[jj][ap][i]);
                    }
            } else {
                k1_loop<false>(sm, a0, acc, rw, p0_flag(tile), tag, wave, lane, hk, wo, tile);
                __builtin_amdgcn_s_setprio(2);
                TS(0, 2);
                const bool isk = cbk < 10;
                const int j5 = isk ? cbk - 5 : cbk - 10;
                u32 dst[2], istep[2], zstep[2];
#pragma unroll
                for (int jj = 0; jj < 2; jj++) {
                    const int cc = 128 * j5 + 64 * wn + 32 * jj + r32, h = cc / 20, d = cc - 20 * h;
                    const int sb32 = rt * 4 + 2 * wm;
                    const bool lo = d < 16;
                    dst[jj] = lo ? (isk ? WS_PK0 : WS_V0) + (((h * 64 + sb32) * 4 + hh) * 128 + d * 8) * 2
                                 : (isk ? WS_XK : WS_XV) + (((h * 64 + sb32) * 4 + hh) * 32 + (d - 16) * 8) * 2;
                    istep[jj] = lo ? 1024 : 256; zstep[jj] = lo ? 512 : 128;
                }
                if (isk) {
#pragma unroll
                    for (int jj = 0; jj < 2; jj++)
#pragma unroll
                        for (int i = 0; i < 2; i++)
#pragma unroll
                            for (int z = 0; z < 2; z++) {
                                const f32x16& a = acc[i][jj];
                                st128c(rw, dst[jj] + i * istep[jj] + z * zstep[jj],
                                       (u32x4){phi2(a[8 * z], a[8 * z + 1]), phi2(a[8 * z + 2], a[8 * z + 3]), phi2(a[8 * z + 4], a[8 * z + 5]), phi2(a[8 * z + 6], a[8 * z + 7])});
                            }
                } else {
#pragma unroll
                    for (int jj = 0; jj < 2; jj++)
#pragma unroll
                        for (int i = 0; i < 2; i++)
#pragma unroll
                            for (int z = 0; z < 2; z++) {
                                const f32x16& a = acc[i][jj];
                                st128c(rw, dst[jj] + i * istep[jj] + z * zstep[jj],
                                       (u32x4){pk2(a[8 * z], a[8 * z + 1]), pk2(a[8 * z + 2], a[8 * z + 3]), pk2(a[8 * z + 4], a[8 * z + 5]), pk2(a[8 * z + 6], a[8 * z + 7])});
                            }
                }
            }
            LGKM0();
            TS(0, 25);
            publish_last(rw, sm, C_G1, 4, F_G1 + cbk * 16 + rt, tag, lane);
            __builtin_amdgcn_s_setprio(0);
            TS(0, 3);
            if (is_conv) {
                lds_wait(sm, C_G1, 4);
                wo_issue(wo, ct, 0, 12, wave, 4, lane);
            }
        }
    }
    if (!is_conv) {
        LGKM0(); BAR();
        if (ko >= 0) {
            const int h = ko >> 1, half = ko & 1;
            wo_issue(wo, ct, 0, 12, wave, 8, lane);
            kvo_block(sm, wave, lane, h, half, rw, tag, ws);
            TS(0, 26);
            VMCNT(0); BAR();
            if (tid == 0) set_flag(rw, F_O + half * 32 + h, tag);
            TS(0, 11);
            wo_issue(wo, ct, 12, 20, wave, 8, lane);
        } else {
            wo_issue(wo, ct, 0, 20, wave, 8, lane);
            if (ko == -2 && wave == 0) {
                wait_flags(rw, (int)F_G1 + lane, (int)F_G1 + 64 + lane, tag);
                wait_flags(rw, (int)F_G1 + 128 + lane, (int)F_G1 + 192 + lane, tag);
                if (lane == 0) set_flag(rw, F_EPOCH, tag);
            }
        }
    } else {
        VMCNT(0); BAR();
        if (wave >= 4) wo_issue(wo, ct, 12, 20, wave - 4, 4, lane);
    }
    TS(0, 16);
    final_block(sm, wave, lane, tid, st, ct, x, out, rw, tag, ws);
    TS(0, 19);
}

extern "C" void kernel_launch(void* const* d_in, const int* in_sizes, int n_in, void* d_out, int out_size, void* d_ws, size_t ws_size, hipStream_t stream) {
    const u16* x = (const u16*)d_in[0]; const u16* wqkv = (const u16*)d_in[1]; const u16* wo = (const u16*)d_in[2];
    const u16* kin = (const u16*)d_in[4]; const u16* vin = (const u16*)d_in[5]; const u16* shk = (const u16*)d_in[6];
    const u16* hk = (const u16*)d_in[7]; const u16* sD = (const u16*)d_in[8]; const u16* gw = (const u16*)d_in[9];
    (void)hipFuncSetAttribute((const void*)fused_kernel, hipFuncAttributeMaxDynamicSharedMemorySize, LDS_BYTES);
    fused_kernel<<<256, 512, LDS_BYTES, stream>>>(x, wqkv, wo, kin, vin, shk, hk, sD, gw, (u16*)d_out, (char*)d_ws);
}
```

```cpp
#include <hip/hip_runtime.h>
#include <stdint.h>

typedef unsigned short u16;
typedef unsigned int u32;
typedef __bf16 bf16x8 __attribute__((ext_vector_type(8)));
typedef __bf16 bf16x2 __attribute__((ext_vector_type(2)));
typedef float f32x4 __attribute__((ext_vector_type(4)));
typedef float f32x2 __attribute__((ext_vector_type(2)));
typedef float f32x16 __attribute__((ext_vector_type(16)));
typedef u32 u32x4 __attribute__((ext_vector_type(4)));
typedef u32 u32x2 __attribute__((ext_vector_type(2)));
typedef short s16x4 __attribute__((ext_vector_type(4)));
#define LAS __attribute__((address_space(3)))
#define GAS __attribute__((address_space(1)))
#define DI __device__ __forceinline__
typedef __amdgpu_buffer_rsrc_t rsrc_t;

constexpr int S = 2048, D = 640;
#define FS 128
#define POLL_SLEEP 8
#define ROT_ON 1

constexpr u32 WS_UT = 0;
constexpr u32 WS_YT = WS_UT + 2621440;
constexpr u32 WS_PK0 = WS_YT + 2621440;
constexpr u32 WS_V0 = WS_PK0 + 2097152;
constexpr u32 WS_XK = WS_V0 + 2097152;
constexpr u32 WS_XV = WS_XK + 524288;
constexpr u32 WS_P2 = WS_XV + 524288;
constexpr u32 WS_O = WS_P2 + 2621440;
constexpr u32 WS_G = WS_O + 2621440;
constexpr u32 WS_FLAG = WS_G + 8192;
constexpr u32 F_P0 = 0;
constexpr u32 F_G1 = 320;
constexpr u32 F_GATE = 560;
constexpr u32 F_O = 576;
constexpr u32 F_Y = 640;
constexpr u32 F_EPOCH = 896, F_TMO = 928;
constexpr u32 WS_STAMP = WS_FLAG + 1024 * FS;
constexpr u32 WS_BYTES = WS_STAMP + 256 * 32 * 16;

#define TS(w, id) do {} while (0)

DI u32 pk2(float a, float b) { f32x2 t = {a, b}; return __builtin_bit_cast(u32, __builtin_convertvector(t, bf16x2)); }
DI float blo(u32 u) { return __uint_as_float(u << 16); }
DI float bhi(u32 u) { return __uint_as_float(u & 0xffff0000u); }
DI float rbf(float f) { return blo(pk2(f, f)); }
DI bf16x8 asbf(u32x4 v) { return __builtin_bit_cast(bf16x8, v); }
DI f32x4 mfma16(u32x4 a, u32x4 b, f32x4 c) { return __builtin_amdgcn_mfma_f32_16x16x32_bf16(asbf(a), asbf(b), c, 0, 0, 0); }
DI f32x16 mfma32(u32x4 a, u32x4 b, f32x16 c) { return __builtin_amdgcn_mfma_f32_32x32x16_bf16(asbf(a), asbf(b), c, 0, 0, 0); }

DI void glds16(const void* sbase, u32 voff, u32 lds) {
    asm volatile("s_mov_b32 m0, %2\n\ts_nop 0\n\tglobal_load_lds_dwordx4 %0, %1" :: "v"(voff), "s"(sbase), "s"(lds) : "memory");
}
DI void glds16nt(const void* sbase, u32 voff, u32 lds) {
    asm volatile("s_mov_b32 m0, %2\n\ts_nop 0\n\tglobal_load_lds_dwordx4 %0, %1 nt" :: "v"(voff), "s"(sbase), "s"(lds) : "memory");
}
DI void touch(const void* sbase, u32 voff) {
    asm volatile("s_mov_b32 m0, %2\n\ts_nop 0\n\tglobal_load_lds_dword %0, %1" :: "v"(voff), "s"(sbase), "s"(156928u) : "memory");
}
#define NT_KV 0
#define NT_X 0
#define NT_H 0
#define PREF1 1
#define PREF2 1
#define PREFC 1
#define CODE_LINES 345
#define PREFT 0
#define QDELAY 0
DI void gld64(u32x2& d, const void* sbase, u32 voff) { asm volatile("global_load_dwordx2 %0, %1, %2" : "=v"(d) : "v"(voff), "s"(sbase) : "memory"); }
DI void gld128(u32x4& d, const void* sbase, u32 voff) { asm volatile("global_load_dwordx4 %0, %1, %2" : "=v"(d) : "v"(voff), "s"(sbase) : "memory"); }
DI void gld128c(u32x4& d, const void* sbase, u32 voff) { asm volatile("global_load_dwordx4 %0, %1, %2 sc1" : "=v"(d) : "v"(voff), "s"(sbase) : "memory"); }
DI void gld32c(u32& d, const void* sbase, u32 voff) { asm volatile("global_load_dword %0, %1, %2 sc1" : "=v"(d) : "v"(voff), "s"(sbase) : "memory"); }
#define PIN(x) asm volatile("" : "+v"(x))
#define SB() __builtin_amdgcn_sched_barrier(0)
DI u32x4 ld128c(rsrc_t r, u32 off) { return __builtin_amdgcn_raw_buffer_load_b128(r, off, 0, 16); }
DI u32x2 ld64c(rsrc_t r, u32 off) { return __builtin_amdgcn_raw_buffer_load_b64(r, off, 0, 16); }
DI u32 ld32c(rsrc_t r, u32 off) { return __builtin_amdgcn_raw_buffer_load_b32(r, off, 0, 16); }
DI void st128c(rsrc_t r, u32 off, u32x4 v) { __builtin_amdgcn_raw_buffer_store_b128(v, r, off, 0, 16); }
DI void st64c(rsrc_t r, u32 off, u32x2 v) { __builtin_amdgcn_raw_buffer_store_b64(v, r, off, 0, 16); }
DI void st32c(rsrc_t r, u32 off, u32 v) { __builtin_amdgcn_raw_buffer_store_b32(v, r, off, 0, 16); }
DI u32x4 lds128(LAS char* sm, u32 a) { return *(LAS u32x4*)(sm + a); }
DI u32x2 lds64(LAS char* sm, u32 a) { return *(LAS u32x2*)(sm + a); }
DI u32x2 ldstr(LAS char* sm, u32 a) { return __builtin_bit_cast(u32x2, __builtin_amdgcn_ds_read_tr16_b64_v4i16((LAS s16x4*)(sm + a))); }
#define VMCNT(n) asm volatile("s_waitcnt vmcnt(" #n ")" ::: "memory")
#define LGKM0() asm volatile("s_waitcnt lgkmcnt(0)" ::: "memory")
#define BAR() do { asm volatile("" ::: "memory"); __builtin_amdgcn_s_barrier(); asm volatile("" ::: "memory"); } while (0)
#define MEMF() asm volatile("" ::: "memory")

DI void set_flag(rsrc_t r, u32 idx, u32 tag) { st32c(r, WS_FLAG + idx * FS, tag); }
DI void wait_flags(rsrc_t r, int idx0, int idx1, u32 tag) {
    unsigned long long t0 = 0;
    for (u32 it = 0;; it++) {
        u32 a = tag, b = tag;
        if (idx0 >= 0) a = ld32c(r, WS_FLAG + idx0 * FS);
        if (idx1 >= 0) b = ld32c(r, WS_FLAG + idx1 * FS);
        if (__all(a == tag && b == tag)) break;
        if (POLL_SLEEP) __builtin_amdgcn_s_sleep(POLL_SLEEP);
        if ((it & 63u) == 0u) {
            const unsigned long long t = __builtin_amdgcn_s_memrealtime();
            if (it == 0) t0 = t;
            else if (t - t0 > 2000000ull) { set_flag(r, F_TMO, tag); break; }
        }
    }
    MEMF();
}
DI void lds_arrive(LAS char* sm, u32 cnt, int lane) { LGKM0(); if (lane == 0) __hip_atomic_fetch_add((LAS u32*)(sm + cnt), 1u, __ATOMIC_RELAXED, __HIP_MEMORY_SCOPE_WORKGROUP); MEMF(); }
DI void lds_wait(LAS char* sm, u32 cnt, u32 n) { MEMF(); while (*(volatile LAS u32*)(sm + cnt) < n) __builtin_amdgcn_s_sleep(0); MEMF(); }
DI void publish_last(rsrc_t r, LAS char* sm, u32 cnt, u32 nw, u32 flag, u32 tag, int lane) {
    VMCNT(0);
    if (lane == 0) {
        const u32 old = __hip_atomic_fetch_add((LAS u32*)(sm + cnt), 1u, __ATOMIC_RELAXED, __HIP_MEMORY_SCOPE_WORKGROUP);
        if (old == nw - 1) set_flag(r, flag, tag);
    }
    MEMF();
}

DI u32 phi2(float a, float b) {
    const f32x2 t = {a, b}, s = t * 1.4426950408889634f;
    const float ea = __builtin_amdgcn_fmed3f(__builtin_amdgcn_exp2f(s.x), 0.f, 1.f), eb = __builtin_amdgcn_fmed3f(__builtin_amdgcn_exp2f(s.y), 0.f, 1.f);
    const float ma = __int_as_float(max(__float_as_int(a), 0)), mb = __int_as_float(max(__float_as_int(b), 0));
    const f32x2 r = ((f32x2){ea, eb} + (f32x2){ma, mb}) + 1e-6f;
    return pk2(r.x, r.y);
}

constexpr int K1_STAGE = 32768;
constexpr int P0_K = 131072, P0_V = P0_K + 9216, P0_OUT = P0_V + 8192;
constexpr int L_CNT = 156672;
constexpr int C_G1 = L_CNT, C_Y = L_CNT + 4, C_P0A = L_CNT + 8, C_P0B = L_CNT + 12, C_P0C = L_CNT + 16, C_FY = L_CNT + 20, C_FY2 = L_CNT + 24, C_FO = L_CNT + 28, C_FR = L_CNT + 32, C_XF = L_CNT + 36;
constexpr int CV_PITCH = 4160;
constexpr int CV_U = 4 * CV_PITCH;
constexpr int CV_WAVE = CV_U + 4608;
constexpr int CV_BASE = 65536;
constexpr int WO_HI = 65536;
constexpr int F_YY = 110592;
constexpr int F_Y2 = F_YY + 10240;
constexpr int F_GG = F_Y2 + 11264;
constexpr int KO_RED = 110592;
constexpr int KO_KV = KO_RED + 32768;
constexpr int RED_PITCH = 336;
constexpr int LDS_BYTES = 160 * 1024;
DI u32 p0_flag(int tile) { return F_P0 + (tile % 10) * 32 + tile / 10; }
DI constexpr u32 wo_off(int kap) { return kap < 12 ? kap * 5120 : WO_HI + (kap - 12) * 5120; }

DI void p0_issue(const u16* kin, const u16* vin, int tile, int lw, int lane, u32 base) {
    const int cg = tile % 10, rg = tile / 10, s0 = rg * 64;
    const u32 col = cg * 128 + (lane & 7) * 16;
#pragma unroll
    for (int z = 0; z < 2; z++) {
        const int n = 2 * lw + z;
        int r = s0 - 4 + n * 8 + (lane >> 3); r = r < 0 ? 0 : r;
        (NT_KV ? glds16nt : glds16)(kin, (u32)(r * 1280) + col, base + n * 1024);
        (NT_KV ? glds16nt : glds16)(vin, (u32)((s0 + n * 8 + (lane >> 3)) * 1280) + col, base + 9216 + n * 1024);
    }
    {
        int r = s0 + 60 + (lane >> 3); r = r > S - 1 ? S - 1 : r;
        (NT_KV ? glds16nt : glds16)(kin, (u32)(r * 1280) + col, base + 8 * 1024);
    }
}
DI float dot2(u32 a, u32 b, float c) { return __builtin_amdgcn_fdot2_f32_bf16(__builtin_bit_cast(bf16x2, a), __builtin_bit_cast(bf16x2, b), c, false); }
DI void p0_compute(LAS char* sm, u32x2 w4, int tile, int wave, int lane, u32 base, u32 outb, char* ws = nullptr) {
    const int s0 = (tile / 10) * 64;
    const int G = wave * 4 + (lane >> 4), cb = G & 3, i = lane & 15, q = i >> 2, p = i & 3;
    const u32 wa = __builtin_amdgcn_alignbit(w4.y, w4.y, 16), wb = __builtin_amdgcn_alignbit(w4.x, w4.x, 16);
    const int ch = 16 * cb + i;
    u32x2 kq[2][3], vq[2][2];
#pragma unroll
    for (int z = 0; z < 2; z++) {
        const int o = (G >> 2) + 4 * z;
        const u32 ka = base + (8 * o + q) * 128 + 32 * cb + 8 * p, va = ka + 9216;
        kq[z][0] = ldstr(sm, ka); kq[z][1] = ldstr(sm, ka + 512); kq[z][2] = ldstr(sm, ka + 1024);
        vq[z][0] = ldstr(sm, va); vq[z][1] = ldstr(sm, va + 512);
    }
#pragma unroll
    for (int z = 0; z < 2; z++) {
        const int o = (G >> 2) + 4 * z;
        u32 K[6] = {kq[z][0].x, kq[z][0].y, kq[z][1].x, kq[z][1].y, kq[z][2].x, kq[z][2].y};
        if (s0 == 0 && o == 0) { K[0] = 0; K[1] = 0; }
        u32 Sh[5];
#pragma unroll
        for (int e = 0; e < 5; e++) Sh[e] = __builtin_amdgcn_alignbit(K[e + 1], K[e], 16);
        const u32 V[4] = {vq[z][0].x, vq[z][0].y, vq[z][1].x, vq[z][1].y};
        u32 r[4];
#pragma unroll
        for (int e = 0; e < 4; e++) {
            const float k0 = dot2(Sh[e], wa, dot2(Sh[e + 1], wb, 0.f)), k1 = dot2(K[e + 1], wa, dot2(K[e + 2], wb, 0.f));
            r[e] = pk2(k0 * blo(V[e]), k1 * bhi(V[e]));
        }
        *(LAS u32x4*)(sm + outb + ch * 128 + ((o ^ (ch & 7)) * 16)) = (u32x4){r[0], r[1], r[2], r[3]};
    }
}
DI void p0_store(LAS char* sm, rsrc_t rw, int tile, int t256, u32 outb) {
    const int cg = tile % 10, s0 = (tile / 10) * 64;
#pragma unroll
    for (int z = 0; z < 2; z++) {
        const int ch2 = (t256 >> 3) + 32 * z, o2 = t256 & 7;
        const u32x4 t4 = lds128(sm, outb + ch2 * 128 + ((o2 ^ (ch2 & 7)) * 16));
        st128c(rw, WS_UT + (cg * 64 + ch2) * 4096 + (s0 + 8 * o2) * 2, t4);
    }
}
DI void p0_weights(u32x2& w4, const u16* shk, int tile, int wave, int lane) {
    const int cg = tile % 10, cb = (wave * 4 + (lane >> 4)) & 3;
    gld64(w4, shk, (u32)((cg * 64 + 16 * cb + (lane & 15)) * 8));
}

struct K1Set { u32x4 xa[2][2]; u32x2 wl[2][2], wh[2][2]; };
struct K1Addr { u32 x[2][2]; u32 w[2][2]; };
template <bool QT, int WO, int XO>
DI void k1_phase(LAS char* sm, f32x16 (&acc)[2][2], const K1Set& cur, K1Set& nxt, const u32 (&xb)[2], const u32 (&wb)[2][2]) {
#pragma unroll
    for (int n = 0; n < 8; n++) {
        const int ks = n >> 2, i = (n >> 1) & 1, jj = n & 1;
        const u32x4 wf = {cur.wl[ks][jj].x, cur.wl[ks][jj].y, cur.wh[ks][jj].x, cur.wh[ks][jj].y};
        acc[i][jj] = QT ? mfma32(wf, cur.xa[ks][i], acc[i][jj]) : mfma32(cur.xa[ks][i], wf, acc[i][jj]);
        if (n == 0) { nxt.wl[0][0] = ldstr(sm, wb[0][0] + WO); nxt.wh[0][0] = ldstr(sm, wb[0][1] + WO); }
        if (n == 1) { nxt.xa[0][0] = lds128(sm, xb[0] + XO); nxt.wl[0][1] = ldstr(sm, wb[1][0] + WO); }
        if (n == 2) { nxt.wh[0][1] = ldstr(sm, wb[1][1] + WO); nxt.xa[0][1] = lds128(sm, xb[0] + XO + 4096); }
        if (n == 3) { nxt.wl[1][0] = ldstr(sm, wb[0][0] + WO + 4096); nxt.wh[1][0] = ldstr(sm, wb[0][1] + WO + 4096); }
        if (n == 4) { nxt.xa[1][0] = lds128(sm, xb[1] + XO); nxt.wl[1][1] = ldstr(sm, wb[1][0] + WO + 4096); }
        if (n == 5) { nxt.wh[1][1] = ldstr(sm, wb[1][1] + WO + 4096); nxt.xa[1][1] = lds128(sm, xb[1] + XO + 4096); }
        SB();
    }
}
template <bool QT, int ST>
DI void k1_chunk(LAS char* sm, f32x16 (&acc)[2][2], K1Set& s0, K1Set& s1, const K1Addr& a0, const K1Addr& a1, bool bar, bool drain = false) {
    constexpr int SN = (ST + 1) & 3;
    const K1Addr& ac = ST < 2 ? a0 : a1; const K1Addr& an = SN < 2 ? a0 : a1;
    k1_phase<QT, (ST & 1) * K1_STAGE + 8192, (ST & 1) * K1_STAGE + 16384>(sm, acc, s0, s1, ac.x[1], ac.w);
    if (drain) VMCNT(0);
    if (bar) { LGKM0(); BAR(); }
    k1_phase<QT, (SN & 1) * K1_STAGE, (SN & 1) * K1_STAGE + 16384>(sm, acc, s1, s0, an.x[0], an.w);
}
template <bool QT>
DI void k1_loop(LAS char* sm, const K1Addr& a0, f32x16 (&acc)[2][2], rsrc_t rw, u32 pflag, u32 tag, int wave, int lane, const u16* hk, const u16* wo, int n240, u32x2 w4) {
    K1Addr a1;
#pragma unroll
    for (int i = 0; i < 2; i++)
#pragma unroll
        for (int z = 0; z < 2; z++) { a1.x[i][z] = a0.x[i][z] + 65536; a1.w[i][z] = a0.w[i][z] + 65536; }
    K1Set s0, s1;
#pragma unroll
    for (int ks = 0; ks < 2; ks++)
#pragma unroll
        for (int jj = 0; jj < 2; jj++) {
            s0.wl[ks][jj] = ldstr(sm, a0.w[jj][0] + ks * 4096); s0.wh[ks][jj] = ldstr(sm, a0.w[jj][1] + ks * 4096);
            s0.xa[ks][jj] = lds128(sm, a0.x[0][ks] + 16384 + jj * 4096);
        }
    SB();
#pragma unroll 1
    for (int c = 0; c < 8; c += 4) {
        k1_chunk<QT, 0>(sm, acc, s0, s1, a0, a1, true);
        if (c == 4) p0_store(sm, rw, n240, wave * 64 + lane, P0_OUT);
        k1_chunk<QT, 1>(sm, acc, s0, s1, a0, a1, true);
        k1_chunk<QT, 2>(sm, acc, s0, s1, a0, a1, true);
        k1_chunk<QT, 3>(sm, acc, s0, s1, a0, a1, true, c == 4);
        if (c == 4 && wave == 0 && lane == 0) set_flag(rw, pflag, tag);
        if (c == 0) { VMCNT(0); PIN(w4); p0_compute(sm, w4, n240, wave, lane, P0_K, P0_OUT); }
        if (PREF2 && c == 0 && wave >= 2) {
            if (wave == 2) { const u32 l_ = n240 * 86 + lane; touch(hk, (l_ < 20480 ? l_ : 20479) * 128); }
            else { const u32 l_ = n240 * 86 + 64 + lane, m_ = n240 * 27 + lane - 22;
                   if (lane < 22) touch(hk, (l_ < 20480 ? l_ : 20479) * 128); else if (lane < 49) touch(wo, (m_ < 6400 ? m_ : 6399) * 128); }
        }
    }
    k1_chunk<QT, 0>(sm, acc, s0, s1, a0, a1, true);
    k1_chunk<QT, 1>(sm, acc, s0, s1, a0, a1, false);
}

DI void kv_addr(bool isk, int j5, int rt, int wm, int wn, int jj, int r32, int hh, u32& dst, u32& istep, u32& zstep) {
    const int cc = 128 * j5 + 64 * wn + 32 * jj + r32, h = cc / 20, d = cc - 20 * h;
    const int sb32 = rt * 4 + 2 * wm;
    const bool lo = d < 16;
    dst = lo ? (isk ? WS_PK0 : WS_V0) + (((h * 64 + sb32) * 4 + hh) * 128 + d * 8) * 2
             : (isk ? WS_XK : WS_XV) + (((h * 64 + sb32) * 4 + hh) * 32 + (d - 16) * 8) * 2;
    istep = lo ? 1024 : 256; zstep = lo ? 512 : 128;
}
constexpr int K_XFER = 65536;

DI void wo_issue(const u16* wo, int ct, int k0, int k1, int w, int nw, int lane) {
    u32 vo[5], vs[5];
#pragma unroll
    for (int i = 0; i < 5; i++) {
        const int n = 64 * i + lane, rho = n / 10, c = n - 10 * rho, g = 2 * (rho >> 4) + ((rho >> 2) & 1), r8 = 4 * ((rho >> 3) & 1) + (rho & 3);
        vo[i] = (u32)((8 * (g < 2 ? g : g + 1) + r8) * 1280 + ct * 160 + c * 16);
        vs[i] = (u32)((40 * g + 16 + r8) * 1280 + ct * 160 + c * 16);
    }
#pragma unroll 1
    for (int kap = k0 + w; kap < k1; kap += nw) {
        const u32 dst = kap < 12 ? kap * 5120 : WO_HI + (kap - 12) * 5120;
        if (kap < 16) {
            const char* src = (const char*)wo + (size_t)kap * 51200;
#pragma unroll
            for (int i = 0; i < 5; i++) glds16(src, vo[i], dst + i * 1024);
        } else {
            const char* src = (const char*)wo + (size_t)(kap - 16) * 204800;
#pragma unroll
            for (int i = 0; i < 5; i++) glds16(src, vs[i], dst + i * 1024);
        }
    }
}

struct CvSet { u32x2 fl[8], fh[8]; u32x4 g[8]; };
DI void cv_load(LAS char* sm, CvSet& s, u32 fa, u32 ga) {
#pragma unroll
    for (int k1 = 0; k1 < 8; k1++) { s.fl[k1] = ldstr(sm, fa + 512 * k1 + 8); s.fh[k1] = ldstr(sm, fa + 512 * k1); s.g[k1] = lds128(sm, ga + 512 * k1); }
}
DI void cv_phase(LAS char* sm, f32x4 (&Y)[8], const CvSet& cur, CvSet& nxt, u32 fa, u32 ga) {
    PIN(fa); PIN(ga);
    SB();
    int n = 0;
#pragma unroll
    for (int e = 0; e < 8; e++)
#pragma unroll
        for (int k1 = 0; k1 + e < 8; k1++) {
            const u32x4 F = {cur.fl[k1].x, cur.fl[k1].y, cur.fh[k1].x, cur.fh[k1].y};
            Y[k1 + e] = mfma16(F, cur.g[e], Y[k1 + e]);
            if (n == 0) nxt.g[0] = lds128(sm, ga);
            else if (n <= 16) { const int r = (n - 1) >> 1; if ((n - 1) & 1) nxt.fh[r] = ldstr(sm, fa + 512 * r); else nxt.fl[r] = ldstr(sm, fa + 512 * r + 8); }
            else if (n < 24) nxt.g[n - 16] = lds128(sm, ga + 512 * (n - 16));
            n++;
            SB();
        }
}
struct CvIn { u32x4 hd[4]; u32x2 hm[4]; };
DI void conv_fetch_h(CvIn& in, const u16* hk, int c, int lane) {
    const char* hb = (const char*)hk + (size_t)c * 4096;
#pragma unroll
    for (int z = 0; z < 4; z++) {
        const int i = lane + 64 * z;
        if (NT_H) asm volatile("global_load_dwordx4 %0, %1, %2 nt" : "=v"(in.hd[z]) : "v"((u32)(i * 16)), "s"(hb) : "memory"); else gld128(in.hd[z], hb, (u32)(i * 16));
        gld64(in.hm[z], hb, (u32)(i > 0 ? i * 16 - 8 : 0));
    }
}
DI void conv_build_h(LAS char* sm0, CvIn& in, int w, int lane) {
    LAS char* sm = sm0 + CV_BASE + w * CV_WAVE;
#pragma unroll
    for (int z = 0; z < 4; z++) { PIN(in.hd[z]); PIN(in.hm[z]); }
    if (lane < 32) *(LAS u32x4*)(sm + CV_U + lane * 16) = (u32x4){0, 0, 0, 0};
    if (lane < 8) *(LAS u32x4*)(sm + (lane >> 1) * CV_PITCH + (lane & 1) * 16) = (u32x4){0, 0, 0, 0};
#pragma unroll
    for (int z = 0; z < 4; z++) {
        const int i = lane + 64 * z;
        u32 m2 = in.hm[z].x, m1 = in.hm[z].y;
        if (i == 0) { m2 = 0; m1 = 0; }
        const u32 d0 = in.hd[z].x, d1 = in.hd[z].y, d2 = in.hd[z].z, d3 = in.hd[z].w;
        const u32 om2 = __builtin_amdgcn_alignbit(m1, m2, 16), om1 = __builtin_amdgcn_alignbit(d0, m1, 16), o0 = __builtin_amdgcn_alignbit(d1, d0, 16),
                  o1 = __builtin_amdgcn_alignbit(d2, d1, 16), o2 = __builtin_amdgcn_alignbit(d3, d2, 16);
        LAS char* p = sm + 32 + i * 16;
        *(LAS u32x4*)(p) = (u32x4){d0, d1, d2, d3};
        *(LAS u32x4*)(p + CV_PITCH) = (u32x4){om1, o0, o1, o2};
        *(LAS u32x4*)(p + 2 * CV_PITCH) = (u32x4){m1, d0, d1, d2};
        *(LAS u32x4*)(p + 3 * CV_PITCH) = (u32x4){om2, om1, o0, o1};
    }
}
DI void conv_run(LAS char* sm0, float Dc, int w, int lane, int c, rsrc_t rw, char* ws) {
    LAS char* sm = sm0;
    LGKM0();
    TS(4, 9);
    const int fr = lane & 15, g = lane >> 4, q = fr >> 2, p = lane & 3;
    sm = sm0;
    const u32 fa = CV_BASE + w * CV_WAVE + q * CV_PITCH + 2 * (32 - 8 * g + 4 * p) - 8;
    const u32 ga = CV_BASE + w * CV_WAVE + CV_U + 480 + 32 * fr + 16 * g;
    f32x4 Y[8];
#pragma unroll
    for (int i = 0; i < 8; i++) Y[i] = (f32x4){0, 0, 0, 0};
    CvSet A, B;
    cv_load(sm, A, fa, ga);
    SB();
#pragma unroll 1
    for (int k0 = 0; k0 < 8; k0 += 2) {
        cv_phase(sm, Y, A, B, fa + 64 * (k0 + 1), ga - 64 * (k0 + 1));
        cv_phase(sm, Y, B, A, fa + 64 * (k0 + 2), ga - 64 * (k0 + 2));
    }
    TS(4, 10);
    sm = sm0 + CV_BASE + w * CV_WAVE;
    {
        u32x2 u4[8];
#pragma unroll
        for (int ib = 0; ib < 8; ib++) u4[ib] = lds64(sm, CV_U + 512 + 512 * ib + 32 * fr + 8 * g);
#pragma unroll
        for (int ib = 0; ib < 8; ib++) {
            const u32x2 r = {pk2(fmaf(blo(u4[ib].x), Dc, Y[ib][0]), fmaf(bhi(u4[ib].x), Dc, Y[ib][1])), pk2(fmaf(blo(u4[ib].y), Dc, Y[ib][2]), fmaf(bhi(u4[ib].y), Dc, Y[ib][3]))};
            *(LAS u32x2*)(sm + 512 * ib + 32 * fr + 8 * g) = r;
        }
    }
    LGKM0();
    u32x4 y8[4];
#pragma unroll
    for (int z = 0; z < 4; z++) y8[z] = lds128(sm, z * 1024 + lane * 16);
#pragma unroll
    for (int z = 0; z < 4; z++) st128c(rw, WS_YT + c * 4096 + z * 1024 + lane * 16, y8[z]);
}

DI void kvo_block(LAS char* sm, int wave, int lane, int h, int half, rsrc_t rw, u32 tag, char* ws) {
    const int fr = lane & 15, g = lane >> 4;
    const int j0 = (20 * h) >> 7, j1 = (20 * h + 19) >> 7;
    wait_flags(rw, lane < 8 ? (int)F_G1 + 16 * (((lane & 2) ? 10 : 5) + ((lane & 1) ? j1 : j0)) + 2 * wave + (lane >> 2)
                            : lane < 10 ? (int)F_G1 + 16 * ((lane & 1) ? j1 : j0) + 8 * half + wave : -1, -1, tag);
    TS(0, 8);
    const u32 pk0 = WS_PK0 + (h * 64 + 8 * wave) * 1024 + lane * 16, v0 = pk0 + (WS_V0 - WS_PK0);
    const u32 xx = ((fr & 4) ? WS_XV : WS_XK) + ((h * 64 + 8 * wave + (fr >> 3)) * 4 + g) * 64 + (fr & 3) * 16;
    u32x4 A[8], B[8], X[4], Q[8];
#pragma unroll
    for (int i = 0; i < 8; i++) { A[i] = ld128c(rw, pk0 + i * 1024); B[i] = ld128c(rw, v0 + i * 1024); if ((i & 1) == 0) X[i >> 1] = ld128c(rw, xx + (i >> 1) * 512); }
    const int odd = h & 1, pr = h >> 1;
    const int u5 = (g < 3 ? g : 2) + 2 * odd;
    const int sb0 = half * 64 + wave * 8;
    const u32 pq = WS_P2 + (((pr * 128 + sb0) * 5 + u5) * 128 + fr * 8) * 2;
    MEMF(); SB();
    f32x4 a00 = {0, 0, 0, 0}, a0x = a00, ax0 = a00, axx = a00;
    const u32 cst = fr == 8 ? 0x3F803F80u : 0u;
#pragma unroll
    for (int i = 0; i < 8; i++) {
        u32x4 xv = X[i >> 1];
        if (i & 1) {
#pragma unroll
            for (int e = 0; e < 4; e++) xv[e] = (u32)__builtin_amdgcn_update_dpp(0, (int)xv[e], 0x108, 0xf, 0xf, false);
        }
        if (fr >= 8) xv = (u32x4){cst, cst, cst, cst};
        a00 = mfma16(A[i], B[i], a00); a0x = mfma16(A[i], xv, a0x); ax0 = mfma16(xv, B[i], ax0); axx = mfma16(xv, xv, axx);
    }
    PIN(a00); PIN(a0x); PIN(ax0); PIN(axx);
    SB();
#pragma unroll
    for (int i = 0; i < 8; i++) Q[i] = ld128c(rw, pq + i * 1280);
    MEMF(); SB();
    TS(0, 9);
    *(LAS f32x4*)(sm + KO_RED + ((wave * 4 + 0) * 64 + lane) * 16) = a00;
    *(LAS f32x4*)(sm + KO_RED + ((wave * 4 + 1) * 64 + lane) * 16) = a0x;
    *(LAS f32x4*)(sm + KO_RED + ((wave * 4 + 2) * 64 + lane) * 16) = ax0;
    *(LAS f32x4*)(sm + KO_RED + ((wave * 4 + 3) * 64 + lane) * 16) = axx;
    if (wave >= 4 && (wave - 4) * 64 + lane < 189) *(LAS u32x2*)(sm + KO_KV + ((wave - 4) * 64 + lane) * 8) = (u32x2){0, 0};
    LGKM0(); BAR();
    if (wave < 4) {
        const int tile = wave;
        f32x4 s = *(LAS f32x4*)(sm + KO_RED + (tile * 64 + lane) * 16);
#pragma unroll
        for (int w2 = 1; w2 < 8; w2++) s += *(LAS f32x4*)(sm + KO_RED + ((w2 * 4 + tile) * 64 + lane) * 16);
        const int e = (tile & 1) ? 12 + fr : fr;
        const bool colok = (tile & 1) ? (fr >= 4 && fr <= 8) : true;
        const bool rowok = (tile & 2) ? (g == 0) : true;
        const int d0 = (tile & 2) ? 16 : 4 * g;
        if (colok && rowok) *(LAS u32x2*)(sm + KO_KV + e * 72 + (d0 + 4) * 2) = (u32x2){pk2(s[0], s[1]), pk2(s[2], s[3])};
    }
    LGKM0(); BAR();
    u32x4 A0, A1[4], A2;
    {
        const u32 dofs = KO_KV + (8 * g - 4 * odd + 4) * 2;
        const u32 r0 = (4 * odd + fr) * 72, r1 = ((odd ? 0 : 16) + (fr & 3)) * 72;
        const u32x2 p0 = lds64(sm, dofs + r0), p1 = lds64(sm, dofs + r0 + 8);
        const u32x2 q0 = lds64(sm, dofs + r1), q1 = lds64(sm, dofs + r1 + 8);
        const u32x2 z0 = lds64(sm, dofs + 20 * 72), z1 = lds64(sm, dofs + 20 * 72 + 8);
        A0 = (u32x4){p0.x, p0.y, p1.x, p1.y}; A2 = (u32x4){z0.x, z0.y, z1.x, z1.y};
#pragma unroll
        for (int z = 0; z < 4; z++) { const bool m = (fr >> 2) == z; A1[z] = (u32x4){m ? q0.x : 0u, m ? q0.y : 0u, m ? q1.x : 0u, m ? q1.y : 0u}; }
    }
    TS(0, 10);
    const u32 offA = WS_O + ((pr * 4 + 2 * odd + (g >> 1)) * 128 + fr * 8) * 2 + (g & 1) * 20480;
    const u32 offX = WS_O + (((16 + (pr >> 2)) * 4 + (pr & 3)) * 128 + odd * 64 + fr * 4) * 2;
    const f32x4 zero = {0, 0, 0, 0};
#pragma unroll
    for (int i4 = 0; i4 < 2; i4++) {
        f32x4 n1 = zero; float rden[4]; u32x2 ov[4];
#pragma unroll
        for (int z = 0; z < 4; z++) {
            const int i = 4 * i4 + z;
            const f32x4 n0 = mfma16(A0, Q[i], zero);
            const f32x4 dn = mfma16(A2, Q[i], zero);
            n1 = mfma16(A1[z], Q[i], n1);
            const float rd = __builtin_amdgcn_rcpf(dn[0] + 1e-6f);
            rden[z] = rd;
            ov[z] = (u32x2){pk2(n0[0] * rd, n0[1] * rd), pk2(n0[2] * rd, n0[3] * rd)};
        }
#pragma unroll
        for (int z = 0; z < 4; z += 2) {
            auto rx = __builtin_amdgcn_permlane16_swap(ov[z].x, ov[z + 1].x, false, false);
            auto ry = __builtin_amdgcn_permlane16_swap(ov[z].y, ov[z + 1].y, false, false);
            st128c(rw, offA + (sb0 + 4 * i4 + z) * 20480, (u32x4){rx[0], ry[0], rx[1], ry[1]});
        }
        const float rd = g == 0 ? rden[0] : g == 1 ? rden[1] : g == 2 ? rden[2] : rden[3];
        st64c(rw, offX + (sb0 + 4 * i4 + g) * 20480, (u32x2){pk2(n1[0] * rd, n1[1] * rd), pk2(n1[2] * rd, n1[3] * rd)});
    }
}

DI void gate_block(int wave, int lane, int m, const u16* x, const u16* gw, rsrc_t rw) {
    const int sub = lane & 15, r4 = lane >> 4;
    u32x4 gq[5], xr[4][5];
#pragma unroll
    for (int jj = 0; jj < 5; jj++) gq[jj] = *(const GAS u32x4*)((const GAS char*)gw + (sub + 16 * jj) * 16);
#pragma unroll
    for (int it = 0; it < 4; it++)
#pragma unroll
        for (int jj = 0; jj < 5; jj++) xr[it][jj] = *(const GAS u32x4*)((const GAS char*)x + (size_t)(128 * m + 16 * wave + 4 * it + r4) * 1280 + (sub + 16 * jj) * 16);
    MEMF(); SB();
#pragma unroll
    for (int it = 0; it < 4; it++) {
        float a = 0.f;
#pragma unroll
        for (int jj = 0; jj < 5; jj++)
#pragma unroll
            for (int e = 0; e < 4; e++) { a = fmaf(blo(xr[it][jj][e]), blo(gq[jj][e]), a); a = fmaf(bhi(xr[it][jj][e]), bhi(gq[jj][e]), a); }
        a += __shfl_xor(a, 1); a += __shfl_xor(a, 2); a += __shfl_xor(a, 4); a += __shfl_xor(a, 8);
        a = rbf(a);
        const float g = rbf(__builtin_amdgcn_rcpf(1.f + __builtin_amdgcn_exp2f(-1.4426950408889634f * a)));
        if (sub == 0) st32c(rw, WS_G + (128 * m + 16 * wave + 4 * it + r4) * 4, __float_as_uint(g));
    }
}

DI void final_block(LAS char* sm, int wave, int lane, int tid, int st, int ct, const u16* x, u16* out, rsrc_t rw, u32 tag, char* ws) {
    const int fr = lane & 15, g = lane >> 4;
    u32x4 xv[2];
#pragma unroll
    for (int r = 0; r < 2; r++) {
        int it = tid + 512 * r; it = it < 640 ? it : 639;
        const int s = it / 10, u = it - 10 * s;
        xv[r] = *(const GAS u32x4*)((const GAS char*)x + (size_t)(st * 64 + s) * 1280 + ct * 160 + u * 16);
    }
    VMCNT(0); BAR();
    if (wave < 4) {
        const int kh = wave & 1, sh = wave >> 1;
        wait_flags(rw, lane < 16 ? (int)F_O + (st >> 4) * 32 + 16 * kh + lane : -1, -1, tag);
        TS(0, 17);
        const u32 ob = WS_O + ((st * 4 + 2 * sh) * 20 + 8 * kh) * 1024 + lane * 16;
        const u32 os = WS_O + ((st * 4 + 2 * sh) * 20 + 16 + 2 * kh) * 1024 + g * 256 + fr * 8;
        u32x4 B[2][10];
#pragma unroll
        for (int kl = 0; kl < 8; kl++) { B[0][kl] = ld128c(rw, ob + kl * 1024); B[1][kl] = ld128c(rw, ob + 20480 + kl * 1024); }
#pragma unroll
        for (int kl = 0; kl < 2; kl++)
#pragma unroll
            for (int z = 0; z < 2; z++) {
                const u32x2 h0 = ld64c(rw, os + z * 20480 + kl * 1024), h1 = ld64c(rw, os + z * 20480 + kl * 1024 + 128);
                B[z][8 + kl] = (u32x4){h0.x, h0.y, h1.x, h1.y};
            }
        MEMF(); SB();
        f32x4 acc[5][2];
#pragma unroll
        for (int cb = 0; cb < 5; cb++) { acc[cb][0] = (f32x4){0, 0, 0, 0}; acc[cb][1] = (f32x4){0, 0, 0, 0}; }
        const u32 aoff = (16 * (g >> 1) + 4 * (g & 1) + (fr >> 2)) * 160 + (lane & 3) * 8;
        u32x2 al[2][5], ah[2][5];
#define F_KAP(kl) ((kl) < 8 ? (kl) : 8 + (kl))
#define F_READ(buf, kl) do { const u32 b0_ = kh ? wo_off(F_KAP(kl) + ((kl) < 8 ? 8 : 2)) : wo_off(F_KAP(kl)); \
            _Pragma("unroll") for (int cb = 0; cb < 5; cb++) { al[buf][cb] = ldstr(sm, b0_ + aoff + cb * 32); ah[buf][cb] = ldstr(sm, b0_ + aoff + cb * 32 + 1280); } } while (0)
        F_READ(0, 0);
#pragma unroll
        for (int kl = 0; kl < 10; kl++) {
            if (kl < 9) F_READ((kl + 1) & 1, kl + 1);
            SB();
#pragma unroll
            for (int cb = 0; cb < 5; cb++) {
                const u32x4 a = {al[kl & 1][cb].x, al[kl & 1][cb].y, ah[kl & 1][cb].x, ah[kl & 1][cb].y};
                acc[cb][0] = mfma16(a, B[0][kl], acc[cb][0]); acc[cb][1] = mfma16(a, B[1][kl], acc[cb][1]);
            }
            SB();
        }
        TS(0, 18);
        lds_arrive(sm, C_FR, lane); lds_wait(sm, C_FR, 4);
#pragma unroll
        for (int cb = 0; cb < 5; cb++)
#pragma unroll
            for (int z = 0; z < 2; z++)
                *(LAS f32x4*)(sm + kh * (64 * RED_PITCH) + (16 * (2 * sh + z) + fr) * RED_PITCH + (16 * cb + 4 * g) * 4) = acc[cb][z];
    } else {
        if (wave == 4) { wait_flags(rw, lane < 20 ? (int)F_Y + ct * 32 + lane : lane == 32 ? (int)F_GATE + (st >> 1) : -1, -1, tag); lds_arrive(sm, C_FY, lane); }
        else lds_wait(sm, C_FY, 1);
        const int t = tid - 256;
        u32x4 yv[3];
#pragma unroll
        for (int r = 0; r < 3; r++) { int id = t + 256 * r; id = id < 640 ? id : 639; yv[r] = ld128c(rw, WS_YT + (80 * ct + (id >> 3)) * 4096 + st * 128 + (id & 7) * 16); }
#pragma unroll
        for (int r = 0; r < 3; r++) { const int id = t + 256 * r; if (id < 640) *(LAS u32x4*)(sm + F_YY + id * 16) = yv[r]; }
        if (wave == 4) *(LAS u32*)(sm + F_GG + lane * 4) = ld32c(rw, WS_G + (st * 64 + lane) * 4);
        lds_arrive(sm, C_FY2, lane); lds_wait(sm, C_FY2, 4);
        const int sb = wave - 4, q = fr >> 2, p = lane & 3;
        u32x2 tt[5];
#pragma unroll
        for (int cb = 0; cb < 5; cb++) tt[cb] = ldstr(sm, F_YY + (16 * cb + 4 * g + q) * 128 + (16 * sb + 4 * p) * 2);
#pragma unroll
        for (int cb = 0; cb < 5; cb++) *(LAS u32x2*)(sm + F_Y2 + (16 * sb + fr) * 176 + (16 * cb + 4 * g) * 2) = tt[cb];
    }
    LGKM0(); BAR();
#pragma unroll
    for (int r = 0; r < 2; r++) {
        const int it = tid + 512 * r;
        if (it < 640) {
            const int s = it / 10, u = it - 10 * s;
            const u32 ra = s * RED_PITCH + u * 32;
            const f32x4 p0 = *(LAS f32x4*)(sm + ra), p1 = *(LAS f32x4*)(sm + ra + 16), q0 = *(LAS f32x4*)(sm + 64 * RED_PITCH + ra), q1 = *(LAS f32x4*)(sm + 64 * RED_PITCH + ra + 16);
            const u32x4 y8 = lds128(sm, F_Y2 + s * 176 + u * 16);
            const float gg = *(LAS float*)(sm + F_GG + s * 4), omg = 1.f - gg;
            float o[8];
#pragma unroll
            for (int e = 0; e < 4; e++) {
                const float a0 = (e < 2 ? p0[2 * e] + q0[2 * e] : p1[2 * e - 4] + q1[2 * e - 4]), a1 = (e < 2 ? p0[2 * e + 1] + q0[2 * e + 1] : p1[2 * e - 3] + q1[2 * e - 3]);
                const float b0 = omg * blo(xv[r][e]), b1 = omg * bhi(xv[r][e]);
                o[2 * e] = fmaf(b0, blo(y8[e]), gg * a0); o[2 * e + 1] = fmaf(b1, bhi(y8[e]), gg * a1);
            }
            *(GAS u32x4*)((GAS char*)out + (size_t)(st * 64 + s) * 1280 + ct * 160 + u * 16) = (u32x4){pk2(o[0], o[1]), pk2(o[2], o[3]), pk2(o[4], o[5]), pk2(o[6], o[7])};
        }
    }
}

__global__ __launch_bounds__(512, 2) void fused_kernel(const u16* __restrict__ x, const u16* __restrict__ w, const u16* __restrict__ wo, const u16* __restrict__ kin,
                                                        const u16* __restrict__ vin, const u16* __restrict__ shk, const u16* __restrict__ hk, const u16* __restrict__ sD,
                                                        const u16* __restrict__ gw, u16* __restrict__ out, char* ws) {
    extern __shared__ __attribute__((aligned(16))) char smem[];
    LAS char* sm = (LAS char*)smem;
    const int tid = threadIdx.x, lane = tid & 63, wave = __builtin_amdgcn_readfirstlane(tid >> 6);
    const int b = blockIdx.x, xcd = b & 7, j = b >> 3;
    const rsrc_t rw = __builtin_amdgcn_make_buffer_rsrc(ws, 0, WS_BYTES, 0x00020000);
    asm volatile("" :: "s"(x), "s"(w), "s"(wo), "s"(kin), "s"(vin), "s"(shk), "s"(hk), "s"(sD), "s"(gw), "s"(out));
    u32 tag;
    asm volatile("s_load_dword %0, %1, 0x0\n\ts_waitcnt lgkmcnt(0)" : "=s"(tag) : "s"(ws + WS_FLAG + F_EPOCH * FS) : "memory");
    tag += 1u;
    if (tid < 16) *(LAS u32*)(sm + L_CNT + tid * 4) = 0u;
    TS(0, 0);
    if (PREFC && wave == 3) {
        unsigned long long pc;
        asm volatile("s_getpc_b64 %0" : "=s"(pc));
        pc &= ~127ull;
        const int ln = PREFC == 1 ? j * 10 + lane : lane;
        if (PREFC == 1) { if (lane < 10 && ln < CODE_LINES) touch((const void*)pc, (u32)(ln * 128)); }
        else {
#pragma unroll
            for (int z = 0; z < 5; z++) if (z * 64 + lane < CODE_LINES) touch((const void*)pc, (u32)((z * 64 + lane) * 128));
        }
    }
    if (PREFT && wave == 2) {
        if (PREFT == 1) { touch(ws, (u32)(lane * 262144)); if (lane < 40) touch(out, (u32)(lane * 65536)); }
        else { touch(ws, (u32)((j * 8 + (lane & 7)) * 65536 + (lane >> 3) * 128) % (WS_STAMP - 1024)); if (lane < 40) touch(out, (u32)(lane * 65536)); }
    }
    const int st = 4 * xcd + (j >> 3), ct = j & 7;
    int ko = -1;
    bool is_conv = false;

    if (j >= 30) {
        const int m = xcd * 2 + (j - 30);
        ko = -100;
        {
            const int grp = wave >> 2, w4i = wave & 3;
            u32x2 w4[3];
#pragma unroll
            for (int t = 0; t < 3; t++) { const int tt = 2 * t + grp < 5 ? 2 * t + grp : 4; p0_weights(w4[t], shk, 240 + 5 * m + tt, w4i, lane); }
#pragma unroll
            for (int t = 0; t < 3; t++) if (2 * t + grp < 5) p0_issue(kin, vin, 240 + 5 * m + 2 * t + grp, w4i, lane, (2 * t + grp) * 17408);
            VMCNT(0);
#pragma unroll
            for (int t = 0; t < 3; t++) PIN(w4[t]);
            BAR();
#pragma unroll
            for (int t = 0; t < 3; t++)
                if (2 * t + grp < 5) p0_compute(sm, w4[t], 240 + 5 * m + 2 * t + grp, w4i, lane, (2 * t + grp) * 17408, 90112 + (2 * t + grp) * 8192);
            LGKM0(); BAR();
            {
                u32x4 t4[3][2];
#pragma unroll
                for (int t = 0; t < 3; t++)
#pragma unroll
                    for (int z = 0; z < 2; z++) { const int ch2 = ((tid & 255) >> 3) + 32 * z, o2 = tid & 7, tt = 2 * t + grp < 5 ? 2 * t + grp : 4; t4[t][z] = lds128(sm, 90112 + tt * 8192 + ch2 * 128 + ((o2 ^ (ch2 & 7)) * 16)); }
#pragma unroll
                for (int t = 0; t < 3; t++)
                    if (2 * t + grp < 5) {
                        const int tile = 240 + 5 * m + 2 * t + grp, cg = tile % 10, s0 = (tile / 10) * 64;
#pragma unroll
                        for (int z = 0; z < 2; z++) { const int ch2 = ((tid & 255) >> 3) + 32 * z, o2 = tid & 7; st128c(rw, WS_UT + (cg * 64 + ch2) * 4096 + (s0 + 8 * o2) * 2, t4[t][z]); }
                    }
            }
        }
        VMCNT(0); BAR();
        if (tid < 5) set_flag(rw, p0_flag(240 + 5 * m + tid), tag);
        TS(0, 1);
        gate_block(wave, lane, m, x, gw, rw);
        VMCNT(0); LGKM0(); BAR();
        if (tid == 0) set_flag(rw, F_GATE + m, tag);
        TS(0, 3);
    } else {
        const int rt = 2 * xcd + j / 15, cbk = j % 15, tile = xcd * 30 + j;
        is_conv = cbk < 5 || cbk >= 10;
        if (!is_conv) { const int ki = rt * 5 + (cbk - 5); ko = ki < 64 ? ki : -2 - (ki - 64); }
        if (wave >= 4) {
            const int lw = wave - 4;
            u32 vx[4], vw[4];
#pragma unroll
            for (int n2 = 0; n2 < 4; n2++) {
                const int n = 4 * lw + n2;
                const int row = 8 * n + (lane >> 3), ch = (lane & 7) ^ ((row >> 1) & 7);
                vx[n2] = (u32)((rt * 128 + row) * 1280 + ch * 16);
                const int kr = 4 * n + (lane >> 4), f = ((kr & 3) << 2) | ((kr >> 2) & 3), cw = (lane & 15) ^ f;
                vw[n2] = (u32)(kr * 3840 + cbk * 256 + cw * 16);
            }
            const int rot = (rt + cbk) % 10;
#define K1_ISSUE(c) do { const u32 st_ = ((c) & 3) * K1_STAGE + 4 * lw * 1024; int kc_ = (c) + ROT_ON * rot; kc_ = kc_ >= 10 ? kc_ - 10 : kc_; \
            const char* xb_ = (const char*)x + kc_ * 128; const char* wp_ = (const char*)w + (size_t)kc_ * 64 * 3840; \
            _Pragma("unroll") for (int n2 = 0; n2 < 4; n2++) { if (NT_X) glds16nt(xb_, vx[n2], st_ + 16384 + n2 * 1024); else glds16(xb_, vx[n2], st_ + 16384 + n2 * 1024); glds16(wp_, vw[n2], st_ + n2 * 1024); } } while (0)
            K1_ISSUE(0); K1_ISSUE(1);
            VMCNT(8); BAR();
            K1_ISSUE(2); K1_ISSUE(3);
            p0_issue(kin, vin, tile, lw, lane, P0_K);
#pragma unroll 1
            for (int c = 0; c < 6; c++) {
                if (c < 3) VMCNT(21); else VMCNT(16);
                BAR();
                K1_ISSUE(c + 4);
            }
            const int cv = rt * 10 + (cbk < 5 ? cbk : cbk - 5), c = 4 * cv + lw;
            if (is_conv) {
                CvIn in; u32 fl; u32x4 u[4];
                const int fidx = (int)F_P0 + (cv >> 4) * 32 + (lane & 31);
                conv_fetch_h(in, hk, c, lane);
                VMCNT(24); BAR();
                VMCNT(16); BAR();
                gld32c(fl, ws, WS_FLAG + fidx * FS);
                VMCNT(9); BAR();
                TS(4, 4);
                VMCNT(0); PIN(fl);
                const bool ready = __all(fl == tag);
                if (ready) {
#pragma unroll
                    for (int z = 0; z < 4; z++) gld128c(u[z], ws, WS_UT + c * 4096 + z * 1024 + lane * 16);
                }
                TS(4, 5);
                conv_build_h(sm, in, lw, lane);
                TS(4, 6);
                if (!ready) {
                    wait_flags(rw, fidx, -1, tag);
#pragma unroll
                    for (int z = 0; z < 4; z++) gld128c(u[z], ws, WS_UT + c * 4096 + z * 1024 + lane * 16);
                }
                TS(4, 7);
                VMCNT(0);
#pragma unroll
                for (int z = 0; z < 4; z++) { PIN(u[z]); *(LAS u32x4*)(sm + CV_BASE + lw * CV_WAVE + CV_U + 512 + z * 1024 + lane * 16) = u[z]; }
                const float Dc = blo((u32)sD[c]);
                TS(4, 8);
                conv_run(sm, Dc, lw, lane, c, rw, ws);
                publish_last(rw, sm, C_Y, 4, F_Y + (cv / 20) * 32 + cv % 20, tag, lane);
                TS(4, 11);
            } else {
                VMCNT(16); BAR();
                VMCNT(8); BAR();
                VMCNT(0); BAR();
                const int r32 = lane & 31, hh = lane >> 5;
                u32 dst, istep, zstep; kv_addr(true, cbk - 5, rt, lw >> 1, lw & 1, 1, r32, hh, dst, istep, zstep);
                lds_wait(sm, C_XF + lw * 4, 1);
                f32x4 a[2][4];
#pragma unroll
                for (int i = 0; i < 2; i++)
#pragma unroll
                    for (int q4 = 0; q4 < 4; q4++) a[i][q4] = *(LAS f32x4*)(sm + K_XFER + lw * 8192 + ((i * 4 + q4) * 64 + lane) * 16);
#pragma unroll
                for (int i = 0; i < 2; i++)
#pragma unroll
                    for (int z = 0; z < 2; z++) {
                        const f32x4 a0 = a[i][2 * z], a1 = a[i][2 * z + 1];
                        st128c(rw, dst + i * istep + z * zstep, (u32x4){phi2(a0[0], a0[1]), phi2(a0[2], a0[3]), phi2(a1[0], a1[1]), phi2(a1[2], a1[3])});
                    }
                publish_last(rw, sm, C_G1, 8, F_G1 + cbk * 16 + rt, tag, lane);
            }
        } else {
            u32x2 w4; p0_weights(w4, shk, tile, wave, lane);
            if (PREF1 && wave < 3) {
                const int n = wave * 64 + lane;
                if (wave == 0) touch(x, (u32)((xcd * 2560 + j * 80 + lane) * 128));
                else if (wave == 1) { if (lane < 16) touch(x, (u32)((xcd * 2560 + j * 80 + 64 + lane) * 128)); else touch(w, (u32)(((xcd * 30 + j) * 80 + lane - 16) * 128)); }
                else if (lane < 32) touch(w, (u32)(((xcd * 30 + j) * 80 + 48 + lane) * 128));
                (void)n;
            }
            const int wm = wave >> 1, wn = wave & 1, r32 = lane & 31, hh = lane >> 5;
            K1Addr a0;
#pragma unroll
            for (int ks = 0; ks < 2; ks++) {
                a0.x[0][ks] = (64 * wm + r32) * 128 + (((2 * ks + hh) ^ ((r32 >> 1) & 7)) * 16);
                a0.x[1][ks] = a0.x[0][ks] ^ 64;
            }
#pragma unroll
            for (int jj = 0; jj < 2; jj++)
#pragma unroll
                for (int t = 0; t < 2; t++) {
                    const int blk = (lane >> 4) & 1, q = (lane & 15) >> 2, p = lane & 3, row = 8 * hh + 4 * t + q, ch = 8 * wn + 4 * jj + 2 * blk + (p >> 1);
                    const int f = ((row & 3) << 2) | ((row >> 2) & 3);
                    a0.w[jj][t] = row * 256 + ((ch ^ f) * 16) + 8 * (p & 1);
                }
            f32x16 acc[2][2];
#pragma unroll
            for (int i = 0; i < 2; i++)
#pragma unroll
                for (int jj = 0; jj < 2; jj++)
#pragma unroll
                    for (int e = 0; e < 16; e++) acc[i][jj][e] = 0.f;
            BAR();
            TS(0, 1);
            if (cbk < 5) {
                k1_loop<true>(sm, a0, acc, rw, p0_flag(tile), tag, wave, lane, hk, wo, tile, w4);
                __builtin_amdgcn_s_setprio(2);
                TS(0, 2);
                u32x4 qv[2][2][2];
#pragma unroll
                for (int jj = 0; jj < 2; jj++)
#pragma unroll
                    for (int ap = 0; ap < 2; ap++)
#pragma unroll
                        for (int i = 0; i < 2; i++) {
                            const f32x16& a = acc[i][jj];
                            const u32 x0 = phi2(a[8 * ap], a[8 * ap + 1]), y0 = phi2(a[8 * ap + 2], a[8 * ap + 3]);
                            const u32 x1 = phi2(a[8 * ap + 4], a[8 * ap + 5]), y1 = phi2(a[8 * ap + 6], a[8 * ap + 7]);
                            auto rx = __builtin_amdgcn_permlane32_swap(x0, x1, false, false);
                            auto ry = __builtin_amdgcn_permlane32_swap(y0, y1, false, false);
                            qv[jj][ap][i] = (u32x4){rx[0], ry[0], rx[1], ry[1]};
                        }
                if (QDELAY) { __builtin_amdgcn_s_setprio(0); for (int z = 0; z < QDELAY; z++) __builtin_amdgcn_s_sleep(8); }
#pragma unroll
                for (int jj = 0; jj < 2; jj++)
#pragma unroll
                    for (int ap = 0; ap < 2; ap++) {
                        const int U = 16 * cbk + 8 * wn + 4 * jj + 2 * ap + hh, pr = U / 5, u5 = U - 5 * pr;
                        const u32 dst = WS_P2 + (((pr * 128 + rt * 8 + 4 * wm + (r32 >> 4)) * 5 + u5) * 128 + (r32 & 15) * 8) * 2;
#pragma unroll
                        for (int i = 0; i < 2; i++) st128c(rw, dst + i * 2560, qv[jj][ap][i]);
                    }
            } else {
                k1_loop<false>(sm, a0, acc, rw, p0_flag(tile), tag, wave, lane, hk, wo, tile, w4);
                __builtin_amdgcn_s_setprio(2);
                TS(0, 2);
                const bool isk = cbk < 10;
                const int j5 = isk ? cbk - 5 : cbk - 10;
                if (isk) {
#pragma unroll
                    for (int i = 0; i < 2; i++)
#pragma unroll
                        for (int q4 = 0; q4 < 4; q4++)
                            *(LAS f32x4*)(sm + K_XFER + wave * 8192 + ((i * 4 + q4) * 64 + lane) * 16) = (f32x4){acc[i][1][4 * q4], acc[i][1][4 * q4 + 1], acc[i][1][4 * q4 + 2], acc[i][1][4 * q4 + 3]};
                    lds_arrive(sm, C_XF + wave * 4, lane);
                    u32 dst, istep, zstep; kv_addr(true, j5, rt, wm, wn, 0, r32, hh, dst, istep, zstep);
#pragma unroll
                    for (int i = 0; i < 2; i++)
#pragma unroll
                        for (int z = 0; z < 2; z++) {
                            const f32x16& a = acc[i][0];
                            st128c(rw, dst + i * istep + z * zstep,
                                   (u32x4){phi2(a[8 * z], a[8 * z + 1]), phi2(a[8 * z + 2], a[8 * z + 3]), phi2(a[8 * z + 4], a[8 * z + 5]), phi2(a[8 * z + 6], a[8 * z + 7])});
                        }
                } else {
#pragma unroll
                    for (int jj = 0; jj < 2; jj++) {
                        u32 dst, istep, zstep; kv_addr(false, j5, rt, wm, wn, jj, r32, hh, dst, istep, zstep);
#pragma unroll
                        for (int i = 0; i < 2; i++)
#pragma unroll
                            for (int z = 0; z < 2; z++) {
                                const f32x16& a = acc[i][jj];
                                st128c(rw, dst + i * istep + z * zstep,
                                       (u32x4){pk2(a[8 * z], a[8 * z + 1]), pk2(a[8 * z + 2], a[8 * z + 3]), pk2(a[8 * z + 4], a[8 * z + 5]), pk2(a[8 * z + 6], a[8 * z + 7])});
                            }
                    }
                }
            }
            LGKM0();
            TS(0, 25);
            publish_last(rw, sm, C_G1, is_conv ? 4 : 8, F_G1 + cbk * 16 + rt, tag, lane);
            __builtin_amdgcn_s_setprio(0);
            TS(0, 3);
            if (is_conv) {
                lds_wait(sm, C_G1, 4);
                wo_issue(wo, ct, 0, 12, wave, 4, lane);
            }
        }
    }
    if (!is_conv) {
        LGKM0(); BAR();
        if (ko >= 0) {
            const int h = ko >> 1, half = ko & 1;
            wo_issue(wo, ct, 0, 12, wave, 8, lane);
            kvo_block(sm, wave, lane, h, half, rw, tag, ws);
            TS(0, 26);
            VMCNT(0); BAR();
            if (tid == 0) set_flag(rw, F_O + half * 32 + h, tag);
            TS(0, 11);
            wo_issue(wo, ct, 12, 20, wave, 8, lane);
        } else {
            wo_issue(wo, ct, 0, 20, wave, 8, lane);
            if (ko == -2 && wave == 0) {
                wait_flags(rw, (int)F_G1 + lane, (int)F_G1 + 64 + lane, tag);
                wait_flags(rw, (int)F_G1 + 128 + lane, (int)F_G1 + 192 + lane, tag);
                if (lane == 0) set_flag(rw, F_EPOCH, tag);
            }
        }
    } else {
        VMCNT(0); BAR();
        if (wave >= 4) wo_issue(wo, ct, 12, 20, wave - 4, 4, lane);
    }
    TS(0, 16);
    final_block(sm, wave, lane, tid, st, ct, x, out, rw, tag, ws);
    TS(0, 19);
}

extern "C" void kernel_launch(void* const* d_in, const int* in_sizes, int n_in, void* d_out, int out_size, void* d_ws, size_t ws_size, hipStream_t stream) {
    const u16* x = (const u16*)d_in[0]; const u16* wqkv = (const u16*)d_in[1]; const u16* wo = (const u16*)d_in[2];
    const u16* kin = (const u16*)d_in[4]; const u16* vin = (const u16*)d_in[5]; const u16* shk = (const u16*)d_in[6];
    const u16* hk = (const u16*)d_in[7]; const u16* sD = (const u16*)d_in[8]; const u16* gw = (const u16*)d_in[9];
    (void)hipFuncSetAttribute((const void*)fused_kernel, hipFuncAttributeMaxDynamicSharedMemorySize, LDS_BYTES);
    fused_kernel<<<256, 512, LDS_BYTES, stream>>>(x, wqkv, wo, kin, vin, shk, hk, sD, gw, (u16*)d_out, (char*)d_ws);
}
```

```cpp
#include <hip/hip_runtime.h>
#include <stdint.h>

typedef unsigned short u16;
typedef unsigned int u32;
typedef __bf16 bf16x8 __attribute__((ext_vector_type(8)));
typedef __bf16 bf16x2 __attribute__((ext_vector_type(2)));
typedef float f32x4 __attribute__((ext_vector_type(4)));
typedef float f32x2 __attribute__((ext_vector_type(2)));
typedef float f32x16 __attribute__((ext_vector_type(16)));
typedef u32 u32x4 __attribute__((ext_vector_type(4)));
typedef u32 u32x2 __attribute__((ext_vector_type(2)));
typedef short s16x4 __attribute__((ext_vector_type(4)));
#define LAS __attribute__((address_space(3)))
#define GAS __attribute__((address_space(1)))
#define DI __device__ __forceinline__
typedef __amdgpu_buffer_rsrc_t rsrc_t;

constexpr int S = 2048, D = 640;
#define FS 256
#define POLL_SLEEP 2
#define ROT_ON 1

constexpr u32 WS_UT = 0;
constexpr u32 WS_YT = WS_UT + 2621440;
constexpr u32 WS_PK0 = WS_YT + 2621440;
constexpr u32 WS_V0 = WS_PK0 + 2097152;
constexpr u32 WS_XK = WS_V0 + 2097152;
constexpr u32 WS_XV = WS_XK + 524288;
constexpr u32 WS_P2 = WS_XV + 524288;
constexpr u32 WS_O = WS_P2 + 2621440;
constexpr u32 WS_G = WS_O + 2621440;
constexpr u32 WS_FLAG = WS_G + 8192;
constexpr u32 F_P0 = 0;
constexpr u32 F_G1 = 320;
constexpr u32 F_GATE = 560;
constexpr u32 F_O = 576;
constexpr u32 F_Y = 640;
constexpr u32 F_EPOCH = 896, F_TMO = 928;
constexpr u32 WS_STAMP = WS_FLAG + 1024 * FS;
constexpr u32 WS_BYTES = WS_STAMP + 256 * 32 * 16;

#define TS(w, id) do {} while (0)

DI u32 pk2(float a, float b) { f32x2 t = {a, b}; return __builtin_bit_cast(u32, __builtin_convertvector(t, bf16x2)); }
DI float blo(u32 u) { return __uint_as_float(u << 16); }
DI float bhi(u32 u) { return __uint_as_float(u & 0xffff0000u); }
DI float rbf(float f) { return blo(pk2(f, f)); }
DI bf16x8 asbf(u32x4 v) { return __builtin_bit_cast(bf16x8, v); }
DI f32x4 mfma16(u32x4 a, u32x4 b, f32x4 c) { return __builtin_amdgcn_mfma_f32_16x16x32_bf16(asbf(a), asbf(b), c, 0, 0, 0); }
DI f32x16 mfma32(u32x4 a, u32x4 b, f32x16 c) { return __builtin_amdgcn_mfma_f32_32x32x16_bf16(asbf(a), asbf(b), c, 0, 0, 0); }

DI void glds16(const void* sbase, u32 voff, u32 lds) {
    asm volatile("s_mov_b32 m0, %2\n\ts_nop 0\n\tglobal_load_lds_dwordx4 %0, %1" :: "v"(voff), "s"(sbase), "s"(lds) : "memory");
}
DI void glds16nt(const void* sbase, u32 voff, u32 lds) {
    asm volatile("s_mov_b32 m0, %2\n\ts_nop 0\n\tglobal_load_lds_dwordx4 %0, %1 nt" :: "v"(voff), "s"(sbase), "s"(lds) : "memory");
}
DI void touch(const void* sbase, u32 voff) {
    asm volatile("s_mov_b32 m0, %2\n\ts_nop 0\n\tglobal_load_lds_dword %0, %1" :: "v"(voff), "s"(sbase), "s"(156928u) : "memory");
}
#define NT_KV 0
#define NT_X 0
#define NT_H 0
#define PREF1 1
#define PREF2 1
#define PREFC 1
#define CODE_LINES 345
#define PREFT 0
#define PREFKV 0
#define QDELAY 0
DI void gld64(u32x2& d, const void* sbase, u32 voff) { asm volatile("global_load_dwordx2 %0, %1, %2" : "=v"(d) : "v"(voff), "s"(sbase) : "memory"); }
DI void gld128(u32x4& d, const void* sbase, u32 voff) { asm volatile("global_load_dwordx4 %0, %1, %2" : "=v"(d) : "v"(voff), "s"(sbase) : "memory"); }
DI void gld128c(u32x4& d, const void* sbase, u32 voff) { asm volatile("global_load_dwordx4 %0, %1, %2 sc1" : "=v"(d) : "v"(voff), "s"(sbase) : "memory"); }
DI void gld32c(u32& d, const void* sbase, u32 voff) { asm volatile("global_load_dword %0, %1, %2 sc1" : "=v"(d) : "v"(voff), "s"(sbase) : "memory"); }
#define PIN(x) asm volatile("" : "+v"(x))
#define SB() __builtin_amdgcn_sched_barrier(0)
DI u32x4 ld128c(rsrc_t r, u32 off) { return __builtin_amdgcn_raw_buffer_load_b128(r, off, 0, 16); }
DI u32x2 ld64c(rsrc_t r, u32 off) { return __builtin_amdgcn_raw_buffer_load_b64(r, off, 0, 16); }
DI u32 ld32c(rsrc_t r, u32 off) { return __builtin_amdgcn_raw_buffer_load_b32(r, off, 0, 16); }
DI void st128c(rsrc_t r, u32 off, u32x4 v) { __builtin_amdgcn_raw_buffer_store_b128(v, r, off, 0, 16); }
DI void st64c(rsrc_t r, u32 off, u32x2 v) { __builtin_amdgcn_raw_buffer_store_b64(v, r, off, 0, 16); }
DI void st32c(rsrc_t r, u32 off, u32 v) { __builtin_amdgcn_raw_buffer_store_b32(v, r, off, 0, 16); }
DI u32x4 lds128(LAS char* sm, u32 a) { return *(LAS u32x4*)(sm + a); }
DI u32x2 lds64(LAS char* sm, u32 a) { return *(LAS u32x2*)(sm + a); }
DI u32x2 ldstr(LAS char* sm, u32 a) { return __builtin_bit_cast(u32x2, __builtin_amdgcn_ds_read_tr16_b64_v4i16((LAS s16x4*)(sm + a))); }
#define VMCNT(n) asm volatile("s_waitcnt vmcnt(" #n ")" ::: "memory")
#define LGKM0() asm volatile("s_waitcnt lgkmcnt(0)" ::: "memory")
#define BAR() do { asm volatile("" ::: "memory"); __builtin_amdgcn_s_barrier(); asm volatile("" ::: "memory"); } while (0)
#define MEMF() asm volatile("" ::: "memory")

DI void set_flag(rsrc_t r, u32 idx, u32 tag) { st32c(r, WS_FLAG + idx * FS, tag); }
DI void wait_flags(rsrc_t r, int idx0, int idx1, u32 tag) {
    unsigned long long t0 = 0;
    for (u32 it = 0;; it++) {
        u32 a = tag, b = tag;
        if (idx0 >= 0) a = ld32c(r, WS_FLAG + idx0 * FS);
        if (idx1 >= 0) b = ld32c(r, WS_FLAG + idx1 * FS);
        if (__all(a == tag && b == tag)) break;
        if (POLL_SLEEP) __builtin_amdgcn_s_sleep(POLL_SLEEP);
        if ((it & 63u) == 0u) {
            const unsigned long long t = __builtin_amdgcn_s_memrealtime();
            if (it == 0) t0 = t;
            else if (t - t0 > 2000000ull) { set_flag(r, F_TMO, tag); break; }
        }
    }
    MEMF();
}
DI void lds_arrive(LAS char* sm, u32 cnt, int lane) { LGKM0(); if (lane == 0) __hip_atomic_fetch_add((LAS u32*)(sm + cnt), 1u, __ATOMIC_RELAXED, __HIP_MEMORY_SCOPE_WORKGROUP); MEMF(); }
DI void lds_wait(LAS char* sm, u32 cnt, u32 n) { MEMF(); while (*(volatile LAS u32*)(sm + cnt) < n) __builtin_amdgcn_s_sleep(0); MEMF(); }
DI void publish_last(rsrc_t r, LAS char* sm, u32 cnt, u32 nw, u32 flag, u32 tag, int lane) {
    VMCNT(0);
    if (lane == 0) {
        const u32 old = __hip_atomic_fetch_add((LAS u32*)(sm + cnt), 1u, __ATOMIC_RELAXED, __HIP_MEMORY_SCOPE_WORKGROUP);
        if (old == nw - 1) set_flag(r, flag, tag);
    }
    MEMF();
}

DI u32 phi2(float a, float b) {
    const f32x2 t = {a, b}, s = t * 1.4426950408889634f;
    const float ea = __builtin_amdgcn_fmed3f(__builtin_amdgcn_exp2f(s.x), 0.f, 1.f), eb = __builtin_amdgcn_fmed3f(__builtin_amdgcn_exp2f(s.y), 0.f, 1.f);
    const float ma = __int_as_float(max(__float_as_int(a), 0)), mb = __int_as_float(max(__float_as_int(b), 0));
    const f32x2 r = ((f32x2){ea, eb} + (f32x2){ma, mb}) + 1e-6f;
    return pk2(r.x, r.y);
}

constexpr int K1_STAGE = 32768;
constexpr int P0_K = 131072, P0_V = P0_K + 9216, P0_OUT = P0_V + 8192;
constexpr int L_CNT = 156672;
constexpr int C_G1 = L_CNT, C_Y = L_CNT + 4, C_P0A = L_CNT + 8, C_P0B = L_CNT + 12, C_P0C = L_CNT + 16, C_FY = L_CNT + 20, C_FY2 = L_CNT + 24, C_FO = L_CNT + 28, C_FR = L_CNT + 32, C_XF = L_CNT + 36;
constexpr int CV_PITCH = 4160;
constexpr int CV_U = 4 * CV_PITCH;
constexpr int CV_WAVE = CV_U + 4608;
constexpr int CV_BASE = 65536;
constexpr int WO_HI = 65536;
constexpr int F_YY = 110592;
constexpr int F_Y2 = F_YY + 10240;
constexpr int F_GG = F_Y2 + 11264;
constexpr int KO_RED = 110592;
constexpr int KO_KV = KO_RED + 32768;
constexpr int RED_PITCH = 336;
constexpr int LDS_BYTES = 160 * 1024;
DI u32 p0_flag(int tile) { return F_P0 + (tile % 10) * 32 + tile / 10; }
DI constexpr u32 wo_off(int kap) { return kap < 12 ? kap * 5120 : WO_HI + (kap - 12) * 5120; }

DI void p0_issue(const u16* kin, const u16* vin, int tile, int lw, int lane, u32 base) {
    const int cg = tile % 10, rg = tile / 10, s0 = rg * 64;
    const u32 col = cg * 128 + (lane & 7) * 16;
#pragma unroll
    for (int z = 0; z < 2; z++) {
        const int n = 2 * lw + z;
        int r = s0 - 4 + n * 8 + (lane >> 3); r = r < 0 ? 0 : r;
        (NT_KV ? glds16nt : glds16)(kin, (u32)(r * 1280) + col, base + n * 1024);
        (NT_KV ? glds16nt : glds16)(vin, (u32)((s0 + n * 8 + (lane >> 3)) * 1280) + col, base + 9216 + n * 1024);
    }
    {
        int r = s0 + 60 + (lane >> 3); r = r > S - 1 ? S - 1 : r;
        (NT_KV ? glds16nt : glds16)(kin, (u32)(r * 1280) + col, base + 8 * 1024);
    }
}
DI float dot2(u32 a, u32 b, float c) { return __builtin_amdgcn_fdot2_f32_bf16(__builtin_bit_cast(bf16x2, a), __builtin_bit_cast(bf16x2, b), c, false); }
DI void p0_compute(LAS char* sm, u32x2 w4, int tile, int wave, int lane, u32 base, u32 outb, char* ws = nullptr) {
    const int s0 = (tile / 10) * 64;
    const int G = wave * 4 + (lane >> 4), cb = G & 3, i = lane & 15, q = i >> 2, p = i & 3;
    const u32 wa = __builtin_amdgcn_alignbit(w4.y, w4.y, 16), wb = __builtin_amdgcn_alignbit(w4.x, w4.x, 16);
    const int ch = 16 * cb + i;
    u32x2 kq[2][3], vq[2][2];
#pragma unroll
    for (int z = 0; z < 2; z++) {
        const int o = (G >> 2) + 4 * z;
        const u32 ka = base + (8 * o + q) * 128 + 32 * cb + 8 * p, va = ka + 9216;
        kq[z][0] = ldstr(sm, ka); kq[z][1] = ldstr(sm, ka + 512); kq[z][2] = ldstr(sm, ka + 1024);
        vq[z][0] = ldstr(sm, va); vq[z][1] = ldstr(sm, va + 512);
    }
#pragma unroll
    for (int z = 0; z < 2; z++) {
        const int o = (G >> 2) + 4 * z;
        u32 K[6] = {kq[z][0].x, kq[z][0].y, kq[z][1].x, kq[z][1].y, kq[z][2].x, kq[z][2].y};
        if (s0 == 0 && o == 0) { K[0] = 0; K[1] = 0; }
        u32 Sh[5];
#pragma unroll
        for (int e = 0; e < 5; e++) Sh[e] = __builtin_amdgcn_alignbit(K[e + 1], K[e], 16);
        const u32 V[4] = {vq[z][0].x, vq[z][0].y, vq[z][1].x, vq[z][1].y};
        u32 r[4];
#pragma unroll
        for (int e = 0; e < 4; e++) {
            const float k0 = dot2(Sh[e], wa, dot2(Sh[e + 1], wb, 0.f)), k1 = dot2(K[e + 1], wa, dot2(K[e + 2], wb, 0.f));
            r[e] = pk2(k0 * blo(V[e]), k1 * bhi(V[e]));
        }
        *(LAS u32x4*)(sm + outb + ch * 128 + ((o ^ (ch & 7)) * 16)) = (u32x4){r[0], r[1], r[2], r[3]};
    }
}
DI void p0_store(LAS char* sm, rsrc_t rw, int tile, int t256, u32 outb) {
    const int cg = tile % 10, s0 = (tile / 10) * 64;
#pragma unroll
    for (int z = 0; z < 2; z++) {
        const int ch2 = (t256 >> 3) + 32 * z, o2 = t256 & 7;
        const u32x4 t4 = lds128(sm, outb + ch2 * 128 + ((o2 ^ (ch2 & 7)) * 16));
        st128c(rw, WS_UT + (cg * 64 + ch2) * 4096 + (s0 + 8 * o2) * 2, t4);
    }
}
DI void p0_weights(u32x2& w4, const u16* shk, int tile, int wave, int lane) {
    const int cg = tile % 10, cb = (wave * 4 + (lane >> 4)) & 3;
    gld64(w4, shk, (u32)((cg * 64 + 16 * cb + (lane & 15)) * 8));
}

struct K1Set { u32x4 xa[2][2]; u32x2 wl[2][2], wh[2][2]; };
struct K1Addr { u32 x[2][2]; u32 w[2][2]; };
template <bool QT, int WO, int XO>
DI void k1_phase(LAS char* sm, f32x16 (&acc)[2][2], const K1Set& cur, K1Set& nxt, const u32 (&xb)[2], const u32 (&wb)[2][2]) {
#pragma unroll
    for (int n = 0; n < 8; n++) {
        const int ks = n >> 2, i = (n >> 1) & 1, jj = n & 1;
        const u32x4 wf = {cur.wl[ks][jj].x, cur.wl[ks][jj].y, cur.wh[ks][jj].x, cur.wh[ks][jj].y};
        acc[i][jj] = QT ? mfma32(wf, cur.xa[ks][i], acc[i][jj]) : mfma32(cur.xa[ks][i], wf, acc[i][jj]);
        if (n == 0) { nxt.wl[0][0] = ldstr(sm, wb[0][0] + WO); nxt.wh[0][0] = ldstr(sm, wb[0][1] + WO); }
        if (n == 1) { nxt.xa[0][0] = lds128(sm, xb[0] + XO); nxt.wl[0][1] = ldstr(sm, wb[1][0] + WO); }
        if (n == 2) { nxt.wh[0][1] = ldstr(sm, wb[1][1] + WO); nxt.xa[0][1] = lds128(sm, xb[0] + XO + 4096); }
        if (n == 3) { nxt.wl[1][0] = ldstr(sm, wb[0][0] + WO + 4096); nxt.wh[1][0] = ldstr(sm, wb[0][1] + WO + 4096); }
        if (n == 4) { nxt.xa[1][0] = lds128(sm, xb[1] + XO); nxt.wl[1][1] = ldstr(sm, wb[1][0] + WO + 4096); }
        if (n == 5) { nxt.wh[1][1] = ldstr(sm, wb[1][1] + WO + 4096); nxt.xa[1][1] = lds128(sm, xb[1] + XO + 4096); }
        SB();
    }
}
template <bool QT, int ST>
DI void k1_chunk(LAS char* sm, f32x16 (&acc)[2][2], K1Set& s0, K1Set& s1, const K1Addr& a0, const K1Addr& a1, bool bar, bool drain = false) {
    constexpr int SN = (ST + 1) & 3;
    const K1Addr& ac = ST < 2 ? a0 : a1; const K1Addr& an = SN < 2 ? a0 : a1;
    k1_phase<QT, (ST & 1) * K1_STAGE + 8192, (ST & 1) * K1_STAGE + 16384>(sm, acc, s0, s1, ac.x[1], ac.w);
    if (drain) VMCNT(0);
    if (bar) { LGKM0(); BAR(); }
    k1_phase<QT, (SN & 1) * K1_STAGE, (SN & 1) * K1_STAGE + 16384>(sm, acc, s1, s0, an.x[0], an.w);
}
template <bool QT>
DI void k1_loop(LAS char* sm, const K1Addr& a0, f32x16 (&acc)[2][2], rsrc_t rw, u32 pflag, u32 tag, int wave, int lane, const u16* hk, const u16* wo, int n240, u32x2 w4) {
    K1Addr a1;
#pragma unroll
    for (int i = 0; i < 2; i++)
#pragma unroll
        for (int z = 0; z < 2; z++) { a1.x[i][z] = a0.x[i][z] + 65536; a1.w[i][z] = a0.w[i][z] + 65536; }
    K1Set s0, s1;
#pragma unroll
    for (int ks = 0; ks < 2; ks++)
#pragma unroll
        for (int jj = 0; jj < 2; jj++) {
            s0.wl[ks][jj] = ldstr(sm, a0.w[jj][0] + ks * 4096); s0.wh[ks][jj] = ldstr(sm, a0.w[jj][1] + ks * 4096);
            s0.xa[ks][jj] = lds128(sm, a0.x[0][ks] + 16384 + jj * 4096);
        }
    SB();
#pragma unroll 1
    for (int c = 0; c < 8; c += 4) {
        k1_chunk<QT, 0>(sm, acc, s0, s1, a0, a1, true);
        if (c == 4) p0_store(sm, rw, n240, wave * 64 + lane, P0_OUT);
        k1_chunk<QT, 1>(sm, acc, s0, s1, a0, a1, true);
        k1_chunk<QT, 2>(sm, acc, s0, s1, a0, a1, true);
        k1_chunk<QT, 3>(sm, acc, s0, s1, a0, a1, true, c == 4);
        if (c == 4 && wave == 0 && lane == 0) set_flag(rw, pflag, tag);
        if (c == 0) { VMCNT(0); PIN(w4); p0_compute(sm, w4, n240, wave, lane, P0_K, P0_OUT); }
        if (PREF2 && c == 0 && wave >= 2) {
            if (wave == 2) { const u32 l_ = n240 * 86 + lane; touch(hk, (l_ < 20480 ? l_ : 20479) * 128); }
            else { const u32 l_ = n240 * 86 + 64 + lane, m_ = n240 * 27 + lane - 22;
                   if (lane < 22) touch(hk, (l_ < 20480 ? l_ : 20479) * 128); else if (lane < 49) touch(wo, (m_ < 6400 ? m_ : 6399) * 128); }
        }
    }
    k1_chunk<QT, 0>(sm, acc, s0, s1, a0, a1, true);
    k1_chunk<QT, 1>(sm, acc, s0, s1, a0, a1, false);
}

DI void kv_addr(bool isk, int j5, int rt, int wm, int wn, int jj, int r32, int hh, u32& dst, u32& istep, u32& zstep) {
    const int cc = 128 * j5 + 64 * wn + 32 * jj + r32, h = cc / 20, d = cc - 20 * h;
    const int sb32 = rt * 4 + 2 * wm;
    const bool lo = d < 16;
    dst = lo ? (isk ? WS_PK0 : WS_V0) + (((h * 64 + sb32) * 4 + hh) * 128 + d * 8) * 2
             : (isk ? WS_XK : WS_XV) + (((h * 64 + sb32) * 4 + hh) * 32 + (d - 16) * 8) * 2;
    istep = lo ? 1024 : 256; zstep = lo ? 512 : 128;
}
constexpr int K_XFER = 65536;

DI void wo_issue(const u16* wo, int ct, int k0, int k1, int w, int nw, int lane) {
    u32 vo[5], vs[5];
#pragma unroll
    for (int i = 0; i < 5; i++) {
        const int n = 64 * i + lane, rho = n / 10, c = n - 10 * rho, g = 2 * (rho >> 4) + ((rho >> 2) & 1), r8 = 4 * ((rho >> 3) & 1) + (rho & 3);
        vo[i] = (u32)((8 * (g < 2 ? g : g + 1) + r8) * 1280 + ct * 160 + c * 16);
        vs[i] = (u32)((40 * g + 16 + r8) * 1280 + ct * 160 + c * 16);
    }
#pragma unroll 1
    for (int kap = k0 + w; kap < k1; kap += nw) {
        const u32 dst = kap < 12 ? kap * 5120 : WO_HI + (kap - 12) * 5120;
        if (kap < 16) {
            const char* src = (const char*)wo + (size_t)kap * 51200;
#pragma unroll
            for (int i = 0; i < 5; i++) glds16(src, vo[i], dst + i * 1024);
        } else {
            const char* src = (const char*)wo + (size_t)(kap - 16) * 204800;
#pragma unroll
            for (int i = 0; i < 5; i++) glds16(src, vs[i], dst + i * 1024);
        }
    }
}

struct CvSet { u32x2 fl[8], fh[8]; u32x4 g[8]; };
DI void cv_load(LAS char* sm, CvSet& s, u32 fa, u32 ga) {
#pragma unroll
    for (int k1 = 0; k1 < 8; k1++) { s.fl[k1] = ldstr(sm, fa + 512 * k1 + 8); s.fh[k1] = ldstr(sm, fa + 512 * k1); s.g[k1] = lds128(sm, ga + 512 * k1); }
}
DI void cv_phase(LAS char* sm, f32x4 (&Y)[8], const CvSet& cur, CvSet& nxt, u32 fa, u32 ga) {
    PIN(fa); PIN(ga);
    SB();
    int n = 0;
#pragma unroll
    for (int e = 0; e < 8; e++)
#pragma unroll
        for (int k1 = 0; k1 + e < 8; k1++) {
            const u32x4 F = {cur.fl[k1].x, cur.fl[k1].y, cur.fh[k1].x, cur.fh[k1].y};
            Y[k1 + e] = mfma16(F, cur.g[e], Y[k1 + e]);
            if (n == 0) nxt.g[0] = lds128(sm, ga);
            else if (n <= 16) { const int r = (n - 1) >> 1; if ((n - 1) & 1) nxt.fh[r] = ldstr(sm, fa + 512 * r); else nxt.fl[r] = ldstr(sm, fa + 512 * r + 8); }
            else if (n < 24) nxt.g[n - 16] = lds128(sm, ga + 512 * (n - 16));
            n++;
            SB();
        }
}
struct CvIn { u32x4 hd[4]; u32x2 hm[4]; };
DI void conv_fetch_h(CvIn& in, const u16* hk, int c, int lane) {
    const char* hb = (const char*)hk + (size_t)c * 4096;
#pragma unroll
    for (int z = 0; z < 4; z++) {
        const int i = lane + 64 * z;
        if (NT_H) asm volatile("global_load_dwordx4 %0, %1, %2 nt" : "=v"(in.hd[z]) : "v"((u32)(i * 16)), "s"(hb) : "memory"); else gld128(in.hd[z], hb, (u32)(i * 16));
        gld64(in.hm[z], hb, (u32)(i > 0 ? i * 16 - 8 : 0));
    }
}
DI void conv_build_h(LAS char* sm0, CvIn& in, int w, int lane) {
    LAS char* sm = sm0 + CV_BASE + w * CV_WAVE;
#pragma unroll
    for (int z = 0; z < 4; z++) { PIN(in.hd[z]); PIN(in.hm[z]); }
    if (lane < 32) *(LAS u32x4*)(sm + CV_U + lane * 16) = (u32x4){0, 0, 0, 0};
    if (lane < 8) *(LAS u32x4*)(sm + (lane >> 1) * CV_PITCH + (lane & 1) * 16) = (u32x4){0, 0, 0, 0};
#pragma unroll
    for (int z = 0; z < 4; z++) {
        const int i = lane + 64 * z;
        u32 m2 = in.hm[z].x, m1 = in.hm[z].y;
        if (i == 0) { m2 = 0; m1 = 0; }
        const u32 d0 = in.hd[z].x, d1 = in.hd[z].y, d2 = in.hd[z].z, d3 = in.hd[z].w;
        const u32 om2 = __builtin_amdgcn_alignbit(m1, m2, 16), om1 = __builtin_amdgcn_alignbit(d0, m1, 16), o0 = __builtin_amdgcn_alignbit(d1, d0, 16),
                  o1 = __builtin_amdgcn_alignbit(d2, d1, 16), o2 = __builtin_amdgcn_alignbit(d3, d2, 16);
        LAS char* p = sm + 32 + i * 16;
        *(LAS u32x4*)(p) = (u32x4){d0, d1, d2, d3};
        *(LAS u32x4*)(p + CV_PITCH) = (u32x4){om1, o0, o1, o2};
        *(LAS u32x4*)(p + 2 * CV_PITCH) = (u32x4){m1, d0, d1, d2};
        *(LAS u32x4*)(p + 3 * CV_PITCH) = (u32x4){om2, om1, o0, o1};
    }
}
DI void conv_run(LAS char* sm0, float Dc, int w, int lane, int c, rsrc_t rw, char* ws) {
    LAS char* sm = sm0;
    LGKM0();
    TS(4, 9);
    const int fr = lane & 15, g = lane >> 4, q = fr >> 2, p = lane & 3;
    sm = sm0;
    const u32 fa = CV_BASE + w * CV_WAVE + q * CV_PITCH + 2 * (32 - 8 * g + 4 * p) - 8;
    const u32 ga = CV_BASE + w * CV_WAVE + CV_U + 480 + 32 * fr + 16 * g;
    f32x4 Y[8];
#pragma unroll
    for (int i = 0; i < 8; i++) Y[i] = (f32x4){0, 0, 0, 0};
    CvSet A, B;
    cv_load(sm, A, fa, ga);
    SB();
#pragma unroll 1
    for (int k0 = 0; k0 < 8; k0 += 2) {
        cv_phase(sm, Y, A, B, fa + 64 * (k0 + 1), ga - 64 * (k0 + 1));
        cv_phase(sm, Y, B, A, fa + 64 * (k0 + 2), ga - 64 * (k0 + 2));
    }
    TS(4, 10);
    sm = sm0 + CV_BASE + w * CV_WAVE;
    {
        u32x2 u4[8];
#pragma unroll
        for (int ib = 0; ib < 8; ib++) u4[ib] = lds64(sm, CV_U + 512 + 512 * ib + 32 * fr + 8 * g);
#pragma unroll
        for (int ib = 0; ib < 8; ib++) {
            const u32x2 r = {pk2(fmaf(blo(u4[ib].x), Dc, Y[ib][0]), fmaf(bhi(u4[ib].x), Dc, Y[ib][1])), pk2(fmaf(blo(u4[ib].y), Dc, Y[ib][2]), fmaf(bhi(u4[ib].y), Dc, Y[ib][3]))};
            *(LAS u32x2*)(sm + 512 * ib + 32 * fr + 8 * g) = r;
        }
    }
    LGKM0();
    u32x4 y8[4];
#pragma unroll
    for (int z = 0; z < 4; z++) y8[z] = lds128(sm, z * 1024 + lane * 16);
#pragma unroll
    for (int z = 0; z < 4; z++) st128c(rw, WS_YT + c * 4096 + z * 1024 + lane * 16, y8[z]);
}

DI void kvo_block(LAS char* sm, int wave, int lane, int h, int half, rsrc_t rw, u32 tag, char* ws) {
    const int fr = lane & 15, g = lane >> 4;
    const int j0 = (20 * h) >> 7, j1 = (20 * h + 19) >> 7;
    wait_flags(rw, lane < 8 ? (int)F_G1 + 16 * (((lane & 2) ? 10 : 5) + ((lane & 1) ? j1 : j0)) + 2 * wave + (lane >> 2)
                            : lane < 10 ? (int)F_G1 + 16 * ((lane & 1) ? j1 : j0) + 8 * half + wave : -1, -1, tag);
    TS(0, 8);
    const u32 pk0 = WS_PK0 + (h * 64 + 8 * wave) * 1024 + lane * 16, v0 = pk0 + (WS_V0 - WS_PK0);
    const u32 xx = ((fr & 4) ? WS_XV : WS_XK) + ((h * 64 + 8 * wave + (fr >> 3)) * 4 + g) * 64 + (fr & 3) * 16;
    u32x4 A[8], B[8], X[4], Q[8];
#pragma unroll
    for (int i = 0; i < 8; i++) { A[i] = ld128c(rw, pk0 + i * 1024); B[i] = ld128c(rw, v0 + i * 1024); if ((i & 1) == 0) X[i >> 1] = ld128c(rw, xx + (i >> 1) * 512); }
    const int odd = h & 1, pr = h >> 1;
    const int u5 = (g < 3 ? g : 2) + 2 * odd;
    const int sb0 = half * 64 + wave * 8;
    const u32 pq = WS_P2 + (((pr * 128 + sb0) * 5 + u5) * 128 + fr * 8) * 2;
    MEMF(); SB();
    f32x4 a00 = {0, 0, 0, 0}, a0x = a00, ax0 = a00, axx = a00;
    const u32 cst = fr == 8 ? 0x3F803F80u : 0u;
#pragma unroll
    for (int i = 0; i < 8; i++) {
        u32x4 xv = X[i >> 1];
        if (i & 1) {
#pragma unroll
            for (int e = 0; e < 4; e++) xv[e] = (u32)__builtin_amdgcn_update_dpp(0, (int)xv[e], 0x108, 0xf, 0xf, false);
        }
        if (fr >= 8) xv = (u32x4){cst, cst, cst, cst};
        a00 = mfma16(A[i], B[i], a00); a0x = mfma16(A[i], xv, a0x); ax0 = mfma16(xv, B[i], ax0); axx = mfma16(xv, xv, axx);
    }
    PIN(a00); PIN(a0x); PIN(ax0); PIN(axx);
    SB();
#pragma unroll
    for (int i = 0; i < 8; i++) Q[i] = ld128c(rw, pq + i * 1280);
    MEMF(); SB();
    TS(0, 9);
    *(LAS f32x4*)(sm + KO_RED + ((wave * 4 + 0) * 64 + lane) * 16) = a00;
    *(LAS f32x4*)(sm + KO_RED + ((wave * 4 + 1) * 64 + lane) * 16) = a0x;
    *(LAS f32x4*)(sm + KO_RED + ((wave * 4 + 2) * 64 + lane) * 16) = ax0;
    *(LAS f32x4*)(sm + KO_RED + ((wave * 4 + 3) * 64 + lane) * 16) = axx;
    if (wave >= 4 && (wave - 4) * 64 + lane < 189) *(LAS u32x2*)(sm + KO_KV + ((wave - 4) * 64 + lane) * 8) = (u32x2){0, 0};
    LGKM0(); BAR();
    if (wave < 4) {
        const int tile = wave;
        f32x4 s = *(LAS f32x4*)(sm + KO_RED + (tile * 64 + lane) * 16);
#pragma unroll
        for (int w2 = 1; w2 < 8; w2++) s += *(LAS f32x4*)(sm + KO_RED + ((w2 * 4 + tile) * 64 + lane) * 16);
        const int e = (tile & 1) ? 12 + fr : fr;
        const bool colok = (tile & 1) ? (fr >= 4 && fr <= 8) : true;
        const bool rowok = (tile & 2) ? (g == 0) : true;
        const int d0 = (tile & 2) ? 16 : 4 * g;
        if (colok && rowok) *(LAS u32x2*)(sm + KO_KV + e * 72 + (d0 + 4) * 2) = (u32x2){pk2(s[0], s[1]), pk2(s[2], s[3])};
    }
    LGKM0(); BAR();
    u32x4 A0, A1[4], A2;
    {
        const u32 dofs = KO_KV + (8 * g - 4 * odd + 4) * 2;
        const u32 r0 = (4 * odd + fr) * 72, r1 = ((odd ? 0 : 16) + (fr & 3)) * 72;
        const u32x2 p0 = lds64(sm, dofs + r0), p1 = lds64(sm, dofs + r0 + 8);
        const u32x2 q0 = lds64(sm, dofs + r1), q1 = lds64(sm, dofs + r1 + 8);
        const u32x2 z0 = lds64(sm, dofs + 20 * 72), z1 = lds64(sm, dofs + 20 * 72 + 8);
        A0 = (u32x4){p0.x, p0.y, p1.x, p1.y}; A2 = (u32x4){z0.x, z0.y, z1.x, z1.y};
#pragma unroll
        for (int z = 0; z < 4; z++) { const bool m = (fr >> 2) == z; A1[z] = (u32x4){m ? q0.x : 0u, m ? q0.y : 0u, m ? q1.x : 0u, m ? q1.y : 0u}; }
    }
    TS(0, 10);
    const u32 offA = WS_O + ((pr * 4 + 2 * odd + (g >> 1)) * 128 + fr * 8) * 2 + (g & 1) * 20480;
    const u32 offX = WS_O + (((16 + (pr >> 2)) * 4 + (pr & 3)) * 128 + odd * 64 + fr * 4) * 2;
    const f32x4 zero = {0, 0, 0, 0};
#pragma unroll
    for (int i4 = 0; i4 < 2; i4++) {
        f32x4 n1 = zero; float rden[4]; u32x2 ov[4];
#pragma unroll
        for (int z = 0; z < 4; z++) {
            const int i = 4 * i4 + z;
            const f32x4 n0 = mfma16(A0, Q[i], zero);
            const f32x4 dn = mfma16(A2, Q[i], zero);
            n1 = mfma16(A1[z], Q[i], n1);
            const float rd = __builtin_amdgcn_rcpf(dn[0] + 1e-6f);
            rden[z] = rd;
            ov[z] = (u32x2){pk2(n0[0] * rd, n0[1] * rd), pk2(n0[2] * rd, n0[3] * rd)};
        }
#pragma unroll
        for (int z = 0; z < 4; z += 2) {
            auto rx = __builtin_amdgcn_permlane16_swap(ov[z].x, ov[z + 1].x, false, false);
            auto ry = __builtin_amdgcn_permlane16_swap(ov[z].y, ov[z + 1].y, false, false);
            st128c(rw, offA + (sb0 + 4 * i4 + z) * 20480, (u32x4){rx[0], ry[0], rx[1], ry[1]});
        }
        const float rd = g == 0 ? rden[0] : g == 1 ? rden[1] : g == 2 ? rden[2] : rden[3];
        st64c(rw, offX + (sb0 + 4 * i4 + g) * 20480, (u32x2){pk2(n1[0] * rd, n1[1] * rd), pk2(n1[2] * rd, n1[3] * rd)});
    }
}

DI void gate_block(int wave, int lane, int m, const u16* x, const u16* gw, rsrc_t rw) {
    const int sub = lane & 15, r4 = lane >> 4;
    u32x4 gq[5], xr[4][5];
#pragma unroll
    for (int jj = 0; jj < 5; jj++) gq[jj] = *(const GAS u32x4*)((const GAS char*)gw + (sub + 16 * jj) * 16);
#pragma unroll
    for (int it = 0; it < 4; it++)
#pragma unroll
        for (int jj = 0; jj < 5; jj++) xr[it][jj] = *(const GAS u32x4*)((const GAS char*)x + (size_t)(128 * m + 16 * wave + 4 * it + r4) * 1280 + (sub + 16 * jj) * 16);
    MEMF(); SB();
#pragma unroll
    for (int it = 0; it < 4; it++) {
        float a = 0.f;
#pragma unroll
        for (int jj = 0; jj < 5; jj++)
#pragma unroll
            for (int e = 0; e < 4; e++) { a = fmaf(blo(xr[it][jj][e]), blo(gq[jj][e]), a); a = fmaf(bhi(xr[it][jj][e]), bhi(gq[jj][e]), a); }
        a += __shfl_xor(a, 1); a += __shfl_xor(a, 2); a += __shfl_xor(a, 4); a += __shfl_xor(a, 8);
        a = rbf(a);
        const float g = rbf(__builtin_amdgcn_rcpf(1.f + __builtin_amdgcn_exp2f(-1.4426950408889634f * a)));
        if (sub == 0) st32c(rw, WS_G + (128 * m + 16 * wave + 4 * it + r4) * 4, __float_as_uint(g));
    }
}

DI void final_block(LAS char* sm, int wave, int lane, int tid, int st, int ct, const u16* x, u16* out, rsrc_t rw, u32 tag, char* ws) {
    const int fr = lane & 15, g = lane >> 4;
    u32x4 xv[2];
#pragma unroll
    for (int r = 0; r < 2; r++) {
        int it = tid + 512 * r; it = it < 640 ? it : 639;
        const int s = it / 10, u = it - 10 * s;
        xv[r] = *(const GAS u32x4*)((const GAS char*)x + (size_t)(st * 64 + s) * 1280 + ct * 160 + u * 16);
    }
    VMCNT(0); BAR();
    if (wave < 4) {
        const int kh = wave & 1, sh = wave >> 1;
        wait_flags(rw, lane < 16 ? (int)F_O + (st >> 4) * 32 + 16 * kh + lane : -1, -1, tag);
        TS(0, 17);
        const u32 ob = WS_O + ((st * 4 + 2 * sh) * 20 + 8 * kh) * 1024 + lane * 16;
        const u32 os = WS_O + ((st * 4 + 2 * sh) * 20 + 16 + 2 * kh) * 1024 + g * 256 + fr * 8;
        u32x4 B[2][10];
#pragma unroll
        for (int kl = 0; kl < 8; kl++) { B[0][kl] = ld128c(rw, ob + kl * 1024); B[1][kl] = ld128c(rw, ob + 20480 + kl * 1024); }
#pragma unroll
        for (int kl = 0; kl < 2; kl++)
#pragma unroll
            for (int z = 0; z < 2; z++) {
                const u32x2 h0 = ld64c(rw, os + z * 20480 + kl * 1024), h1 = ld64c(rw, os + z * 20480 + kl * 1024 + 128);
                B[z][8 + kl] = (u32x4){h0.x, h0.y, h1.x, h1.y};
            }
        MEMF(); SB();
        f32x4 acc[5][2];
#pragma unroll
        for (int cb = 0; cb < 5; cb++) { acc[cb][0] = (f32x4){0, 0, 0, 0}; acc[cb][1] = (f32x4){0, 0, 0, 0}; }
        const u32 aoff = (16 * (g >> 1) + 4 * (g & 1) + (fr >> 2)) * 160 + (lane & 3) * 8;
        u32x2 al[2][5], ah[2][5];
#define F_KAP(kl) ((kl) < 8 ? (kl) : 8 + (kl))
#define F_READ(buf, kl) do { const u32 b0_ = kh ? wo_off(F_KAP(kl) + ((kl) < 8 ? 8 : 2)) : wo_off(F_KAP(kl)); \
            _Pragma("unroll") for (int cb = 0; cb < 5; cb++) { al[buf][cb] = ldstr(sm, b0_ + aoff + cb * 32); ah[buf][cb] = ldstr(sm, b0_ + aoff + cb * 32 + 1280); } } while (0)
        F_READ(0, 0);
#pragma unroll
        for (int kl = 0; kl < 10; kl++) {
            if (kl < 9) F_READ((kl + 1) & 1, kl + 1);
            SB();
#pragma unroll
            for (int cb = 0; cb < 5; cb++) {
                const u32x4 a = {al[kl & 1][cb].x, al[kl & 1][cb].y, ah[kl & 1][cb].x, ah[kl & 1][cb].y};
                acc[cb][0] = mfma16(a, B[0][kl], acc[cb][0]); acc[cb][1] = mfma16(a, B[1][kl], acc[cb][1]);
            }
            SB();
        }
        TS(0, 18);
        lds_arrive(sm, C_FR, lane); lds_wait(sm, C_FR, 4);
#pragma unroll
        for (int cb = 0; cb < 5; cb++)
#pragma unroll
            for (int z = 0; z < 2; z++)
                *(LAS f32x4*)(sm + kh * (64 * RED_PITCH) + (16 * (2 * sh + z) + fr) * RED_PITCH + (16 * cb + 4 * g) * 4) = acc[cb][z];
    } else {
        if (wave == 4) { wait_flags(rw, lane < 20 ? (int)F_Y + ct * 32 + lane : lane == 32 ? (int)F_GATE + (st >> 1) : -1, -1, tag); lds_arrive(sm, C_FY, lane); }
        else lds_wait(sm, C_FY, 1);
        const int t = tid - 256;
        u32x4 yv[3];
#pragma unroll
        for (int r = 0; r < 3; r++) { int id = t + 256 * r; id = id < 640 ? id : 639; yv[r] = ld128c(rw, WS_YT + (80 * ct + (id >> 3)) * 4096 + st * 128 + (id & 7) * 16); }
#pragma unroll
        for (int r = 0; r < 3; r++) { const int id = t + 256 * r; if (id < 640) *(LAS u32x4*)(sm + F_YY + id * 16) = yv[r]; }
        if (wave == 4) *(LAS u32*)(sm + F_GG + lane * 4) = ld32c(rw, WS_G + (st * 64 + lane) * 4);
        lds_arrive(sm, C_FY2, lane); lds_wait(sm, C_FY2, 4);
        const int sb = wave - 4, q = fr >> 2, p = lane & 3;
        u32x2 tt[5];
#pragma unroll
        for (int cb = 0; cb < 5; cb++) tt[cb] = ldstr(sm, F_YY + (16 * cb + 4 * g + q) * 128 + (16 * sb + 4 * p) * 2);
#pragma unroll
        for (int cb = 0; cb < 5; cb++) *(LAS u32x2*)(sm + F_Y2 + (16 * sb + fr) * 176 + (16 * cb + 4 * g) * 2) = tt[cb];
    }
    LGKM0(); BAR();
#pragma unroll
    for (int r = 0; r < 2; r++) {
        const int it = tid + 512 * r;
        if (it < 640) {
            const int s = it / 10, u = it - 10 * s;
            const u32 ra = s * RED_PITCH + u * 32;
            const f32x4 p0 = *(LAS f32x4*)(sm + ra), p1 = *(LAS f32x4*)(sm + ra + 16), q0 = *(LAS f32x4*)(sm + 64 * RED_PITCH + ra), q1 = *(LAS f32x4*)(sm + 64 * RED_PITCH + ra + 16);
            const u32x4 y8 = lds128(sm, F_Y2 + s * 176 + u * 16);
            const float gg = *(LAS float*)(sm + F_GG + s * 4), omg = 1.f - gg;
            float o[8];
#pragma unroll
            for (int e = 0; e < 4; e++) {
                const float a0 = (e < 2 ? p0[2 * e] + q0[2 * e] : p1[2 * e - 4] + q1[2 * e - 4]), a1 = (e < 2 ? p0[2 * e + 1] + q0[2 * e + 1] : p1[2 * e - 3] + q1[2 * e - 3]);
                const float b0 = omg * blo(xv[r][e]), b1 = omg * bhi(xv[r][e]);
                o[2 * e] = fmaf(b0, blo(y8[e]), gg * a0); o[2 * e + 1] = fmaf(b1, bhi(y8[e]), gg * a1);
            }
            *(GAS u32x4*)((GAS char*)out + (size_t)(st * 64 + s) * 1280 + ct * 160 + u * 16) = (u32x4){pk2(o[0], o[1]), pk2(o[2], o[3]), pk2(o[4], o[5]), pk2(o[6], o[7])};
        }
    }
}

__global__ __launch_bounds__(512, 2) void fused_kernel(const u16* __restrict__ x, const u16* __restrict__ w, const u16* __restrict__ wo, const u16* __restrict__ kin,
                                                        const u16* __restrict__ vin, const u16* __restrict__ shk, const u16* __restrict__ hk, const u16* __restrict__ sD,
                                                        const u16* __restrict__ gw, u16* __restrict__ out, char* ws) {
    extern __shared__ __attribute__((aligned(16))) char smem[];
    LAS char* sm = (LAS char*)smem;
    const int tid = threadIdx.x, lane = tid & 63, wave = __builtin_amdgcn_readfirstlane(tid >> 6);
    const int b = blockIdx.x, xcd = b & 7, j = b >> 3;
    const rsrc_t rw = __builtin_amdgcn_make_buffer_rsrc(ws, 0, WS_BYTES, 0x00020000);
    asm volatile("" :: "s"(x), "s"(w), "s"(wo), "s"(kin), "s"(vin), "s"(shk), "s"(hk), "s"(sD), "s"(gw), "s"(out));
    if (PREF1 && wave < 3 && j < 30) {
        if (wave == 0) touch(x, (u32)((xcd * 2560 + j * 80 + lane) * 128));
        else if (wave == 1) { if (lane < 16) touch(x, (u32)((xcd * 2560 + j * 80 + 64 + lane) * 128)); else touch(w, (u32)(((xcd * 30 + j) * 80 + lane - 16) * 128)); }
        else if (lane < 32) touch(w, (u32)(((xcd * 30 + j) * 80 + 48 + lane) * 128));
    }
    if (PREFKV && wave < 2 && j < 30) {
        const int tile_ = xcd * 30 + j;
        touch(wave == 0 ? kin : vin, (u32)(((tile_ / 10) * 64 + lane) * 1280 + (tile_ % 10) * 128));
    }
    if (PREFC && wave == 3) {
        unsigned long long pc;
        asm volatile("s_getpc_b64 %0" : "=s"(pc));
        pc &= ~127ull;
        const int ln = PREFC == 1 ? j * 10 + lane : lane;
        if (PREFC == 1) { if (lane < 10 && ln < CODE_LINES) touch((const void*)pc, (u32)(ln * 128)); }
        else {
#pragma unroll
            for (int z = 0; z < 5; z++) if (z * 64 + lane < CODE_LINES) touch((const void*)pc, (u32)((z * 64 + lane) * 128));
        }
    }
    if (PREFT && wave == 2) {
        if (PREFT == 1) { touch(ws, (u32)(lane * 262144)); if (lane < 40) touch(out, (u32)(lane * 65536)); }
        else { touch(ws, (u32)((j * 8 + (lane & 7)) * 65536 + (lane >> 3) * 128) % (WS_STAMP - 1024)); if (lane < 40) touch(out, (u32)(lane * 65536)); }
    }
    u32 tag;
    asm volatile("s_load_dword %0, %1, 0x0\n\ts_waitcnt lgkmcnt(0)" : "=s"(tag) : "s"(ws + WS_FLAG + F_EPOCH * FS) : "memory");
    tag += 1u;
    if (tid < 16) *(LAS u32*)(sm + L_CNT + tid * 4) = 0u;
    TS(0, 0);
    const int st = 4 * xcd + (j >> 3), ct = j & 7;
    int ko = -1;
    bool is_conv = false;

    if (j >= 30) {
        const int m = xcd * 2 + (j - 30);
        ko = -100;
        {
            const int grp = wave >> 2, w4i = wave & 3;
            u32x2 w4[3];
#pragma unroll
            for (int t = 0; t < 3; t++) { const int tt = 2 * t + grp < 5 ? 2 * t + grp : 4; p0_weights(w4[t], shk, 240 + 5 * m + tt, w4i, lane); }
#pragma unroll
            for (int t = 0; t < 3; t++) if (2 * t + grp < 5) p0_issue(kin, vin, 240 + 5 * m + 2 * t + grp, w4i, lane, (2 * t + grp) * 17408);
            VMCNT(0);
#pragma unroll
            for (int t = 0; t < 3; t++) PIN(w4[t]);
            BAR();
#pragma unroll
            for (int t = 0; t < 3; t++)
                if (2 * t + grp < 5) p0_compute(sm, w4[t], 240 + 5 * m + 2 * t + grp, w4i, lane, (2 * t + grp) * 17408, 90112 + (2 * t + grp) * 8192);
            LGKM0(); BAR();
            {
                u32x4 t4[3][2];
#pragma unroll
                for (int t = 0; t < 3; t++)
#pragma unroll
                    for (int z = 0; z < 2; z++) { const int ch2 = ((tid & 255) >> 3) + 32 * z, o2 = tid & 7, tt = 2 * t + grp < 5 ? 2 * t + grp : 4; t4[t][z] = lds128(sm, 90112 + tt * 8192 + ch2 * 128 + ((o2 ^ (ch2 & 7)) * 16)); }
#pragma unroll
                for (int t = 0; t < 3; t++)
                    if (2 * t + grp < 5) {
                        const int tile = 240 + 5 * m + 2 * t + grp, cg = tile % 10, s0 = (tile / 10) * 64;
#pragma unroll
                        for (int z = 0; z < 2; z++) { const int ch2 = ((tid & 255) >> 3) + 32 * z, o2 = tid & 7; st128c(rw, WS_UT + (cg * 64 + ch2) * 4096 + (s0 + 8 * o2) * 2, t4[t][z]); }
                    }
            }
        }
        VMCNT(0); BAR();
        if (tid < 5) set_flag(rw, p0_flag(240 + 5 * m + tid), tag);
        TS(0, 1);
        gate_block(wave, lane, m, x, gw, rw);
        VMCNT(0); LGKM0(); BAR();
        if (tid == 0) set_flag(rw, F_GATE + m, tag);
        TS(0, 3);
    } else {
        const int rt = 2 * xcd + j / 15, cbk = j % 15, tile = xcd * 30 + j;
        is_conv = cbk < 5 || cbk >= 10;
        if (!is_conv) { const int ki = rt * 5 + (cbk - 5); ko = ki < 64 ? ki : -2 - (ki - 64); }
        if (wave >= 4) {
            const int lw = wave - 4;
            u32 vx[4], vw[4];
#pragma unroll
            for (int n2 = 0; n2 < 4; n2++) {
                const int n = 4 * lw + n2;
                const int row = 8 * n + (lane >> 3), ch = (lane & 7) ^ ((row >> 1) & 7);
                vx[n2] = (u32)((rt * 128 + row) * 1280 + ch * 16);
                const int kr = 4 * n + (lane >> 4), f = ((kr & 3) << 2) | ((kr >> 2) & 3), cw = (lane & 15) ^ f;
                vw[n2] = (u32)(kr * 3840 + cbk * 256 + cw * 16);
            }
            const int rot = (rt + cbk) % 10;
#define K1_ISSUE(c) do { const u32 st_ = ((c) & 3) * K1_STAGE + 4 * lw * 1024; int kc_ = (c) + ROT_ON * rot; kc_ = kc_ >= 10 ? kc_ - 10 : kc_; \
            const char* xb_ = (const char*)x + kc_ * 128; const char* wp_ = (const char*)w + (size_t)kc_ * 64 * 3840; \
            _Pragma("unroll") for (int n2 = 0; n2 < 4; n2++) { if (NT_X) glds16nt(xb_, vx[n2], st_ + 16384 + n2 * 1024); else glds16(xb_, vx[n2], st_ + 16384 + n2 * 1024); glds16(wp_, vw[n2], st_ + n2 * 1024); } } while (0)
            K1_ISSUE(0); K1_ISSUE(1);
            VMCNT(8); BAR();
            K1_ISSUE(2); K1_ISSUE(3);
            p0_issue(kin, vin, tile, lw, lane, P0_K);
#pragma unroll 1
            for (int c = 0; c < 6; c++) {
                if (c < 3) VMCNT(21); else VMCNT(16);
                BAR();
                K1_ISSUE(c + 4);
            }
            const int cv = rt * 10 + (cbk < 5 ? cbk : cbk - 5), c = 4 * cv + lw;
            if (is_conv) {
                CvIn in; u32 fl; u32x4 u[4];
                const int fidx = (int)F_P0 + (cv >> 4) * 32 + (lane & 31);
                conv_fetch_h(in, hk, c, lane);
                VMCNT(24); BAR();
                VMCNT(16); BAR();
                gld32c(fl, ws, WS_FLAG + fidx * FS);
                VMCNT(9); BAR();
                TS(4, 4);
                VMCNT(0); PIN(fl);
                const bool ready = __all(fl == tag);
                if (ready) {
#pragma unroll
                    for (int z = 0; z < 4; z++) gld128c(u[z], ws, WS_UT + c * 4096 + z * 1024 + lane * 16);
                }
                TS(4, 5);
                conv_build_h(sm, in, lw, lane);
                TS(4, 6);
                if (!ready) {
                    wait_flags(rw, fidx, -1, tag);
#pragma unroll
                    for (int z = 0; z < 4; z++) gld128c(u[z], ws, WS_UT + c * 4096 + z * 1024 + lane * 16);
                }
                TS(4, 7);
                VMCNT(0);
#pragma unroll
                for (int z = 0; z < 4; z++) { PIN(u[z]); *(LAS u32x4*)(sm + CV_BASE + lw * CV_WAVE + CV_U + 512 + z * 1024 + lane * 16) = u[z]; }
                const float Dc = blo((u32)sD[c]);
                TS(4, 8);
                conv_run(sm, Dc, lw, lane, c, rw, ws);
                publish_last(rw, sm, C_Y, 4, F_Y + (cv / 20) * 32 + cv % 20, tag, lane);
                TS(4, 11);
            } else {
                VMCNT(16); BAR();
                VMCNT(8); BAR();
                VMCNT(0); BAR();
                const int r32 = lane & 31, hh = lane >> 5;
                u32 dst, istep, zstep; kv_addr(true, cbk - 5, rt, lw >> 1, lw & 1, 1, r32, hh, dst, istep, zstep);
                lds_wait(sm, C_XF + lw * 4, 1);
                f32x4 a[2][4];
#pragma unroll
                for (int i = 0; i < 2; i++)
#pragma unroll
                    for (int q4 = 0; q4 < 4; q4++) a[i][q4] = *(LAS f32x4*)(sm + K_XFER + lw * 8192 + ((i * 4 + q4) * 64 + lane) * 16);
#pragma unroll
                for (int i = 0; i < 2; i++)
#pragma unroll
                    for (int z = 0; z < 2; z++) {
                        const f32x4 a0 = a[i][2 * z], a1 = a[i][2 * z + 1];
                        st128c(rw, dst + i * istep + z * zstep, (u32x4){phi2(a0[0], a0[1]), phi2(a0[2], a0[3]), phi2(a1[0], a1[1]), phi2(a1[2], a1[3])});
                    }
                publish_last(rw, sm, C_G1, 8, F_G1 + cbk * 16 + rt, tag, lane);
            }
        } else {
            u32x2 w4; p0_weights(w4, shk, tile, wave, lane);
            const int wm = wave >> 1, wn = wave & 1, r32 = lane & 31, hh = lane >> 5;
            K1Addr a0;
#pragma unroll
            for (int ks = 0; ks < 2; ks++) {
                a0.x[0][ks] = (64 * wm + r32) * 128 + (((2 * ks + hh) ^ ((r32 >> 1) & 7)) * 16);
                a0.x[1][ks] = a0.x[0][ks] ^ 64;
            }
#pragma unroll
            for (int jj = 0; jj < 2; jj++)
#pragma unroll
                for (int t = 0; t < 2; t++) {
                    const int blk = (lane >> 4) & 1, q = (lane & 15) >> 2, p = lane & 3, row = 8 * hh + 4 * t + q, ch = 8 * wn + 4 * jj + 2 * blk + (p >> 1);
                    const int f = ((row & 3) << 2) | ((row >> 2) & 3);
                    a0.w[jj][t] = row * 256 + ((ch ^ f) * 16) + 8 * (p & 1);
                }
            f32x16 acc[2][2];
#pragma unroll
            for (int i = 0; i < 2; i++)
#pragma unroll
                for (int jj = 0; jj < 2; jj++)
#pragma unroll
                    for (int e = 0; e < 16; e++) acc[i][jj][e] = 0.f;
            BAR();
            TS(0, 1);
            if (cbk < 5) {
                k1_loop<true>(sm, a0, acc, rw, p0_flag(tile), tag, wave, lane, hk, wo, tile, w4);
                __builtin_amdgcn_s_setprio(2);
                TS(0, 2);
                u32x4 qv[2][2][2];
#pragma unroll
                for (int jj = 0; jj < 2; jj++)
#pragma unroll
                    for (int ap = 0; ap < 2; ap++)
#pragma unroll
                        for (int i = 0; i < 2; i++) {
                            const f32x16& a = acc[i][jj];
                            const u32 x0 = phi2(a[8 * ap], a[8 * ap + 1]), y0 = phi2(a[8 * ap + 2], a[8 * ap + 3]);
                            const u32 x1 = phi2(a[8 * ap + 4], a[8 * ap + 5]), y1 = phi2(a[8 * ap + 6], a[8 * ap + 7]);
                            auto rx = __builtin_amdgcn_permlane32_swap(x0, x1, false, false);
                            auto ry = __builtin_amdgcn_permlane32_swap(y0, y1, false, false);
                            qv[jj][ap][i] = (u32x4){rx[0], ry[0], rx[1], ry[1]};
                        }
                if (QDELAY) { __builtin_amdgcn_s_setprio(0); for (int z = 0; z < QDELAY; z++) __builtin_amdgcn_s_sleep(8); }
#pragma unroll
                for (int jj = 0; jj < 2; jj++)
#pragma unroll
                    for (int ap = 0; ap < 2; ap++) {
                        const int U = 16 * cbk + 8 * wn + 4 * jj + 2 * ap + hh, pr = U / 5, u5 = U - 5 * pr;
                        const u32 dst = WS_P2 + (((pr * 128 + rt * 8 + 4 * wm + (r32 >> 4)) * 5 + u5) * 128 + (r32 & 15) * 8) * 2;
#pragma unroll
                        for (int i = 0; i < 2; i++) st128c(rw, dst + i * 2560, qv[jj][ap][i]);
                    }
            } else {
                k1_loop<false>(sm, a0, acc, rw, p0_flag(tile), tag, wave, lane, hk, wo, tile, w4);
                __builtin_amdgcn_s_setprio(2);
                TS(0, 2);
                const bool isk = cbk < 10;
                const int j5 = isk ? cbk - 5 : cbk - 10;
                if (isk) {
#pragma unroll
                    for (int i = 0; i < 2; i++)
#pragma unroll
                        for (int q4 = 0; q4 < 4; q4++)
                            *(LAS f32x4*)(sm + K_XFER + wave * 8192 + ((i * 4 + q4) * 64 + lane) * 16) = (f32x4){acc[i][1][4 * q4], acc[i][1][4 * q4 + 1], acc[i][1][4 * q4 + 2], acc[i][1][4 * q4 + 3]};
                    lds_arrive(sm, C_XF + wave * 4, lane);
                    u32 dst, istep, zstep; kv_addr(true, j5, rt, wm, wn, 0, r32, hh, dst, istep, zstep);
#pragma unroll
                    for (int i = 0; i < 2; i++)
#pragma unroll
                        for (int z = 0; z < 2; z++) {
                            const f32x16& a = acc[i][0];
                            st128c(rw, dst + i * istep + z * zstep,
                                   (u32x4){phi2(a[8 * z], a[8 * z + 1]), phi2(a[8 * z + 2], a[8 * z + 3]), phi2(a[8 * z + 4], a[8 * z + 5]), phi2(a[8 * z + 6], a[8 * z + 7])});
                        }
                } else {
#pragma unroll
                    for (int jj = 0; jj < 2; jj++) {
                        u32 dst, istep, zstep; kv_addr(false, j5, rt, wm, wn, jj, r32, hh, dst, istep, zstep);
#pragma unroll
                        for (int i = 0; i < 2; i++)
#pragma unroll
                            for (int z = 0; z < 2; z++) {
                                const f32x16& a = acc[i][jj];
                                st128c(rw, dst + i * istep + z * zstep,
                                       (u32x4){pk2(a[8 * z], a[8 * z + 1]), pk2(a[8 * z + 2], a[8 * z + 3]), pk2(a[8 * z + 4], a[8 * z + 5]), pk2(a[8 * z + 6], a[8 * z + 7])});
                            }
                    }
                }
            }
            LGKM0();
            TS(0, 25);
            publish_last(rw, sm, C_G1, is_conv ? 4 : 8, F_G1 + cbk * 16 + rt, tag, lane);
            __builtin_amdgcn_s_setprio(0);
            TS(0, 3);
            if (is_conv) {
                lds_wait(sm, C_G1, 4);
                wo_issue(wo, ct, 0, 12, wave, 4, lane);
            }
        }
    }
    if (!is_conv) {
        LGKM0(); BAR();
        if (ko >= 0) {
            const int h = ko >> 1, half = ko & 1;
            wo_issue(wo, ct, 0, 12, wave, 8, lane);
            kvo_block(sm, wave, lane, h, half, rw, tag, ws);
            TS(0, 26);
            VMCNT(0); BAR();
            if (tid == 0) set_flag(rw, F_O + half * 32 + h, tag);
            TS(0, 11);
            wo_issue(wo, ct, 12, 20, wave, 8, lane);
        } else {
            wo_issue(wo, ct, 0, 20, wave, 8, lane);
            if (ko == -2 && wave == 0) {
                wait_flags(rw, (int)F_G1 + lane, (int)F_G1 + 64 + lane, tag);
                wait_flags(rw, (int)F_G1 + 128 + lane, (int)F_G1 + 192 + lane, tag);
                if (lane == 0) set_flag(rw, F_EPOCH, tag);
            }
        }
    } else {
        VMCNT(0); BAR();
        if (wave >= 4) wo_issue(wo, ct, 12, 20, wave - 4, 4, lane);
    }
    TS(0, 16);
    final_block(sm, wave, lane, tid, st, ct, x, out, rw, tag, ws);
    TS(0, 19);
}

extern "C" void kernel_launch(void* const* d_in, const int* in_sizes, int n_in, void* d_out, int out_size, void* d_ws, size_t ws_size, hipStream_t stream) {
    const u16* x = (const u16*)d_in[0]; const u16* wqkv = (const u16*)d_in[1]; const u16* wo = (const u16*)d_in[2];
    const u16* kin = (const u16*)d_in[4]; const u16* vin = (const u16*)d_in[5]; const u16* shk = (const u16*)d_in[6];
    const u16* hk = (const u16*)d_in[7]; const u16* sD = (const u16*)d_in[8]; const u16* gw = (const u16*)d_in[9];
    (void)hipFuncSetAttribute((const void*)fused_kernel, hipFuncAttributeMaxDynamicSharedMemorySize, LDS_BYTES);
    fused_kernel<<<256, 512, LDS_BYTES, stream>>>(x, wqkv, wo, kin, vin, shk, hk, sD, gw, (u16*)d_out, (char*)d_ws);
}
```

```cpp
#include <hip/hip_runtime.h>
#include <stdint.h>

typedef unsigned short u16;
typedef unsigned int u32;
typedef __bf16 bf16x8 __attribute__((ext_vector_type(8)));
typedef __bf16 bf16x2 __attribute__((ext_vector_type(2)));
typedef float f32x4 __attribute__((ext_vector_type(4)));
typedef float f32x2 __attribute__((ext_vector_type(2)));
typedef float f32x16 __attribute__((ext_vector_type(16)));
typedef u32 u32x4 __attribute__((ext_vector_type(4)));
typedef u32 u32x2 __attribute__((ext_vector_type(2)));
typedef short s16x4 __attribute__((ext_vector_type(4)));
#define LAS __attribute__((address_space(3)))
#define GAS __attribute__((address_space(1)))
#define DI __device__ __forceinline__
typedef __amdgpu_buffer_rsrc_t rsrc_t;

constexpr int S = 2048, D = 640;
#define FS 256
#define POLL_SLEEP 2
#define ROT_ON 1

constexpr u32 WS_UT = 0;
constexpr u32 WS_YT = WS_UT + 2621440;
constexpr u32 WS_PK0 = WS_YT + 2621440;
constexpr u32 WS_V0 = WS_PK0 + 2097152;
constexpr u32 WS_XK = WS_V0 + 2097152;
constexpr u32 WS_XV = WS_XK + 524288;
constexpr u32 WS_P2 = WS_XV + 524288;
constexpr u32 WS_O = WS_P2 + 2621440;
constexpr u32 WS_G = WS_O + 2621440;
constexpr u32 WS_FLAG = WS_G + 8192;
constexpr u32 F_P0 = 0;
constexpr u32 F_G1 = 320;
constexpr u32 F_GATE = 560;
constexpr u32 F_O = 576;
constexpr u32 F_Y = 640;
constexpr u32 F_EPOCH = 896, F_TMO = 928;
constexpr u32 WS_STAMP = WS_FLAG + 1024 * FS;
constexpr u32 WS_BYTES = WS_STAMP + 256 * 32 * 16;

#define TS(w, id) do {} while (0)

DI u32 pk2(float a, float b) { f32x2 t = {a, b}; return __builtin_bit_cast(u32, __builtin_convertvector(t, bf16x2)); }
DI float blo(u32 u) { return __uint_as_float(u << 16); }
DI float bhi(u32 u) { return __uint_as_float(u & 0xffff0000u); }
DI float rbf(float f) { return blo(pk2(f, f)); }
DI bf16x8 asbf(u32x4 v) { return __builtin_bit_cast(bf16x8, v); }
DI f32x4 mfma16(u32x4 a, u32x4 b, f32x4 c) { return __builtin_amdgcn_mfma_f32_16x16x32_bf16(asbf(a), asbf(b), c, 0, 0, 0); }
DI f32x16 mfma32(u32x4 a, u32x4 b, f32x16 c) { return __builtin_amdgcn_mfma_f32_32x32x16_bf16(asbf(a), asbf(b), c, 0, 0, 0); }

DI void glds16(const void* sbase, u32 voff, u32 lds) {
    asm volatile("s_mov_b32 m0, %2\n\ts_nop 0\n\tglobal_load_lds_dwordx4 %0, %1" :: "v"(voff), "s"(sbase), "s"(lds) : "memory");
}
DI void glds16nt(const void* sbase, u32 voff, u32 lds) {
    asm volatile("s_mov_b32 m0, %2\n\ts_nop 0\n\tglobal_load_lds_dwordx4 %0, %1 nt" :: "v"(voff), "s"(sbase), "s"(lds) : "memory");
}
DI void touch(const void* sbase, u32 voff) {
    asm volatile("s_mov_b32 m0, %2\n\ts_nop 0\n\tglobal_load_lds_dword %0, %1" :: "v"(voff), "s"(sbase), "s"(156928u) : "memory");
}
#define NT_KV 0
#define NT_X 0
#define NT_H 0
#define PREF1 1
#define PREF2 0
#define PREFC 1
#define CODE_LINES 345
#define PREFT 0
#define PREFKV 0
#define PREF3 1
#define QDELAY 0
DI void gld64(u32x2& d, const void* sbase, u32 voff) { asm volatile("global_load_dwordx2 %0, %1, %2" : "=v"(d) : "v"(voff), "s"(sbase) : "memory"); }
DI void gld128(u32x4& d, const void* sbase, u32 voff) { asm volatile("global_load_dwordx4 %0, %1, %2" : "=v"(d) : "v"(voff), "s"(sbase) : "memory"); }
DI void gld128c(u32x4& d, const void* sbase, u32 voff) { asm volatile("global_load_dwordx4 %0, %1, %2 sc1" : "=v"(d) : "v"(voff), "s"(sbase) : "memory"); }
DI void gld32c(u32& d, const void* sbase, u32 voff) { asm volatile("global_load_dword %0, %1, %2 sc1" : "=v"(d) : "v"(voff), "s"(sbase) : "memory"); }
#define PIN(x) asm volatile("" : "+v"(x))
#define SB() __builtin_amdgcn_sched_barrier(0)
DI u32x4 ld128c(rsrc_t r, u32 off) { return __builtin_amdgcn_raw_buffer_load_b128(r, off, 0, 16); }
DI u32x2 ld64c(rsrc_t r, u32 off) { return __builtin_amdgcn_raw_buffer_load_b64(r, off, 0, 16); }
DI u32 ld32c(rsrc_t r, u32 off) { return __builtin_amdgcn_raw_buffer_load_b32(r, off, 0, 16); }
DI void st128c(rsrc_t r, u32 off, u32x4 v) { __builtin_amdgcn_raw_buffer_store_b128(v, r, off, 0, 16); }
DI void st64c(rsrc_t r, u32 off, u32x2 v) { __builtin_amdgcn_raw_buffer_store_b64(v, r, off, 0, 16); }
DI void st32c(rsrc_t r, u32 off, u32 v) { __builtin_amdgcn_raw_buffer_store_b32(v, r, off, 0, 16); }
DI u32x4 lds128(LAS char* sm, u32 a) { return *(LAS u32x4*)(sm + a); }
DI u32x2 lds64(LAS char* sm, u32 a) { return *(LAS u32x2*)(sm + a); }
DI u32x2 ldstr(LAS char* sm, u32 a) { return __builtin_bit_cast(u32x2, __builtin_amdgcn_ds_read_tr16_b64_v4i16((LAS s16x4*)(sm + a))); }
#define VMCNT(n) asm volatile("s_waitcnt vmcnt(" #n ")" ::: "memory")
#define LGKM0() asm volatile("s_waitcnt lgkmcnt(0)" ::: "memory")
#define BAR() do { asm volatile("" ::: "memory"); __builtin_amdgcn_s_barrier(); asm volatile("" ::: "memory"); } while (0)
#define MEMF() asm volatile("" ::: "memory")

DI void set_flag(rsrc_t r, u32 idx, u32 tag) { st32c(r, WS_FLAG + idx * FS, tag); }
DI void wait_flags(rsrc_t r, int idx0, int idx1, u32 tag) {
    unsigned long long t0 = 0;
    for (u32 it = 0;; it++) {
        u32 a = tag, b = tag;
        if (idx0 >= 0) a = ld32c(r, WS_FLAG + idx0 * FS);
        if (idx1 >= 0) b = ld32c(r, WS_FLAG + idx1 * FS);
        if (__all(a == tag && b == tag)) break;
        if (POLL_SLEEP) __builtin_amdgcn_s_sleep(POLL_SLEEP);
        if ((it & 63u) == 0u) {
            const unsigned long long t = __builtin_amdgcn_s_memrealtime();
            if (it == 0) t0 = t;
            else if (t - t0 > 2000000ull) { set_flag(r, F_TMO, tag); break; }
        }
    }
    MEMF();
}
DI void lds_arrive(LAS char* sm, u32 cnt, int lane) { LGKM0(); if (lane == 0) __hip_atomic_fetch_add((LAS u32*)(sm + cnt), 1u, __ATOMIC_RELAXED, __HIP_MEMORY_SCOPE_WORKGROUP); MEMF(); }
DI void lds_wait(LAS char* sm, u32 cnt, u32 n) { MEMF(); while (*(volatile LAS u32*)(sm + cnt) < n) __builtin_amdgcn_s_sleep(0); MEMF(); }
DI void publish_last(rsrc_t r, LAS char* sm, u32 cnt, u32 nw, u32 flag, u32 tag, int lane) {
    VMCNT(0);
    if (lane == 0) {
        const u32 old = __hip_atomic_fetch_add((LAS u32*)(sm + cnt), 1u, __ATOMIC_RELAXED, __HIP_MEMORY_SCOPE_WORKGROUP);
        if (old == nw - 1) set_flag(r, flag, tag);
    }
    MEMF();
}

DI u32 phi2(float a, float b) {
    const f32x2 t = {a, b}, s = t * 1.4426950408889634f;
    const float ea = __builtin_amdgcn_fmed3f(__builtin_amdgcn_exp2f(s.x), 0.f, 1.f), eb = __builtin_amdgcn_fmed3f(__builtin_amdgcn_exp2f(s.y), 0.f, 1.f);
    const float ma = __int_as_float(max(__float_as_int(a), 0)), mb = __int_as_float(max(__float_as_int(b), 0));
    const f32x2 r = ((f32x2){ea, eb} + (f32x2){ma, mb}) + 1e-6f;
    return pk2(r.x, r.y);
}

constexpr int K1_STAGE = 32768;
constexpr int P0_K = 131072, P0_V = P0_K + 9216, P0_OUT = P0_V + 8192;
constexpr int L_CNT = 156672;
constexpr int C_G1 = L_CNT, C_Y = L_CNT + 4, C_P0A = L_CNT + 8, C_P0B = L_CNT + 12, C_P0C = L_CNT + 16, C_FY = L_CNT + 20, C_FY2 = L_CNT + 24, C_FO = L_CNT + 28, C_FR = L_CNT + 32, C_XF = L_CNT + 36;
constexpr int CV_PITCH = 4160;
constexpr int CV_U = 4 * CV_PITCH;
constexpr int CV_WAVE = CV_U + 4608;
constexpr int CV_BASE = 65536;
constexpr int WO_HI = 65536;
constexpr int F_YY = 110592;
constexpr int F_Y2 = F_YY + 10240;
constexpr int F_GG = F_Y2 + 11264;
constexpr int KO_RED = 110592;
constexpr int KO_KV = KO_RED + 32768;
constexpr int RED_PITCH = 336;
constexpr int LDS_BYTES = 160 * 1024;
DI u32 p0_flag(int tile) { return F_P0 + (tile % 10) * 32 + tile / 10; }
DI constexpr u32 wo_off(int kap) { return kap < 12 ? kap * 5120 : WO_HI + (kap - 12) * 5120; }

DI void p0_issue(const u16* kin, const u16* vin, int tile, int lw, int lane, u32 base) {
    const int cg = tile % 10, rg = tile / 10, s0 = rg * 64;
    const u32 col = cg * 128 + (lane & 7) * 16;
#pragma unroll
    for (int z = 0; z < 2; z++) {
        const int n = 2 * lw + z;
        int r = s0 - 4 + n * 8 + (lane >> 3); r = r < 0 ? 0 : r;
        (NT_KV ? glds16nt : glds16)(kin, (u32)(r * 1280) + col, base + n * 1024);
        (NT_KV ? glds16nt : glds16)(vin, (u32)((s0 + n * 8 + (lane >> 3)) * 1280) + col, base + 9216 + n * 1024);
    }
    {
        int r = s0 + 60 + (lane >> 3); r = r > S - 1 ? S - 1 : r;
        (NT_KV ? glds16nt : glds16)(kin, (u32)(r * 1280) + col, base + 8 * 1024);
    }
}
DI float dot2(u32 a, u32 b, float c) { return __builtin_amdgcn_fdot2_f32_bf16(__builtin_bit_cast(bf16x2, a), __builtin_bit_cast(bf16x2, b), c, false); }
DI void p0_compute(LAS char* sm, u32x2 w4, int tile, int wave, int lane, u32 base, u32 outb, char* ws = nullptr) {
    const int s0 = (tile / 10) * 64;
    const int G = wave * 4 + (lane >> 4), cb = G & 3, i = lane & 15, q = i >> 2, p = i & 3;
    const u32 wa = __builtin_amdgcn_alignbit(w4.y, w4.y, 16), wb = __builtin_amdgcn_alignbit(w4.x, w4.x, 16);
    const int ch = 16 * cb + i;
    u32x2 kq[2][3], vq[2][2];
#pragma unroll
    for (int z = 0; z < 2; z++) {
        const int o = (G >> 2) + 4 * z;
        const u32 ka = base + (8 * o + q) * 128 + 32 * cb + 8 * p, va = ka + 9216;
        kq[z][0] = ldstr(sm, ka); kq[z][1] = ldstr(sm, ka + 512); kq[z][2] = ldstr(sm, ka + 1024);
        vq[z][0] = ldstr(sm, va); vq[z][1] = ldstr(sm, va + 512);
    }
#pragma unroll
    for (int z = 0; z < 2; z++) {
        const int o = (G >> 2) + 4 * z;
        u32 K[6] = {kq[z][0].x, kq[z][0].y, kq[z][1].x, kq[z][1].y, kq[z][2].x, kq[z][2].y};
        if (s0 == 0 && o == 0) { K[0] = 0; K[1] = 0; }
        u32 Sh[5];
#pragma unroll
        for (int e = 0; e < 5; e++) Sh[e] = __builtin_amdgcn_alignbit(K[e + 1], K[e], 16);
        const u32 V[4] = {vq[z][0].x, vq[z][0].y, vq[z][1].x, vq[z][1].y};
        u32 r[4];
#pragma unroll
        for (int e = 0; e < 4; e++) {
            const float k0 = dot2(Sh[e], wa, dot2(Sh[e + 1], wb, 0.f)), k1 = dot2(K[e + 1], wa, dot2(K[e + 2], wb, 0.f));
            r[e] = pk2(k0 * blo(V[e]), k1 * bhi(V[e]));
        }
        *(LAS u32x4*)(sm + outb + ch * 128 + ((o ^ (ch & 7)) * 16)) = (u32x4){r[0], r[1], r[2], r[3]};
    }
}
DI void p0_store(LAS char* sm, rsrc_t rw, int tile, int t256, u32 outb) {
    const int cg = tile % 10, s0 = (tile / 10) * 64;
#pragma unroll
    for (int z = 0; z < 2; z++) {
        const int ch2 = (t256 >> 3) + 32 * z, o2 = t256 & 7;
        const u32x4 t4 = lds128(sm, outb + ch2 * 128 + ((o2 ^ (ch2 & 7)) * 16));
        st128c(rw, WS_UT + (cg * 64 + ch2) * 4096 + (s0 + 8 * o2) * 2, t4);
    }
}
DI void p0_weights(u32x2& w4, const u16* shk, int tile, int wave, int lane) {
    const int cg = tile % 10, cb = (wave * 4 + (lane >> 4)) & 3;
    gld64(w4, shk, (u32)((cg * 64 + 16 * cb + (lane & 15)) * 8));
}

struct K1Set { u32x4 xa[2][2]; u32x2 wl[2][2], wh[2][2]; };
struct K1Addr { u32 x[2][2]; u32 w[2][2]; };
template <bool QT, int WO, int XO>
DI void k1_phase(LAS char* sm, f32x16 (&acc)[2][2], const K1Set& cur, K1Set& nxt, const u32 (&xb)[2], const u32 (&wb)[2][2]) {
#pragma unroll
    for (int n = 0; n < 8; n++) {
        const int ks = n >> 2, i = (n >> 1) & 1, jj = n & 1;
        const u32x4 wf = {cur.wl[ks][jj].x, cur.wl[ks][jj].y, cur.wh[ks][jj].x, cur.wh[ks][jj].y};
        acc[i][jj] = QT ? mfma32(wf, cur.xa[ks][i], acc[i][jj]) : mfma32(cur.xa[ks][i], wf, acc[i][jj]);
        if (n == 0) { nxt.wl[0][0] = ldstr(sm, wb[0][0] + WO); nxt.wh[0][0] = ldstr(sm, wb[0][1] + WO); }
        if (n == 1) { nxt.xa[0][0] = lds128(sm, xb[0] + XO); nxt.wl[0][1] = ldstr(sm, wb[1][0] + WO); }
        if (n == 2) { nxt.wh[0][1] = ldstr(sm, wb[1][1] + WO); nxt.xa[0][1] = lds128(sm, xb[0] + XO + 4096); }
        if (n == 3) { nxt.wl[1][0] = ldstr(sm, wb[0][0] + WO + 4096); nxt.wh[1][0] = ldstr(sm, wb[0][1] + WO + 4096); }
        if (n == 4) { nxt.xa[1][0] = lds128(sm, xb[1] + XO); nxt.wl[1][1] = ldstr(sm, wb[1][0] + WO + 4096); }
        if (n == 5) { nxt.wh[1][1] = ldstr(sm, wb[1][1] + WO + 4096); nxt.xa[1][1] = lds128(sm, xb[1] + XO + 4096); }
        SB();
    }
}
template <bool QT, int ST>
DI void k1_chunk(LAS char* sm, f32x16 (&acc)[2][2], K1Set& s0, K1Set& s1, const K1Addr& a0, const K1Addr& a1, bool bar, bool drain = false) {
    constexpr int SN = (ST + 1) & 3;
    const K1Addr& ac = ST < 2 ? a0 : a1; const K1Addr& an = SN < 2 ? a0 : a1;
    k1_phase<QT, (ST & 1) * K1_STAGE + 8192, (ST & 1) * K1_STAGE + 16384>(sm, acc, s0, s1, ac.x[1], ac.w);
    if (drain) VMCNT(0);
    if (bar) { LGKM0(); BAR(); }
    k1_phase<QT, (SN & 1) * K1_STAGE, (SN & 1) * K1_STAGE + 16384>(sm, acc, s1, s0, an.x[0], an.w);
}
template <bool QT>
DI void k1_loop(LAS char* sm, const K1Addr& a0, f32x16 (&acc)[2][2], rsrc_t rw, u32 pflag, u32 tag, int wave, int lane, const u16* hk, const u16* wo, int n240, u32x2 w4) {
    K1Addr a1;
#pragma unroll
    for (int i = 0; i < 2; i++)
#pragma unroll
        for (int z = 0; z < 2; z++) { a1.x[i][z] = a0.x[i][z] + 65536; a1.w[i][z] = a0.w[i][z] + 65536; }
    K1Set s0, s1;
#pragma unroll
    for (int ks = 0; ks < 2; ks++)
#pragma unroll
        for (int jj = 0; jj < 2; jj++) {
            s0.wl[ks][jj] = ldstr(sm, a0.w[jj][0] + ks * 4096); s0.wh[ks][jj] = ldstr(sm, a0.w[jj][1] + ks * 4096);
            s0.xa[ks][jj] = lds128(sm, a0.x[0][ks] + 16384 + jj * 4096);
        }
    SB();
#pragma unroll 1
    for (int c = 0; c < 8; c += 4) {
        k1_chunk<QT, 0>(sm, acc, s0, s1, a0, a1, true);
        if (c == 4) p0_store(sm, rw, n240, wave * 64 + lane, P0_OUT);
        k1_chunk<QT, 1>(sm, acc, s0, s1, a0, a1, true);
        k1_chunk<QT, 2>(sm, acc, s0, s1, a0, a1, true);
        k1_chunk<QT, 3>(sm, acc, s0, s1, a0, a1, true, c == 4);
        if (c == 4 && wave == 0 && lane == 0) set_flag(rw, pflag, tag);
        if (c == 0) { VMCNT(0); PIN(w4); p0_compute(sm, w4, n240, wave, lane, P0_K, P0_OUT); }
        if (PREF2 && c == 0 && wave >= 2) {
            if (wave == 2) { const u32 l_ = n240 * 86 + lane; touch(hk, (l_ < 20480 ? l_ : 20479) * 128); }
            else { const u32 l_ = n240 * 86 + 64 + lane, m_ = n240 * 27 + lane - 22;
                   if (lane < 22) touch(hk, (l_ < 20480 ? l_ : 20479) * 128); else if (lane < 49) touch(wo, (m_ < 6400 ? m_ : 6399) * 128); }
        }
    }
    k1_chunk<QT, 0>(sm, acc, s0, s1, a0, a1, true);
    k1_chunk<QT, 1>(sm, acc, s0, s1, a0, a1, false);
}

DI void kv_addr(bool isk, int j5, int rt, int wm, int wn, int jj, int r32, int hh, u32& dst, u32& istep, u32& zstep) {
    const int cc = 128 * j5 + 64 * wn + 32 * jj + r32, h = cc / 20, d = cc - 20 * h;
    const int sb32 = rt * 4 + 2 * wm;
    const bool lo = d < 16;
    dst = lo ? (isk ? WS_PK0 : WS_V0) + (((h * 64 + sb32) * 4 + hh) * 128 + d * 8) * 2
             : (isk ? WS_XK : WS_XV) + (((h * 64 + sb32) * 4 + hh) * 32 + (d - 16) * 8) * 2;
    istep = lo ? 1024 : 256; zstep = lo ? 512 : 128;
}
constexpr int K_XFER = 65536;

DI void wo_issue(const u16* wo, int ct, int k0, int k1, int w, int nw, int lane) {
    u32 vo[5], vs[5];
#pragma unroll
    for (int i = 0; i < 5; i++) {
        const int n = 64 * i + lane, rho = n / 10, c = n - 10 * rho, g = 2 * (rho >> 4) + ((rho >> 2) & 1), r8 = 4 * ((rho >> 3) & 1) + (rho & 3);
        vo[i] = (u32)((8 * (g < 2 ? g : g + 1) + r8) * 1280 + ct * 160 + c * 16);
        vs[i] = (u32)((40 * g + 16 + r8) * 1280 + ct * 160 + c * 16);
    }
#pragma unroll 1
    for (int kap = k0 + w; kap < k1; kap += nw) {
        const u32 dst = kap < 12 ? kap * 5120 : WO_HI + (kap - 12) * 5120;
        if (kap < 16) {
            const char* src = (const char*)wo + (size_t)kap * 51200;
#pragma unroll
            for (int i = 0; i < 5; i++) glds16(src, vo[i], dst + i * 1024);
        } else {
            const char* src = (const char*)wo + (size_t)(kap - 16) * 204800;
#pragma unroll
            for (int i = 0; i < 5; i++) glds16(src, vs[i], dst + i * 1024);
        }
    }
}

struct CvSet { u32x2 fl[8], fh[8]; u32x4 g[8]; };
DI void cv_load(LAS char* sm, CvSet& s, u32 fa, u32 ga) {
#pragma unroll
    for (int k1 = 0; k1 < 8; k1++) { s.fl[k1] = ldstr(sm, fa + 512 * k1 + 8); s.fh[k1] = ldstr(sm, fa + 512 * k1); s.g[k1] = lds128(sm, ga + 512 * k1); }
}
DI void cv_phase(LAS char* sm, f32x4 (&Y)[8], const CvSet& cur, CvSet& nxt, u32 fa, u32 ga) {
    PIN(fa); PIN(ga);
    SB();
    int n = 0;
#pragma unroll
    for (int e = 0; e < 8; e++)
#pragma unroll
        for (int k1 = 0; k1 + e < 8; k1++) {
            const u32x4 F = {cur.fl[k1].x, cur.fl[k1].y, cur.fh[k1].x, cur.fh[k1].y};
            Y[k1 + e] = mfma16(F, cur.g[e], Y[k1 + e]);
            if (n == 0) nxt.g[0] = lds128(sm, ga);
            else if (n <= 16) { const int r = (n - 1) >> 1; if ((n - 1) & 1) nxt.fh[r] = ldstr(sm, fa + 512 * r); else nxt.fl[r] = ldstr(sm, fa + 512 * r + 8); }
            else if (n < 24) nxt.g[n - 16] = lds128(sm, ga + 512 * (n - 16));
            n++;
            SB();
        }
}
struct CvIn { u32x4 hd[4]; u32x2 hm[4]; };
DI void conv_fetch_h(CvIn& in, const u16* hk, int c, int lane) {
    const char* hb = (const char*)hk + (size_t)c * 4096;
#pragma unroll
    for (int z = 0; z < 4; z++) {
        const int i = lane + 64 * z;
        if (NT_H) asm volatile("global_load_dwordx4 %0, %1, %2 nt" : "=v"(in.hd[z]) : "v"((u32)(i * 16)), "s"(hb) : "memory"); else gld128(in.hd[z], hb, (u32)(i * 16));
        gld64(in.hm[z], hb, (u32)(i > 0 ? i * 16 - 8 : 0));
    }
}
DI void conv_build_h(LAS char* sm0, CvIn& in, int w, int lane) {
    LAS char* sm = sm0 + CV_BASE + w * CV_WAVE;
#pragma unroll
    for (int z = 0; z < 4; z++) { PIN(in.hd[z]); PIN(in.hm[z]); }
    if (lane < 32) *(LAS u32x4*)(sm + CV_U + lane * 16) = (u32x4){0, 0, 0, 0};
    if (lane < 8) *(LAS u32x4*)(sm + (lane >> 1) * CV_PITCH + (lane & 1) * 16) = (u32x4){0, 0, 0, 0};
#pragma unroll
    for (int z = 0; z < 4; z++) {
        const int i = lane + 64 * z;
        u32 m2 = in.hm[z].x, m1 = in.hm[z].y;
        if (i == 0) { m2 = 0; m1 = 0; }
        const u32 d0 = in.hd[z].x, d1 = in.hd[z].y, d2 = in.hd[z].z, d3 = in.hd[z].w;
        const u32 om2 = __builtin_amdgcn_alignbit(m1, m2, 16), om1 = __builtin_amdgcn_alignbit(d0, m1, 16), o0 = __builtin_amdgcn_alignbit(d1, d0, 16),
                  o1 = __builtin_amdgcn_alignbit(d2, d1, 16), o2 = __builtin_amdgcn_alignbit(d3, d2, 16);
        LAS char* p = sm + 32 + i * 16;
        *(LAS u32x4*)(p) = (u32x4){d0, d1, d2, d3};
        *(LAS u32x4*)(p + CV_PITCH) = (u32x4){om1, o0, o1, o2};
        *(LAS u32x4*)(p + 2 * CV_PITCH) = (u32x4){m1, d0, d1, d2};
        *(LAS u32x4*)(p + 3 * CV_PITCH) = (u32x4){om2, om1, o0, o1};
    }
}
DI void conv_run(LAS char* sm0, float Dc, int w, int lane, int c, rsrc_t rw, char* ws) {
    LAS char* sm = sm0;
    LGKM0();
    TS(4, 9);
    const int fr = lane & 15, g = lane >> 4, q = fr >> 2, p = lane & 3;
    sm = sm0;
    const u32 fa = CV_BASE + w * CV_WAVE + q * CV_PITCH + 2 * (32 - 8 * g + 4 * p) - 8;
    const u32 ga = CV_BASE + w * CV_WAVE + CV_U + 480 + 32 * fr + 16 * g;
    f32x4 Y[8];
#pragma unroll
    for (int i = 0; i < 8; i++) Y[i] = (f32x4){0, 0, 0, 0};
    CvSet A, B;
    cv_load(sm, A, fa, ga);
    SB();
#pragma unroll 1
    for (int k0 = 0; k0 < 8; k0 += 2) {
        cv_phase(sm, Y, A, B, fa + 64 * (k0 + 1), ga - 64 * (k0 + 1));
        cv_phase(sm, Y, B, A, fa + 64 * (k0 + 2), ga - 64 * (k0 + 2));
    }
    TS(4, 10);
    sm = sm0 + CV_BASE + w * CV_WAVE;
    {
        u32x2 u4[8];
#pragma unroll
        for (int ib = 0; ib < 8; ib++) u4[ib] = lds64(sm, CV_U + 512 + 512 * ib + 32 * fr + 8 * g);
#pragma unroll
        for (int ib = 0; ib < 8; ib++) {
            const u32x2 r = {pk2(fmaf(blo(u4[ib].x), Dc, Y[ib][0]), fmaf(bhi(u4[ib].x), Dc, Y[ib][1])), pk2(fmaf(blo(u4[ib].y), Dc, Y[ib][2]), fmaf(bhi(u4[ib].y), Dc, Y[ib][3]))};
            *(LAS u32x2*)(sm + 512 * ib + 32 * fr + 8 * g) = r;
        }
    }
    LGKM0();
    u32x4 y8[4];
#pragma unroll
    for (int z = 0; z < 4; z++) y8[z] = lds128(sm, z * 1024 + lane * 16);
#pragma unroll
    for (int z = 0; z < 4; z++) st128c(rw, WS_YT + c * 4096 + z * 1024 + lane * 16, y8[z]);
}

DI void kvo_block(LAS char* sm, int wave, int lane, int h, int half, rsrc_t rw, u32 tag, char* ws) {
    const int fr = lane & 15, g = lane >> 4;
    const int j0 = (20 * h) >> 7, j1 = (20 * h + 19) >> 7;
    wait_flags(rw, lane < 8 ? (int)F_G1 + 16 * (((lane & 2) ? 10 : 5) + ((lane & 1) ? j1 : j0)) + 2 * wave + (lane >> 2)
                            : lane < 10 ? (int)F_G1 + 16 * ((lane & 1) ? j1 : j0) + 8 * half + wave : -1, -1, tag);
    TS(0, 8);
    const u32 pk0 = WS_PK0 + (h * 64 + 8 * wave) * 1024 + lane * 16, v0 = pk0 + (WS_V0 - WS_PK0);
    const u32 xx = ((fr & 4) ? WS_XV : WS_XK) + ((h * 64 + 8 * wave + (fr >> 3)) * 4 + g) * 64 + (fr & 3) * 16;
    u32x4 A[8], B[8], X[4], Q[8];
#pragma unroll
    for (int i = 0; i < 8; i++) { A[i] = ld128c(rw, pk0 + i * 1024); B[i] = ld128c(rw, v0 + i * 1024); if ((i & 1) == 0) X[i >> 1] = ld128c(rw, xx + (i >> 1) * 512); }
    const int odd = h & 1, pr = h >> 1;
    const int u5 = (g < 3 ? g : 2) + 2 * odd;
    const int sb0 = half * 64 + wave * 8;
    const u32 pq = WS_P2 + (((pr * 128 + sb0) * 5 + u5) * 128 + fr * 8) * 2;
    MEMF(); SB();
    f32x4 a00 = {0, 0, 0, 0}, a0x = a00, ax0 = a00, axx = a00;
    const u32 cst = fr == 8 ? 0x3F803F80u : 0u;
#pragma unroll
    for (int i = 0; i < 8; i++) {
        u32x4 xv = X[i >> 1];
        if (i & 1) {
#pragma unroll
            for (int e = 0; e < 4; e++) xv[e] = (u32)__builtin_amdgcn_update_dpp(0, (int)xv[e], 0x108, 0xf, 0xf, false);
        }
        if (fr >= 8) xv = (u32x4){cst, cst, cst, cst};
        a00 = mfma16(A[i], B[i], a00); a0x = mfma16(A[i], xv, a0x); ax0 = mfma16(xv, B[i], ax0); axx = mfma16(xv, xv, axx);
    }
    PIN(a00); PIN(a0x); PIN(ax0); PIN(axx);
    SB();
#pragma unroll
    for (int i = 0; i < 8; i++) Q[i] = ld128c(rw, pq + i * 1280);
    MEMF(); SB();
    TS(0, 9);
    *(LAS f32x4*)(sm + KO_RED + ((wave * 4 + 0) * 64 + lane) * 16) = a00;
    *(LAS f32x4*)(sm + KO_RED + ((wave * 4 + 1) * 64 + lane) * 16) = a0x;
    *(LAS f32x4*)(sm + KO_RED + ((wave * 4 + 2) * 64 + lane) * 16) = ax0;
    *(LAS f32x4*)(sm + KO_RED + ((wave * 4 + 3) * 64 + lane) * 16) = axx;
    if (wave >= 4 && (wave - 4) * 64 + lane < 189) *(LAS u32x2*)(sm + KO_KV + ((wave - 4) * 64 + lane) * 8) = (u32x2){0, 0};
    LGKM0(); BAR();
    if (wave < 4) {
        const int tile = wave;
        f32x4 s = *(LAS f32x4*)(sm + KO_RED + (tile * 64 + lane) * 16);
#pragma unroll
        for (int w2 = 1; w2 < 8; w2++) s += *(LAS f32x4*)(sm + KO_RED + ((w2 * 4 + tile) * 64 + lane) * 16);
        const int e = (tile & 1) ? 12 + fr : fr;
        const bool colok = (tile & 1) ? (fr >= 4 && fr <= 8) : true;
        const bool rowok = (tile & 2) ? (g == 0) : true;
        const int d0 = (tile & 2) ? 16 : 4 * g;
        if (colok && rowok) *(LAS u32x2*)(sm + KO_KV + e * 72 + (d0 + 4) * 2) = (u32x2){pk2(s[0], s[1]), pk2(s[2], s[3])};
    }
    LGKM0(); BAR();
    u32x4 A0, A1[4], A2;
    {
        const u32 dofs = KO_KV + (8 * g - 4 * odd + 4) * 2;
        const u32 r0 = (4 * odd + fr) * 72, r1 = ((odd ? 0 : 16) + (fr & 3)) * 72;
        const u32x2 p0 = lds64(sm, dofs + r0), p1 = lds64(sm, dofs + r0 + 8);
        const u32x2 q0 = lds64(sm, dofs + r1), q1 = lds64(sm, dofs + r1 + 8);
        const u32x2 z0 = lds64(sm, dofs + 20 * 72), z1 = lds64(sm, dofs + 20 * 72 + 8);
        A0 = (u32x4){p0.x, p0.y, p1.x, p1.y}; A2 = (u32x4){z0.x, z0.y, z1.x, z1.y};
#pragma unroll
        for (int z = 0; z < 4; z++) { const bool m = (fr >> 2) == z; A1[z] = (u32x4){m ? q0.x : 0u, m ? q0.y : 0u, m ? q1.x : 0u, m ? q1.y : 0u}; }
    }
    TS(0, 10);
    const u32 offA = WS_O + ((pr * 4 + 2 * odd + (g >> 1)) * 128 + fr * 8) * 2 + (g & 1) * 20480;
    const u32 offX = WS_O + (((16 + (pr >> 2)) * 4 + (pr & 3)) * 128 + odd * 64 + fr * 4) * 2;
    const f32x4 zero = {0, 0, 0, 0};
#pragma unroll
    for (int i4 = 0; i4 < 2; i4++) {
        f32x4 n1 = zero; float rden[4]; u32x2 ov[4];
#pragma unroll
        for (int z = 0; z < 4; z++) {
            const int i = 4 * i4 + z;
            const f32x4 n0 = mfma16(A0, Q[i], zero);
            const f32x4 dn = mfma16(A2, Q[i], zero);
            n1 = mfma16(A1[z], Q[i], n1);
            const float rd = __builtin_amdgcn_rcpf(dn[0] + 1e-6f);
            rden[z] = rd;
            ov[z] = (u32x2){pk2(n0[0] * rd, n0[1] * rd), pk2(n0[2] * rd, n0[3] * rd)};
        }
#pragma unroll
        for (int z = 0; z < 4; z += 2) {
            auto rx = __builtin_amdgcn_permlane16_swap(ov[z].x, ov[z + 1].x, false, false);
            auto ry = __builtin_amdgcn_permlane16_swap(ov[z].y, ov[z + 1].y, false, false);
            st128c(rw, offA + (sb0 + 4 * i4 + z) * 20480, (u32x4){rx[0], ry[0], rx[1], ry[1]});
        }
        const float rd = g == 0 ? rden[0] : g == 1 ? rden[1] : g == 2 ? rden[2] : rden[3];
        st64c(rw, offX + (sb0 + 4 * i4 + g) * 20480, (u32x2){pk2(n1[0] * rd, n1[1] * rd), pk2(n1[2] * rd, n1[3] * rd)});
    }
}

DI void gate_block(int wave, int lane, int m, const u16* x, const u16* gw, rsrc_t rw) {
    const int sub = lane & 15, r4 = lane >> 4;
    u32x4 gq[5], xr[4][5];
#pragma unroll
    for (int jj = 0; jj < 5; jj++) gq[jj] = *(const GAS u32x4*)((const GAS char*)gw + (sub + 16 * jj) * 16);
#pragma unroll
    for (int it = 0; it < 4; it++)
#pragma unroll
        for (int jj = 0; jj < 5; jj++) xr[it][jj] = *(const GAS u32x4*)((const GAS char*)x + (size_t)(128 * m + 16 * wave + 4 * it + r4) * 1280 + (sub + 16 * jj) * 16);
    MEMF(); SB();
#pragma unroll
    for (int it = 0; it < 4; it++) {
        float a = 0.f;
#pragma unroll
        for (int jj = 0; jj < 5; jj++)
#pragma unroll
            for (int e = 0; e < 4; e++) { a = fmaf(blo(xr[it][jj][e]), blo(gq[jj][e]), a); a = fmaf(bhi(xr[it][jj][e]), bhi(gq[jj][e]), a); }
        a += __shfl_xor(a, 1); a += __shfl_xor(a, 2); a += __shfl_xor(a, 4); a += __shfl_xor(a, 8);
        a = rbf(a);
        const float g = rbf(__builtin_amdgcn_rcpf(1.f + __builtin_amdgcn_exp2f(-1.4426950408889634f * a)));
        if (sub == 0) st32c(rw, WS_G + (128 * m + 16 * wave + 4 * it + r4) * 4, __float_as_uint(g));
    }
}

DI void final_block(LAS char* sm, int wave, int lane, int tid, int st, int ct, const u16* x, u16* out, rsrc_t rw, u32 tag, char* ws) {
    const int fr = lane & 15, g = lane >> 4;
    u32x4 xv[2];
#pragma unroll
    for (int r = 0; r < 2; r++) {
        int it = tid + 512 * r; it = it < 640 ? it : 639;
        const int s = it / 10, u = it - 10 * s;
        xv[r] = *(const GAS u32x4*)((const GAS char*)x + (size_t)(st * 64 + s) * 1280 + ct * 160 + u * 16);
    }
    VMCNT(0); BAR();
    if (wave < 4) {
        const int kh = wave & 1, sh = wave >> 1;
        wait_flags(rw, lane < 16 ? (int)F_O + (st >> 4) * 32 + 16 * kh + lane : -1, -1, tag);
        TS(0, 17);
        const u32 ob = WS_O + ((st * 4 + 2 * sh) * 20 + 8 * kh) * 1024 + lane * 16;
        const u32 os = WS_O + ((st * 4 + 2 * sh) * 20 + 16 + 2 * kh) * 1024 + g * 256 + fr * 8;
        u32x4 B[2][10];
#pragma unroll
        for (int kl = 0; kl < 8; kl++) { B[0][kl] = ld128c(rw, ob + kl * 1024); B[1][kl] = ld128c(rw, ob + 20480 + kl * 1024); }
#pragma unroll
        for (int kl = 0; kl < 2; kl++)
#pragma unroll
            for (int z = 0; z < 2; z++) {
                const u32x2 h0 = ld64c(rw, os + z * 20480 + kl * 1024), h1 = ld64c(rw, os + z * 20480 + kl * 1024 + 128);
                B[z][8 + kl] = (u32x4){h0.x, h0.y, h1.x, h1.y};
            }
        MEMF(); SB();
        f32x4 acc[5][2];
#pragma unroll
        for (int cb = 0; cb < 5; cb++) { acc[cb][0] = (f32x4){0, 0, 0, 0}; acc[cb][1] = (f32x4){0, 0, 0, 0}; }
        const u32 aoff = (16 * (g >> 1) + 4 * (g & 1) + (fr >> 2)) * 160 + (lane & 3) * 8;
        u32x2 al[2][5], ah[2][5];
#define F_KAP(kl) ((kl) < 8 ? (kl) : 8 + (kl))
#define F_READ(buf, kl) do { const u32 b0_ = kh ? wo_off(F_KAP(kl) + ((kl) < 8 ? 8 : 2)) : wo_off(F_KAP(kl)); \
            _Pragma("unroll") for (int cb = 0; cb < 5; cb++) { al[buf][cb] = ldstr(sm, b0_ + aoff + cb * 32); ah[buf][cb] = ldstr(sm, b0_ + aoff + cb * 32 + 1280); } } while (0)
        F_READ(0, 0);
#pragma unroll
        for (int kl = 0; kl < 10; kl++) {
            if (kl < 9) F_READ((kl + 1) & 1, kl + 1);
            SB();
#pragma unroll
            for (int cb = 0; cb < 5; cb++) {
                const u32x4 a = {al[kl & 1][cb].x, al[kl & 1][cb].y, ah[kl & 1][cb].x, ah[kl & 1][cb].y};
                acc[cb][0] = mfma16(a, B[0][kl], acc[cb][0]); acc[cb][1] = mfma16(a, B[1][kl], acc[cb][1]);
            }
            SB();
        }
        TS(0, 18);
        lds_arrive(sm, C_FR, lane); lds_wait(sm, C_FR, 4);
#pragma unroll
        for (int cb = 0; cb < 5; cb++)
#pragma unroll
            for (int z = 0; z < 2; z++)
                *(LAS f32x4*)(sm + kh * (64 * RED_PITCH) + (16 * (2 * sh + z) + fr) * RED_PITCH + (16 * cb + 4 * g) * 4) = acc[cb][z];
    } else {
        if (wave == 4) { wait_flags(rw, lane < 20 ? (int)F_Y + ct * 32 + lane : lane == 32 ? (int)F_GATE + (st >> 1) : -1, -1, tag); lds_arrive(sm, C_FY, lane); }
        else lds_wait(sm, C_FY, 1);
        const int t = tid - 256;
        u32x4 yv[3];
#pragma unroll
        for (int r = 0; r < 3; r++) { int id = t + 256 * r; id = id < 640 ? id : 639; yv[r] = ld128c(rw, WS_YT + (80 * ct + (id >> 3)) * 4096 + st * 128 + (id & 7) * 16); }
#pragma unroll
        for (int r = 0; r < 3; r++) { const int id = t + 256 * r; if (id < 640) *(LAS u32x4*)(sm + F_YY + id * 16) = yv[r]; }
        if (wave == 4) *(LAS u32*)(sm + F_GG + lane * 4) = ld32c(rw, WS_G + (st * 64 + lane) * 4);
        lds_arrive(sm, C_FY2, lane); lds_wait(sm, C_FY2, 4);
        const int sb = wave - 4, q = fr >> 2, p = lane & 3;
        u32x2 tt[5];
#pragma unroll
        for (int cb = 0; cb < 5; cb++) tt[cb] = ldstr(sm, F_YY + (16 * cb + 4 * g + q) * 128 + (16 * sb + 4 * p) * 2);
#pragma unroll
        for (int cb = 0; cb < 5; cb++) *(LAS u32x2*)(sm + F_Y2 + (16 * sb + fr) * 176 + (16 * cb + 4 * g) * 2) = tt[cb];
    }
    LGKM0(); BAR();
#pragma unroll
    for (int r = 0; r < 2; r++) {
        const int it = tid + 512 * r;
        if (it < 640) {
            const int s = it / 10, u = it - 10 * s;
            const u32 ra = s * RED_PITCH + u * 32;
            const f32x4 p0 = *(LAS f32x4*)(sm + ra), p1 = *(LAS f32x4*)(sm + ra + 16), q0 = *(LAS f32x4*)(sm + 64 * RED_PITCH + ra), q1 = *(LAS f32x4*)(sm + 64 * RED_PITCH + ra + 16);
            const u32x4 y8 = lds128(sm, F_Y2 + s * 176 + u * 16);
            const float gg = *(LAS float*)(sm + F_GG + s * 4), omg = 1.f - gg;
            float o[8];
#pragma unroll
            for (int e = 0; e < 4; e++) {
                const float a0 = (e < 2 ? p0[2 * e] + q0[2 * e] : p1[2 * e - 4] + q1[2 * e - 4]), a1 = (e < 2 ? p0[2 * e + 1] + q0[2 * e + 1] : p1[2 * e - 3] + q1[2 * e - 3]);
                const float b0 = omg * blo(xv[r][e]), b1 = omg * bhi(xv[r][e]);
                o[2 * e] = fmaf(b0, blo(y8[e]), gg * a0); o[2 * e + 1] = fmaf(b1, bhi(y8[e]), gg * a1);
            }
            *(GAS u32x4*)((GAS char*)out + (size_t)(st * 64 + s) * 1280 + ct * 160 + u * 16) = (u32x4){pk2(o[0], o[1]), pk2(o[2], o[3]), pk2(o[4], o[5]), pk2(o[6], o[7])};
        }
    }
}

__global__ __launch_bounds__(512, 2) void fused_kernel(const u16* __restrict__ x, const u16* __restrict__ w, const u16* __restrict__ wo, const u16* __restrict__ kin,
                                                        const u16* __restrict__ vin, const u16* __restrict__ shk, const u16* __restrict__ hk, const u16* __restrict__ sD,
                                                        const u16* __restrict__ gw, u16* __restrict__ out, char* ws) {
    extern __shared__ __attribute__((aligned(16))) char smem[];
    LAS char* sm = (LAS char*)smem;
    const int tid = threadIdx.x, lane = tid & 63, wave = __builtin_amdgcn_readfirstlane(tid >> 6);
    const int b = blockIdx.x, xcd = b & 7, j = b >> 3;
    const rsrc_t rw = __builtin_amdgcn_make_buffer_rsrc(ws, 0, WS_BYTES, 0x00020000);
    asm volatile("" :: "s"(x), "s"(w), "s"(wo), "s"(kin), "s"(vin), "s"(shk), "s"(hk), "s"(sD), "s"(gw), "s"(out));
    if (PREF1 && wave < 3 && j < 30) {
        if (wave == 0) touch(x, (u32)((xcd * 2560 + j * 80 + lane) * 128));
        else if (wave == 1) { if (lane < 16) touch(x, (u32)((xcd * 2560 + j * 80 + 64 + lane) * 128)); else touch(w, (u32)(((xcd * 30 + j) * 80 + lane - 16) * 128)); }
        else if (lane < 32) touch(w, (u32)(((xcd * 30 + j) * 80 + 48 + lane) * 128));
    }
    if (PREFKV && wave < 2 && j < 30) {
        const int tile_ = xcd * 30 + j;
        touch(wave == 0 ? kin : vin, (u32)(((tile_ / 10) * 64 + lane) * 1280 + (tile_ % 10) * 128));
    }
    if (PREF3 && wave < 2 && j < 30) {
        const int n240 = xcd * 30 + j;
        if (wave == 0) { const u32 l_ = n240 * 86 + lane; touch(hk, (l_ < 20480 ? l_ : 20479) * 128); }
        else { const u32 l_ = n240 * 86 + 64 + lane, m_ = n240 * 27 + lane - 22;
               if (lane < 22) touch(hk, (l_ < 20480 ? l_ : 20479) * 128); else if (lane < 49) touch(wo, (m_ < 6400 ? m_ : 6399) * 128); }
    }
    if (PREFC && wave == 3) {
        unsigned long long pc;
        asm volatile("s_getpc_b64 %0" : "=s"(pc));
        pc &= ~127ull;
        const int ln = PREFC == 1 ? j * 10 + lane : lane;
        if (PREFC == 1) { if (lane < 10 && ln < CODE_LINES) touch((const void*)pc, (u32)(ln * 128)); }
        else {
#pragma unroll
            for (int z = 0; z < 5; z++) if (z * 64 + lane < CODE_LINES) touch((const void*)pc, (u32)((z * 64 + lane) * 128));
        }
    }
    if (PREFT && wave == 2) {
        if (PREFT == 1) { touch(ws, (u32)(lane * 262144)); if (lane < 40) touch(out, (u32)(lane * 65536)); }
        else { touch(ws, (u32)((j * 8 + (lane & 7)) * 65536 + (lane >> 3) * 128) % (WS_STAMP - 1024)); if (lane < 40) touch(out, (u32)(lane * 65536)); }
    }
    u32 tag;
    asm volatile("s_load_dword %0, %1, 0x0\n\ts_waitcnt lgkmcnt(0)" : "=s"(tag) : "s"(ws + WS_FLAG + F_EPOCH * FS) : "memory");
    tag += 1u;
    if (tid < 16) *(LAS u32*)(sm + L_CNT + tid * 4) = 0u;
    TS(0, 0);
    const int st = 4 * xcd + (j >> 3), ct = j & 7;
    int ko = -1;
    bool is_conv = false;

    if (j >= 30) {
        const int m = xcd * 2 + (j - 30);
        ko = -100;
        {
            const int grp = wave >> 2, w4i = wave & 3;
            u32x2 w4[3];
#pragma unroll
            for (int t = 0; t < 3; t++) { const int tt = 2 * t + grp < 5 ? 2 * t + grp : 4; p0_weights(w4[t], shk, 240 + 5 * m + tt, w4i, lane); }
#pragma unroll
            for (int t = 0; t < 3; t++) if (2 * t + grp < 5) p0_issue(kin, vin, 240 + 5 * m + 2 * t + grp, w4i, lane, (2 * t + grp) * 17408);
            VMCNT(0);
#pragma unroll
            for (int t = 0; t < 3; t++) PIN(w4[t]);
            BAR();
#pragma unroll
            for (int t = 0; t < 3; t++)
                if (2 * t + grp < 5) p0_compute(sm, w4[t], 240 + 5 * m + 2 * t + grp, w4i, lane, (2 * t + grp) * 17408, 90112 + (2 * t + grp) * 8192);
            LGKM0(); BAR();
            {
                u32x4 t4[3][2];
#pragma unroll
                for (int t = 0; t < 3; t++)
#pragma unroll
                    for (int z = 0; z < 2; z++) { const int ch2 = ((tid & 255) >> 3) + 32 * z, o2 = tid & 7, tt = 2 * t + grp < 5 ? 2 * t + grp : 4; t4[t][z] = lds128(sm, 90112 + tt * 8192 + ch2 * 128 + ((o2 ^ (ch2 & 7)) * 16)); }
#pragma unroll
                for (int t = 0; t < 3; t++)
                    if (2 * t + grp < 5) {
                        const int tile = 240 + 5 * m + 2 * t + grp, cg = tile % 10, s0 = (tile / 10) * 64;
#pragma unroll
                        for (int z = 0; z < 2; z++) { const int ch2 = ((tid & 255) >> 3) + 32 * z, o2 = tid & 7; st128c(rw, WS_UT + (cg * 64 + ch2) * 4096 + (s0 + 8 * o2) * 2, t4[t][z]); }
                    }
            }
        }
        VMCNT(0); BAR();
        if (tid < 5) set_flag(rw, p0_flag(240 + 5 * m + tid), tag);
        TS(0, 1);
        gate_block(wave, lane, m, x, gw, rw);
        VMCNT(0); LGKM0(); BAR();
        if (tid == 0) set_flag(rw, F_GATE + m, tag);
        TS(0, 3);
    } else {
        const int rt = 2 * xcd + j / 15, cbk = j % 15, tile = xcd * 30 + j;
        is_conv = cbk < 5 || cbk >= 10;
        if (!is_conv) { const int ki = rt * 5 + (cbk - 5); ko = ki < 64 ? ki : -2 - (ki - 64); }
        if (wave >= 4) {
            const int lw = wave - 4;
            u32 vx[4], vw[4];
#pragma unroll
            for (int n2 = 0; n2 < 4; n2++) {
                const int n = 4 * lw + n2;
                const int row = 8 * n + (lane >> 3), ch = (lane & 7) ^ ((row >> 1) & 7);
                vx[n2] = (u32)((rt * 128 + row) * 1280 + ch * 16);
                const int kr = 4 * n + (lane >> 4), f = ((kr & 3) << 2) | ((kr >> 2) & 3), cw = (lane & 15) ^ f;
                vw[n2] = (u32)(kr * 3840 + cbk * 256 + cw * 16);
            }
            const int rot = (rt + cbk) % 10;
#define K1_ISSUE(c) do { const u32 st_ = ((c) & 3) * K1_STAGE + 4 * lw * 1024; int kc_ = (c) + ROT_ON * rot; kc_ = kc_ >= 10 ? kc_ - 10 : kc_; \
            const char* xb_ = (const char*)x + kc_ * 128; const char* wp_ = (const char*)w + (size_t)kc_ * 64 * 3840; \
            _Pragma("unroll") for (int n2 = 0; n2 < 4; n2++) { if (NT_X) glds16nt(xb_, vx[n2], st_ + 16384 + n2 * 1024); else glds16(xb_, vx[n2], st_ + 16384 + n2 * 1024); glds16(wp_, vw[n2], st_ + n2 * 1024); } } while (0)
            K1_ISSUE(0); K1_ISSUE(1);
            VMCNT(8); BAR();
            K1_ISSUE(2); K1_ISSUE(3);
            p0_issue(kin, vin, tile, lw, lane, P0_K);
#pragma unroll 1
            for (int c = 0; c < 6; c++) {
                if (c < 3) VMCNT(21); else VMCNT(16);
                BAR();
                K1_ISSUE(c + 4);
            }
            const int cv = rt * 10 + (cbk < 5 ? cbk : cbk - 5), c = 4 * cv + lw;
            if (is_conv) {
                CvIn in; u32 fl; u32x4 u[4];
                const int fidx = (int)F_P0 + (cv >> 4) * 32 + (lane & 31);
                conv_fetch_h(in, hk, c, lane);
                VMCNT(24); BAR();
                VMCNT(16); BAR();
                gld32c(fl, ws, WS_FLAG + fidx * FS);
                VMCNT(9); BAR();
                TS(4, 4);
                VMCNT(0); PIN(fl);
                const bool ready = __all(fl == tag);
                if (ready) {
#pragma unroll
                    for (int z = 0; z < 4; z++) gld128c(u[z], ws, WS_UT + c * 4096 + z * 1024 + lane * 16);
                }
                TS(4, 5);
                conv_build_h(sm, in, lw, lane);
                TS(4, 6);
                if (!ready) {
                    wait_flags(rw, fidx, -1, tag);
#pragma unroll
                    for (int z = 0; z < 4; z++) gld128c(u[z], ws, WS_UT + c * 4096 + z * 1024 + lane * 16);
                }
                TS(4, 7);
                VMCNT(0);
#pragma unroll
                for (int z = 0; z < 4; z++) { PIN(u[z]); *(LAS u32x4*)(sm + CV_BASE + lw * CV_WAVE + CV_U + 512 + z * 1024 + lane * 16) = u[z]; }
                const float Dc = blo((u32)sD[c]);
                TS(4, 8);
                conv_run(sm, Dc, lw, lane, c, rw, ws);
                publish_last(rw, sm, C_Y, 4, F_Y + (cv / 20) * 32 + cv % 20, tag, lane);
                TS(4, 11);
            } else {
                VMCNT(16); BAR();
                VMCNT(8); BAR();
                VMCNT(0); BAR();
                const int r32 = lane & 31, hh = lane >> 5;
                u32 dst, istep, zstep; kv_addr(true, cbk - 5, rt, lw >> 1, lw & 1, 1, r32, hh, dst, istep, zstep);
                lds_wait(sm, C_XF + lw * 4, 1);
                f32x4 a[2][4];
#pragma unroll
                for (int i = 0; i < 2; i++)
#pragma unroll
                    for (int q4 = 0; q4 < 4; q4++) a[i][q4] = *(LAS f32x4*)(sm + K_XFER + lw * 8192 + ((i * 4 + q4) * 64 + lane) * 16);
#pragma unroll
                for (int i = 0; i < 2; i++)
#pragma unroll
                    for (int z = 0; z < 2; z++) {
                        const f32x4 a0 = a[i][2 * z], a1 = a[i][2 * z + 1];
                        st128c(rw, dst + i * istep + z * zstep, (u32x4){phi2(a0[0], a0[1]), phi2(a0[2], a0[3]), phi2(a1[0], a1[1]), phi2(a1[2], a1[3])});
                    }
                publish_last(rw, sm, C_G1, 8, F_G1 + cbk * 16 + rt, tag, lane);
            }
        } else {
            u32x2 w4; p0_weights(w4, shk, tile, wave, lane);
            const int wm = wave >> 1, wn = wave & 1, r32 = lane & 31, hh = lane >> 5;
            K1Addr a0;
#pragma unroll
            for (int ks = 0; ks < 2; ks++) {
                a0.x[0][ks] = (64 * wm + r32) * 128 + (((2 * ks + hh) ^ ((r32 >> 1) & 7)) * 16);
                a0.x[1][ks] = a0.x[0][ks] ^ 64;
            }
#pragma unroll
            for (int jj = 0; jj < 2; jj++)
#pragma unroll
                for (int t = 0; t < 2; t++) {
                    const int blk = (lane >> 4) & 1, q = (lane & 15) >> 2, p = lane & 3, row = 8 * hh + 4 * t + q, ch = 8 * wn + 4 * jj + 2 * blk + (p >> 1);
                    const int f = ((row & 3) << 2) | ((row >> 2) & 3);
                    a0.w[jj][t] = row * 256 + ((ch ^ f) * 16) + 8 * (p & 1);
                }
            f32x16 acc[2][2];
#pragma unroll
            for (int i = 0; i < 2; i++)
#pragma unroll
                for (int jj = 0; jj < 2; jj++)
#pragma unroll
                    for (int e = 0; e < 16; e++) acc[i][jj][e] = 0.f;
            BAR();
            TS(0, 1);
            if (cbk < 5) {
                k1_loop<true>(sm, a0, acc, rw, p0_flag(tile), tag, wave, lane, hk, wo, tile, w4);
                __builtin_amdgcn_s_setprio(2);
                TS(0, 2);
                u32x4 qv[2][2][2];
#pragma unroll
                for (int jj = 0; jj < 2; jj++)
#pragma unroll
                    for (int ap = 0; ap < 2; ap++)
#pragma unroll
                        for (int i = 0; i < 2; i++) {
                            const f32x16& a = acc[i][jj];
                            const u32 x0 = phi2(a[8 * ap], a[8 * ap + 1]), y0 = phi2(a[8 * ap + 2], a[8 * ap + 3]);
                            const u32 x1 = phi2(a[8 * ap + 4], a[8 * ap + 5]), y1 = phi2(a[8 * ap + 6], a[8 * ap + 7]);
                            auto rx = __builtin_amdgcn_permlane32_swap(x0, x1, false, false);
                            auto ry = __builtin_amdgcn_permlane32_swap(y0, y1, false, false);
                            qv[jj][ap][i] = (u32x4){rx[0], ry[0], rx[1], ry[1]};
                        }
                if (QDELAY) { __builtin_amdgcn_s_setprio(0); for (int z = 0; z < QDELAY; z++) __builtin_amdgcn_s_sleep(8); }
#pragma unroll
                for (int jj = 0; jj < 2; jj++)
#pragma unroll
                    for (int ap = 0; ap < 2; ap++) {
                        const int U = 16 * cbk + 8 * wn + 4 * jj + 2 * ap + hh, pr = U / 5, u5 = U - 5 * pr;
                        const u32 dst = WS_P2 + (((pr * 128 + rt * 8 + 4 * wm + (r32 >> 4)) * 5 + u5) * 128 + (r32 & 15) * 8) * 2;
#pragma unroll
                        for (int i = 0; i < 2; i++) st128c(rw, dst + i * 2560, qv[jj][ap][i]);
                    }
            } else {
                k1_loop<false>(sm, a0, acc, rw, p0_flag(tile), tag, wave, lane, hk, wo, tile, w4);
                __builtin_amdgcn_s_setprio(2);
                TS(0, 2);
                const bool isk = cbk < 10;
                const int j5 = isk ? cbk - 5 : cbk - 10;
                if (isk) {
#pragma unroll
                    for (int i = 0; i < 2; i++)
#pragma unroll
                        for (int q4 = 0; q4 < 4; q4++)
                            *(LAS f32x4*)(sm + K_XFER + wave * 8192 + ((i * 4 + q4) * 64 + lane) * 16) = (f32x4){acc[i][1][4 * q4], acc[i][1][4 * q4 + 1], acc[i][1][4 * q4 + 2], acc[i][1][4 * q4 + 3]};
                    lds_arrive(sm, C_XF + wave * 4, lane);
                    u32 dst, istep, zstep; kv_addr(true, j5, rt, wm, wn, 0, r32, hh, dst, istep, zstep);
#pragma unroll
                    for (int i = 0; i < 2; i++)
#pragma unroll
                        for (int z = 0; z < 2; z++) {
                            const f32x16& a = acc[i][0];
                            st128c(rw, dst + i * istep + z * zstep,
                                   (u32x4){phi2(a[8 * z], a[8 * z + 1]), phi2(a[8 * z + 2], a[8 * z + 3]), phi2(a[8 * z + 4], a[8 * z + 5]), phi2(a[8 * z + 6], a[8 * z + 7])});
                        }
                } else {
#pragma unroll
                    for (int jj = 0; jj < 2; jj++) {
                        u32 dst, istep, zstep; kv_addr(false, j5, rt, wm, wn, jj, r32, hh, dst, istep, zstep);
#pragma unroll
                        for (int i = 0; i < 2; i++)
#pragma unroll
                            for (int z = 0; z < 2; z++) {
                                const f32x16& a = acc[i][jj];
                                st128c(rw, dst + i * istep + z * zstep,
                                       (u32x4){pk2(a[8 * z], a[8 * z + 1]), pk2(a[8 * z + 2], a[8 * z + 3]), pk2(a[8 * z + 4], a[8 * z + 5]), pk2(a[8 * z + 6], a[8 * z + 7])});
                            }
                    }
                }
            }
            LGKM0();
            TS(0, 25);
            publish_last(rw, sm, C_G1, is_conv ? 4 : 8, F_G1 + cbk * 16 + rt, tag, lane);
            __builtin_amdgcn_s_setprio(0);
            TS(0, 3);
            if (is_conv) {
                lds_wait(sm, C_G1, 4);
                wo_issue(wo, ct, 0, 12, wave, 4, lane);
            }
        }
    }
    if (!is_conv) {
        LGKM0(); BAR();
        if (ko >= 0) {
            const int h = ko >> 1, half = ko & 1;
            wo_issue(wo, ct, 0, 12, wave, 8, lane);
            kvo_block(sm, wave, lane, h, half, rw, tag, ws);
            TS(0, 26);
            VMCNT(0); BAR();
            if (tid == 0) set_flag(rw, F_O + half * 32 + h, tag);
            TS(0, 11);
            wo_issue(wo, ct, 12, 20, wave, 8, lane);
        } else {
            wo_issue(wo, ct, 0, 20, wave, 8, lane);
            if (ko == -2 && wave == 0) {
                wait_flags(rw, (int)F_G1 + lane, (int)F_G1 + 64 + lane, tag);
                wait_flags(rw, (int)F_G1 + 128 + lane, (int)F_G1 + 192 + lane, tag);
                if (lane == 0) set_flag(rw, F_EPOCH, tag);
            }
        }
    } else {
        VMCNT(0); BAR();
        if (wave >= 4) wo_issue(wo, ct, 12, 20, wave - 4, 4, lane);
    }
    TS(0, 16);
    final_block(sm, wave, lane, tid, st, ct, x, out, rw, tag, ws);
    TS(0, 19);
}

extern "C" void kernel_launch(void* const* d_in, const int* in_sizes, int n_in, void* d_out, int out_size, void* d_ws, size_t ws_size, hipStream_t stream) {
    const u16* x = (const u16*)d_in[0]; const u16* wqkv = (const u16*)d_in[1]; const u16* wo = (const u16*)d_in[2];
    const u16* kin = (const u16*)d_in[4]; const u16* vin = (const u16*)d_in[5]; const u16* shk = (const u16*)d_in[6];
    const u16* hk = (const u16*)d_in[7]; const u16* sD = (const u16*)d_in[8]; const u16* gw = (const u16*)d_in[9];
    (void)hipFuncSetAttribute((const void*)fused_kernel, hipFuncAttributeMaxDynamicSharedMemorySize, LDS_BYTES);
    fused_kernel<<<256, 512, LDS_BYTES, stream>>>(x, wqkv, wo, kin, vin, shk, hk, sD, gw, (u16*)d_out, (char*)d_ws);
}
```

```cpp
#define GPROBE 0
#include <hip/hip_runtime.h>
#include <stdint.h>

typedef unsigned short u16;
typedef unsigned int u32;
typedef __bf16 bf16x8 __attribute__((ext_vector_type(8)));
typedef __bf16 bf16x2 __attribute__((ext_vector_type(2)));
typedef float f32x4 __attribute__((ext_vector_type(4)));
typedef float f32x2 __attribute__((ext_vector_type(2)));
typedef float f32x16 __attribute__((ext_vector_type(16)));
typedef u32 u32x4 __attribute__((ext_vector_type(4)));
typedef u32 u32x2 __attribute__((ext_vector_type(2)));
typedef short s16x4 __attribute__((ext_vector_type(4)));
#define LAS __attribute__((address_space(3)))
#define GAS __attribute__((address_space(1)))
#define DI __device__ __forceinline__
typedef __amdgpu_buffer_rsrc_t rsrc_t;

constexpr int S = 2048, D = 640;
#define FS 256
#define POLL_SLEEP 2
#define ROT_ON 1

constexpr u32 WS_UT = 0;
constexpr u32 WS_YT = WS_UT + 2621440;
constexpr u32 WS_PK0 = WS_YT + 2621440;
constexpr u32 WS_V0 = WS_PK0 + 2097152;
constexpr u32 WS_XK = WS_V0 + 2097152;
constexpr u32 WS_XV = WS_XK + 524288;
constexpr u32 WS_P2 = WS_XV + 524288;
constexpr u32 WS_O = WS_P2 + 2621440;
constexpr u32 WS_G = WS_O + 2621440;
constexpr u32 WS_FLAG = WS_G + 8192;
constexpr u32 F_P0 = 0;
constexpr u32 F_G1 = 320;
constexpr u32 F_GATE = 560;
constexpr u32 F_O = 576;
constexpr u32 F_Y = 640;
constexpr u32 F_EPOCH = 896, F_TMO = 928;
constexpr u32 WS_STAMP = WS_FLAG + 1024 * FS;
constexpr u32 WS_BYTES = WS_STAMP + 256 * 32 * 16;

extern __shared__ __attribute__((aligned(16))) char smem_gp[];
#define TS(w, id) do { if ((id) == GPROBE && threadIdx.x == 64 * (w)) { *(volatile LAS unsigned long long*)((LAS char*)smem_gp + 156672 + 64) = __builtin_amdgcn_s_memrealtime(); } } while (0)

DI u32 pk2(float a, float b) { f32x2 t = {a, b}; return __builtin_bit_cast(u32, __builtin_convertvector(t, bf16x2)); }
DI float blo(u32 u) { return __uint_as_float(u << 16); }
DI float bhi(u32 u) { return __uint_as_float(u & 0xffff0000u); }
DI float rbf(float f) { return blo(pk2(f, f)); }
DI bf16x8 asbf(u32x4 v) { return __builtin_bit_cast(bf16x8, v); }
DI f32x4 mfma16(u32x4 a, u32x4 b, f32x4 c) { return __builtin_amdgcn_mfma_f32_16x16x32_bf16(asbf(a), asbf(b), c, 0, 0, 0); }
DI f32x16 mfma32(u32x4 a, u32x4 b, f32x16 c) { return __builtin_amdgcn_mfma_f32_32x32x16_bf16(asbf(a), asbf(b), c, 0, 0, 0); }

DI void glds16(const void* sbase, u32 voff, u32 lds) {
    asm volatile("s_mov_b32 m0, %2\n\ts_nop 0\n\tglobal_load_lds_dwordx4 %0, %1" :: "v"(voff), "s"(sbase), "s"(lds) : "memory");
}
DI void glds16nt(const void* sbase, u32 voff, u32 lds) {
    asm volatile("s_mov_b32 m0, %2\n\ts_nop 0\n\tglobal_load_lds_dwordx4 %0, %1 nt" :: "v"(voff), "s"(sbase), "s"(lds) : "memory");
}
DI void touch(const void* sbase, u32 voff) {
    asm volatile("s_mov_b32 m0, %2\n\ts_nop 0\n\tglobal_load_lds_dword %0, %1" :: "v"(voff), "s"(sbase), "s"(156928u) : "memory");
}
#define NT_KV 0
#define NT_X 0
#define NT_H 0
#define PREF1 1
#define PREF2 0
#define PREFC 1
#define CODE_LINES 345
#define PREFT 0
#define PREFKV 0
#define PREF3 1
#define QDELAY 0
DI void gld64(u32x2& d, const void* sbase, u32 voff) { asm volatile("global_load_dwordx2 %0, %1, %2" : "=v"(d) : "v"(voff), "s"(sbase) : "memory"); }
DI void gld128(u32x4& d, const void* sbase, u32 voff) { asm volatile("global_load_dwordx4 %0, %1, %2" : "=v"(d) : "v"(voff), "s"(sbase) : "memory"); }
DI void gld128c(u32x4& d, const void* sbase, u32 voff) { asm volatile("global_load_dwordx4 %0, %1, %2 sc1" : "=v"(d) : "v"(voff), "s"(sbase) : "memory"); }
DI void gld32c(u32& d, const void* sbase, u32 voff) { asm volatile("global_load_dword %0, %1, %2 sc1" : "=v"(d) : "v"(voff), "s"(sbase) : "memory"); }
#define PIN(x) asm volatile("" : "+v"(x))
#define SB() __builtin_amdgcn_sched_barrier(0)
DI u32x4 ld128c(rsrc_t r, u32 off) { return __builtin_amdgcn_raw_buffer_load_b128(r, off, 0, 16); }
DI u32x2 ld64c(rsrc_t r, u32 off) { return __builtin_amdgcn_raw_buffer_load_b64(r, off, 0, 16); }
DI u32 ld32c(rsrc_t r, u32 off) { return __builtin_amdgcn_raw_buffer_load_b32(r, off, 0, 16); }
DI void st128c(rsrc_t r, u32 off, u32x4 v) { __builtin_amdgcn_raw_buffer_store_b128(v, r, off, 0, 16); }
DI void st64c(rsrc_t r, u32 off, u32x2 v) { __builtin_amdgcn_raw_buffer_store_b64(v, r, off, 0, 16); }
DI void st32c(rsrc_t r, u32 off, u32 v) { __builtin_amdgcn_raw_buffer_store_b32(v, r, off, 0, 16); }
DI u32x4 lds128(LAS char* sm, u32 a) { return *(LAS u32x4*)(sm + a); }
DI u32x2 lds64(LAS char* sm, u32 a) { return *(LAS u32x2*)(sm + a); }
DI u32x2 ldstr(LAS char* sm, u32 a) { return __builtin_bit_cast(u32x2, __builtin_amdgcn_ds_read_tr16_b64_v4i16((LAS s16x4*)(sm + a))); }
#define VMCNT(n) asm volatile("s_waitcnt vmcnt(" #n ")" ::: "memory")
#define LGKM0() asm volatile("s_waitcnt lgkmcnt(0)" ::: "memory")
#define BAR() do { asm volatile("" ::: "memory"); __builtin_amdgcn_s_barrier(); asm volatile("" ::: "memory"); } while (0)
#define MEMF() asm volatile("" ::: "memory")

DI void set_flag(rsrc_t r, u32 idx, u32 tag) { st32c(r, WS_FLAG + idx * FS, tag); }
DI void wait_flags(rsrc_t r, int idx0, int idx1, u32 tag) {
    unsigned long long t0 = 0;
    for (u32 it = 0;; it++) {
        u32 a = tag, b = tag;
        if (idx0 >= 0) a = ld32c(r, WS_FLAG + idx0 * FS);
        if (idx1 >= 0) b = ld32c(r, WS_FLAG + idx1 * FS);
        if (__all(a == tag && b == tag)) break;
        if (POLL_SLEEP) __builtin_amdgcn_s_sleep(POLL_SLEEP);
        if ((it & 63u) == 0u) {
            const unsigned long long t = __builtin_amdgcn_s_memrealtime();
            if (it == 0) t0 = t;
            else if (t - t0 > 2000000ull) { set_flag(r, F_TMO, tag); break; }
        }
    }
    MEMF();
}
DI void lds_arrive(LAS char* sm, u32 cnt, int lane) { LGKM0(); if (lane == 0) __hip_atomic_fetch_add((LAS u32*)(sm + cnt), 1u, __ATOMIC_RELAXED, __HIP_MEMORY_SCOPE_WORKGROUP); MEMF(); }
DI void lds_wait(LAS char* sm, u32 cnt, u32 n) { MEMF(); while (*(volatile LAS u32*)(sm + cnt) < n) __builtin_amdgcn_s_sleep(0); MEMF(); }
DI void publish_last(rsrc_t r, LAS char* sm, u32 cnt, u32 nw, u32 flag, u32 tag, int lane) {
    VMCNT(0);
    if (lane == 0) {
        const u32 old = __hip_atomic_fetch_add((LAS u32*)(sm + cnt), 1u, __ATOMIC_RELAXED, __HIP_MEMORY_SCOPE_WORKGROUP);
        if (old == nw - 1) set_flag(r, flag, tag);
    }
    MEMF();
}

DI u32 phi2(float a, float b) {
    const f32x2 t = {a, b}, s = t * 1.4426950408889634f;
    const float ea = __builtin_amdgcn_fmed3f(__builtin_amdgcn_exp2f(s.x), 0.f, 1.f), eb = __builtin_amdgcn_fmed3f(__builtin_amdgcn_exp2f(s.y), 0.f, 1.f);
    const float ma = __int_as_float(max(__float_as_int(a), 0)), mb = __int_as_float(max(__float_as_int(b), 0));
    const f32x2 r = ((f32x2){ea, eb} + (f32x2){ma, mb}) + 1e-6f;
    return pk2(r.x, r.y);
}

constexpr int K1_STAGE = 32768;
constexpr int P0_K = 131072, P0_V = P0_K + 9216, P0_OUT = P0_V + 8192;
constexpr int L_CNT = 156672;
constexpr int C_G1 = L_CNT, C_Y = L_CNT + 4, C_P0A = L_CNT + 8, C_P0B = L_CNT + 12, C_P0C = L_CNT + 16, C_FY = L_CNT + 20, C_FY2 = L_CNT + 24, C_FO = L_CNT + 28, C_FR = L_CNT + 32, C_XF = L_CNT + 36;
constexpr int CV_PITCH = 4160;
constexpr int CV_U = 4 * CV_PITCH;
constexpr int CV_WAVE = CV_U + 4608;
constexpr int CV_BASE = 65536;
constexpr int WO_HI = 65536;
constexpr int F_YY = 110592;
constexpr int F_Y2 = F_YY + 10240;
constexpr int F_GG = F_Y2 + 11264;
constexpr int KO_RED = 110592;
constexpr int KO_KV = KO_RED + 32768;
constexpr int RED_PITCH = 336;
constexpr int LDS_BYTES = 160 * 1024;
DI u32 p0_flag(int tile) { return F_P0 + (tile % 10) * 32 + tile / 10; }
DI constexpr u32 wo_off(int kap) { return kap < 12 ? kap * 5120 : WO_HI + (kap - 12) * 5120; }

DI void p0_issue(const u16* kin, const u16* vin, int tile, int lw, int lane, u32 base) {
    const int cg = tile % 10, rg = tile / 10, s0 = rg * 64;
    const u32 col = cg * 128 + (lane & 7) * 16;
#pragma unroll
    for (int z = 0; z < 2; z++) {
        const int n = 2 * lw + z;
        int r = s0 - 4 + n * 8 + (lane >> 3); r = r < 0 ? 0 : r;
        (NT_KV ? glds16nt : glds16)(kin, (u32)(r * 1280) + col, base + n * 1024);
        (NT_KV ? glds16nt : glds16)(vin, (u32)((s0 + n * 8 + (lane >> 3)) * 1280) + col, base + 9216 + n * 1024);
    }
    {
        int r = s0 + 60 + (lane >> 3); r = r > S - 1 ? S - 1 : r;
        (NT_KV ? glds16nt : glds16)(kin, (u32)(r * 1280) + col, base + 8 * 1024);
    }
}
DI float dot2(u32 a, u32 b, float c) { return __builtin_amdgcn_fdot2_f32_bf16(__builtin_bit_cast(bf16x2, a), __builtin_bit_cast(bf16x2, b), c, false); }
DI void p0_compute(LAS char* sm, u32x2 w4, int tile, int wave, int lane, u32 base, u32 outb, char* ws = nullptr) {
    const int s0 = (tile / 10) * 64;
    const int G = wave * 4 + (lane >> 4), cb = G & 3, i = lane & 15, q = i >> 2, p = i & 3;
    const u32 wa = __builtin_amdgcn_alignbit(w4.y, w4.y, 16), wb = __builtin_amdgcn_alignbit(w4.x, w4.x, 16);
    const int ch = 16 * cb + i;
    u32x2 kq[2][3], vq[2][2];
#pragma unroll
    for (int z = 0; z < 2; z++) {
        const int o = (G >> 2) + 4 * z;
        const u32 ka = base + (8 * o + q) * 128 + 32 * cb + 8 * p, va = ka + 9216;
        kq[z][0] = ldstr(sm, ka); kq[z][1] = ldstr(sm, ka + 512); kq[z][2] = ldstr(sm, ka + 1024);
        vq[z][0] = ldstr(sm, va); vq[z][1] = ldstr(sm, va + 512);
    }
#pragma unroll
    for (int z = 0; z < 2; z++) {
        const int o = (G >> 2) + 4 * z;
        u32 K[6] = {kq[z][0].x, kq[z][0].y, kq[z][1].x, kq[z][1].y, kq[z][2].x, kq[z][2].y};
        if (s0 == 0 && o == 0) { K[0] = 0; K[1] = 0; }
        u32 Sh[5];
#pragma unroll
        for (int e = 0; e < 5; e++) Sh[e] = __builtin_amdgcn_alignbit(K[e + 1], K[e], 16);
        const u32 V[4] = {vq[z][0].x, vq[z][0].y, vq[z][1].x, vq[z][1].y};
        u32 r[4];
#pragma unroll
        for (int e = 0; e < 4; e++) {
            const float k0 = dot2(Sh[e], wa, dot2(Sh[e + 1], wb, 0.f)), k1 = dot2(K[e + 1], wa, dot2(K[e + 2], wb, 0.f));
            r[e] = pk2(k0 * blo(V[e]), k1 * bhi(V[e]));
        }
        *(LAS u32x4*)(sm + outb + ch * 128 + ((o ^ (ch & 7)) * 16)) = (u32x4){r[0], r[1], r[2], r[3]};
    }
}
DI void p0_store(LAS char* sm, rsrc_t rw, int tile, int t256, u32 outb) {
    const int cg = tile % 10, s0 = (tile / 10) * 64;
#pragma unroll
    for (int z = 0; z < 2; z++) {
        const int ch2 = (t256 >> 3) + 32 * z, o2 = t256 & 7;
        const u32x4 t4 = lds128(sm, outb + ch2 * 128 + ((o2 ^ (ch2 & 7)) * 16));
        st128c(rw, WS_UT + (cg * 64 + ch2) * 4096 + (s0 + 8 * o2) * 2, t4);
    }
}
DI void p0_weights(u32x2& w4, const u16* shk, int tile, int wave, int lane) {
    const int cg = tile % 10, cb = (wave * 4 + (lane >> 4)) & 3;
    gld64(w4, shk, (u32)((cg * 64 + 16 * cb + (lane & 15)) * 8));
}

struct K1Set { u32x4 xa[2][2]; u32x2 wl[2][2], wh[2][2]; };
struct K1Addr { u32 x[2][2]; u32 w[2][2]; };
template <bool QT, int WO, int XO>
DI void k1_phase(LAS char* sm, f32x16 (&acc)[2][2], const K1Set& cur, K1Set& nxt, const u32 (&xb)[2], const u32 (&wb)[2][2]) {
#pragma unroll
    for (int n = 0; n < 8; n++) {
        const int ks = n >> 2, i = (n >> 1) & 1, jj = n & 1;
        const u32x4 wf = {cur.wl[ks][jj].x, cur.wl[ks][jj].y, cur.wh[ks][jj].x, cur.wh[ks][jj].y};
        acc[i][jj] = QT ? mfma32(wf, cur.xa[ks][i], acc[i][jj]) : mfma32(cur.xa[ks][i], wf, acc[i][jj]);
        if (n == 0) { nxt.wl[0][0] = ldstr(sm, wb[0][0] + WO); nxt.wh[0][0] = ldstr(sm, wb[0][1] + WO); }
        if (n == 1) { nxt.xa[0][0] = lds128(sm, xb[0] + XO); nxt.wl[0][1] = ldstr(sm, wb[1][0] + WO); }
        if (n == 2) { nxt.wh[0][1] = ldstr(sm, wb[1][1] + WO); nxt.xa[0][1] = lds128(sm, xb[0] + XO + 4096); }
        if (n == 3) { nxt.wl[1][0] = ldstr(sm, wb[0][0] + WO + 4096); nxt.wh[1][0] = ldstr(sm, wb[0][1] + WO + 4096); }
        if (n == 4) { nxt.xa[1][0] = lds128(sm, xb[1] + XO); nxt.wl[1][1] = ldstr(sm, wb[1][0] + WO + 4096); }
        if (n == 5) { nxt.wh[1][1] = ldstr(sm, wb[1][1] + WO + 4096); nxt.xa[1][1] = lds128(sm, xb[1] + XO + 4096); }
        SB();
    }
}
template <bool QT, int ST>
DI void k1_chunk(LAS char* sm, f32x16 (&acc)[2][2], K1Set& s0, K1Set& s1, const K1Addr& a0, const K1Addr& a1, bool bar, bool drain = false) {
    constexpr int SN = (ST + 1) & 3;
    const K1Addr& ac = ST < 2 ? a0 : a1; const K1Addr& an = SN < 2 ? a0 : a1;
    k1_phase<QT, (ST & 1) * K1_STAGE + 8192, (ST & 1) * K1_STAGE + 16384>(sm, acc, s0, s1, ac.x[1], ac.w);
    if (drain) VMCNT(0);
    if (bar) { LGKM0(); BAR(); }
    k1_phase<QT, (SN & 1) * K1_STAGE, (SN & 1) * K1_STAGE + 16384>(sm, acc, s1, s0, an.x[0], an.w);
}
template <bool QT>
DI void k1_loop(LAS char* sm, const K1Addr& a0, f32x16 (&acc)[2][2], rsrc_t rw, u32 pflag, u32 tag, int wave, int lane, const u16* hk, const u16* wo, int n240, u32x2 w4) {
    K1Addr a1;
#pragma unroll
    for (int i = 0; i < 2; i++)
#pragma unroll
        for (int z = 0; z < 2; z++) { a1.x[i][z] = a0.x[i][z] + 65536; a1.w[i][z] = a0.w[i][z] + 65536; }
    K1Set s0, s1;
#pragma unroll
    for (int ks = 0; ks < 2; ks++)
#pragma unroll
        for (int jj = 0; jj < 2; jj++) {
            s0.wl[ks][jj] = ldstr(sm, a0.w[jj][0] + ks * 4096); s0.wh[ks][jj] = ldstr(sm, a0.w[jj][1] + ks * 4096);
            s0.xa[ks][jj] = lds128(sm, a0.x[0][ks] + 16384 + jj * 4096);
        }
    SB();
#pragma unroll 1
    for (int c = 0; c < 8; c += 4) {
        k1_chunk<QT, 0>(sm, acc, s0, s1, a0, a1, true);
        if (c == 4) p0_store(sm, rw, n240, wave * 64 + lane, P0_OUT);
        k1_chunk<QT, 1>(sm, acc, s0, s1, a0, a1, true);
        k1_chunk<QT, 2>(sm, acc, s0, s1, a0, a1, true);
        k1_chunk<QT, 3>(sm, acc, s0, s1, a0, a1, true, c == 4);
        if (c == 4 && wave == 0 && lane == 0) set_flag(rw, pflag, tag);
        if (c == 0) { VMCNT(0); PIN(w4); p0_compute(sm, w4, n240, wave, lane, P0_K, P0_OUT); }
        if (PREF2 && c == 0 && wave >= 2) {
            if (wave == 2) { const u32 l_ = n240 * 86 + lane; touch(hk, (l_ < 20480 ? l_ : 20479) * 128); }
            else { const u32 l_ = n240 * 86 + 64 + lane, m_ = n240 * 27 + lane - 22;
                   if (lane < 22) touch(hk, (l_ < 20480 ? l_ : 20479) * 128); else if (lane < 49) touch(wo, (m_ < 6400 ? m_ : 6399) * 128); }
        }
    }
    k1_chunk<QT, 0>(sm, acc, s0, s1, a0, a1, true);
    k1_chunk<QT, 1>(sm, acc, s0, s1, a0, a1, false);
}

DI void kv_addr(bool isk, int j5, int rt, int wm, int wn, int jj, int r32, int hh, u32& dst, u32& istep, u32& zstep) {
    const int cc = 128 * j5 + 64 * wn + 32 * jj + r32, h = cc / 20, d = cc - 20 * h;
    const int sb32 = rt * 4 + 2 * wm;
    const bool lo = d < 16;
    dst = lo ? (isk ? WS_PK0 : WS_V0) + (((h * 64 + sb32) * 4 + hh) * 128 + d * 8) * 2
             : (isk ? WS_XK : WS_XV) + (((h * 64 + sb32) * 4 + hh) * 32 + (d - 16) * 8) * 2;
    istep = lo ? 1024 : 256; zstep = lo ? 512 : 128;
}
constexpr int K_XFER = 65536;

DI void wo_issue(const u16* wo, int ct, int k0, int k1, int w, int nw, int lane) {
    u32 vo[5], vs[5];
#pragma unroll
    for (int i = 0; i < 5; i++) {
        const int n = 64 * i + lane, rho = n / 10, c = n - 10 * rho, g = 2 * (rho >> 4) + ((rho >> 2) & 1), r8 = 4 * ((rho >> 3) & 1) + (rho & 3);
        vo[i] = (u32)((8 * (g < 2 ? g : g + 1) + r8) * 1280 + ct * 160 + c * 16);
        vs[i] = (u32)((40 * g + 16 + r8) * 1280 + ct * 160 + c * 16);
    }
#pragma unroll 1
    for (int kap = k0 + w; kap < k1; kap += nw) {
        const u32 dst = kap < 12 ? kap * 5120 : WO_HI + (kap - 12) * 5120;
        if (kap < 16) {
            const char* src = (const char*)wo + (size_t)kap * 51200;
#pragma unroll
            for (int i = 0; i < 5; i++) glds16(src, vo[i], dst + i * 1024);
        } else {
            const char* src = (const char*)wo + (size_t)(kap - 16) * 204800;
#pragma unroll
            for (int i = 0; i < 5; i++) glds16(src, vs[i], dst + i * 1024);
        }
    }
}

struct CvSet { u32x2 fl[8], fh[8]; u32x4 g[8]; };
DI void cv_load(LAS char* sm, CvSet& s, u32 fa, u32 ga) {
#pragma unroll
    for (int k1 = 0; k1 < 8; k1++) { s.fl[k1] = ldstr(sm, fa + 512 * k1 + 8); s.fh[k1] = ldstr(sm, fa + 512 * k1); s.g[k1] = lds128(sm, ga + 512 * k1); }
}
DI void cv_phase(LAS char* sm, f32x4 (&Y)[8], const CvSet& cur, CvSet& nxt, u32 fa, u32 ga) {
    PIN(fa); PIN(ga);
    SB();
    int n = 0;
#pragma unroll
    for (int e = 0; e < 8; e++)
#pragma unroll
        for (int k1 = 0; k1 + e < 8; k1++) {
            const u32x4 F = {cur.fl[k1].x, cur.fl[k1].y, cur.fh[k1].x, cur.fh[k1].y};
            Y[k1 + e] = mfma16(F, cur.g[e], Y[k1 + e]);
            if (n == 0) nxt.g[0] = lds128(sm, ga);
            else if (n <= 16) { const int r = (n - 1) >> 1; if ((n - 1) & 1) nxt.fh[r] = ldstr(sm, fa + 512 * r); else nxt.fl[r] = ldstr(sm, fa + 512 * r + 8); }
            else if (n < 24) nxt.g[n - 16] = lds128(sm, ga + 512 * (n - 16));
            n++;
            SB();
        }
}
struct CvIn { u32x4 hd[4]; u32x2 hm[4]; };
DI void conv_fetch_h(CvIn& in, const u16* hk, int c, int lane) {
    const char* hb = (const char*)hk + (size_t)c * 4096;
#pragma unroll
    for (int z = 0; z < 4; z++) {
        const int i = lane + 64 * z;
        if (NT_H) asm volatile("global_load_dwordx4 %0, %1, %2 nt" : "=v"(in.hd[z]) : "v"((u32)(i * 16)), "s"(hb) : "memory"); else gld128(in.hd[z], hb, (u32)(i * 16));
        gld64(in.hm[z], hb, (u32)(i > 0 ? i * 16 - 8 : 0));
    }
}
DI void conv_build_h(LAS char* sm0, CvIn& in, int w, int lane) {
    LAS char* sm = sm0 + CV_BASE + w * CV_WAVE;
#pragma unroll
    for (int z = 0; z < 4; z++) { PIN(in.hd[z]); PIN(in.hm[z]); }
    if (lane < 32) *(LAS u32x4*)(sm + CV_U + lane * 16) = (u32x4){0, 0, 0, 0};
    if (lane < 8) *(LAS u32x4*)(sm + (lane >> 1) * CV_PITCH + (lane & 1) * 16) = (u32x4){0, 0, 0, 0};
#pragma unroll
    for (int z = 0; z < 4; z++) {
        const int i = lane + 64 * z;
        u32 m2 = in.hm[z].x, m1 = in.hm[z].y;
        if (i == 0) { m2 = 0; m1 = 0; }
        const u32 d0 = in.hd[z].x, d1 = in.hd[z].y, d2 = in.hd[z].z, d3 = in.hd[z].w;
        const u32 om2 = __builtin_amdgcn_alignbit(m1, m2, 16), om1 = __builtin_amdgcn_alignbit(d0, m1, 16), o0 = __builtin_amdgcn_alignbit(d1, d0, 16),
                  o1 = __builtin_amdgcn_alignbit(d2, d1, 16), o2 = __builtin_amdgcn_alignbit(d3, d2, 16);
        LAS char* p = sm + 32 + i * 16;
        *(LAS u32x4*)(p) = (u32x4){d0, d1, d2, d3};
        *(LAS u32x4*)(p + CV_PITCH) = (u32x4){om1, o0, o1, o2};
        *(LAS u32x4*)(p + 2 * CV_PITCH) = (u32x4){m1, d0, d1, d2};
        *(LAS u32x4*)(p + 3 * CV_PITCH) = (u32x4){om2, om1, o0, o1};
    }
}
DI void conv_run(LAS char* sm0, float Dc, int w, int lane, int c, rsrc_t rw, char* ws) {
    LAS char* sm = sm0;
    LGKM0();
    TS(4, 9);
    const int fr = lane & 15, g = lane >> 4, q = fr >> 2, p = lane & 3;
    sm = sm0;
    const u32 fa = CV_BASE + w * CV_WAVE + q * CV_PITCH + 2 * (32 - 8 * g + 4 * p) - 8;
    const u32 ga = CV_BASE + w * CV_WAVE + CV_U + 480 + 32 * fr + 16 * g;
    f32x4 Y[8];
#pragma unroll
    for (int i = 0; i < 8; i++) Y[i] = (f32x4){0, 0, 0, 0};
    CvSet A, B;
    cv_load(sm, A, fa, ga);
    SB();
#pragma unroll 1
    for (int k0 = 0; k0 < 8; k0 += 2) {
        cv_phase(sm, Y, A, B, fa + 64 * (k0 + 1), ga - 64 * (k0 + 1));
        cv_phase(sm, Y, B, A, fa + 64 * (k0 + 2), ga - 64 * (k0 + 2));
    }
    TS(4, 10);
    sm = sm0 + CV_BASE + w * CV_WAVE;
    {
        u32x2 u4[8];
#pragma unroll
        for (int ib = 0; ib < 8; ib++) u4[ib] = lds64(sm, CV_U + 512 + 512 * ib + 32 * fr + 8 * g);
#pragma unroll
        for (int ib = 0; ib < 8; ib++) {
            const u32x2 r = {pk2(fmaf(blo(u4[ib].x), Dc, Y[ib][0]), fmaf(bhi(u4[ib].x), Dc, Y[ib][1])), pk2(fmaf(blo(u4[ib].y), Dc, Y[ib][2]), fmaf(bhi(u4[ib].y), Dc, Y[ib][3]))};
            *(LAS u32x2*)(sm + 512 * ib + 32 * fr + 8 * g) = r;
        }
    }
    LGKM0();
    u32x4 y8[4];
#pragma unroll
    for (int z = 0; z < 4; z++) y8[z] = lds128(sm, z * 1024 + lane * 16);
#pragma unroll
    for (int z = 0; z < 4; z++) st128c(rw, WS_YT + c * 4096 + z * 1024 + lane * 16, y8[z]);
}

DI void kvo_block(LAS char* sm, int wave, int lane, int h, int half, rsrc_t rw, u32 tag, char* ws) {
    const int fr = lane & 15, g = lane >> 4;
    const int j0 = (20 * h) >> 7, j1 = (20 * h + 19) >> 7;
    wait_flags(rw, lane < 8 ? (int)F_G1 + 16 * (((lane & 2) ? 10 : 5) + ((lane & 1) ? j1 : j0)) + 2 * wave + (lane >> 2)
                            : lane < 10 ? (int)F_G1 + 16 * ((lane & 1) ? j1 : j0) + 8 * half + wave : -1, -1, tag);
    TS(0, 8);
    const u32 pk0 = WS_PK0 + (h * 64 + 8 * wave) * 1024 + lane * 16, v0 = pk0 + (WS_V0 - WS_PK0);
    const u32 xx = ((fr & 4) ? WS_XV : WS_XK) + ((h * 64 + 8 * wave + (fr >> 3)) * 4 + g) * 64 + (fr & 3) * 16;
    u32x4 A[8], B[8], X[4], Q[8];
#pragma unroll
    for (int i = 0; i < 8; i++) { A[i] = ld128c(rw, pk0 + i * 1024); B[i] = ld128c(rw, v0 + i * 1024); if ((i & 1) == 0) X[i >> 1] = ld128c(rw, xx + (i >> 1) * 512); }
    const int odd = h & 1, pr = h >> 1;
    const int u5 = (g < 3 ? g : 2) + 2 * odd;
    const int sb0 = half * 64 + wave * 8;
    const u32 pq = WS_P2 + (((pr * 128 + sb0) * 5 + u5) * 128 + fr * 8) * 2;
    MEMF(); SB();
    f32x4 a00 = {0, 0, 0, 0}, a0x = a00, ax0 = a00, axx = a00;
    const u32 cst = fr == 8 ? 0x3F803F80u : 0u;
#pragma unroll
    for (int i = 0; i < 8; i++) {
        u32x4 xv = X[i >> 1];
        if (i & 1) {
#pragma unroll
            for (int e = 0; e < 4; e++) xv[e] = (u32)__builtin_amdgcn_update_dpp(0, (int)xv[e], 0x108, 0xf, 0xf, false);
        }
        if (fr >= 8) xv = (u32x4){cst, cst, cst, cst};
        a00 = mfma16(A[i], B[i], a00); a0x = mfma16(A[i], xv, a0x); ax0 = mfma16(xv, B[i], ax0); axx = mfma16(xv, xv, axx);
    }
    PIN(a00); PIN(a0x); PIN(ax0); PIN(axx);
    SB();
#pragma unroll
    for (int i = 0; i < 8; i++) Q[i] = ld128c(rw, pq + i * 1280);
    MEMF(); SB();
    TS(0, 9);
    *(LAS f32x4*)(sm + KO_RED + ((wave * 4 + 0) * 64 + lane) * 16) = a00;
    *(LAS f32x4*)(sm + KO_RED + ((wave * 4 + 1) * 64 + lane) * 16) = a0x;
    *(LAS f32x4*)(sm + KO_RED + ((wave * 4 + 2) * 64 + lane) * 16) = ax0;
    *(LAS f32x4*)(sm + KO_RED + ((wave * 4 + 3) * 64 + lane) * 16) = axx;
    if (wave >= 4 && (wave - 4) * 64 + lane < 189) *(LAS u32x2*)(sm + KO_KV + ((wave - 4) * 64 + lane) * 8) = (u32x2){0, 0};
    LGKM0(); BAR();
    if (wave < 4) {
        const int tile = wave;
        f32x4 s = *(LAS f32x4*)(sm + KO_RED + (tile * 64 + lane) * 16);
#pragma unroll
        for (int w2 = 1; w2 < 8; w2++) s += *(LAS f32x4*)(sm + KO_RED + ((w2 * 4 + tile) * 64 + lane) * 16);
        const int e = (tile & 1) ? 12 + fr : fr;
        const bool colok = (tile & 1) ? (fr >= 4 && fr <= 8) : true;
        const bool rowok = (tile & 2) ? (g == 0) : true;
        const int d0 = (tile & 2) ? 16 : 4 * g;
        if (colok && rowok) *(LAS u32x2*)(sm + KO_KV + e * 72 + (d0 + 4) * 2) = (u32x2){pk2(s[0], s[1]), pk2(s[2], s[3])};
    }
    LGKM0(); BAR();
    u32x4 A0, A1[4], A2;
    {
        const u32 dofs = KO_KV + (8 * g - 4 * odd + 4) * 2;
        const u32 r0 = (4 * odd + fr) * 72, r1 = ((odd ? 0 : 16) + (fr & 3)) * 72;
        const u32x2 p0 = lds64(sm, dofs + r0), p1 = lds64(sm, dofs + r0 + 8);
        const u32x2 q0 = lds64(sm, dofs + r1), q1 = lds64(sm, dofs + r1 + 8);
        const u32x2 z0 = lds64(sm, dofs + 20 * 72), z1 = lds64(sm, dofs + 20 * 72 + 8);
        A0 = (u32x4){p0.x, p0.y, p1.x, p1.y}; A2 = (u32x4){z0.x, z0.y, z1.x, z1.y};
#pragma unroll
        for (int z = 0; z < 4; z++) { const bool m = (fr >> 2) == z; A1[z] = (u32x4){m ? q0.x : 0u, m ? q0.y : 0u, m ? q1.x : 0u, m ? q1.y : 0u}; }
    }
    TS(0, 10);
    const u32 offA = WS_O + ((pr * 4 + 2 * odd + (g >> 1)) * 128 + fr * 8) * 2 + (g & 1) * 20480;
    const u32 offX = WS_O + (((16 + (pr >> 2)) * 4 + (pr & 3)) * 128 + odd * 64 + fr * 4) * 2;
    const f32x4 zero = {0, 0, 0, 0};
#pragma unroll
    for (int i4 = 0; i4 < 2; i4++) {
        f32x4 n1 = zero; float rden[4]; u32x2 ov[4];
#pragma unroll
        for (int z = 0; z < 4; z++) {
            const int i = 4 * i4 + z;
            const f32x4 n0 = mfma16(A0, Q[i], zero);
            const f32x4 dn = mfma16(A2, Q[i], zero);
            n1 = mfma16(A1[z], Q[i], n1);
            const float rd = __builtin_amdgcn_rcpf(dn[0] + 1e-6f);
            rden[z] = rd;
            ov[z] = (u32x2){pk2(n0[0] * rd, n0[1] * rd), pk2(n0[2] * rd, n0[3] * rd)};
        }
#pragma unroll
        for (int z = 0; z < 4; z += 2) {
            auto rx = __builtin_amdgcn_permlane16_swap(ov[z].x, ov[z + 1].x, false, false);
            auto ry = __builtin_amdgcn_permlane16_swap(ov[z].y, ov[z + 1].y, false, false);
            st128c(rw, offA + (sb0 + 4 * i4 + z) * 20480, (u32x4){rx[0], ry[0], rx[1], ry[1]});
        }
        const float rd = g == 0 ? rden[0] : g == 1 ? rden[1] : g == 2 ? rden[2] : rden[3];
        st64c(rw, offX + (sb0 + 4 * i4 + g) * 20480, (u32x2){pk2(n1[0] * rd, n1[1] * rd), pk2(n1[2] * rd, n1[3] * rd)});
    }
}

DI void gate_block(int wave, int lane, int m, const u16* x, const u16* gw, rsrc_t rw) {
    const int sub = lane & 15, r4 = lane >> 4;
    u32x4 gq[5], xr[4][5];
#pragma unroll
    for (int jj = 0; jj < 5; jj++) gq[jj] = *(const GAS u32x4*)((const GAS char*)gw + (sub + 16 * jj) * 16);
#pragma unroll
    for (int it = 0; it < 4; it++)
#pragma unroll
        for (int jj = 0; jj < 5; jj++) xr[it][jj] = *(const GAS u32x4*)((const GAS char*)x + (size_t)(128 * m + 16 * wave + 4 * it + r4) * 1280 + (sub + 16 * jj) * 16);
    MEMF(); SB();
#pragma unroll
    for (int it = 0; it < 4; it++) {
        float a = 0.f;
#pragma unroll
        for (int jj = 0; jj < 5; jj++)
#pragma unroll
            for (int e = 0; e < 4; e++) { a = fmaf(blo(xr[it][jj][e]), blo(gq[jj][e]), a); a = fmaf(bhi(xr[it][jj][e]), bhi(gq[jj][e]), a); }
        a += __shfl_xor(a, 1); a += __shfl_xor(a, 2); a += __shfl_xor(a, 4); a += __shfl_xor(a, 8);
        a = rbf(a);
        const float g = rbf(__builtin_amdgcn_rcpf(1.f + __builtin_amdgcn_exp2f(-1.4426950408889634f * a)));
        if (sub == 0) st32c(rw, WS_G + (128 * m + 16 * wave + 4 * it + r4) * 4, __float_as_uint(g));
    }
}

DI void final_block(LAS char* sm, int wave, int lane, int tid, int st, int ct, const u16* x, u16* out, rsrc_t rw, u32 tag, char* ws) {
    const int fr = lane & 15, g = lane >> 4;
    u32x4 xv[2];
#pragma unroll
    for (int r = 0; r < 2; r++) {
        int it = tid + 512 * r; it = it < 640 ? it : 639;
        const int s = it / 10, u = it - 10 * s;
        xv[r] = *(const GAS u32x4*)((const GAS char*)x + (size_t)(st * 64 + s) * 1280 + ct * 160 + u * 16);
    }
    VMCNT(0); BAR();
    if (wave < 4) {
        const int kh = wave & 1, sh = wave >> 1;
        wait_flags(rw, lane < 16 ? (int)F_O + (st >> 4) * 32 + 16 * kh + lane : -1, -1, tag);
        TS(0, 17);
        const u32 ob = WS_O + ((st * 4 + 2 * sh) * 20 + 8 * kh) * 1024 + lane * 16;
        const u32 os = WS_O + ((st * 4 + 2 * sh) * 20 + 16 + 2 * kh) * 1024 + g * 256 + fr * 8;
        u32x4 B[2][10];
#pragma unroll
        for (int kl = 0; kl < 8; kl++) { B[0][kl] = ld128c(rw, ob + kl * 1024); B[1][kl] = ld128c(rw, ob + 20480 + kl * 1024); }
#pragma unroll
        for (int kl = 0; kl < 2; kl++)
#pragma unroll
            for (int z = 0; z < 2; z++) {
                const u32x2 h0 = ld64c(rw, os + z * 20480 + kl * 1024), h1 = ld64c(rw, os + z * 20480 + kl * 1024 + 128);
                B[z][8 + kl] = (u32x4){h0.x, h0.y, h1.x, h1.y};
            }
        MEMF(); SB();
        f32x4 acc[5][2];
#pragma unroll
        for (int cb = 0; cb < 5; cb++) { acc[cb][0] = (f32x4){0, 0, 0, 0}; acc[cb][1] = (f32x4){0, 0, 0, 0}; }
        const u32 aoff = (16 * (g >> 1) + 4 * (g & 1) + (fr >> 2)) * 160 + (lane & 3) * 8;
        u32x2 al[2][5], ah[2][5];
#define F_KAP(kl) ((kl) < 8 ? (kl) : 8 + (kl))
#define F_READ(buf, kl) do { const u32 b0_ = kh ? wo_off(F_KAP(kl) + ((kl) < 8 ? 8 : 2)) : wo_off(F_KAP(kl)); \
            _Pragma("unroll") for (int cb = 0; cb < 5; cb++) { al[buf][cb] = ldstr(sm, b0_ + aoff + cb * 32); ah[buf][cb] = ldstr(sm, b0_ + aoff + cb * 32 + 1280); } } while (0)
        F_READ(0, 0);
#pragma unroll
        for (int kl = 0; kl < 10; kl++) {
            if (kl < 9) F_READ((kl + 1) & 1, kl + 1);
            SB();
#pragma unroll
            for (int cb = 0; cb < 5; cb++) {
                const u32x4 a = {al[kl & 1][cb].x, al[kl & 1][cb].y, ah[kl & 1][cb].x, ah[kl & 1][cb].y};
                acc[cb][0] = mfma16(a, B[0][kl], acc[cb][0]); acc[cb][1] = mfma16(a, B[1][kl], acc[cb][1]);
            }
            SB();
        }
        TS(0, 18);
        lds_arrive(sm, C_FR, lane); lds_wait(sm, C_FR, 4);
#pragma unroll
        for (int cb = 0; cb < 5; cb++)
#pragma unroll
            for (int z = 0; z < 2; z++)
                *(LAS f32x4*)(sm + kh * (64 * RED_PITCH) + (16 * (2 * sh + z) + fr) * RED_PITCH + (16 * cb + 4 * g) * 4) = acc[cb][z];
    } else {
        if (wave == 4) { wait_flags(rw, lane < 20 ? (int)F_Y + ct * 32 + lane : lane == 32 ? (int)F_GATE + (st >> 1) : -1, -1, tag); lds_arrive(sm, C_FY, lane); }
        else lds_wait(sm, C_FY, 1);
        const int t = tid - 256;
        u32x4 yv[3];
#pragma unroll
        for (int r = 0; r < 3; r++) { int id = t + 256 * r; id = id < 640 ? id : 639; yv[r] = ld128c(rw, WS_YT + (80 * ct + (id >> 3)) * 4096 + st * 128 + (id & 7) * 16); }
#pragma unroll
        for (int r = 0; r < 3; r++) { const int id = t + 256 * r; if (id < 640) *(LAS u32x4*)(sm + F_YY + id * 16) = yv[r]; }
        if (wave == 4) *(LAS u32*)(sm + F_GG + lane * 4) = ld32c(rw, WS_G + (st * 64 + lane) * 4);
        lds_arrive(sm, C_FY2, lane); lds_wait(sm, C_FY2, 4);
        const int sb = wave - 4, q = fr >> 2, p = lane & 3;
        u32x2 tt[5];
#pragma unroll
        for (int cb = 0; cb < 5; cb++) tt[cb] = ldstr(sm, F_YY + (16 * cb + 4 * g + q) * 128 + (16 * sb + 4 * p) * 2);
#pragma unroll
        for (int cb = 0; cb < 5; cb++) *(LAS u32x2*)(sm + F_Y2 + (16 * sb + fr) * 176 + (16 * cb + 4 * g) * 2) = tt[cb];
    }
    LGKM0(); BAR();
#pragma unroll
    for (int r = 0; r < 2; r++) {
        const int it = tid + 512 * r;
        if (it < 640) {
            const int s = it / 10, u = it - 10 * s;
            const u32 ra = s * RED_PITCH + u * 32;
            const f32x4 p0 = *(LAS f32x4*)(sm + ra), p1 = *(LAS f32x4*)(sm + ra + 16), q0 = *(LAS f32x4*)(sm + 64 * RED_PITCH + ra), q1 = *(LAS f32x4*)(sm + 64 * RED_PITCH + ra + 16);
            const u32x4 y8 = lds128(sm, F_Y2 + s * 176 + u * 16);
            const float gg = *(LAS float*)(sm + F_GG + s * 4), omg = 1.f - gg;
            float o[8];
#pragma unroll
            for (int e = 0; e < 4; e++) {
                const float a0 = (e < 2 ? p0[2 * e] + q0[2 * e] : p1[2 * e - 4] + q1[2 * e - 4]), a1 = (e < 2 ? p0[2 * e + 1] + q0[2 * e + 1] : p1[2 * e - 3] + q1[2 * e - 3]);
                const float b0 = omg * blo(xv[r][e]), b1 = omg * bhi(xv[r][e]);
                o[2 * e] = fmaf(b0, blo(y8[e]), gg * a0); o[2 * e + 1] = fmaf(b1, bhi(y8[e]), gg * a1);
            }
            *(GAS u32x4*)((GAS char*)out + (size_t)(st * 64 + s) * 1280 + ct * 160 + u * 16) = (u32x4){pk2(o[0], o[1]), pk2(o[2], o[3]), pk2(o[4], o[5]), pk2(o[6], o[7])};
        }
    }
}

__global__ __launch_bounds__(512, 2) void fused_kernel(const u16* __restrict__ x, const u16* __restrict__ w, const u16* __restrict__ wo, const u16* __restrict__ kin,
                                                        const u16* __restrict__ vin, const u16* __restrict__ shk, const u16* __restrict__ hk, const u16* __restrict__ sD,
                                                        const u16* __restrict__ gw, u16* __restrict__ out, char* ws) {
    const unsigned long long t_entry = __builtin_amdgcn_s_memrealtime();
    extern __shared__ __attribute__((aligned(16))) char smem[];
    LAS char* sm = (LAS char*)smem;
    const int tid = threadIdx.x, lane = tid & 63, wave = __builtin_amdgcn_readfirstlane(tid >> 6);
    const int b = blockIdx.x, xcd = b & 7, j = b >> 3;
    const rsrc_t rw = __builtin_amdgcn_make_buffer_rsrc(ws, 0, WS_BYTES, 0x00020000);
    asm volatile("" :: "s"(x), "s"(w), "s"(wo), "s"(kin), "s"(vin), "s"(shk), "s"(hk), "s"(sD), "s"(gw), "s"(out));
    if (PREF1 && wave < 3 && j < 30) {
        if (wave == 0) touch(x, (u32)((xcd * 2560 + j * 80 + lane) * 128));
        else if (wave == 1) { if (lane < 16) touch(x, (u32)((xcd * 2560 + j * 80 + 64 + lane) * 128)); else touch(w, (u32)(((xcd * 30 + j) * 80 + lane - 16) * 128)); }
        else if (lane < 32) touch(w, (u32)(((xcd * 30 + j) * 80 + 48 + lane) * 128));
    }
    if (PREFKV && wave < 2 && j < 30) {
        const int tile_ = xcd * 30 + j;
        touch(wave == 0 ? kin : vin, (u32)(((tile_ / 10) * 64 + lane) * 1280 + (tile_ % 10) * 128));
    }
    if (PREF3 && wave < 2 && j < 30) {
        const int n240 = xcd * 30 + j;
        if (wave == 0) { const u32 l_ = n240 * 86 + lane; touch(hk, (l_ < 20480 ? l_ : 20479) * 128); }
        else { const u32 l_ = n240 * 86 + 64 + lane, m_ = n240 * 27 + lane - 22;
               if (lane < 22) touch(hk, (l_ < 20480 ? l_ : 20479) * 128); else if (lane < 49) touch(wo, (m_ < 6400 ? m_ : 6399) * 128); }
    }
    if (PREFC && wave == 3) {
        unsigned long long pc;
        asm volatile("s_getpc_b64 %0" : "=s"(pc));
        pc &= ~127ull;
        const int ln = PREFC == 1 ? j * 10 + lane : lane;
        if (PREFC == 1) { if (lane < 10 && ln < CODE_LINES) touch((const void*)pc, (u32)(ln * 128)); }
        else {
#pragma unroll
            for (int z = 0; z < 5; z++) if (z * 64 + lane < CODE_LINES) touch((const void*)pc, (u32)((z * 64 + lane) * 128));
        }
    }
    if (PREFT && wave == 2) {
        if (PREFT == 1) { touch(ws, (u32)(lane * 262144)); if (lane < 40) touch(out, (u32)(lane * 65536)); }
        else { touch(ws, (u32)((j * 8 + (lane & 7)) * 65536 + (lane >> 3) * 128) % (WS_STAMP - 1024)); if (lane < 40) touch(out, (u32)(lane * 65536)); }
    }
    u32 tag;
    asm volatile("s_load_dword %0, %1, 0x0\n\ts_waitcnt lgkmcnt(0)" : "=s"(tag) : "s"(ws + WS_FLAG + F_EPOCH * FS) : "memory");
    tag += 1u;
    if (tid < 16) *(LAS u32*)(sm + L_CNT + tid * 4) = 0u;
    if (tid == 0) *(volatile LAS unsigned long long*)(sm + 156672 + 64) = 0ull;
    TS(0, 0);
    const int st = 4 * xcd + (j >> 3), ct = j & 7;
    int ko = -1;
    bool is_conv = false;

    if (j >= 30) {
        const int m = xcd * 2 + (j - 30);
        ko = -100;
        {
            const int grp = wave >> 2, w4i = wave & 3;
            u32x2 w4[3];
#pragma unroll
            for (int t = 0; t < 3; t++) { const int tt = 2 * t + grp < 5 ? 2 * t + grp : 4; p0_weights(w4[t], shk, 240 + 5 * m + tt, w4i, lane); }
#pragma unroll
            for (int t = 0; t < 3; t++) if (2 * t + grp < 5) p0_issue(kin, vin, 240 + 5 * m + 2 * t + grp, w4i, lane, (2 * t + grp) * 17408);
            VMCNT(0);
#pragma unroll
            for (int t = 0; t < 3; t++) PIN(w4[t]);
            BAR();
#pragma unroll
            for (int t = 0; t < 3; t++)
                if (2 * t + grp < 5) p0_compute(sm, w4[t], 240 + 5 * m + 2 * t + grp, w4i, lane, (2 * t + grp) * 17408, 90112 + (2 * t + grp) * 8192);
            LGKM0(); BAR();
            {
                u32x4 t4[3][2];
#pragma unroll
                for (int t = 0; t < 3; t++)
#pragma unroll
                    for (int z = 0; z < 2; z++) { const int ch2 = ((tid & 255) >> 3) + 32 * z, o2 = tid & 7, tt = 2 * t + grp < 5 ? 2 * t + grp : 4; t4[t][z] = lds128(sm, 90112 + tt * 8192 + ch2 * 128 + ((o2 ^ (ch2 & 7)) * 16)); }
#pragma unroll
                for (int t = 0; t < 3; t++)
                    if (2 * t + grp < 5) {
                        const int tile = 240 + 5 * m + 2 * t + grp, cg = tile % 10, s0 = (tile / 10) * 64;
#pragma unroll
                        for (int z = 0; z < 2; z++) { const int ch2 = ((tid & 255) >> 3) + 32 * z, o2 = tid & 7; st128c(rw, WS_UT + (cg * 64 + ch2) * 4096 + (s0 + 8 * o2) * 2, t4[t][z]); }
                    }
            }
        }
        VMCNT(0); BAR();
        if (tid < 5) set_flag(rw, p0_flag(240 + 5 * m + tid), tag);
        TS(0, 1);
        gate_block(wave, lane, m, x, gw, rw);
        VMCNT(0); LGKM0(); BAR();
        if (tid == 0) set_flag(rw, F_GATE + m, tag);
        TS(0, 3);
    } else {
        const int rt = 2 * xcd + j / 15, cbk = j % 15, tile = xcd * 30 + j;
        is_conv = cbk < 5 || cbk >= 10;
        if (!is_conv) { const int ki = rt * 5 + (cbk - 5); ko = ki < 64 ? ki : -2 - (ki - 64); }
        if (wave >= 4) {
            const int lw = wave - 4;
            u32 vx[4], vw[4];
#pragma unroll
            for (int n2 = 0; n2 < 4; n2++) {
                const int n = 4 * lw + n2;
                const int row = 8 * n + (lane >> 3), ch = (lane & 7) ^ ((row >> 1) & 7);
                vx[n2] = (u32)((rt * 128 + row) * 1280 + ch * 16);
                const int kr = 4 * n + (lane >> 4), f = ((kr & 3) << 2) | ((kr >> 2) & 3), cw = (lane & 15) ^ f;
                vw[n2] = (u32)(kr * 3840 + cbk * 256 + cw * 16);
            }
            const int rot = (rt + cbk) % 10;
#define K1_ISSUE(c) do { const u32 st_ = ((c) & 3) * K1_STAGE + 4 * lw * 1024; int kc_ = (c) + ROT_ON * rot; kc_ = kc_ >= 10 ? kc_ - 10 : kc_; \
            const char* xb_ = (const char*)x + kc_ * 128; const char* wp_ = (const char*)w + (size_t)kc_ * 64 * 3840; \
            _Pragma("unroll") for (int n2 = 0; n2 < 4; n2++) { if (NT_X) glds16nt(xb_, vx[n2], st_ + 16384 + n2 * 1024); else glds16(xb_, vx[n2], st_ + 16384 + n2 * 1024); glds16(wp_, vw[n2], st_ + n2 * 1024); } } while (0)
            K1_ISSUE(0); K1_ISSUE(1);
            VMCNT(8); BAR();
            K1_ISSUE(2); K1_ISSUE(3);
            p0_issue(kin, vin, tile, lw, lane, P0_K);
#pragma unroll 1
            for (int c = 0; c < 6; c++) {
                if (c < 3) VMCNT(21); else VMCNT(16);
                BAR();
                K1_ISSUE(c + 4);
            }
            const int cv = rt * 10 + (cbk < 5 ? cbk : cbk - 5), c = 4 * cv + lw;
            if (is_conv) {
                CvIn in; u32 fl; u32x4 u[4];
                const int fidx = (int)F_P0 + (cv >> 4) * 32 + (lane & 31);
                conv_fetch_h(in, hk, c, lane);
                VMCNT(24); BAR();
                VMCNT(16); BAR();
                gld32c(fl, ws, WS_FLAG + fidx * FS);
                VMCNT(9); BAR();
                TS(4, 4);
                VMCNT(0); PIN(fl);
                const bool ready = __all(fl == tag);
                if (ready) {
#pragma unroll
                    for (int z = 0; z < 4; z++) gld128c(u[z], ws, WS_UT + c * 4096 + z * 1024 + lane * 16);
                }
                TS(4, 5);
                conv_build_h(sm, in, lw, lane);
                TS(4, 6);
                if (!ready) {
                    wait_flags(rw, fidx, -1, tag);
#pragma unroll
                    for (int z = 0; z < 4; z++) gld128c(u[z], ws, WS_UT + c * 4096 + z * 1024 + lane * 16);
                }
                TS(4, 7);
                VMCNT(0);
#pragma unroll
                for (int z = 0; z < 4; z++) { PIN(u[z]); *(LAS u32x4*)(sm + CV_BASE + lw * CV_WAVE + CV_U + 512 + z * 1024 + lane * 16) = u[z]; }
                const float Dc = blo((u32)sD[c]);
                TS(4, 8);
                conv_run(sm, Dc, lw, lane, c, rw, ws);
                publish_last(rw, sm, C_Y, 4, F_Y + (cv / 20) * 32 + cv % 20, tag, lane);
                TS(4, 11);
            } else {
                VMCNT(16); BAR();
                VMCNT(8); BAR();
                VMCNT(0); BAR();
                const int r32 = lane & 31, hh = lane >> 5;
                u32 dst, istep, zstep; kv_addr(true, cbk - 5, rt, lw >> 1, lw & 1, 1, r32, hh, dst, istep, zstep);
                lds_wait(sm, C_XF + lw * 4, 1);
                f32x4 a[2][4];
#pragma unroll
                for (int i = 0; i < 2; i++)
#pragma unroll
                    for (int q4 = 0; q4 < 4; q4++) a[i][q4] = *(LAS f32x4*)(sm + K_XFER + lw * 8192 + ((i * 4 + q4) * 64 + lane) * 16);
#pragma unroll
                for (int i = 0; i < 2; i++)
#pragma unroll
                    for (int z = 0; z < 2; z++) {
                        const f32x4 a0 = a[i][2 * z], a1 = a[i][2 * z + 1];
                        st128c(rw, dst + i * istep + z * zstep, (u32x4){phi2(a0[0], a0[1]), phi2(a0[2], a0[3]), phi2(a1[0], a1[1]), phi2(a1[2], a1[3])});
                    }
                publish_last(rw, sm, C_G1, 8, F_G1 + cbk * 16 + rt, tag, lane);
            }
        } else {
            u32x2 w4; p0_weights(w4, shk, tile, wave, lane);
            const int wm = wave >> 1, wn = wave & 1, r32 = lane & 31, hh = lane >> 5;
            K1Addr a0;
#pragma unroll
            for (int ks = 0; ks < 2; ks++) {
                a0.x[0][ks] = (64 * wm + r32) * 128 + (((2 * ks + hh) ^ ((r32 >> 1) & 7)) * 16);
                a0.x[1][ks] = a0.x[0][ks] ^ 64;
            }
#pragma unroll
            for (int jj = 0; jj < 2; jj++)
#pragma unroll
                for (int t = 0; t < 2; t++) {
                    const int blk = (lane >> 4) & 1, q = (lane & 15) >> 2, p = lane & 3, row = 8 * hh + 4 * t + q, ch = 8 * wn + 4 * jj + 2 * blk + (p >> 1);
                    const int f = ((row & 3) << 2) | ((row >> 2) & 3);
                    a0.w[jj][t] = row * 256 + ((ch ^ f) * 16) + 8 * (p & 1);
                }
            f32x16 acc[2][2];
#pragma unroll
            for (int i = 0; i < 2; i++)
#pragma unroll
                for (int jj = 0; jj < 2; jj++)
#pragma unroll
                    for (int e = 0; e < 16; e++) acc[i][jj][e] = 0.f;
            BAR();
            TS(0, 1);
            if (cbk < 5) {
                k1_loop<true>(sm, a0, acc, rw, p0_flag(tile), tag, wave, lane, hk, wo, tile, w4);
                __builtin_amdgcn_s_setprio(2);
                TS(0, 2);
                u32x4 qv[2][2][2];
#pragma unroll
                for (int jj = 0; jj < 2; jj++)
#pragma unroll
                    for (int ap = 0; ap < 2; ap++)
#pragma unroll
                        for (int i = 0; i < 2; i++) {
                            const f32x16& a = acc[i][jj];
                            const u32 x0 = phi2(a[8 * ap], a[8 * ap + 1]), y0 = phi2(a[8 * ap + 2], a[8 * ap + 3]);
                            const u32 x1 = phi2(a[8 * ap + 4], a[8 * ap + 5]), y1 = phi2(a[8 * ap + 6], a[8 * ap + 7]);
                            auto rx = __builtin_amdgcn_permlane32_swap(x0, x1, false, false);
                            auto ry = __builtin_amdgcn_permlane32_swap(y0, y1, false, false);
                            qv[jj][ap][i] = (u32x4){rx[0], ry[0], rx[1], ry[1]};
                        }
                if (QDELAY) { __builtin_amdgcn_s_setprio(0); for (int z = 0; z < QDELAY; z++) __builtin_amdgcn_s_sleep(8); }
#pragma unroll
                for (int jj = 0; jj < 2; jj++)
#pragma unroll
                    for (int ap = 0; ap < 2; ap++) {
                        const int U = 16 * cbk + 8 * wn + 4 * jj + 2 * ap + hh, pr = U / 5, u5 = U - 5 * pr;
                        const u32 dst = WS_P2 + (((pr * 128 + rt * 8 + 4 * wm + (r32 >> 4)) * 5 + u5) * 128 + (r32 & 15) * 8) * 2;
#pragma unroll
                        for (int i = 0; i < 2; i++) st128c(rw, dst + i * 2560, qv[jj][ap][i]);
                    }
            } else {
                k1_loop<false>(sm, a0, acc, rw, p0_flag(tile), tag, wave, lane, hk, wo, tile, w4);
                __builtin_amdgcn_s_setprio(2);
                TS(0, 2);
                const bool isk = cbk < 10;
                const int j5 = isk ? cbk - 5 : cbk - 10;
                if (isk) {
#pragma unroll
                    for (int i = 0; i < 2; i++)
#pragma unroll
                        for (int q4 = 0; q4 < 4; q4++)
                            *(LAS f32x4*)(sm + K_XFER + wave * 8192 + ((i * 4 + q4) * 64 + lane) * 16) = (f32x4){acc[i][1][4 * q4], acc[i][1][4 * q4 + 1], acc[i][1][4 * q4 + 2], acc[i][1][4 * q4 + 3]};
                    lds_arrive(sm, C_XF + wave * 4, lane);
                    u32 dst, istep, zstep; kv_addr(true, j5, rt, wm, wn, 0, r32, hh, dst, istep, zstep);
#pragma unroll
                    for (int i = 0; i < 2; i++)
#pragma unroll
                        for (int z = 0; z < 2; z++) {
                            const f32x16& a = acc[i][0];
                            st128c(rw, dst + i * istep + z * zstep,
                                   (u32x4){phi2(a[8 * z], a[8 * z + 1]), phi2(a[8 * z + 2], a[8 * z + 3]), phi2(a[8 * z + 4], a[8 * z + 5]), phi2(a[8 * z + 6], a[8 * z + 7])});
                        }
                } else {
#pragma unroll
                    for (int jj = 0; jj < 2; jj++) {
                        u32 dst, istep, zstep; kv_addr(false, j5, rt, wm, wn, jj, r32, hh, dst, istep, zstep);
#pragma unroll
                        for (int i = 0; i < 2; i++)
#pragma unroll
                            for (int z = 0; z < 2; z++) {
                                const f32x16& a = acc[i][jj];
                                st128c(rw, dst + i * istep + z * zstep,
                                       (u32x4){pk2(a[8 * z], a[8 * z + 1]), pk2(a[8 * z + 2], a[8 * z + 3]), pk2(a[8 * z + 4], a[8 * z + 5]), pk2(a[8 * z + 6], a[8 * z + 7])});
                            }
                    }
                }
            }
            LGKM0();
            TS(0, 25);
            publish_last(rw, sm, C_G1, is_conv ? 4 : 8, F_G1 + cbk * 16 + rt, tag, lane);
            __builtin_amdgcn_s_setprio(0);
            TS(0, 3);
            if (is_conv) {
                lds_wait(sm, C_G1, 4);
                wo_issue(wo, ct, 0, 12, wave, 4, lane);
            }
        }
    }
    if (!is_conv) {
        LGKM0(); BAR();
        if (ko >= 0) {
            const int h = ko >> 1, half = ko & 1;
            wo_issue(wo, ct, 0, 12, wave, 8, lane);
            kvo_block(sm, wave, lane, h, half, rw, tag, ws);
            TS(0, 26);
            VMCNT(0); BAR();
            if (tid == 0) set_flag(rw, F_O + half * 32 + h, tag);
            TS(0, 11);
            wo_issue(wo, ct, 12, 20, wave, 8, lane);
        } else {
            wo_issue(wo, ct, 0, 20, wave, 8, lane);
            if (ko == -2 && wave == 0) {
                wait_flags(rw, (int)F_G1 + lane, (int)F_G1 + 64 + lane, tag);
                wait_flags(rw, (int)F_G1 + 128 + lane, (int)F_G1 + 192 + lane, tag);
                if (lane == 0) set_flag(rw, F_EPOCH, tag);
            }
        }
    } else {
        VMCNT(0); BAR();
        if (wave >= 4) wo_issue(wo, ct, 12, 20, wave - 4, 4, lane);
    }
    TS(0, 16);
    final_block(sm, wave, lane, tid, st, ct, x, out, rw, tag, ws);

    VMCNT(0); BAR();
    if (tid == 0) {
        const unsigned long long te_ = *(volatile LAS unsigned long long*)(sm + 156672 + 64);
        st64c(rw, WS_STAMP + blockIdx.x * 64, __builtin_bit_cast(u32x2, t_entry)); st64c(rw, WS_STAMP + blockIdx.x * 64 + 8, __builtin_bit_cast(u32x2, te_));
        VMCNT(0);
        st32c(rw, WS_STAMP + blockIdx.x * 64 + 16, tag);
    }
    if (blockIdx.x == 51 && wave == 0) {
        unsigned long long mn = ~0ull, mx = 0;
        for (int q = 0; q < 4; q++) {
            while (ld32c(rw, WS_STAMP + (q * 64 + lane) * 64 + 16) != tag) __builtin_amdgcn_s_sleep(1);
            const unsigned long long a_ = __builtin_bit_cast(unsigned long long, __builtin_amdgcn_raw_buffer_load_b64(rw, WS_STAMP + (q * 64 + lane) * 64, 0, 16));
            const unsigned long long e2_ = __builtin_bit_cast(unsigned long long, __builtin_amdgcn_raw_buffer_load_b64(rw, WS_STAMP + (q * 64 + lane) * 64 + 8, 0, 16));
            mn = a_ < mn ? a_ : mn; mx = e2_ > mx ? e2_ : mx; }
        for (int o = 1; o < 64; o <<= 1) { const unsigned long long a_ = __shfl_xor(mn, o), b_ = __shfl_xor(mx, o); mn = a_ < mn ? a_ : mn; mx = b_ > mx ? b_ : mx; }
        const unsigned long long d_ = mx - mn;
        const unsigned long long e_ = __builtin_amdgcn_s_memrealtime() + 2 * d_;
        while (__builtin_amdgcn_s_memrealtime() < e_) __builtin_amdgcn_s_sleep(1);
    }
    TS(0, 19);
}

extern "C" void kernel_launch(void* const* d_in, const int* in_sizes, int n_in, void* d_out, int out_size, void* d_ws, size_t ws_size, hipStream_t stream) {
    const u16* x = (const u16*)d_in[0]; const u16* wqkv = (const u16*)d_in[1]; const u16* wo = (const u16*)d_in[2];
    const u16* kin = (const u16*)d_in[4]; const u16* vin = (const u16*)d_in[5]; const u16* shk = (const u16*)d_in[6];
    const u16* hk = (const u16*)d_in[7]; const u16* sD = (const u16*)d_in[8]; const u16* gw = (const u16*)d_in[9];
    (void)hipFuncSetAttribute((const void*)fused_kernel, hipFuncAttributeMaxDynamicSharedMemorySize, LDS_BYTES);
    fused_kernel<<<256, 512, LDS_BYTES, stream>>>(x, wqkv, wo, kin, vin, shk, hk, sD, gw, (u16*)d_out, (char*)d_ws);
}
```

```cpp
#include <hip/hip_runtime.h>
#include <stdint.h>

typedef unsigned short u16;
typedef unsigned int u32;
typedef __bf16 bf16x8 __attribute__((ext_vector_type(8)));
typedef __bf16 bf16x2 __attribute__((ext_vector_type(2)));
typedef float f32x4 __attribute__((ext_vector_type(4)));
typedef float f32x2 __attribute__((ext_vector_type(2)));
typedef float f32x16 __attribute__((ext_vector_type(16)));
typedef u32 u32x4 __attribute__((ext_vector_type(4)));
typedef u32 u32x2 __attribute__((ext_vector_type(2)));
typedef short s16x4 __attribute__((ext_vector_type(4)));
#define LAS __attribute__((address_space(3)))
#define GAS __attribute__((address_space(1)))
#define DI __device__ __forceinline__
typedef __amdgpu_buffer_rsrc_t rsrc_t;

constexpr int S = 2048, D = 640;
#define FS 256
#define POLL_SLEEP 2
#define ROT_ON 1

constexpr u32 WS_UT = 0;
constexpr u32 WS_YT = WS_UT + 2621440;
constexpr u32 WS_PK0 = WS_YT + 2621440;
constexpr u32 WS_V0 = WS_PK0 + 2097152;
constexpr u32 WS_XK = WS_V0 + 2097152;
constexpr u32 WS_XV = WS_XK + 524288;
constexpr u32 WS_P2 = WS_XV + 524288;
constexpr u32 WS_O = WS_P2 + 2621440;
constexpr u32 WS_G = WS_O + 2621440;
constexpr u32 WS_FLAG = WS_G + 8192;
constexpr u32 F_P0 = 0;
constexpr u32 F_G1 = 320;
constexpr u32 F_GATE = 560;
constexpr u32 F_O = 576;
constexpr u32 F_Y = 640;
constexpr u32 F_EPOCH = 896, F_TMO = 928;
constexpr u32 WS_STAMP = WS_FLAG + 1024 * FS;
constexpr u32 WS_BYTES = WS_STAMP + 256 * 32 * 16;

#define TS(w, id) do {} while (0)

DI u32 pk2(float a, float b) { f32x2 t = {a, b}; return __builtin_bit_cast(u32, __builtin_convertvector(t, bf16x2)); }
DI float blo(u32 u) { return __uint_as_float(u << 16); }
DI float bhi(u32 u) { return __uint_as_float(u & 0xffff0000u); }
DI float rbf(float f) { return blo(pk2(f, f)); }
DI bf16x8 asbf(u32x4 v) { return __builtin_bit_cast(bf16x8, v); }
DI f32x4 mfma16(u32x4 a, u32x4 b, f32x4 c) { return __builtin_amdgcn_mfma_f32_16x16x32_bf16(asbf(a), asbf(b), c, 0, 0, 0); }
DI f32x16 mfma32(u32x4 a, u32x4 b, f32x16 c) { return __builtin_amdgcn_mfma_f32_32x32x16_bf16(asbf(a), asbf(b), c, 0, 0, 0); }

DI void glds16(const void* sbase, u32 voff, u32 lds) {
    asm volatile("s_mov_b32 m0, %2\n\ts_nop 0\n\tglobal_load_lds_dwordx4 %0, %1" :: "v"(voff), "s"(sbase), "s"(lds) : "memory");
}
DI void glds16nt(const void* sbase, u32 voff, u32 lds) {
    asm volatile("s_mov_b32 m0, %2\n\ts_nop 0\n\tglobal_load_lds_dwordx4 %0, %1 nt" :: "v"(voff), "s"(sbase), "s"(lds) : "memory");
}
DI void touch(const void* sbase, u32 voff) {
    asm volatile("s_mov_b32 m0, %2\n\ts_nop 0\n\tglobal_load_lds_dword %0, %1" :: "v"(voff), "s"(sbase), "s"(156928u) : "memory");
}
#define NT_KV 0
#define NT_X 0
#define NT_H 0
#define PREF1 1
#define PREF2 0
#define PREFC 1
#define CODE_LINES 345
#define PREFT 0
#define PREFKV 0
#define PREF3 1
#define QDELAY 0
DI void gld64(u32x2& d, const void* sbase, u32 voff) { asm volatile("global_load_dwordx2 %0, %1, %2" : "=v"(d) : "v"(voff), "s"(sbase) : "memory"); }
DI void gld128(u32x4& d, const void* sbase, u32 voff) { asm volatile("global_load_dwordx4 %0, %1, %2" : "=v"(d) : "v"(voff), "s"(sbase) : "memory"); }
DI void gld128c(u32x4& d, const void* sbase, u32 voff) { asm volatile("global_load_dwordx4 %0, %1, %2 sc1" : "=v"(d) : "v"(voff), "s"(sbase) : "memory"); }
DI void gld32c(u32& d, const void* sbase, u32 voff) { asm volatile("global_load_dword %0, %1, %2 sc1" : "=v"(d) : "v"(voff), "s"(sbase) : "memory"); }
#define PIN(x) asm volatile("" : "+v"(x))
#define SB() __builtin_amdgcn_sched_barrier(0)
DI u32x4 ld128c(rsrc_t r, u32 off) { return __builtin_amdgcn_raw_buffer_load_b128(r, off, 0, 16); }
DI u32x2 ld64c(rsrc_t r, u32 off) { return __builtin_amdgcn_raw_buffer_load_b64(r, off, 0, 16); }
DI u32 ld32c(rsrc_t r, u32 off) { return __builtin_amdgcn_raw_buffer_load_b32(r, off, 0, 16); }
DI void st128c(rsrc_t r, u32 off, u32x4 v) { __builtin_amdgcn_raw_buffer_store_b128(v, r, off, 0, 16); }
DI void st64c(rsrc_t r, u32 off, u32x2 v) { __builtin_amdgcn_raw_buffer_store_b64(v, r, off, 0, 16); }
DI void st32c(rsrc_t r, u32 off, u32 v) { __builtin_amdgcn_raw_buffer_store_b32(v, r, off, 0, 16); }
DI u32x4 lds128(LAS char* sm, u32 a) { return *(LAS u32x4*)(sm + a); }
DI u32x2 lds64(LAS char* sm, u32 a) { return *(LAS u32x2*)(sm + a); }
DI u32x2 ldstr(LAS char* sm, u32 a) { return __builtin_bit_cast(u32x2, __builtin_amdgcn_ds_read_tr16_b64_v4i16((LAS s16x4*)(sm + a))); }
#define VMCNT(n) asm volatile("s_waitcnt vmcnt(" #n ")" ::: "memory")
#define LGKM0() asm volatile("s_waitcnt lgkmcnt(0)" ::: "memory")
#define BAR() do { asm volatile("" ::: "memory"); __builtin_amdgcn_s_barrier(); asm volatile("" ::: "memory"); } while (0)
#define MEMF() asm volatile("" ::: "memory")

DI void set_flag(rsrc_t r, u32 idx, u32 tag) { st32c(r, WS_FLAG + idx * FS, tag); }
DI void wait_flags(rsrc_t r, int idx0, int idx1, u32 tag) {
    unsigned long long t0 = 0;
    for (u32 it = 0;; it++) {
        u32 a = tag, b = tag;
        if (idx0 >= 0) a = ld32c(r, WS_FLAG + idx0 * FS);
        if (idx1 >= 0) b = ld32c(r, WS_FLAG + idx1 * FS);
        if (__all(a == tag && b == tag)) break;
        if (POLL_SLEEP) __builtin_amdgcn_s_sleep(POLL_SLEEP);
        if ((it & 63u) == 0u) {
            const unsigned long long t = __builtin_amdgcn_s_memrealtime();
            if (it == 0) t0 = t;
            else if (t - t0 > 2000000ull) { set_flag(r, F_TMO, tag); break; }
        }
    }
    MEMF();
}
DI void lds_arrive(LAS char* sm, u32 cnt, int lane) { LGKM0(); if (lane == 0) __hip_atomic_fetch_add((LAS u32*)(sm + cnt), 1u, __ATOMIC_RELAXED, __HIP_MEMORY_SCOPE_WORKGROUP); MEMF(); }
DI void lds_wait(LAS char* sm, u32 cnt, u32 n) { MEMF(); while (*(volatile LAS u32*)(sm + cnt) < n) __builtin_amdgcn_s_sleep(0); MEMF(); }
DI void publish_last(rsrc_t r, LAS char* sm, u32 cnt, u32 nw, u32 flag, u32 tag, int lane) {
    VMCNT(0);
    if (lane == 0) {
        const u32 old = __hip_atomic_fetch_add((LAS u32*)(sm + cnt), 1u, __ATOMIC_RELAXED, __HIP_MEMORY_SCOPE_WORKGROUP);
        if (old == nw - 1) set_flag(r, flag, tag);
    }
    MEMF();
}

DI u32 phi2(float a, float b) {
    const f32x2 t = {a, b}, s = t * 1.4426950408889634f;
    const float ea = __builtin_amdgcn_fmed3f(__builtin_amdgcn_exp2f(s.x), 0.f, 1.f), eb = __builtin_amdgcn_fmed3f(__builtin_amdgcn_exp2f(s.y), 0.f, 1.f);
    const float ma = __int_as_float(max(__float_as_int(a), 0)), mb = __int_as_float(max(__float_as_int(b), 0));
    const f32x2 r = ((f32x2){ea, eb} + (f32x2){ma, mb}) + 1e-6f;
    return pk2(r.x, r.y);
}

constexpr int K1_STAGE = 32768;
constexpr int P0_K = 131072, P0_V = P0_K + 9216, P0_OUT = P0_V + 8192;
constexpr int L_CNT = 156672;
constexpr int C_G1 = L_CNT, C_Y = L_CNT + 4, C_P0A = L_CNT + 8, C_P0B = L_CNT + 12, C_P0C = L_CNT + 16, C_FY = L_CNT + 20, C_FY2 = L_CNT + 24, C_FO = L_CNT + 28, C_FR = L_CNT + 32, C_XF = L_CNT + 36;
constexpr int CV_PITCH = 4160;
constexpr int CV_U = 4 * CV_PITCH;
constexpr int CV_WAVE = CV_U + 4608;
constexpr int CV_BASE = 65536;
constexpr int WO_HI = 65536;
constexpr int F_YY = 110592;
constexpr int F_Y2 = F_YY + 10240;
constexpr int F_GG = F_Y2 + 11264;
constexpr int KO_RED = 110592;
constexpr int KO_KV = KO_RED + 32768;
constexpr int RED_PITCH = 336;
constexpr int LDS_BYTES = 160 * 1024;
DI u32 p0_flag(int tile) { return F_P0 + (tile % 10) * 32 + tile / 10; }
DI constexpr u32 wo_off(int kap) { return kap < 12 ? kap * 5120 : WO_HI + (kap - 12) * 5120; }

DI void p0_issue(const u16* kin, const u16* vin, int tile, int lw, int lane, u32 base) {
    const int cg = tile % 10, rg = tile / 10, s0 = rg * 64;
    const u32 col = cg * 128 + (lane & 7) * 16;
#pragma unroll
    for (int z = 0; z < 2; z++) {
        const int n = 2 * lw + z;
        int r = s0 - 4 + n * 8 + (lane >> 3); r = r < 0 ? 0 : r;
        (NT_KV ? glds16nt : glds16)(kin, (u32)(r * 1280) + col, base + n * 1024);
        (NT_KV ? glds16nt : glds16)(vin, (u32)((s0 + n * 8 + (lane >> 3)) * 1280) + col, base + 9216 + n * 1024);
    }
    {
        int r = s0 + 60 + (lane >> 3); r = r > S - 1 ? S - 1 : r;
        (NT_KV ? glds16nt : glds16)(kin, (u32)(r * 1280) + col, base + 8 * 1024);
    }
}
DI float dot2(u32 a, u32 b, float c) { return __builtin_amdgcn_fdot2_f32_bf16(__builtin_bit_cast(bf16x2, a), __builtin_bit_cast(bf16x2, b), c, false); }
DI void p0_compute(LAS char* sm, u32x2 w4, int tile, int wave, int lane, u32 base, u32 outb, char* ws = nullptr) {
    const int s0 = (tile / 10) * 64;
    const int G = wave * 4 + (lane >> 4), cb = G & 3, i = lane & 15, q = i >> 2, p = i & 3;
    const u32 wa = __builtin_amdgcn_alignbit(w4.y, w4.y, 16), wb = __builtin_amdgcn_alignbit(w4.x, w4.x, 16);
    const int ch = 16 * cb + i;
    u32x2 kq[2][3], vq[2][2];
#pragma unroll
    for (int z = 0; z < 2; z++) {
        const int o = (G >> 2) + 4 * z;
        const u32 ka = base + (8 * o + q) * 128 + 32 * cb + 8 * p, va = ka + 9216;
        kq[z][0] = ldstr(sm, ka); kq[z][1] = ldstr(sm, ka + 512); kq[z][2] = ldstr(sm, ka + 1024);
        vq[z][0] = ldstr(sm, va); vq[z][1] = ldstr(sm, va + 512);
    }
#pragma unroll
    for (int z = 0; z < 2; z++) {
        const int o = (G >> 2) + 4 * z;
        u32 K[6] = {kq[z][0].x, kq[z][0].y, kq[z][1].x, kq[z][1].y, kq[z][2].x, kq[z][2].y};
        if (s0 == 0 && o == 0) { K[0] = 0; K[1] = 0; }
        u32 Sh[5];
#pragma unroll
        for (int e = 0; e < 5; e++) Sh[e] = __builtin_amdgcn_alignbit(K[e + 1], K[e], 16);
        const u32 V[4] = {vq[z][0].x, vq[z][0].y, vq[z][1].x, vq[z][1].y};
        u32 r[4];
#pragma unroll
        for (int e = 0; e < 4; e++) {
            const float k0 = dot2(Sh[e], wa, dot2(Sh[e + 1], wb, 0.f)), k1 = dot2(K[e + 1], wa, dot2(K[e + 2], wb, 0.f));
            r[e] = pk2(k0 * blo(V[e]), k1 * bhi(V[e]));
        }
        *(LAS u32x4*)(sm + outb + ch * 128 + ((o ^ (ch & 7)) * 16)) = (u32x4){r[0], r[1], r[2], r[3]};
    }
}
DI void p0_store(LAS char* sm, rsrc_t rw, int tile, int t256, u32 outb) {
    const int cg = tile % 10, s0 = (tile / 10) * 64;
#pragma unroll
    for (int z = 0; z < 2; z++) {
        const int ch2 = (t256 >> 3) + 32 * z, o2 = t256 & 7;
        const u32x4 t4 = lds128(sm, outb + ch2 * 128 + ((o2 ^ (ch2 & 7)) * 16));
        st128c(rw, WS_UT + (cg * 64 + ch2) * 4096 + (s0 + 8 * o2) * 2, t4);
    }
}
DI void p0_weights(u32x2& w4, const u16* shk, int tile, int wave, int lane) {
    const int cg = tile % 10, cb = (wave * 4 + (lane >> 4)) & 3;
    gld64(w4, shk, (u32)((cg * 64 + 16 * cb + (lane & 15)) * 8));
}

struct K1Set { u32x4 xa[2][2]; u32x2 wl[2][2], wh[2][2]; };
struct K1Addr { u32 x[2][2]; u32 w[2][2]; };
template <bool QT, int WO, int XO>
DI void k1_phase(LAS char* sm, f32x16 (&acc)[2][2], const K1Set& cur, K1Set& nxt, const u32 (&xb)[2], const u32 (&wb)[2][2]) {
#pragma unroll
    for (int n = 0; n < 8; n++) {
        const int ks = n >> 2, i = (n >> 1) & 1, jj = n & 1;
        const u32x4 wf = {cur.wl[ks][jj].x, cur.wl[ks][jj].y, cur.wh[ks][jj].x, cur.wh[ks][jj].y};
        acc[i][jj] = QT ? mfma32(wf, cur.xa[ks][i], acc[i][jj]) : mfma32(cur.xa[ks][i], wf, acc[i][jj]);
        if (n == 0) { nxt.wl[0][0] = ldstr(sm, wb[0][0] + WO); nxt.wh[0][0] = ldstr(sm, wb[0][1] + WO); }
        if (n == 1) { nxt.xa[0][0] = lds128(sm, xb[0] + XO); nxt.wl[0][1] = ldstr(sm, wb[1][0] + WO); }
        if (n == 2) { nxt.wh[0][1] = ldstr(sm, wb[1][1] + WO); nxt.xa[0][1] = lds128(sm, xb[0] + XO + 4096); }
        if (n == 3) { nxt.wl[1][0] = ldstr(sm, wb[0][0] + WO + 4096); nxt.wh[1][0] = ldstr(sm, wb[0][1] + WO + 4096); }
        if (n == 4) { nxt.xa[1][0] = lds128(sm, xb[1] + XO); nxt.wl[1][1] = ldstr(sm, wb[1][0] + WO + 4096); }
        if (n == 5) { nxt.wh[1][1] = ldstr(sm, wb[1][1] + WO + 4096); nxt.xa[1][1] = lds128(sm, xb[1] + XO + 4096); }
        SB();
    }
}
template <bool QT, int ST>
DI void k1_chunk(LAS char* sm, f32x16 (&acc)[2][2], K1Set& s0, K1Set& s1, const K1Addr& a0, const K1Addr& a1, bool bar, bool drain = false) {
    constexpr int SN = (ST + 1) & 3;
    const K1Addr& ac = ST < 2 ? a0 : a1; const K1Addr& an = SN < 2 ? a0 : a1;
    k1_phase<QT, (ST & 1) * K1_STAGE + 8192, (ST & 1) * K1_STAGE + 16384>(sm, acc, s0, s1, ac.x[1], ac.w);
    if (drain) VMCNT(0);
    if (bar) { LGKM0(); BAR(); }
    k1_phase<QT, (SN & 1) * K1_STAGE, (SN & 1) * K1_STAGE + 16384>(sm, acc, s1, s0, an.x[0], an.w);
}
template <bool QT>
DI void k1_loop(LAS char* sm, const K1Addr& a0, f32x16 (&acc)[2][2], rsrc_t rw, u32 pflag, u32 tag, int wave, int lane, const u16* hk, const u16* wo, int n240, u32x2 w4) {
    K1Addr a1;
#pragma unroll
    for (int i = 0; i < 2; i++)
#pragma unroll
        for (int z = 0; z < 2; z++) { a1.x[i][z] = a0.x[i][z] + 65536; a1.w[i][z] = a0.w[i][z] + 65536; }
    K1Set s0, s1;
#pragma unroll
    for (int ks = 0; ks < 2; ks++)
#pragma unroll
        for (int jj = 0; jj < 2; jj++) {
            s0.wl[ks][jj] = ldstr(sm, a0.w[jj][0] + ks * 4096); s0.wh[ks][jj] = ldstr(sm, a0.w[jj][1] + ks * 4096);
            s0.xa[ks][jj] = lds128(sm, a0.x[0][ks] + 16384 + jj * 4096);
        }
    SB();
#pragma unroll 1
    for (int c = 0; c < 8; c += 4) {
        k1_chunk<QT, 0>(sm, acc, s0, s1, a0, a1, true);
        if (c == 4) p0_store(sm, rw, n240, wave * 64 + lane, P0_OUT);
        k1_chunk<QT, 1>(sm, acc, s0, s1, a0, a1, true);
        k1_chunk<QT, 2>(sm, acc, s0, s1, a0, a1, true);
        k1_chunk<QT, 3>(sm, acc, s0, s1, a0, a1, true, c == 4);
        if (c == 4 && wave == 0 && lane == 0) set_flag(rw, pflag, tag);
        if (c == 0) { VMCNT(0); PIN(w4); p0_compute(sm, w4, n240, wave, lane, P0_K, P0_OUT); }
        if (PREF2 && c == 0 && wave >= 2) {
            if (wave == 2) { const u32 l_ = n240 * 86 + lane; touch(hk, (l_ < 20480 ? l_ : 20479) * 128); }
            else { const u32 l_ = n240 * 86 + 64 + lane, m_ = n240 * 27 + lane - 22;
                   if (lane < 22) touch(hk, (l_ < 20480 ? l_ : 20479) * 128); else if (lane < 49) touch(wo, (m_ < 6400 ? m_ : 6399) * 128); }
        }
    }
    k1_chunk<QT, 0>(sm, acc, s0, s1, a0, a1, true);
    k1_chunk<QT, 1>(sm, acc, s0, s1, a0, a1, false);
}

DI void kv_addr(bool isk, int j5, int rt, int wm, int wn, int jj, int r32, int hh, u32& dst, u32& istep, u32& zstep) {
    const int cc = 128 * j5 + 64 * wn + 32 * jj + r32, h = cc / 20, d = cc - 20 * h;
    const int sb32 = rt * 4 + 2 * wm;
    const bool lo = d < 16;
    dst = lo ? (isk ? WS_PK0 : WS_V0) + (((h * 64 + sb32) * 4 + hh) * 128 + d * 8) * 2
             : (isk ? WS_XK : WS_XV) + (((h * 64 + sb32) * 4 + hh) * 32 + (d - 16) * 8) * 2;
    istep = lo ? 1024 : 256; zstep = lo ? 512 : 128;
}
constexpr int K_XFER = 65536;

DI void wo_issue(const u16* wo, int ct, int k0, int k1, int w, int nw, int lane) {
    u32 vo[5], vs[5];
#pragma unroll
    for (int i = 0; i < 5; i++) {
        const int n = 64 * i + lane, rho = n / 10, c = n - 10 * rho, g = 2 * (rho >> 4) + ((rho >> 2) & 1), r8 = 4 * ((rho >> 3) & 1) + (rho & 3);
        vo[i] = (u32)((8 * (g < 2 ? g : g + 1) + r8) * 1280 + ct * 160 + c * 16);
        vs[i] = (u32)((40 * g + 16 + r8) * 1280 + ct * 160 + c * 16);
    }
#pragma unroll 1
    for (int kap = k0 + w; kap < k1; kap += nw) {
        const u32 dst = kap < 12 ? kap * 5120 : WO_HI + (kap - 12) * 5120;
        if (kap < 16) {
            const char* src = (const char*)wo + (size_t)kap * 51200;
#pragma unroll
            for (int i = 0; i < 5; i++) glds16(src, vo[i], dst + i * 1024);
        } else {
            const char* src = (const char*)wo + (size_t)(kap - 16) * 204800;
#pragma unroll
            for (int i = 0; i < 5; i++) glds16(src, vs[i], dst + i * 1024);
        }
    }
}

struct CvSet { u32x2 fl[8], fh[8]; u32x4 g[8]; };
DI void cv_load(LAS char* sm, CvSet& s, u32 fa, u32 ga) {
#pragma unroll
    for (int k1 = 0; k1 < 8; k1++) { s.fl[k1] = ldstr(sm, fa + 512 * k1 + 8); s.fh[k1] = ldstr(sm, fa + 512 * k1); s.g[k1] = lds128(sm, ga + 512 * k1); }
}
DI void cv_phase(LAS char* sm, f32x4 (&Y)[8], const CvSet& cur, CvSet& nxt, u32 fa, u32 ga) {
    PIN(fa); PIN(ga);
    SB();
    int n = 0;
#pragma unroll
    for (int e = 0; e < 8; e++)
#pragma unroll
        for (int k1 = 0; k1 + e < 8; k1++) {
            const u32x4 F = {cur.fl[k1].x, cur.fl[k1].y, cur.fh[k1].x, cur.fh[k1].y};
            Y[k1 + e] = mfma16(F, cur.g[e], Y[k1 + e]);
            if (n == 0) nxt.g[0] = lds128(sm, ga);
            else if (n <= 16) { const int r = (n - 1) >> 1; if ((n - 1) & 1) nxt.fh[r] = ldstr(sm, fa + 512 * r); else nxt.fl[r] = ldstr(sm, fa + 512 * r + 8); }
            else if (n < 24) nxt.g[n - 16] = lds128(sm, ga + 512 * (n - 16));
            n++;
            SB();
        }
}
struct CvIn { u32x4 hd[4]; u32x2 hm[4]; };
DI void conv_fetch_h(CvIn& in, const u16* hk, int c, int lane) {
    const char* hb = (const char*)hk + (size_t)c * 4096;
#pragma unroll
    for (int z = 0; z < 4; z++) {
        const int i = lane + 64 * z;
        if (NT_H) asm volatile("global_load_dwordx4 %0, %1, %2 nt" : "=v"(in.hd[z]) : "v"((u32)(i * 16)), "s"(hb) : "memory"); else gld128(in.hd[z], hb, (u32)(i * 16));
        gld64(in.hm[z], hb, (u32)(i > 0 ? i * 16 - 8 : 0));
    }
}
DI void conv_build_h(LAS char* sm0, CvIn& in, int w, int lane) {
    LAS char* sm = sm0 + CV_BASE + w * CV_WAVE;
#pragma unroll
    for (int z = 0; z < 4; z++) { PIN(in.hd[z]); PIN(in.hm[z]); }
    if (lane < 32) *(LAS u32x4*)(sm + CV_U + lane * 16) = (u32x4){0, 0, 0, 0};
    if (lane < 8) *(LAS u32x4*)(sm + (lane >> 1) * CV_PITCH + (lane & 1) * 16) = (u32x4){0, 0, 0, 0};
#pragma unroll
    for (int z = 0; z < 4; z++) {
        const int i = lane + 64 * z;
        u32 m2 = in.hm[z].x, m1 = in.hm[z].y;
        if (i == 0) { m2 = 0; m1 = 0; }
        const u32 d0 = in.hd[z].x, d1 = in.hd[z].y, d2 = in.hd[z].z, d3 = in.hd[z].w;
        const u32 om2 = __builtin_amdgcn_alignbit(m1, m2, 16), om1 = __builtin_amdgcn_alignbit(d0, m1, 16), o0 = __builtin_amdgcn_alignbit(d1, d0, 16),
                  o1 = __builtin_amdgcn_alignbit(d2, d1, 16), o2 = __builtin_amdgcn_alignbit(d3, d2, 16);
        LAS char* p = sm + 32 + i * 16;
        *(LAS u32x4*)(p) = (u32x4){d0, d1, d2, d3};
        *(LAS u32x4*)(p + CV_PITCH) = (u32x4){om1, o0, o1, o2};
        *(LAS u32x4*)(p + 2 * CV_PITCH) = (u32x4){m1, d0, d1, d2};
        *(LAS u32x4*)(p + 3 * CV_PITCH) = (u32x4){om2, om1, o0, o1};
    }
}
DI void conv_run(LAS char* sm0, float Dc, int w, int lane, int c, rsrc_t rw, char* ws) {
    LAS char* sm = sm0;
    LGKM0();
    TS(4, 9);
    const int fr = lane & 15, g = lane >> 4, q = fr >> 2, p = lane & 3;
    sm = sm0;
    const u32 fa = CV_BASE + w * CV_WAVE + q * CV_PITCH + 2 * (32 - 8 * g + 4 * p) - 8;
    const u32 ga = CV_BASE + w * CV_WAVE + CV_U + 480 + 32 * fr + 16 * g;
    f32x4 Y[8];
#pragma unroll
    for (int i = 0; i < 8; i++) Y[i] = (f32x4){0, 0, 0, 0};
    CvSet A, B;
    cv_load(sm, A, fa, ga);
    SB();
#pragma unroll 1
    for (int k0 = 0; k0 < 8; k0 += 2) {
        cv_phase(sm, Y, A, B, fa + 64 * (k0 + 1), ga - 64 * (k0 + 1));
        cv_phase(sm, Y, B, A, fa + 64 * (k0 + 2), ga - 64 * (k0 + 2));
    }
    TS(4, 10);
    sm = sm0 + CV_BASE + w * CV_WAVE;
    {
        u32x2 u4[8];
#pragma unroll
        for (int ib = 0; ib < 8; ib++) u4[ib] = lds64(sm, CV_U + 512 + 512 * ib + 32 * fr + 8 * g);
#pragma unroll
        for (int ib = 0; ib < 8; ib++) {
            const u32x2 r = {pk2(fmaf(blo(u4[ib].x), Dc, Y[ib][0]), fmaf(bhi(u4[ib].x), Dc, Y[ib][1])), pk2(fmaf(blo(u4[ib].y), Dc, Y[ib][2]), fmaf(bhi(u4[ib].y), Dc, Y[ib][3]))};
            *(LAS u32x2*)(sm + 512 * ib + 32 * fr + 8 * g) = r;
        }
    }
    LGKM0();
    u32x4 y8[4];
#pragma unroll
    for (int z = 0; z < 4; z++) y8[z] = lds128(sm, z * 1024 + lane * 16);
#pragma unroll
    for (int z = 0; z < 4; z++) st128c(rw, WS_YT + c * 4096 + z * 1024 + lane * 16, y8[z]);
}

DI void kvo_block(LAS char* sm, int wave, int lane, int h, int half, rsrc_t rw, u32 tag, char* ws) {
    const int fr = lane & 15, g = lane >> 4;
    const int j0 = (20 * h) >> 7, j1 = (20 * h + 19) >> 7;
    wait_flags(rw, lane < 8 ? (int)F_G1 + 16 * (((lane & 2) ? 10 : 5) + ((lane & 1) ? j1 : j0)) + 2 * wave + (lane >> 2)
                            : lane < 10 ? (int)F_G1 + 16 * ((lane & 1) ? j1 : j0) + 8 * half + wave : -1, -1, tag);
    TS(0, 8);
    const u32 pk0 = WS_PK0 + (h * 64 + 8 * wave) * 1024 + lane * 16, v0 = pk0 + (WS_V0 - WS_PK0);
    const u32 xx = ((fr & 4) ? WS_XV : WS_XK) + ((h * 64 + 8 * wave + (fr >> 3)) * 4 + g) * 64 + (fr & 3) * 16;
    u32x4 A[8], B[8], X[4], Q[8];
#pragma unroll
    for (int i = 0; i < 8; i++) { A[i] = ld128c(rw, pk0 + i * 1024); B[i] = ld128c(rw, v0 + i * 1024); if ((i & 1) == 0) X[i >> 1] = ld128c(rw, xx + (i >> 1) * 512); }
    const int odd = h & 1, pr = h >> 1;
    const int u5 = (g < 3 ? g : 2) + 2 * odd;
    const int sb0 = half * 64 + wave * 8;
    const u32 pq = WS_P2 + (((pr * 128 + sb0) * 5 + u5) * 128 + fr * 8) * 2;
    MEMF(); SB();
    f32x4 a00 = {0, 0, 0, 0}, a0x = a00, ax0 = a00, axx = a00;
    const u32 cst = fr == 8 ? 0x3F803F80u : 0u;
#pragma unroll
    for (int i = 0; i < 8; i++) {
        u32x4 xv = X[i >> 1];
        if (i & 1) {
#pragma unroll
            for (int e = 0; e < 4; e++) xv[e] = (u32)__builtin_amdgcn_update_dpp(0, (int)xv[e], 0x108, 0xf, 0xf, false);
        }
        if (fr >= 8) xv = (u32x4){cst, cst, cst, cst};
        a00 = mfma16(A[i], B[i], a00); a0x = mfma16(A[i], xv, a0x); ax0 = mfma16(xv, B[i], ax0); axx = mfma16(xv, xv, axx);
    }
    PIN(a00); PIN(a0x); PIN(ax0); PIN(axx);
    SB();
#pragma unroll
    for (int i = 0; i < 8; i++) Q[i] = ld128c(rw, pq + i * 1280);
    MEMF(); SB();
    TS(0, 9);
    *(LAS f32x4*)(sm + KO_RED + ((wave * 4 + 0) * 64 + lane) * 16) = a00;
    *(LAS f32x4*)(sm + KO_RED + ((wave * 4 + 1) * 64 + lane) * 16) = a0x;
    *(LAS f32x4*)(sm + KO_RED + ((wave * 4 + 2) * 64 + lane) * 16) = ax0;
    *(LAS f32x4*)(sm + KO_RED + ((wave * 4 + 3) * 64 + lane) * 16) = axx;
    if (wave >= 4 && (wave - 4) * 64 + lane < 189) *(LAS u32x2*)(sm + KO_KV + ((wave - 4) * 64 + lane) * 8) = (u32x2){0, 0};
    LGKM0(); BAR();
    if (wave < 4) {
        const int tile = wave;
        f32x4 s = *(LAS f32x4*)(sm + KO_RED + (tile * 64 + lane) * 16);
#pragma unroll
        for (int w2 = 1; w2 < 8; w2++) s += *(LAS f32x4*)(sm + KO_RED + ((w2 * 4 + tile) * 64 + lane) * 16);
        const int e = (tile & 1) ? 12 + fr : fr;
        const bool colok = (tile & 1) ? (fr >= 4 && fr <= 8) : true;
        const bool rowok = (tile & 2) ? (g == 0) : true;
        const int d0 = (tile & 2) ? 16 : 4 * g;
        if (colok && rowok) *(LAS u32x2*)(sm + KO_KV + e * 72 + (d0 + 4) * 2) = (u32x2){pk2(s[0], s[1]), pk2(s[2], s[3])};
    }
    LGKM0(); BAR();
    u32x4 A0, A1[4], A2;
    {
        const u32 dofs = KO_KV + (8 * g - 4 * odd + 4) * 2;
        const u32 r0 = (4 * odd + fr) * 72, r1 = ((odd ? 0 : 16) + (fr & 3)) * 72;
        const u32x2 p0 = lds64(sm, dofs + r0), p1 = lds64(sm, dofs + r0 + 8);
        const u32x2 q0 = lds64(sm, dofs + r1), q1 = lds64(sm, dofs + r1 + 8);
        const u32x2 z0 = lds64(sm, dofs + 20 * 72), z1 = lds64(sm, dofs + 20 * 72 + 8);
        A0 = (u32x4){p0.x, p0.y, p1.x, p1.y}; A2 = (u32x4){z0.x, z0.y, z1.x, z1.y};
#pragma unroll
        for (int z = 0; z < 4; z++) { const bool m = (fr >> 2) == z; A1[z] = (u32x4){m ? q0.x : 0u, m ? q0.y : 0u, m ? q1.x : 0u, m ? q1.y : 0u}; }
    }
    TS(0, 10);
    const u32 offA = WS_O + ((pr * 4 + 2 * odd + (g >> 1)) * 128 + fr * 8) * 2 + (g & 1) * 20480;
    const u32 offX = WS_O + (((16 + (pr >> 2)) * 4 + (pr & 3)) * 128 + odd * 64 + fr * 4) * 2;
    const f32x4 zero = {0, 0, 0, 0};
#pragma unroll
    for (int i4 = 0; i4 < 2; i4++) {
        f32x4 n1 = zero; float rden[4]; u32x2 ov[4];
#pragma unroll
        for (int z = 0; z < 4; z++) {
            const int i = 4 * i4 + z;
            const f32x4 n0 = mfma16(A0, Q[i], zero);
            const f32x4 dn = mfma16(A2, Q[i], zero);
            n1 = mfma16(A1[z], Q[i], n1);
            const float rd = __builtin_amdgcn_rcpf(dn[0] + 1e-6f);
            rden[z] = rd;
            ov[z] = (u32x2){pk2(n0[0] * rd, n0[1] * rd), pk2(n0[2] * rd, n0[3] * rd)};
        }
#pragma unroll
        for (int z = 0; z < 4; z += 2) {
            auto rx = __builtin_amdgcn_permlane16_swap(ov[z].x, ov[z + 1].x, false, false);
            auto ry = __builtin_amdgcn_permlane16_swap(ov[z].y, ov[z + 1].y, false, false);
            st128c(rw, offA + (sb0 + 4 * i4 + z) * 20480, (u32x4){rx[0], ry[0], rx[1], ry[1]});
        }
        const float rd = g == 0 ? rden[0] : g == 1 ? rden[1] : g == 2 ? rden[2] : rden[3];
        st64c(rw, offX + (sb0 + 4 * i4 + g) * 20480, (u32x2){pk2(n1[0] * rd, n1[1] * rd), pk2(n1[2] * rd, n1[3] * rd)});
    }
}

DI void gate_block(int wave, int lane, int m, const u16* x, const u16* gw, rsrc_t rw) {
    const int sub = lane & 15, r4 = lane >> 4;
    u32x4 gq[5], xr[4][5];
#pragma unroll
    for (int jj = 0; jj < 5; jj++) gq[jj] = *(const GAS u32x4*)((const GAS char*)gw + (sub + 16 * jj) * 16);
#pragma unroll
    for (int it = 0; it < 4; it++)
#pragma unroll
        for (int jj = 0; jj < 5; jj++) xr[it][jj] = *(const GAS u32x4*)((const GAS char*)x + (size_t)(128 * m + 16 * wave + 4 * it + r4) * 1280 + (sub + 16 * jj) * 16);
    MEMF(); SB();
#pragma unroll
    for (int it = 0; it < 4; it++) {
        float a = 0.f;
#pragma unroll
        for (int jj = 0; jj < 5; jj++)
#pragma unroll
            for (int e = 0; e < 4; e++) { a = fmaf(blo(xr[it][jj][e]), blo(gq[jj][e]), a); a = fmaf(bhi(xr[it][jj][e]), bhi(gq[jj][e]), a); }
        a += __shfl_xor(a, 1); a += __shfl_xor(a, 2); a += __shfl_xor(a, 4); a += __shfl_xor(a, 8);
        a = rbf(a);
        const float g = rbf(__builtin_amdgcn_rcpf(1.f + __builtin_amdgcn_exp2f(-1.4426950408889634f * a)));
        if (sub == 0) st32c(rw, WS_G + (128 * m + 16 * wave + 4 * it + r4) * 4, __float_as_uint(g));
    }
}

DI void final_block(LAS char* sm, int wave, int lane, int tid, int st, int ct, const u16* x, u16* out, rsrc_t rw, u32 tag, char* ws) {
    const int fr = lane & 15, g = lane >> 4;
    u32x4 xv[2];
#pragma unroll
    for (int r = 0; r < 2; r++) {
        int it = tid + 512 * r; it = it < 640 ? it : 639;
        const int s = it / 10, u = it - 10 * s;
        xv[r] = *(const GAS u32x4*)((const GAS char*)x + (size_t)(st * 64 + s) * 1280 + ct * 160 + u * 16);
    }
    VMCNT(0); BAR();
    if (wave < 4) {
        const int kh = wave & 1, sh = wave >> 1;
        wait_flags(rw, lane < 16 ? (int)F_O + (st >> 4) * 32 + 16 * kh + lane : -1, -1, tag);
        TS(0, 17);
        const u32 ob = WS_O + ((st * 4 + 2 * sh) * 20 + 8 * kh) * 1024 + lane * 16;
        const u32 os = WS_O + ((st * 4 + 2 * sh) * 20 + 16 + 2 * kh) * 1024 + g * 256 + fr * 8;
        u32x4 B[2][10];
#pragma unroll
        for (int kl = 0; kl < 8; kl++) { B[0][kl] = ld128c(rw, ob + kl * 1024); B[1][kl] = ld128c(rw, ob + 20480 + kl * 1024); }
#pragma unroll
        for (int kl = 0; kl < 2; kl++)
#pragma unroll
            for (int z = 0; z < 2; z++) {
                const u32x2 h0 = ld64c(rw, os + z * 20480 + kl * 1024), h1 = ld64c(rw, os + z * 20480 + kl * 1024 + 128);
                B[z][8 + kl] = (u32x4){h0.x, h0.y, h1.x, h1.y};
            }
        MEMF(); SB();
        f32x4 acc[5][2];
#pragma unroll
        for (int cb = 0; cb < 5; cb++) { acc[cb][0] = (f32x4){0, 0, 0, 0}; acc[cb][1] = (f32x4){0, 0, 0, 0}; }
        const u32 aoff = (16 * (g >> 1) + 4 * (g & 1) + (fr >> 2)) * 160 + (lane & 3) * 8;
        u32x2 al[2][5], ah[2][5];
#define F_KAP(kl) ((kl) < 8 ? (kl) : 8 + (kl))
#define F_READ(buf, kl) do { const u32 b0_ = kh ? wo_off(F_KAP(kl) + ((kl) < 8 ? 8 : 2)) : wo_off(F_KAP(kl)); \
            _Pragma("unroll") for (int cb = 0; cb < 5; cb++) { al[buf][cb] = ldstr(sm, b0_ + aoff + cb * 32); ah[buf][cb] = ldstr(sm, b0_ + aoff + cb * 32 + 1280); } } while (0)
        F_READ(0, 0);
#pragma unroll
        for (int kl = 0; kl < 10; kl++) {
            if (kl < 9) F_READ((kl + 1) & 1, kl + 1);
            SB();
#pragma unroll
            for (int cb = 0; cb < 5; cb++) {
                const u32x4 a = {al[kl & 1][cb].x, al[kl & 1][cb].y, ah[kl & 1][cb].x, ah[kl & 1][cb].y};
                acc[cb][0] = mfma16(a, B[0][kl], acc[cb][0]); acc[cb][1] = mfma16(a, B[1][kl], acc[cb][1]);
            }
            SB();
        }
        TS(0, 18);
        lds_arrive(sm, C_FR, lane); lds_wait(sm, C_FR, 4);
#pragma unroll
        for (int cb = 0; cb < 5; cb++)
#pragma unroll
            for (int z = 0; z < 2; z++)
                *(LAS f32x4*)(sm + kh * (64 * RED_PITCH) + (16 * (2 * sh + z) + fr) * RED_PITCH + (16 * cb + 4 * g) * 4) = acc[cb][z];
    } else {
        if (wave == 4) { wait_flags(rw, lane < 20 ? (int)F_Y + ct * 32 + lane : lane == 32 ? (int)F_GATE + (st >> 1) : -1, -1, tag); lds_arrive(sm, C_FY, lane); }
        else lds_wait(sm, C_FY, 1);
        const int t = tid - 256;
        u32x4 yv[3];
#pragma unroll
        for (int r = 0; r < 3; r++) { int id = t + 256 * r; id = id < 640 ? id : 639; yv[r] = ld128c(rw, WS_YT + (80 * ct + (id >> 3)) * 4096 + st * 128 + (id & 7) * 16); }
#pragma unroll
        for (int r = 0; r < 3; r++) { const int id = t + 256 * r; if (id < 640) *(LAS u32x4*)(sm + F_YY + id * 16) = yv[r]; }
        if (wave == 4) *(LAS u32*)(sm + F_GG + lane * 4) = ld32c(rw, WS_G + (st * 64 + lane) * 4);
        lds_arrive(sm, C_FY2, lane); lds_wait(sm, C_FY2, 4);
        const int sb = wave - 4, q = fr >> 2, p = lane & 3;
        u32x2 tt[5];
#pragma unroll
        for (int cb = 0; cb < 5; cb++) tt[cb] = ldstr(sm, F_YY + (16 * cb + 4 * g + q) * 128 + (16 * sb + 4 * p) * 2);
#pragma unroll
        for (int cb = 0; cb < 5; cb++) *(LAS u32x2*)(sm + F_Y2 + (16 * sb + fr) * 176 + (16 * cb + 4 * g) * 2) = tt[cb];
    }
    LGKM0(); BAR();
#pragma unroll
    for (int r = 0; r < 2; r++) {
        const int it = tid + 512 * r;
        if (it < 640) {
            const int s = it / 10, u = it - 10 * s;
            const u32 ra = s * RED_PITCH + u * 32;
            const f32x4 p0 = *(LAS f32x4*)(sm + ra), p1 = *(LAS f32x4*)(sm + ra + 16), q0 = *(LAS f32x4*)(sm + 64 * RED_PITCH + ra), q1 = *(LAS f32x4*)(sm + 64 * RED_PITCH + ra + 16);
            const u32x4 y8 = lds128(sm, F_Y2 + s * 176 + u * 16);
            const float gg = *(LAS float*)(sm + F_GG + s * 4), omg = 1.f - gg;
            float o[8];
#pragma unroll
            for (int e = 0; e < 4; e++) {
                const float a0 = (e < 2 ? p0[2 * e] + q0[2 * e] : p1[2 * e - 4] + q1[2 * e - 4]), a1 = (e < 2 ? p0[2 * e + 1] + q0[2 * e + 1] : p1[2 * e - 3] + q1[2 * e - 3]);
                const float b0 = omg * blo(xv[r][e]), b1 = omg * bhi(xv[r][e]);
                o[2 * e] = fmaf(b0, blo(y8[e]), gg * a0); o[2 * e + 1] = fmaf(b1, bhi(y8[e]), gg * a1);
            }
            { const u32x4 ov_ = {pk2(o[0], o[1]), pk2(o[2], o[3]), pk2(o[4], o[5]), pk2(o[6], o[7])};
              asm volatile("global_store_dwordx4 %0, %1, %2 nt\n\ts_nop 1" :: "v"((u32)((st * 64 + s) * 1280 + ct * 160 + u * 16)), "v"(ov_), "s"(out) : "memory"); }
        }
    }
}

__global__ __launch_bounds__(512, 2) void fused_kernel(const u16* __restrict__ x, const u16* __restrict__ w, const u16* __restrict__ wo, const u16* __restrict__ kin,
                                                        const u16* __restrict__ vin, const u16* __restrict__ shk, const u16* __restrict__ hk, const u16* __restrict__ sD,
                                                        const u16* __restrict__ gw, u16* __restrict__ out, char* ws) {
    extern __shared__ __attribute__((aligned(16))) char smem[];
    LAS char* sm = (LAS char*)smem;
    const int tid = threadIdx.x, lane = tid & 63, wave = __builtin_amdgcn_readfirstlane(tid >> 6);
    const int b = blockIdx.x, xcd = b & 7, j = b >> 3;
    const rsrc_t rw = __builtin_amdgcn_make_buffer_rsrc(ws, 0, WS_BYTES, 0x00020000);
    asm volatile("" :: "s"(x), "s"(w), "s"(wo), "s"(kin), "s"(vin), "s"(shk), "s"(hk), "s"(sD), "s"(gw), "s"(out));
    if (PREF1 && wave < 3 && j < 30) {
        if (wave == 0) touch(x, (u32)((xcd * 2560 + j * 80 + lane) * 128));
        else if (wave == 1) { if (lane < 16) touch(x, (u32)((xcd * 2560 + j * 80 + 64 + lane) * 128)); else touch(w, (u32)(((xcd * 30 + j) * 80 + lane - 16) * 128)); }
        else if (lane < 32) touch(w, (u32)(((xcd * 30 + j) * 80 + 48 + lane) * 128));
    }
    if (PREFKV && wave < 2 && j < 30) {
        const int tile_ = xcd * 30 + j;
        touch(wave == 0 ? kin : vin, (u32)(((tile_ / 10) * 64 + lane) * 1280 + (tile_ % 10) * 128));
    }
    if (PREF3 && wave < 2 && j < 30) {
        const int n240 = xcd * 30 + j;
        if (wave == 0) { const u32 l_ = n240 * 86 + lane; touch(hk, (l_ < 20480 ? l_ : 20479) * 128); }
        else { const u32 l_ = n240 * 86 + 64 + lane, m_ = n240 * 27 + lane - 22;
               if (lane < 22) touch(hk, (l_ < 20480 ? l_ : 20479) * 128); else if (lane < 49) touch(wo, (m_ < 6400 ? m_ : 6399) * 128); }
    }
    if (PREFC && wave == 3) {
        unsigned long long pc;
        asm volatile("s_getpc_b64 %0" : "=s"(pc));
        pc &= ~127ull;
        const int ln = PREFC == 1 ? j * 10 + lane : lane;
        if (PREFC == 1) { if (lane < 10 && ln < CODE_LINES) touch((const void*)pc, (u32)(ln * 128)); }
        else {
#pragma unroll
            for (int z = 0; z < 5; z++) if (z * 64 + lane < CODE_LINES) touch((const void*)pc, (u32)((z * 64 + lane) * 128));
        }
    }
    if (PREFT && wave == 2) {
        if (PREFT == 1) { touch(ws, (u32)(lane * 262144)); if (lane < 40) touch(out, (u32)(lane * 65536)); }
        else { touch(ws, (u32)((j * 8 + (lane & 7)) * 65536 + (lane >> 3) * 128) % (WS_STAMP - 1024)); if (lane < 40) touch(out, (u32)(lane * 65536)); }
    }
    u32 tag;
    asm volatile("s_load_dword %0, %1, 0x0\n\ts_waitcnt lgkmcnt(0)" : "=s"(tag) : "s"(ws + WS_FLAG + F_EPOCH * FS) : "memory");
    tag += 1u;
    if (tid < 16) *(LAS u32*)(sm + L_CNT + tid * 4) = 0u;
    TS(0, 0);
    const int st = 4 * xcd + (j >> 3), ct = j & 7;
    int ko = -1;
    bool is_conv = false;

    if (j >= 30) {
        const int m = xcd * 2 + (j - 30);
        ko = -100;
        {
            const int grp = wave >> 2, w4i = wave & 3;
            u32x2 w4[3];
#pragma unroll
            for (int t = 0; t < 3; t++) { const int tt = 2 * t + grp < 5 ? 2 * t + grp : 4; p0_weights(w4[t], shk, 240 + 5 * m + tt, w4i, lane); }
#pragma unroll
            for (int t = 0; t < 3; t++) if (2 * t + grp < 5) p0_issue(kin, vin, 240 + 5 * m + 2 * t + grp, w4i, lane, (2 * t + grp) * 17408);
            VMCNT(0);
#pragma unroll
            for (int t = 0; t < 3; t++) PIN(w4[t]);
            BAR();
#pragma unroll
            for (int t = 0; t < 3; t++)
                if (2 * t + grp < 5) p0_compute(sm, w4[t], 240 + 5 * m + 2 * t + grp, w4i, lane, (2 * t + grp) * 17408, 90112 + (2 * t + grp) * 8192);
            LGKM0(); BAR();
            {
                u32x4 t4[3][2];
#pragma unroll
                for (int t = 0; t < 3; t++)
#pragma unroll
                    for (int z = 0; z < 2; z++) { const int ch2 = ((tid & 255) >> 3) + 32 * z, o2 = tid & 7, tt = 2 * t + grp < 5 ? 2 * t + grp : 4; t4[t][z] = lds128(sm, 90112 + tt * 8192 + ch2 * 128 + ((o2 ^ (ch2 & 7)) * 16)); }
#pragma unroll
                for (int t = 0; t < 3; t++)
                    if (2 * t + grp < 5) {
                        const int tile = 240 + 5 * m + 2 * t + grp, cg = tile % 10, s0 = (tile / 10) * 64;
#pragma unroll
                        for (int z = 0; z < 2; z++) { const int ch2 = ((tid & 255) >> 3) + 32 * z, o2 = tid & 7; st128c(rw, WS_UT + (cg * 64 + ch2) * 4096 + (s0 + 8 * o2) * 2, t4[t][z]); }
                    }
            }
        }
        VMCNT(0); BAR();
        if (tid < 5) set_flag(rw, p0_flag(240 + 5 * m + tid), tag);
        TS(0, 1);
        gate_block(wave, lane, m, x, gw, rw);
        VMCNT(0); LGKM0(); BAR();
        if (tid == 0) set_flag(rw, F_GATE + m, tag);
        TS(0, 3);
    } else {
        const int rt = 2 * xcd + j / 15, cbk = j % 15, tile = xcd * 30 + j;
        is_conv = cbk < 5 || cbk >= 10;
        if (!is_conv) { const int ki = rt * 5 + (cbk - 5); ko = ki < 64 ? ki : -2 - (ki - 64); }
        if (wave >= 4) {
            const int lw = wave - 4;
            u32 vx[4], vw[4];
#pragma unroll
            for (int n2 = 0; n2 < 4; n2++) {
                const int n = 4 * lw + n2;
                const int row = 8 * n + (lane >> 3), ch = (lane & 7) ^ ((row >> 1) & 7);
                vx[n2] = (u32)((rt * 128 + row) * 1280 + ch * 16);
                const int kr = 4 * n + (lane >> 4), f = ((kr & 3) << 2) | ((kr >> 2) & 3), cw = (lane & 15) ^ f;
                vw[n2] = (u32)(kr * 3840 + cbk * 256 + cw * 16);
            }
            const int rot = (rt + cbk) % 10;
#define K1_ISSUE(c) do { const u32 st_ = ((c) & 3) * K1_STAGE + 4 * lw * 1024; int kc_ = (c) + ROT_ON * rot; kc_ = kc_ >= 10 ? kc_ - 10 : kc_; \
            const char* xb_ = (const char*)x + kc_ * 128; const char* wp_ = (const char*)w + (size_t)kc_ * 64 * 3840; \
            _Pragma("unroll") for (int n2 = 0; n2 < 4; n2++) { if (NT_X) glds16nt(xb_, vx[n2], st_ + 16384 + n2 * 1024); else glds16(xb_, vx[n2], st_ + 16384 + n2 * 1024); glds16(wp_, vw[n2], st_ + n2 * 1024); } } while (0)
            K1_ISSUE(0); K1_ISSUE(1);
            VMCNT(8); BAR();
            K1_ISSUE(2); K1_ISSUE(3);
            p0_issue(kin, vin, tile, lw, lane, P0_K);
#pragma unroll 1
            for (int c = 0; c < 6; c++) {
                if (c < 3) VMCNT(21); else VMCNT(16);
                BAR();
                K1_ISSUE(c + 4);
            }
            const int cv = rt * 10 + (cbk < 5 ? cbk : cbk - 5), c = 4 * cv + lw;
            if (is_conv) {
                CvIn in; u32 fl; u32x4 u[4];
                const int fidx = (int)F_P0 + (cv >> 4) * 32 + (lane & 31);
                conv_fetch_h(in, hk, c, lane);
                VMCNT(24); BAR();
                VMCNT(16); BAR();
                gld32c(fl, ws, WS_FLAG + fidx * FS);
                VMCNT(9); BAR();
                TS(4, 4);
                VMCNT(0); PIN(fl);
                const bool ready = __all(fl == tag);
                if (ready) {
#pragma unroll
                    for (int z = 0; z < 4; z++) gld128c(u[z], ws, WS_UT + c * 4096 + z * 1024 + lane * 16);
                }
                TS(4, 5);
                conv_build_h(sm, in, lw, lane);
                TS(4, 6);
                if (!ready) {
                    wait_flags(rw, fidx, -1, tag);
#pragma unroll
                    for (int z = 0; z < 4; z++) gld128c(u[z], ws, WS_UT + c * 4096 + z * 1024 + lane * 16);
                }
                TS(4, 7);
                VMCNT(0);
#pragma unroll
                for (int z = 0; z < 4; z++) { PIN(u[z]); *(LAS u32x4*)(sm + CV_BASE + lw * CV_WAVE + CV_U + 512 + z * 1024 + lane * 16) = u[z]; }
                const float Dc = blo((u32)sD[c]);
                TS(4, 8);
                conv_run(sm, Dc, lw, lane, c, rw, ws);
                publish_last(rw, sm, C_Y, 4, F_Y + (cv / 20) * 32 + cv % 20, tag, lane);
                TS(4, 11);
            } else {
                VMCNT(16); BAR();
                VMCNT(8); BAR();
                VMCNT(0); BAR();
                const int r32 = lane & 31, hh = lane >> 5;
                u32 dst, istep, zstep; kv_addr(true, cbk - 5, rt, lw >> 1, lw & 1, 1, r32, hh, dst, istep, zstep);
                lds_wait(sm, C_XF + lw * 4, 1);
                f32x4 a[2][4];
#pragma unroll
                for (int i = 0; i < 2; i++)
#pragma unroll
                    for (int q4 = 0; q4 < 4; q4++) a[i][q4] = *(LAS f32x4*)(sm + K_XFER + lw * 8192 + ((i * 4 + q4) * 64 + lane) * 16);
#pragma unroll
                for (int i = 0; i < 2; i++)
#pragma unroll
                    for (int z = 0; z < 2; z++) {
                        const f32x4 a0 = a[i][2 * z], a1 = a[i][2 * z + 1];
                        st128c(rw, dst + i * istep + z * zstep, (u32x4){phi2(a0[0], a0[1]), phi2(a0[2], a0[3]), phi2(a1[0], a1[1]), phi2(a1[2], a1[3])});
                    }
                publish_last(rw, sm, C_G1, 8, F_G1 + cbk * 16 + rt, tag, lane);
            }
        } else {
            u32x2 w4; p0_weights(w4, shk, tile, wave, lane);
            const int wm = wave >> 1, wn = wave & 1, r32 = lane & 31, hh = lane >> 5;
            K1Addr a0;
#pragma unroll
            for (int ks = 0; ks < 2; ks++) {
                a0.x[0][ks] = (64 * wm + r32) * 128 + (((2 * ks + hh) ^ ((r32 >> 1) & 7)) * 16);
                a0.x[1][ks] = a0.x[0][ks] ^ 64;
            }
#pragma unroll
            for (int jj = 0; jj < 2; jj++)
#pragma unroll
                for (int t = 0; t < 2; t++) {
                    const int blk = (lane >> 4) & 1, q = (lane & 15) >> 2, p = lane & 3, row = 8 * hh + 4 * t + q, ch = 8 * wn + 4 * jj + 2 * blk + (p >> 1);
                    const int f = ((row & 3) << 2) | ((row >> 2) & 3);
                    a0.w[jj][t] = row * 256 + ((ch ^ f) * 16) + 8 * (p & 1);
                }
            f32x16 acc[2][2];
#pragma unroll
            for (int i = 0; i < 2; i++)
#pragma unroll
                for (int jj = 0; jj < 2; jj++)
#pragma unroll
                    for (int e = 0; e < 16; e++) acc[i][jj][e] = 0.f;
            BAR();
            TS(0, 1);
            if (cbk < 5) {
                k1_loop<true>(sm, a0, acc, rw, p0_flag(tile), tag, wave, lane, hk, wo, tile, w4);
                __builtin_amdgcn_s_setprio(2);
                TS(0, 2);
                u32x4 qv[2][2][2];
#pragma unroll
                for (int jj = 0; jj < 2; jj++)
#pragma unroll
                    for (int ap = 0; ap < 2; ap++)
#pragma unroll
                        for (int i = 0; i < 2; i++) {
                            const f32x16& a = acc[i][jj];
                            const u32 x0 = phi2(a[8 * ap], a[8 * ap + 1]), y0 = phi2(a[8 * ap + 2], a[8 * ap + 3]);
                            const u32 x1 = phi2(a[8 * ap + 4], a[8 * ap + 5]), y1 = phi2(a[8 * ap + 6], a[8 * ap + 7]);
                            auto rx = __builtin_amdgcn_permlane32_swap(x0, x1, false, false);
                            auto ry = __builtin_amdgcn_permlane32_swap(y0, y1, false, false);
                            qv[jj][ap][i] = (u32x4){rx[0], ry[0], rx[1], ry[1]};
                        }
                if (QDELAY) { __builtin_amdgcn_s_setprio(0); for (int z = 0; z < QDELAY; z++) __builtin_amdgcn_s_sleep(8); }
#pragma unroll
                for (int jj = 0; jj < 2; jj++)
#pragma unroll
                    for (int ap = 0; ap < 2; ap++) {
                        const int U = 16 * cbk + 8 * wn + 4 * jj + 2 * ap + hh, pr = U / 5, u5 = U - 5 * pr;
                        const u32 dst = WS_P2 + (((pr * 128 + rt * 8 + 4 * wm + (r32 >> 4)) * 5 + u5) * 128 + (r32 & 15) * 8) * 2;
#pragma unroll
                        for (int i = 0; i < 2; i++) st128c(rw, dst + i * 2560, qv[jj][ap][i]);
                    }
            } else {
                k1_loop<false>(sm, a0, acc, rw, p0_flag(tile), tag, wave, lane, hk, wo, tile, w4);
                __builtin_amdgcn_s_setprio(2);
                TS(0, 2);
                const bool isk = cbk < 10;
                const int j5 = isk ? cbk - 5 : cbk - 10;
                if (isk) {
#pragma unroll
                    for (int i = 0; i < 2; i++)
#pragma unroll
                        for (int q4 = 0; q4 < 4; q4++)
                            *(LAS f32x4*)(sm + K_XFER + wave * 8192 + ((i * 4 + q4) * 64 + lane) * 16) = (f32x4){acc[i][1][4 * q4], acc[i][1][4 * q4 + 1], acc[i][1][4 * q4 + 2], acc[i][1][4 * q4 + 3]};
                    lds_arrive(sm, C_XF + wave * 4, lane);
                    u32 dst, istep, zstep; kv_addr(true, j5, rt, wm, wn, 0, r32, hh, dst, istep, zstep);
#pragma unroll
                    for (int i = 0; i < 2; i++)
#pragma unroll
                        for (int z = 0; z < 2; z++) {
                            const f32x16& a = acc[i][0];
                            st128c(rw, dst + i * istep + z * zstep,
                                   (u32x4){phi2(a[8 * z], a[8 * z + 1]), phi2(a[8 * z + 2], a[8 * z + 3]), phi2(a[8 * z + 4], a[8 * z + 5]), phi2(a[8 * z + 6], a[8 * z + 7])});
                        }
                } else {
#pragma unroll
                    for (int jj = 0; jj < 2; jj++) {
                        u32 dst, istep, zstep; kv_addr(false, j5, rt, wm, wn, jj, r32, hh, dst, istep, zstep);
#pragma unroll
                        for (int i = 0; i < 2; i++)
#pragma unroll
                            for (int z = 0; z < 2; z++) {
                                const f32x16& a = acc[i][jj];
                                st128c(rw, dst + i * istep + z * zstep,
                                       (u32x4){pk2(a[8 * z], a[8 * z + 1]), pk2(a[8 * z + 2], a[8 * z + 3]), pk2(a[8 * z + 4], a[8 * z + 5]), pk2(a[8 * z + 6], a[8 * z + 7])});
                            }
                    }
                }
            }
            LGKM0();
            TS(0, 25);
            publish_last(rw, sm, C_G1, is_conv ? 4 : 8, F_G1 + cbk * 16 + rt, tag, lane);
            __builtin_amdgcn_s_setprio(0);
            TS(0, 3);
            if (is_conv) {
                lds_wait(sm, C_G1, 4);
                wo_issue(wo, ct, 0, 12, wave, 4, lane);
            }
        }
    }
    if (!is_conv) {
        LGKM0(); BAR();
        if (ko >= 0) {
            const int h = ko >> 1, half = ko & 1;
            wo_issue(wo, ct, 0, 12, wave, 8, lane);
            kvo_block(sm, wave, lane, h, half, rw, tag, ws);
            TS(0, 26);
            VMCNT(0); BAR();
            if (tid == 0) set_flag(rw, F_O + half * 32 + h, tag);
            TS(0, 11);
            wo_issue(wo, ct, 12, 20, wave, 8, lane);
        } else {
            wo_issue(wo, ct, 0, 20, wave, 8, lane);
            if (ko == -2 && wave == 0) {
                wait_flags(rw, (int)F_G1 + lane, (int)F_G1 + 64 + lane, tag);
                wait_flags(rw, (int)F_G1 + 128 + lane, (int)F_G1 + 192 + lane, tag);
                if (lane == 0) set_flag(rw, F_EPOCH, tag);
            }
        }
    } else {
        VMCNT(0); BAR();
        if (wave >= 4) wo_issue(wo, ct, 12, 20, wave - 4, 4, lane);
    }
    TS(0, 16);
    final_block(sm, wave, lane, tid, st, ct, x, out, rw, tag, ws);
    TS(0, 19);
}

extern "C" void kernel_launch(void* const* d_in, const int* in_sizes, int n_in, void* d_out, int out_size, void* d_ws, size_t ws_size, hipStream_t stream) {
    const u16* x = (const u16*)d_in[0]; const u16* wqkv = (const u16*)d_in[1]; const u16* wo = (const u16*)d_in[2];
    const u16* kin = (const u16*)d_in[4]; const u16* vin = (const u16*)d_in[5]; const u16* shk = (const u16*)d_in[6];
    const u16* hk = (const u16*)d_in[7]; const u16* sD = (const u16*)d_in[8]; const u16* gw = (const u16*)d_in[9];
    (void)hipFuncSetAttribute((const void*)fused_kernel, hipFuncAttributeMaxDynamicSharedMemorySize, LDS_BYTES);
    fused_kernel<<<256, 512, LDS_BYTES, stream>>>(x, wqkv, wo, kin, vin, shk, hk, sD, gw, (u16*)d_out, (char*)d_ws);
}
```

```cpp
#include <hip/hip_runtime.h>
#include <stdint.h>

typedef unsigned short u16;
typedef unsigned int u32;
typedef __bf16 bf16x8 __attribute__((ext_vector_type(8)));
typedef __bf16 bf16x2 __attribute__((ext_vector_type(2)));
typedef float f32x4 __attribute__((ext_vector_type(4)));
typedef float f32x2 __attribute__((ext_vector_type(2)));
typedef float f32x16 __attribute__((ext_vector_type(16)));
typedef u32 u32x4 __attribute__((ext_vector_type(4)));
typedef u32 u32x2 __attribute__((ext_vector_type(2)));
typedef short s16x4 __attribute__((ext_vector_type(4)));
#define LAS __attribute__((address_space(3)))
#define GAS __attribute__((address_space(1)))
#define DI __device__ __forceinline__
typedef __amdgpu_buffer_rsrc_t rsrc_t;

constexpr int S = 2048, D = 640;
#define FS 256
#define POLL_SLEEP 1
#define ROT_ON 1

constexpr u32 WS_UT = 0;
constexpr u32 WS_YT = WS_UT + 2621440;
constexpr u32 WS_PK0 = WS_YT + 2621440;
constexpr u32 WS_V0 = WS_PK0 + 2097152;
constexpr u32 WS_XK = WS_V0 + 2097152;
constexpr u32 WS_XV = WS_XK + 524288;
constexpr u32 WS_P2 = WS_XV + 524288;
constexpr u32 WS_O = WS_P2 + 2621440;
constexpr u32 WS_G = WS_O + 2621440;
constexpr u32 WS_FLAG = WS_G + 8192;
constexpr u32 F_P0 = 0;
constexpr u32 F_G1 = 320;
constexpr u32 F_GATE = 560;
constexpr u32 F_O = 576;
constexpr u32 F_Y = 640;
constexpr u32 F_EPOCH = 896, F_TMO = 928;
constexpr u32 WS_STAMP = WS_FLAG + 1024 * FS;
constexpr u32 WS_BYTES = WS_STAMP + 256 * 32 * 16;

#define TS(w, id) do {} while (0)

DI u32 pk2(float a, float b) { f32x2 t = {a, b}; return __builtin_bit_cast(u32, __builtin_convertvector(t, bf16x2)); }
DI float blo(u32 u) { return __uint_as_float(u << 16); }
DI float bhi(u32 u) { return __uint_as_float(u & 0xffff0000u); }
DI float rbf(float f) { return blo(pk2(f, f)); }
DI bf16x8 asbf(u32x4 v) { return __builtin_bit_cast(bf16x8, v); }
DI f32x4 mfma16(u32x4 a, u32x4 b, f32x4 c) { return __builtin_amdgcn_mfma_f32_16x16x32_bf16(asbf(a), asbf(b), c, 0, 0, 0); }
DI f32x16 mfma32(u32x4 a, u32x4 b, f32x16 c) { return __builtin_amdgcn_mfma_f32_32x32x16_bf16(asbf(a), asbf(b), c, 0, 0, 0); }

DI void glds16(const void* sbase, u32 voff, u32 lds) {
    asm volatile("s_mov_b32 m0, %2\n\ts_nop 0\n\tglobal_load_lds_dwordx4 %0, %1" :: "v"(voff), "s"(sbase), "s"(lds) : "memory");
}
DI void glds16nt(const void* sbase, u32 voff, u32 lds) {
    asm volatile("s_mov_b32 m0, %2\n\ts_nop 0\n\tglobal_load_lds_dwordx4 %0, %1 nt" :: "v"(voff), "s"(sbase), "s"(lds) : "memory");
}
DI void touch(const void* sbase, u32 voff) {
    asm volatile("s_mov_b32 m0, %2\n\ts_nop 0\n\tglobal_load_lds_dword %0, %1" :: "v"(voff), "s"(sbase), "s"(156928u) : "memory");
}
#define NT_KV 0
#define NT_X 0
#define NT_H 0
#define PREF1 1
#define PREF2 0
#define PREFC 1
#define CODE_LINES 345
#define PREFT 2
#define PREFKV 0
#define PREF3 1
#define PREFTIN 1
#define QDELAY 0
DI void gld64(u32x2& d, const void* sbase, u32 voff) { asm volatile("global_load_dwordx2 %0, %1, %2" : "=v"(d) : "v"(voff), "s"(sbase) : "memory"); }
DI void gld128(u32x4& d, const void* sbase, u32 voff) { asm volatile("global_load_dwordx4 %0, %1, %2" : "=v"(d) : "v"(voff), "s"(sbase) : "memory"); }
DI void gld128c(u32x4& d, const void* sbase, u32 voff) { asm volatile("global_load_dwordx4 %0, %1, %2 sc1" : "=v"(d) : "v"(voff), "s"(sbase) : "memory"); }
DI void gld32c(u32& d, const void* sbase, u32 voff) { asm volatile("global_load_dword %0, %1, %2 sc1" : "=v"(d) : "v"(voff), "s"(sbase) : "memory"); }
#define PIN(x) asm volatile("" : "+v"(x))
#define SB() __builtin_amdgcn_sched_barrier(0)
DI u32x4 ld128c(rsrc_t r, u32 off) { return __builtin_amdgcn_raw_buffer_load_b128(r, off, 0, 16); }
DI u32x2 ld64c(rsrc_t r, u32 off) { return __builtin_amdgcn_raw_buffer_load_b64(r, off, 0, 16); }
DI u32 ld32c(rsrc_t r, u32 off) { return __builtin_amdgcn_raw_buffer_load_b32(r, off, 0, 16); }
DI void st128c(rsrc_t r, u32 off, u32x4 v) { __builtin_amdgcn_raw_buffer_store_b128(v, r, off, 0, 16); }
DI void st64c(rsrc_t r, u32 off, u32x2 v) { __builtin_amdgcn_raw_buffer_store_b64(v, r, off, 0, 16); }
DI void st32c(rsrc_t r, u32 off, u32 v) { __builtin_amdgcn_raw_buffer_store_b32(v, r, off, 0, 16); }
DI u32x4 lds128(LAS char* sm, u32 a) { return *(LAS u32x4*)(sm + a); }
DI u32x2 lds64(LAS char* sm, u32 a) { return *(LAS u32x2*)(sm + a); }
DI u32x2 ldstr(LAS char* sm, u32 a) { return __builtin_bit_cast(u32x2, __builtin_amdgcn_ds_read_tr16_b64_v4i16((LAS s16x4*)(sm + a))); }
#define VMCNT(n) asm volatile("s_waitcnt vmcnt(" #n ")" ::: "memory")
#define LGKM0() asm volatile("s_waitcnt lgkmcnt(0)" ::: "memory")
#define BAR() do { asm volatile("" ::: "memory"); __builtin_amdgcn_s_barrier(); asm volatile("" ::: "memory"); } while (0)
#define MEMF() asm volatile("" ::: "memory")

DI void set_flag(rsrc_t r, u32 idx, u32 tag) { st32c(r, WS_FLAG + idx * FS, tag); }
DI void wait_flags(rsrc_t r, int idx0, int idx1, u32 tag) {
    unsigned long long t0 = 0;
    for (u32 it = 0;; it++) {
        u32 a = tag, b = tag;
        if (idx0 >= 0) a = ld32c(r, WS_FLAG + idx0 * FS);
        if (idx1 >= 0) b = ld32c(r, WS_FLAG + idx1 * FS);
        if (__all(a == tag && b == tag)) break;
        if (POLL_SLEEP) __builtin_amdgcn_s_sleep(POLL_SLEEP);
        if ((it & 63u) == 0u) {
            const unsigned long long t = __builtin_amdgcn_s_memrealtime();
            if (it == 0) t0 = t;
            else if (t - t0 > 2000000ull) { set_flag(r, F_TMO, tag); break; }
        }
    }
    MEMF();
}
DI void lds_arrive(LAS char* sm, u32 cnt, int lane) { LGKM0(); if (lane == 0) __hip_atomic_fetch_add((LAS u32*)(sm + cnt), 1u, __ATOMIC_RELAXED, __HIP_MEMORY_SCOPE_WORKGROUP); MEMF(); }
DI void lds_wait(LAS char* sm, u32 cnt, u32 n) { MEMF(); while (*(volatile LAS u32*)(sm + cnt) < n) __builtin_amdgcn_s_sleep(0); MEMF(); }
DI void publish_last(rsrc_t r, LAS char* sm, u32 cnt, u32 nw, u32 flag, u32 tag, int lane) {
    VMCNT(0);
    if (lane == 0) {
        const u32 old = __hip_atomic_fetch_add((LAS u32*)(sm + cnt), 1u, __ATOMIC_RELAXED, __HIP_MEMORY_SCOPE_WORKGROUP);
        if (old == nw - 1) set_flag(r, flag, tag);
    }
    MEMF();
}

DI u32 phi2(float a, float b) {
    const f32x2 t = {a, b}, s = t * 1.4426950408889634f;
    const float ea = __builtin_amdgcn_fmed3f(__builtin_amdgcn_exp2f(s.x), 0.f, 1.f), eb = __builtin_amdgcn_fmed3f(__builtin_amdgcn_exp2f(s.y), 0.f, 1.f);
    const float ma = __int_as_float(max(__float_as_int(a), 0)), mb = __int_as_float(max(__float_as_int(b), 0));
    const f32x2 r = ((f32x2){ea, eb} + (f32x2){ma, mb}) + 1e-6f;
    return pk2(r.x, r.y);
}

constexpr int K1_STAGE = 32768;
constexpr int P0_K = 131072, P0_V = P0_K + 9216, P0_OUT = P0_V + 8192;
constexpr int L_CNT = 156672;
constexpr int C_G1 = L_CNT, C_Y = L_CNT + 4, C_P0A = L_CNT + 8, C_P0B = L_CNT + 12, C_P0C = L_CNT + 16, C_FY = L_CNT + 20, C_FY2 = L_CNT + 24, C_FO = L_CNT + 28, C_FR = L_CNT + 32, C_XF = L_CNT + 36;
constexpr int CV_PITCH = 4160;
constexpr int CV_U = 4 * CV_PITCH;
constexpr int CV_WAVE = CV_U + 4608;
constexpr int CV_BASE = 65536;
constexpr int WO_HI = 65536;
constexpr int F_YY = 110592;
constexpr int F_Y2 = F_YY + 10240;
constexpr int F_GG = F_Y2 + 11264;
constexpr int KO_RED = 110592;
constexpr int KO_KV = KO_RED + 32768;
constexpr int RED_PITCH = 336;
constexpr int LDS_BYTES = 160 * 1024;
DI u32 p0_flag(int tile) { return F_P0 + (tile % 10) * 32 + tile / 10; }
DI constexpr u32 wo_off(int kap) { return kap < 12 ? kap * 5120 : WO_HI + (kap - 12) * 5120; }

DI void p0_issue(const u16* kin, const u16* vin, int tile, int lw, int lane, u32 base) {
    const int cg = tile % 10, rg = tile / 10, s0 = rg * 64;
    const u32 col = cg * 128 + (lane & 7) * 16;
#pragma unroll
    for (int z = 0; z < 2; z++) {
        const int n = 2 * lw + z;
        int r = s0 - 4 + n * 8 + (lane >> 3); r = r < 0 ? 0 : r;
        (NT_KV ? glds16nt : glds16)(kin, (u32)(r * 1280) + col, base + n * 1024);
        (NT_KV ? glds16nt : glds16)(vin, (u32)((s0 + n * 8 + (lane >> 3)) * 1280) + col, base + 9216 + n * 1024);
    }
    {
        int r = s0 + 60 + (lane >> 3); r = r > S - 1 ? S - 1 : r;
        (NT_KV ? glds16nt : glds16)(kin, (u32)(r * 1280) + col, base + 8 * 1024);
    }
}
DI float dot2(u32 a, u32 b, float c) { return __builtin_amdgcn_fdot2_f32_bf16(__builtin_bit_cast(bf16x2, a), __builtin_bit_cast(bf16x2, b), c, false); }
DI void p0_compute(LAS char* sm, u32x2 w4, int tile, int wave, int lane, u32 base, u32 outb, char* ws = nullptr) {
    const int s0 = (tile / 10) * 64;
    const int G = wave * 4 + (lane >> 4), cb = G & 3, i = lane & 15, q = i >> 2, p = i & 3;
    const u32 wa = __builtin_amdgcn_alignbit(w4.y, w4.y, 16), wb = __builtin_amdgcn_alignbit(w4.x, w4.x, 16);
    const int ch = 16 * cb + i;
    u32x2 kq[2][3], vq[2][2];
#pragma unroll
    for (int z = 0; z < 2; z++) {
        const int o = (G >> 2) + 4 * z;
        const u32 ka = base + (8 * o + q) * 128 + 32 * cb + 8 * p, va = ka + 9216;
        kq[z][0] = ldstr(sm, ka); kq[z][1] = ldstr(sm, ka + 512); kq[z][2] = ldstr(sm, ka + 1024);
        vq[z][0] = ldstr(sm, va); vq[z][1] = ldstr(sm, va + 512);
    }
#pragma unroll
    for (int z = 0; z < 2; z++) {
        const int o = (G >> 2) + 4 * z;
        u32 K[6] = {kq[z][0].x, kq[z][0].y, kq[z][1].x, kq[z][1].y, kq[z][2].x, kq[z][2].y};
        if (s0 == 0 && o == 0) { K[0] = 0; K[1] = 0; }
        u32 Sh[5];
#pragma unroll
        for (int e = 0; e < 5; e++) Sh[e] = __builtin_amdgcn_alignbit(K[e + 1], K[e], 16);
        const u32 V[4] = {vq[z][0].x, vq[z][0].y, vq[z][1].x, vq[z][1].y};
        u32 r[4];
#pragma unroll
        for (int e = 0; e < 4; e++) {
            const float k0 = dot2(Sh[e], wa, dot2(Sh[e + 1], wb, 0.f)), k1 = dot2(K[e + 1], wa, dot2(K[e + 2], wb, 0.f));
            r[e] = pk2(k0 * blo(V[e]), k1 * bhi(V[e]));
        }
        *(LAS u32x4*)(sm + outb + ch * 128 + ((o ^ (ch & 7)) * 16)) = (u32x4){r[0], r[1], r[2], r[3]};
    }
}
DI void p0_store(LAS char* sm, rsrc_t rw, int tile, int t256, u32 outb) {
    const int cg = tile % 10, s0 = (tile / 10) * 64;
#pragma unroll
    for (int z = 0; z < 2; z++) {
        const int ch2 = (t256 >> 3) + 32 * z, o2 = t256 & 7;
        const u32x4 t4 = lds128(sm, outb + ch2 * 128 + ((o2 ^ (ch2 & 7)) * 16));
        st128c(rw, WS_UT + (cg * 64 + ch2) * 4096 + (s0 + 8 * o2) * 2, t4);
    }
}
DI void p0_weights(u32x2& w4, const u16* shk, int tile, int wave, int lane) {
    const int cg = tile % 10, cb = (wave * 4 + (lane >> 4)) & 3;
    gld64(w4, shk, (u32)((cg * 64 + 16 * cb + (lane & 15)) * 8));
}

struct K1Set { u32x4 xa[2][2]; u32x2 wl[2][2], wh[2][2]; };
struct K1Addr { u32 x[2][2]; u32 w[2][2]; };
template <bool QT, int WO, int XO>
DI void k1_phase(LAS char* sm, f32x16 (&acc)[2][2], const K1Set& cur, K1Set& nxt, const u32 (&xb)[2], const u32 (&wb)[2][2]) {
#pragma unroll
    for (int n = 0; n < 8; n++) {
        const int ks = n >> 2, i = (n >> 1) & 1, jj = n & 1;
        const u32x4 wf = {cur.wl[ks][jj].x, cur.wl[ks][jj].y, cur.wh[ks][jj].x, cur.wh[ks][jj].y};
        acc[i][jj] = QT ? mfma32(wf, cur.xa[ks][i], acc[i][jj]) : mfma32(cur.xa[ks][i], wf, acc[i][jj]);
        if (n == 0) { nxt.wl[0][0] = ldstr(sm, wb[0][0] + WO); nxt.wh[0][0] = ldstr(sm, wb[0][1] + WO); }
        if (n == 1) { nxt.xa[0][0] = lds128(sm, xb[0] + XO); nxt.wl[0][1] = ldstr(sm, wb[1][0] + WO); }
        if (n == 2) { nxt.wh[0][1] = ldstr(sm, wb[1][1] + WO); nxt.xa[0][1] = lds128(sm, xb[0] + XO + 4096); }
        if (n == 3) { nxt.wl[1][0] = ldstr(sm, wb[0][0] + WO + 4096); nxt.wh[1][0] = ldstr(sm, wb[0][1] + WO + 4096); }
        if (n == 4) { nxt.xa[1][0] = lds128(sm, xb[1] + XO); nxt.wl[1][1] = ldstr(sm, wb[1][0] + WO + 4096); }
        if (n == 5) { nxt.wh[1][1] = ldstr(sm, wb[1][1] + WO + 4096); nxt.xa[1][1] = lds128(sm, xb[1] + XO + 4096); }
        SB();
    }
}
template <bool QT, int ST>
DI void k1_chunk(LAS char* sm, f32x16 (&acc)[2][2], K1Set& s0, K1Set& s1, const K1Addr& a0, const K1Addr& a1, bool bar, bool drain = false) {
    constexpr int SN = (ST + 1) & 3;
    const K1Addr& ac = ST < 2 ? a0 : a1; const K1Addr& an = SN < 2 ? a0 : a1;
    k1_phase<QT, (ST & 1) * K1_STAGE + 8192, (ST & 1) * K1_STAGE + 16384>(sm, acc, s0, s1, ac.x[1], ac.w);
    if (drain) VMCNT(0);
    if (bar) { LGKM0(); BAR(); }
    k1_phase<QT, (SN & 1) * K1_STAGE, (SN & 1) * K1_STAGE + 16384>(sm, acc, s1, s0, an.x[0], an.w);
}
template <bool QT>
DI void k1_loop(LAS char* sm, const K1Addr& a0, f32x16 (&acc)[2][2], rsrc_t rw, u32 pflag, u32 tag, int wave, int lane, const u16* hk, const u16* wo, int n240, u32x2 w4) {
    K1Addr a1;
#pragma unroll
    for (int i = 0; i < 2; i++)
#pragma unroll
        for (int z = 0; z < 2; z++) { a1.x[i][z] = a0.x[i][z] + 65536; a1.w[i][z] = a0.w[i][z] + 65536; }
    K1Set s0, s1;
#pragma unroll
    for (int ks = 0; ks < 2; ks++)
#pragma unroll
        for (int jj = 0; jj < 2; jj++) {
            s0.wl[ks][jj] = ldstr(sm, a0.w[jj][0] + ks * 4096); s0.wh[ks][jj] = ldstr(sm, a0.w[jj][1] + ks * 4096);
            s0.xa[ks][jj] = lds128(sm, a0.x[0][ks] + 16384 + jj * 4096);
        }
    SB();
#pragma unroll 1
    for (int c = 0; c < 8; c += 4) {
        k1_chunk<QT, 0>(sm, acc, s0, s1, a0, a1, true);
        if (c == 4) p0_store(sm, rw, n240, wave * 64 + lane, P0_OUT);
        k1_chunk<QT, 1>(sm, acc, s0, s1, a0, a1, true);
        k1_chunk<QT, 2>(sm, acc, s0, s1, a0, a1, true);
        k1_chunk<QT, 3>(sm, acc, s0, s1, a0, a1, true, c == 4);
        if (c == 4 && wave == 0 && lane == 0) set_flag(rw, pflag, tag);
        if (c == 0) { VMCNT(0); PIN(w4); p0_compute(sm, w4, n240, wave, lane, P0_K, P0_OUT); }
        if (PREF2 && c == 0 && wave >= 2) {
            if (wave == 2) { const u32 l_ = n240 * 86 + lane; touch(hk, (l_ < 20480 ? l_ : 20479) * 128); }
            else { const u32 l_ = n240 * 86 + 64 + lane, m_ = n240 * 27 + lane - 22;
                   if (lane < 22) touch(hk, (l_ < 20480 ? l_ : 20479) * 128); else if (lane < 49) touch(wo, (m_ < 6400 ? m_ : 6399) * 128); }
        }
    }
    k1_chunk<QT, 0>(sm, acc, s0, s1, a0, a1, true);
    k1_chunk<QT, 1>(sm, acc, s0, s1, a0, a1, false);
}

DI void kv_addr(bool isk, int j5, int rt, int wm, int wn, int jj, int r32, int hh, u32& dst, u32& istep, u32& zstep) {
    const int cc = 128 * j5 + 64 * wn + 32 * jj + r32, h = cc / 20, d = cc - 20 * h;
    const int sb32 = rt * 4 + 2 * wm;
    const bool lo = d < 16;
    dst = lo ? (isk ? WS_PK0 : WS_V0) + (((h * 64 + sb32) * 4 + hh) * 128 + d * 8) * 2
             : (isk ? WS_XK : WS_XV) + (((h * 64 + sb32) * 4 + hh) * 32 + (d - 16) * 8) * 2;
    istep = lo ? 1024 : 256; zstep = lo ? 512 : 128;
}
constexpr int K_XFER = 65536;

DI void wo_issue(const u16* wo, int ct, int k0, int k1, int w, int nw, int lane) {
    u32 vo[5], vs[5];
#pragma unroll
    for (int i = 0; i < 5; i++) {
        const int n = 64 * i + lane, rho = n / 10, c = n - 10 * rho, g = 2 * (rho >> 4) + ((rho >> 2) & 1), r8 = 4 * ((rho >> 3) & 1) + (rho & 3);
        vo[i] = (u32)((8 * (g < 2 ? g : g + 1) + r8) * 1280 + ct * 160 + c * 16);
        vs[i] = (u32)((40 * g + 16 + r8) * 1280 + ct * 160 + c * 16);
    }
#pragma unroll 1
    for (int kap = k0 + w; kap < k1; kap += nw) {
        const u32 dst = kap < 12 ? kap * 5120 : WO_HI + (kap - 12) * 5120;
        if (kap < 16) {
            const char* src = (const char*)wo + (size_t)kap * 51200;
#pragma unroll
            for (int i = 0; i < 5; i++) glds16(src, vo[i], dst + i * 1024);
        } else {
            const char* src = (const char*)wo + (size_t)(kap - 16) * 204800;
#pragma unroll
            for (int i = 0; i < 5; i++) glds16(src, vs[i], dst + i * 1024);
        }
    }
}

struct CvSet { u32x2 fl[8], fh[8]; u32x4 g[8]; };
DI void cv_load(LAS char* sm, CvSet& s, u32 fa, u32 ga) {
#pragma unroll
    for (int k1 = 0; k1 < 8; k1++) { s.fl[k1] = ldstr(sm, fa + 512 * k1 + 8); s.fh[k1] = ldstr(sm, fa + 512 * k1); s.g[k1] = lds128(sm, ga + 512 * k1); }
}
DI void cv_phase(LAS char* sm, f32x4 (&Y)[8], const CvSet& cur, CvSet& nxt, u32 fa, u32 ga) {
    PIN(fa); PIN(ga);
    SB();
    int n = 0;
#pragma unroll
    for (int e = 0; e < 8; e++)
#pragma unroll
        for (int k1 = 0; k1 + e < 8; k1++) {
            const u32x4 F = {cur.fl[k1].x, cur.fl[k1].y, cur.fh[k1].x, cur.fh[k1].y};
            Y[k1 + e] = mfma16(F, cur.g[e], Y[k1 + e]);
            if (n == 0) nxt.g[0] = lds128(sm, ga);
            else if (n <= 16) { const int r = (n - 1) >> 1; if ((n - 1) & 1) nxt.fh[r] = ldstr(sm, fa + 512 * r); else nxt.fl[r] = ldstr(sm, fa + 512 * r + 8); }
            else if (n < 24) nxt.g[n - 16] = lds128(sm, ga + 512 * (n - 16));
            n++;
            SB();
        }
}
struct CvIn { u32x4 hd[4]; u32x2 hm[4]; };
DI void conv_fetch_h(CvIn& in, const u16* hk, int c, int lane) {
    const char* hb = (const char*)hk + (size_t)c * 4096;
#pragma unroll
    for (int z = 0; z < 4; z++) {
        const int i = lane + 64 * z;
        if (NT_H) asm volatile("global_load_dwordx4 %0, %1, %2 nt" : "=v"(in.hd[z]) : "v"((u32)(i * 16)), "s"(hb) : "memory"); else gld128(in.hd[z], hb, (u32)(i * 16));
        gld64(in.hm[z], hb, (u32)(i > 0 ? i * 16 - 8 : 0));
    }
}
DI void conv_build_h(LAS char* sm0, CvIn& in, int w, int lane) {
    LAS char* sm = sm0 + CV_BASE + w * CV_WAVE;
#pragma unroll
    for (int z = 0; z < 4; z++) { PIN(in.hd[z]); PIN(in.hm[z]); }
    if (lane < 32) *(LAS u32x4*)(sm + CV_U + lane * 16) = (u32x4){0, 0, 0, 0};
    if (lane < 8) *(LAS u32x4*)(sm + (lane >> 1) * CV_PITCH + (lane & 1) * 16) = (u32x4){0, 0, 0, 0};
#pragma unroll
    for (int z = 0; z < 4; z++) {
        const int i = lane + 64 * z;
        u32 m2 = in.hm[z].x, m1 = in.hm[z].y;
        if (i == 0) { m2 = 0; m1 = 0; }
        const u32 d0 = in.hd[z].x, d1 = in.hd[z].y, d2 = in.hd[z].z, d3 = in.hd[z].w;
        const u32 om2 = __builtin_amdgcn_alignbit(m1, m2, 16), om1 = __builtin_amdgcn_alignbit(d0, m1, 16), o0 = __builtin_amdgcn_alignbit(d1, d0, 16),
                  o1 = __builtin_amdgcn_alignbit(d2, d1, 16), o2 = __builtin_amdgcn_alignbit(d3, d2, 16);
        LAS char* p = sm + 32 + i * 16;
        *(LAS u32x4*)(p) = (u32x4){d0, d1, d2, d3};
        *(LAS u32x4*)(p + CV_PITCH) = (u32x4){om1, o0, o1, o2};
        *(LAS u32x4*)(p + 2 * CV_PITCH) = (u32x4){m1, d0, d1, d2};
        *(LAS u32x4*)(p + 3 * CV_PITCH) = (u32x4){om2, om1, o0, o1};
    }
}
DI void conv_run(LAS char* sm0, float Dc, int w, int lane, int c, rsrc_t rw, char* ws) {
    LAS char* sm = sm0;
    LGKM0();
    TS(4, 9);
    const int fr = lane & 15, g = lane >> 4, q = fr >> 2, p = lane & 3;
    sm = sm0;
    const u32 fa = CV_BASE + w * CV_WAVE + q * CV_PITCH + 2 * (32 - 8 * g + 4 * p) - 8;
    const u32 ga = CV_BASE + w * CV_WAVE + CV_U + 480 + 32 * fr + 16 * g;
    f32x4 Y[8];
#pragma unroll
    for (int i = 0; i < 8; i++) Y[i] = (f32x4){0, 0, 0, 0};
    CvSet A, B;
    cv_load(sm, A, fa, ga);
    SB();
#pragma unroll 1
    for (int k0 = 0; k0 < 8; k0 += 2) {
        cv_phase(sm, Y, A, B, fa + 64 * (k0 + 1), ga - 64 * (k0 + 1));
        cv_phase(sm, Y, B, A, fa + 64 * (k0 + 2), ga - 64 * (k0 + 2));
    }
    TS(4, 10);
    sm = sm0 + CV_BASE + w * CV_WAVE;
    {
        u32x2 u4[8];
#pragma unroll
        for (int ib = 0; ib < 8; ib++) u4[ib] = lds64(sm, CV_U + 512 + 512 * ib + 32 * fr + 8 * g);
#pragma unroll
        for (int ib = 0; ib < 8; ib++) {
            const u32x2 r = {pk2(fmaf(blo(u4[ib].x), Dc, Y[ib][0]), fmaf(bhi(u4[ib].x), Dc, Y[ib][1])), pk2(fmaf(blo(u4[ib].y), Dc, Y[ib][2]), fmaf(bhi(u4[ib].y), Dc, Y[ib][3]))};
            *(LAS u32x2*)(sm + 512 * ib + 32 * fr + 8 * g) = r;
        }
    }
    LGKM0();
    u32x4 y8[4];
#pragma unroll
    for (int z = 0; z < 4; z++) y8[z] = lds128(sm, z * 1024 + lane * 16);
#pragma unroll
    for (int z = 0; z < 4; z++) st128c(rw, WS_YT + c * 4096 + z * 1024 + lane * 16, y8[z]);
}

DI void kvo_block(LAS char* sm, int wave, int lane, int h, int half, rsrc_t rw, u32 tag, char* ws) {
    const int fr = lane & 15, g = lane >> 4;
    const int j0 = (20 * h) >> 7, j1 = (20 * h + 19) >> 7;
    wait_flags(rw, lane < 8 ? (int)F_G1 + 16 * (((lane & 2) ? 10 : 5) + ((lane & 1) ? j1 : j0)) + 2 * wave + (lane >> 2)
                            : lane < 10 ? (int)F_G1 + 16 * ((lane & 1) ? j1 : j0) + 8 * half + wave : -1, -1, tag);
    TS(0, 8);
    const u32 pk0 = WS_PK0 + (h * 64 + 8 * wave) * 1024 + lane * 16, v0 = pk0 + (WS_V0 - WS_PK0);
    const u32 xx = ((fr & 4) ? WS_XV : WS_XK) + ((h * 64 + 8 * wave + (fr >> 3)) * 4 + g) * 64 + (fr & 3) * 16;
    u32x4 A[8], B[8], X[4], Q[8];
#pragma unroll
    for (int i = 0; i < 8; i++) { A[i] = ld128c(rw, pk0 + i * 1024); B[i] = ld128c(rw, v0 + i * 1024); if ((i & 1) == 0) X[i >> 1] = ld128c(rw, xx + (i >> 1) * 512); }
    const int odd = h & 1, pr = h >> 1;
    const int u5 = (g < 3 ? g : 2) + 2 * odd;
    const int sb0 = half * 64 + wave * 8;
    const u32 pq = WS_P2 + (((pr * 128 + sb0) * 5 + u5) * 128 + fr * 8) * 2;
    MEMF(); SB();
    f32x4 a00 = {0, 0, 0, 0}, a0x = a00, ax0 = a00, axx = a00;
    const u32 cst = fr == 8 ? 0x3F803F80u : 0u;
#pragma unroll
    for (int i = 0; i < 8; i++) {
        u32x4 xv = X[i >> 1];
        if (i & 1) {
#pragma unroll
            for (int e = 0; e < 4; e++) xv[e] = (u32)__builtin_amdgcn_update_dpp(0, (int)xv[e], 0x108, 0xf, 0xf, false);
        }
        if (fr >= 8) xv = (u32x4){cst, cst, cst, cst};
        a00 = mfma16(A[i], B[i], a00); a0x = mfma16(A[i], xv, a0x); ax0 = mfma16(xv, B[i], ax0); axx = mfma16(xv, xv, axx);
    }
    PIN(a00); PIN(a0x); PIN(ax0); PIN(axx);
    SB();
#pragma unroll
    for (int i = 0; i < 8; i++) Q[i] = ld128c(rw, pq + i * 1280);
    MEMF(); SB();
    TS(0, 9);
    *(LAS f32x4*)(sm + KO_RED + ((wave * 4 + 0) * 64 + lane) * 16) = a00;
    *(LAS f32x4*)(sm + KO_RED + ((wave * 4 + 1) * 64 + lane) * 16) = a0x;
    *(LAS f32x4*)(sm + KO_RED + ((wave * 4 + 2) * 64 + lane) * 16) = ax0;
    *(LAS f32x4*)(sm + KO_RED + ((wave * 4 + 3) * 64 + lane) * 16) = axx;
    if (wave >= 4 && (wave - 4) * 64 + lane < 189) *(LAS u32x2*)(sm + KO_KV + ((wave - 4) * 64 + lane) * 8) = (u32x2){0, 0};
    LGKM0(); BAR();
    if (wave < 4) {
        const int tile = wave;
        f32x4 s = *(LAS f32x4*)(sm + KO_RED + (tile * 64 + lane) * 16);
#pragma unroll
        for (int w2 = 1; w2 < 8; w2++) s += *(LAS f32x4*)(sm + KO_RED + ((w2 * 4 + tile) * 64 + lane) * 16);
        const int e = (tile & 1) ? 12 + fr : fr;
        const bool colok = (tile & 1) ? (fr >= 4 && fr <= 8) : true;
        const bool rowok = (tile & 2) ? (g == 0) : true;
        const int d0 = (tile & 2) ? 16 : 4 * g;
        if (colok && rowok) *(LAS u32x2*)(sm + KO_KV + e * 72 + (d0 + 4) * 2) = (u32x2){pk2(s[0], s[1]), pk2(s[2], s[3])};
    }
    LGKM0(); BAR();
    u32x4 A0, A1[4], A2;
    {
        const u32 dofs = KO_KV + (8 * g - 4 * odd + 4) * 2;
        const u32 r0 = (4 * odd + fr) * 72, r1 = ((odd ? 0 : 16) + (fr & 3)) * 72;
        const u32x2 p0 = lds64(sm, dofs + r0), p1 = lds64(sm, dofs + r0 + 8);
        const u32x2 q0 = lds64(sm, dofs + r1), q1 = lds64(sm, dofs + r1 + 8);
        const u32x2 z0 = lds64(sm, dofs + 20 * 72), z1 = lds64(sm, dofs + 20 * 72 + 8);
        A0 = (u32x4){p0.x, p0.y, p1.x, p1.y}; A2 = (u32x4){z0.x, z0.y, z1.x, z1.y};
#pragma unroll
        for (int z = 0; z < 4; z++) { const bool m = (fr >> 2) == z; A1[z] = (u32x4){m ? q0.x : 0u, m ? q0.y : 0u, m ? q1.x : 0u, m ? q1.y : 0u}; }
    }
    TS(0, 10);
    const u32 offA = WS_O + ((pr * 4 + 2 * odd + (g >> 1)) * 128 + fr * 8) * 2 + (g & 1) * 20480;
    const u32 offX = WS_O + (((16 + (pr >> 2)) * 4 + (pr & 3)) * 128 + odd * 64 + fr * 4) * 2;
    const f32x4 zero = {0, 0, 0, 0};
#pragma unroll
    for (int i4 = 0; i4 < 2; i4++) {
        f32x4 n1 = zero; float rden[4]; u32x2 ov[4];
#pragma unroll
        for (int z = 0; z < 4; z++) {
            const int i = 4 * i4 + z;
            const f32x4 n0 = mfma16(A0, Q[i], zero);
            const f32x4 dn = mfma16(A2, Q[i], zero);
            n1 = mfma16(A1[z], Q[i], n1);
            const float rd = __builtin_amdgcn_rcpf(dn[0] + 1e-6f);
            rden[z] = rd;
            ov[z] = (u32x2){pk2(n0[0] * rd, n0[1] * rd), pk2(n0[2] * rd, n0[3] * rd)};
        }
#pragma unroll
        for (int z = 0; z < 4; z += 2) {
            auto rx = __builtin_amdgcn_permlane16_swap(ov[z].x, ov[z + 1].x, false, false);
            auto ry = __builtin_amdgcn_permlane16_swap(ov[z].y, ov[z + 1].y, false, false);
            st128c(rw, offA + (sb0 + 4 * i4 + z) * 20480, (u32x4){rx[0], ry[0], rx[1], ry[1]});
        }
        const float rd = g == 0 ? rden[0] : g == 1 ? rden[1] : g == 2 ? rden[2] : rden[3];
        st64c(rw, offX + (sb0 + 4 * i4 + g) * 20480, (u32x2){pk2(n1[0] * rd, n1[1] * rd), pk2(n1[2] * rd, n1[3] * rd)});
    }
}

DI void gate_block(int wave, int lane, int m, const u16* x, const u16* gw, rsrc_t rw) {
    const int sub = lane & 15, r4 = lane >> 4;
    u32x4 gq[5], xr[4][5];
#pragma unroll
    for (int jj = 0; jj < 5; jj++) gq[jj] = *(const GAS u32x4*)((const GAS char*)gw + (sub + 16 * jj) * 16);
#pragma unroll
    for (int it = 0; it < 4; it++)
#pragma unroll
        for (int jj = 0; jj < 5; jj++) xr[it][jj] = *(const GAS u32x4*)((const GAS char*)x + (size_t)(128 * m + 16 * wave + 4 * it + r4) * 1280 + (sub + 16 * jj) * 16);
    MEMF(); SB();
#pragma unroll
    for (int it = 0; it < 4; it++) {
        float a = 0.f;
#pragma unroll
        for (int jj = 0; jj < 5; jj++)
#pragma unroll
            for (int e = 0; e < 4; e++) { a = fmaf(blo(xr[it][jj][e]), blo(gq[jj][e]), a); a = fmaf(bhi(xr[it][jj][e]), bhi(gq[jj][e]), a); }
        a += __shfl_xor(a, 1); a += __shfl_xor(a, 2); a += __shfl_xor(a, 4); a += __shfl_xor(a, 8);
        a = rbf(a);
        const float g = rbf(__builtin_amdgcn_rcpf(1.f + __builtin_amdgcn_exp2f(-1.4426950408889634f * a)));
        if (sub == 0) st32c(rw, WS_G + (128 * m + 16 * wave + 4 * it + r4) * 4, __float_as_uint(g));
    }
}

DI void final_block(LAS char* sm, int wave, int lane, int tid, int st, int ct, const u16* x, u16* out, rsrc_t rw, u32 tag, char* ws) {
    const int fr = lane & 15, g = lane >> 4;
    u32x4 xv[2];
#pragma unroll
    for (int r = 0; r < 2; r++) {
        int it = tid + 512 * r; it = it < 640 ? it : 639;
        const int s = it / 10, u = it - 10 * s;
        xv[r] = *(const GAS u32x4*)((const GAS char*)x + (size_t)(st * 64 + s) * 1280 + ct * 160 + u * 16);
    }
    VMCNT(0); BAR();
    if (wave < 4) {
        const int kh = wave & 1, sh = wave >> 1;
        wait_flags(rw, lane < 16 ? (int)F_O + (st >> 4) * 32 + 16 * kh + lane : -1, -1, tag);
        TS(0, 17);
        const u32 ob = WS_O + ((st * 4 + 2 * sh) * 20 + 8 * kh) * 1024 + lane * 16;
        const u32 os = WS_O + ((st * 4 + 2 * sh) * 20 + 16 + 2 * kh) * 1024 + g * 256 + fr * 8;
        u32x4 B[2][10];
#pragma unroll
        for (int kl = 0; kl < 8; kl++) { B[0][kl] = ld128c(rw, ob + kl * 1024); B[1][kl] = ld128c(rw, ob + 20480 + kl * 1024); }
#pragma unroll
        for (int kl = 0; kl < 2; kl++)
#pragma unroll
            for (int z = 0; z < 2; z++) {
                const u32x2 h0 = ld64c(rw, os + z * 20480 + kl * 1024), h1 = ld64c(rw, os + z * 20480 + kl * 1024 + 128);
                B[z][8 + kl] = (u32x4){h0.x, h0.y, h1.x, h1.y};
            }
        MEMF(); SB();
        f32x4 acc[5][2];
#pragma unroll
        for (int cb = 0; cb < 5; cb++) { acc[cb][0] = (f32x4){0, 0, 0, 0}; acc[cb][1] = (f32x4){0, 0, 0, 0}; }
        const u32 aoff = (16 * (g >> 1) + 4 * (g & 1) + (fr >> 2)) * 160 + (lane & 3) * 8;
        u32x2 al[2][5], ah[2][5];
#define F_KAP(kl) ((kl) < 8 ? (kl) : 8 + (kl))
#define F_READ(buf, kl) do { const u32 b0_ = kh ? wo_off(F_KAP(kl) + ((kl) < 8 ? 8 : 2)) : wo_off(F_KAP(kl)); \
            _Pragma("unroll") for (int cb = 0; cb < 5; cb++) { al[buf][cb] = ldstr(sm, b0_ + aoff + cb * 32); ah[buf][cb] = ldstr(sm, b0_ + aoff + cb * 32 + 1280); } } while (0)
        F_READ(0, 0);
#pragma unroll
        for (int kl = 0; kl < 10; kl++) {
            if (kl < 9) F_READ((kl + 1) & 1, kl + 1);
            SB();
#pragma unroll
            for (int cb = 0; cb < 5; cb++) {
                const u32x4 a = {al[kl & 1][cb].x, al[kl & 1][cb].y, ah[kl & 1][cb].x, ah[kl & 1][cb].y};
                acc[cb][0] = mfma16(a, B[0][kl], acc[cb][0]); acc[cb][1] = mfma16(a, B[1][kl], acc[cb][1]);
            }
            SB();
        }
        TS(0, 18);
        lds_arrive(sm, C_FR, lane); lds_wait(sm, C_FR, 4);
#pragma unroll
        for (int cb = 0; cb < 5; cb++)
#pragma unroll
            for (int z = 0; z < 2; z++)
                *(LAS f32x4*)(sm + kh * (64 * RED_PITCH) + (16 * (2 * sh + z) + fr) * RED_PITCH + (16 * cb + 4 * g) * 4) = acc[cb][z];
    } else {
        if (wave == 4) { wait_flags(rw, lane < 20 ? (int)F_Y + ct * 32 + lane : lane == 32 ? (int)F_GATE + (st >> 1) : -1, -1, tag); lds_arrive(sm, C_FY, lane); }
        else lds_wait(sm, C_FY, 1);
        const int t = tid - 256;
        u32x4 yv[3];
#pragma unroll
        for (int r = 0; r < 3; r++) { int id = t + 256 * r; id = id < 640 ? id : 639; yv[r] = ld128c(rw, WS_YT + (80 * ct + (id >> 3)) * 4096 + st * 128 + (id & 7) * 16); }
#pragma unroll
        for (int r = 0; r < 3; r++) { const int id = t + 256 * r; if (id < 640) *(LAS u32x4*)(sm + F_YY + id * 16) = yv[r]; }
        if (wave == 4) *(LAS u32*)(sm + F_GG + lane * 4) = ld32c(rw, WS_G + (st * 64 + lane) * 4);
        lds_arrive(sm, C_FY2, lane); lds_wait(sm, C_FY2, 4);
        const int sb = wave - 4, q = fr >> 2, p = lane & 3;
        u32x2 tt[5];
#pragma unroll
        for (int cb = 0; cb < 5; cb++) tt[cb] = ldstr(sm, F_YY + (16 * cb + 4 * g + q) * 128 + (16 * sb + 4 * p) * 2);
#pragma unroll
        for (int cb = 0; cb < 5; cb++) *(LAS u32x2*)(sm + F_Y2 + (16 * sb + fr) * 176 + (16 * cb + 4 * g) * 2) = tt[cb];
    }
    LGKM0(); BAR();
#pragma unroll
    for (int r = 0; r < 2; r++) {
        const int it = tid + 512 * r;
        if (it < 640) {
            const int s = it / 10, u = it - 10 * s;
            const u32 ra = s * RED_PITCH + u * 32;
            const f32x4 p0 = *(LAS f32x4*)(sm + ra), p1 = *(LAS f32x4*)(sm + ra + 16), q0 = *(LAS f32x4*)(sm + 64 * RED_PITCH + ra), q1 = *(LAS f32x4*)(sm + 64 * RED_PITCH + ra + 16);
            const u32x4 y8 = lds128(sm, F_Y2 + s * 176 + u * 16);
            const float gg = *(LAS float*)(sm + F_GG + s * 4), omg = 1.f - gg;
            float o[8];
#pragma unroll
            for (int e = 0; e < 4; e++) {
                const float a0 = (e < 2 ? p0[2 * e] + q0[2 * e] : p1[2 * e - 4] + q1[2 * e - 4]), a1 = (e < 2 ? p0[2 * e + 1] + q0[2 * e + 1] : p1[2 * e - 3] + q1[2 * e - 3]);
                const float b0 = omg * blo(xv[r][e]), b1 = omg * bhi(xv[r][e]);
                o[2 * e] = fmaf(b0, blo(y8[e]), gg * a0); o[2 * e + 1] = fmaf(b1, bhi(y8[e]), gg * a1);
            }
            { const u32x4 ov_ = {pk2(o[0], o[1]), pk2(o[2], o[3]), pk2(o[4], o[5]), pk2(o[6], o[7])};
              asm volatile("global_store_dwordx4 %0, %1, %2 nt\n\ts_nop 1" :: "v"((u32)((st * 64 + s) * 1280 + ct * 160 + u * 16)), "v"(ov_), "s"(out) : "memory"); }
        }
    }
}

__global__ __launch_bounds__(512, 2) void fused_kernel(const u16* __restrict__ x, const u16* __restrict__ w, const u16* __restrict__ wo, const u16* __restrict__ kin,
                                                        const u16* __restrict__ vin, const u16* __restrict__ shk, const u16* __restrict__ hk, const u16* __restrict__ sD,
                                                        const u16* __restrict__ gw, u16* __restrict__ out, char* ws) {
    extern __shared__ __attribute__((aligned(16))) char smem[];
    LAS char* sm = (LAS char*)smem;
    const int tid = threadIdx.x, lane = tid & 63, wave = __builtin_amdgcn_readfirstlane(tid >> 6);
    const int b = blockIdx.x, xcd = b & 7, j = b >> 3;
    const rsrc_t rw = __builtin_amdgcn_make_buffer_rsrc(ws, 0, WS_BYTES, 0x00020000);
    asm volatile("" :: "s"(x), "s"(w), "s"(wo), "s"(kin), "s"(vin), "s"(shk), "s"(hk), "s"(sD), "s"(gw), "s"(out));
    if (PREF1 && wave < 3 && j < 30) {
        if (wave == 0) touch(x, (u32)((xcd * 2560 + j * 80 + lane) * 128));
        else if (wave == 1) { if (lane < 16) touch(x, (u32)((xcd * 2560 + j * 80 + 64 + lane) * 128)); else touch(w, (u32)(((xcd * 30 + j) * 80 + lane - 16) * 128)); }
        else if (lane < 32) touch(w, (u32)(((xcd * 30 + j) * 80 + 48 + lane) * 128));
        else if (PREFTIN && lane < 42) touch(kin, (u32)((lane - 32) * 262144 + (j & 7) * 128));
        else if (PREFTIN && lane < 52) touch(vin, (u32)((lane - 42) * 262144 + (j & 7) * 128));
    }
    if (PREFKV && wave < 2 && j < 30) {
        const int tile_ = xcd * 30 + j;
        touch(wave == 0 ? kin : vin, (u32)(((tile_ / 10) * 64 + lane) * 1280 + (tile_ % 10) * 128));
    }
    if (PREF3 && wave < 2 && j < 30) {
        const int n240 = xcd * 30 + j;
        if (wave == 0) { const u32 l_ = n240 * 86 + lane; touch(hk, (l_ < 20480 ? l_ : 20479) * 128); }
        else { const u32 l_ = n240 * 86 + 64 + lane, m_ = n240 * 27 + lane - 22;
               if (lane < 22) touch(hk, (l_ < 20480 ? l_ : 20479) * 128); else if (lane < 49) touch(wo, (m_ < 6400 ? m_ : 6399) * 128); }
    }
    if (PREFC && wave == 3) {
        unsigned long long pc;
        asm volatile("s_getpc_b64 %0" : "=s"(pc));
        pc &= ~127ull;
        const int ln = PREFC == 1 ? j * 10 + lane : lane;
        if (PREFC == 1) { if (lane < 10 && ln < CODE_LINES) touch((const void*)pc, (u32)(ln * 128)); }
        else {
#pragma unroll
            for (int z = 0; z < 5; z++) if (z * 64 + lane < CODE_LINES) touch((const void*)pc, (u32)((z * 64 + lane) * 128));
        }
    }
    if (PREFT && wave == 2) {
        if (PREFT == 1) { touch(ws, (u32)(lane * 262144)); if (lane < 40) touch(out, (u32)(lane * 65536)); }
        else { touch(ws, (u32)((j * 8 + (lane & 7)) * 65536 + (lane >> 3) * 128) % (WS_STAMP - 1024)); if (lane < 40) touch(out, (u32)(lane * 65536)); }
    }
    u32 tag;
    asm volatile("s_load_dword %0, %1, 0x0\n\ts_waitcnt lgkmcnt(0)" : "=s"(tag) : "s"(ws + WS_FLAG + F_EPOCH * FS) : "memory");
    tag += 1u;
    if (tid < 16) *(LAS u32*)(sm + L_CNT + tid * 4) = 0u;
    TS(0, 0);
    const int st = 4 * xcd + (j >> 3), ct = j & 7;
    int ko = -1;
    bool is_conv = false;

    if (j >= 30) {
        const int m = xcd * 2 + (j - 30);
        ko = -100;
        {
            const int grp = wave >> 2, w4i = wave & 3;
            u32x2 w4[3];
#pragma unroll
            for (int t = 0; t < 3; t++) { const int tt = 2 * t + grp < 5 ? 2 * t + grp : 4; p0_weights(w4[t], shk, 240 + 5 * m + tt, w4i, lane); }
#pragma unroll
            for (int t = 0; t < 3; t++) if (2 * t + grp < 5) p0_issue(kin, vin, 240 + 5 * m + 2 * t + grp, w4i, lane, (2 * t + grp) * 17408);
            VMCNT(0);
#pragma unroll
            for (int t = 0; t < 3; t++) PIN(w4[t]);
            BAR();
#pragma unroll
            for (int t = 0; t < 3; t++)
                if (2 * t + grp < 5) p0_compute(sm, w4[t], 240 + 5 * m + 2 * t + grp, w4i, lane, (2 * t + grp) * 17408, 90112 + (2 * t + grp) * 8192);
            LGKM0(); BAR();
            {
                u32x4 t4[3][2];
#pragma unroll
                for (int t = 0; t < 3; t++)
#pragma unroll
                    for (int z = 0; z < 2; z++) { const int ch2 = ((tid & 255) >> 3) + 32 * z, o2 = tid & 7, tt = 2 * t + grp < 5 ? 2 * t + grp : 4; t4[t][z] = lds128(sm, 90112 + tt * 8192 + ch2 * 128 + ((o2 ^ (ch2 & 7)) * 16)); }
#pragma unroll
                for (int t = 0; t < 3; t++)
                    if (2 * t + grp < 5) {
                        const int tile = 240 + 5 * m + 2 * t + grp, cg = tile % 10, s0 = (tile / 10) * 64;
#pragma unroll
                        for (int z = 0; z < 2; z++) { const int ch2 = ((tid & 255) >> 3) + 32 * z, o2 = tid & 7; st128c(rw, WS_UT + (cg * 64 + ch2) * 4096 + (s0 + 8 * o2) * 2, t4[t][z]); }
                    }
            }
        }
        VMCNT(0); BAR();
        if (tid < 5) set_flag(rw, p0_flag(240 + 5 * m + tid), tag);
        TS(0, 1);
        gate_block(wave, lane, m, x, gw, rw);
        VMCNT(0); LGKM0(); BAR();
        if (tid == 0) set_flag(rw, F_GATE + m, tag);
        TS(0, 3);
    } else {
        const int rt = 2 * xcd + j / 15, cbk = j % 15, tile = xcd * 30 + j;
        is_conv = cbk < 5 || cbk >= 10;
        if (!is_conv) { const int ki = rt * 5 + (cbk - 5); ko = ki < 64 ? ki : -2 - (ki - 64); }
        if (wave >= 4) {
            const int lw = wave - 4;
            u32 vx[4], vw[4];
#pragma unroll
            for (int n2 = 0; n2 < 4; n2++) {
                const int n = 4 * lw + n2;
                const int row = 8 * n + (lane >> 3), ch = (lane & 7) ^ ((row >> 1) & 7);
                vx[n2] = (u32)((rt * 128 + row) * 1280 + ch * 16);
                const int kr = 4 * n + (lane >> 4), f = ((kr & 3) << 2) | ((kr >> 2) & 3), cw = (lane & 15) ^ f;
                vw[n2] = (u32)(kr * 3840 + cbk * 256 + cw * 16);
            }
            const int rot = ((rt >> 1) + cbk) % 10;
#define K1_ISSUE(c) do { const u32 st_ = ((c) & 3) * K1_STAGE + 4 * lw * 1024; int kc_ = (c) + ROT_ON * rot; kc_ = kc_ >= 10 ? kc_ - 10 : kc_; \
            const char* xb_ = (const char*)x + kc_ * 128; const char* wp_ = (const char*)w + (size_t)kc_ * 64 * 3840; \
            _Pragma("unroll") for (int n2 = 0; n2 < 4; n2++) { if (NT_X) glds16nt(xb_, vx[n2], st_ + 16384 + n2 * 1024); else glds16(xb_, vx[n2], st_ + 16384 + n2 * 1024); glds16(wp_, vw[n2], st_ + n2 * 1024); } } while (0)
            K1_ISSUE(0); K1_ISSUE(1);
            VMCNT(8); BAR();
            K1_ISSUE(2); K1_ISSUE(3);
            p0_issue(kin, vin, tile, lw, lane, P0_K);
#pragma unroll 1
            for (int c = 0; c < 6; c++) {
                if (c < 3) VMCNT(21); else VMCNT(16);
                BAR();
                K1_ISSUE(c + 4);
            }
            const int cv = rt * 10 + (cbk < 5 ? cbk : cbk - 5), c = 4 * cv + lw;
            if (is_conv) {
                CvIn in; u32 fl; u32x4 u[4];
                const int fidx = (int)F_P0 + (cv >> 4) * 32 + (lane & 31);
                VMCNT(16); BAR();
                VMCNT(8); BAR();
                conv_fetch_h(in, hk, c, lane);
                gld32c(fl, ws, WS_FLAG + fidx * FS);
                VMCNT(9); BAR();
                TS(4, 4);
                VMCNT(0); PIN(fl);
                const bool ready = __all(fl == tag);
                if (ready) {
#pragma unroll
                    for (int z = 0; z < 4; z++) gld128c(u[z], ws, WS_UT + c * 4096 + z * 1024 + lane * 16);
                }
                TS(4, 5);
                conv_build_h(sm, in, lw, lane);
                TS(4, 6);
                if (!ready) {
                    wait_flags(rw, fidx, -1, tag);
#pragma unroll
                    for (int z = 0; z < 4; z++) gld128c(u[z], ws, WS_UT + c * 4096 + z * 1024 + lane * 16);
                }
                TS(4, 7);
                VMCNT(0);
#pragma unroll
                for (int z = 0; z < 4; z++) { PIN(u[z]); *(LAS u32x4*)(sm + CV_BASE + lw * CV_WAVE + CV_U + 512 + z * 1024 + lane * 16) = u[z]; }
                const float Dc = blo((u32)sD[c]);
                TS(4, 8);
                conv_run(sm, Dc, lw, lane, c, rw, ws);
                publish_last(rw, sm, C_Y, 4, F_Y + (cv / 20) * 32 + cv % 20, tag, lane);
                TS(4, 11);
            } else {
                VMCNT(16); BAR();
                VMCNT(8); BAR();
                VMCNT(0); BAR();
                const int r32 = lane & 31, hh = lane >> 5;
                u32 dst, istep, zstep; kv_addr(true, cbk - 5, rt, lw >> 1, lw & 1, 1, r32, hh, dst, istep, zstep);
                lds_wait(sm, C_XF + lw * 4, 1);
                f32x4 a[2][4];
#pragma unroll
                for (int i = 0; i < 2; i++)
#pragma unroll
                    for (int q4 = 0; q4 < 4; q4++) a[i][q4] = *(LAS f32x4*)(sm + K_XFER + lw * 8192 + ((i * 4 + q4) * 64 + lane) * 16);
#pragma unroll
                for (int i = 0; i < 2; i++)
#pragma unroll
                    for (int z = 0; z < 2; z++) {
                        const f32x4 a0 = a[i][2 * z], a1 = a[i][2 * z + 1];
                        st128c(rw, dst + i * istep + z * zstep, (u32x4){phi2(a0[0], a0[1]), phi2(a0[2], a0[3]), phi2(a1[0], a1[1]), phi2(a1[2], a1[3])});
                    }
                publish_last(rw, sm, C_G1, 8, F_G1 + cbk * 16 + rt, tag, lane);
            }
        } else {
            u32x2 w4; p0_weights(w4, shk, tile, wave, lane);
            const int wm = wave >> 1, wn = wave & 1, r32 = lane & 31, hh = lane >> 5;
            K1Addr a0;
#pragma unroll
            for (int ks = 0; ks < 2; ks++) {
                a0.x[0][ks] = (64 * wm + r32) * 128 + (((2 * ks + hh) ^ ((r32 >> 1) & 7)) * 16);
                a0.x[1][ks] = a0.x[0][ks] ^ 64;
            }
#pragma unroll
            for (int jj = 0; jj < 2; jj++)
#pragma unroll
                for (int t = 0; t < 2; t++) {
                    const int blk = (lane >> 4) & 1, q = (lane & 15) >> 2, p = lane & 3, row = 8 * hh + 4 * t + q, ch = 8 * wn + 4 * jj + 2 * blk + (p >> 1);
                    const int f = ((row & 3) << 2) | ((row >> 2) & 3);
                    a0.w[jj][t] = row * 256 + ((ch ^ f) * 16) + 8 * (p & 1);
                }
            f32x16 acc[2][2];
#pragma unroll
            for (int i = 0; i < 2; i++)
#pragma unroll
                for (int jj = 0; jj < 2; jj++)
#pragma unroll
                    for (int e = 0; e < 16; e++) acc[i][jj][e] = 0.f;
            BAR();
            TS(0, 1);
            if (cbk < 5) {
                k1_loop<true>(sm, a0, acc, rw, p0_flag(tile), tag, wave, lane, hk, wo, tile, w4);
                __builtin_amdgcn_s_setprio(2);
                TS(0, 2);
                u32x4 qv[2][2][2];
#pragma unroll
                for (int jj = 0; jj < 2; jj++)
#pragma unroll
                    for (int ap = 0; ap < 2; ap++)
#pragma unroll
                        for (int i = 0; i < 2; i++) {
                            const f32x16& a = acc[i][jj];
                            const u32 x0 = phi2(a[8 * ap], a[8 * ap + 1]), y0 = phi2(a[8 * ap + 2], a[8 * ap + 3]);
                            const u32 x1 = phi2(a[8 * ap + 4], a[8 * ap + 5]), y1 = phi2(a[8 * ap + 6], a[8 * ap + 7]);
                            auto rx = __builtin_amdgcn_permlane32_swap(x0, x1, false, false);
                            auto ry = __builtin_amdgcn_permlane32_swap(y0, y1, false, false);
                            qv[jj][ap][i] = (u32x4){rx[0], ry[0], rx[1], ry[1]};
                        }
                if (QDELAY) { __builtin_amdgcn_s_setprio(0); for (int z = 0; z < QDELAY; z++) __builtin_amdgcn_s_sleep(8); }
#pragma unroll
                for (int jj = 0; jj < 2; jj++)
#pragma unroll
                    for (int ap = 0; ap < 2; ap++) {
                        const int U = 16 * cbk + 8 * wn + 4 * jj + 2 * ap + hh, pr = U / 5, u5 = U - 5 * pr;
                        const u32 dst = WS_P2 + (((pr * 128 + rt * 8 + 4 * wm + (r32 >> 4)) * 5 + u5) * 128 + (r32 & 15) * 8) * 2;
#pragma unroll
                        for (int i = 0; i < 2; i++) st128c(rw, dst + i * 2560, qv[jj][ap][i]);
                    }
            } else {
                k1_loop<false>(sm, a0, acc, rw, p0_flag(tile), tag, wave, lane, hk, wo, tile, w4);
                __builtin_amdgcn_s_setprio(2);
                TS(0, 2);
                const bool isk = cbk < 10;
                const int j5 = isk ? cbk - 5 : cbk - 10;
                if (isk) {
#pragma unroll
                    for (int i = 0; i < 2; i++)
#pragma unroll
                        for (int q4 = 0; q4 < 4; q4++)
                            *(LAS f32x4*)(sm + K_XFER + wave * 8192 + ((i * 4 + q4) * 64 + lane) * 16) = (f32x4){acc[i][1][4 * q4], acc[i][1][4 * q4 + 1], acc[i][1][4 * q4 + 2], acc[i][1][4 * q4 + 3]};
                    lds_arrive(sm, C_XF + wave * 4, lane);
                    u32 dst, istep, zstep; kv_addr(true, j5, rt, wm, wn, 0, r32, hh, dst, istep, zstep);
#pragma unroll
                    for (int i = 0; i < 2; i++)
#pragma unroll
                        for (int z = 0; z < 2; z++) {
                            const f32x16& a = acc[i][0];
                            st128c(rw, dst + i * istep + z * zstep,
                                   (u32x4){phi2(a[8 * z], a[8 * z + 1]), phi2(a[8 * z + 2], a[8 * z + 3]), phi2(a[8 * z + 4], a[8 * z + 5]), phi2(a[8 * z + 6], a[8 * z + 7])});
                        }
                } else {
#pragma unroll
                    for (int jj = 0; jj < 2; jj++) {
                        u32 dst, istep, zstep; kv_addr(false, j5, rt, wm, wn, jj, r32, hh, dst, istep, zstep);
#pragma unroll
                        for (int i = 0; i < 2; i++)
#pragma unroll
                            for (int z = 0; z < 2; z++) {
                                const f32x16& a = acc[i][jj];
                                st128c(rw, dst + i * istep + z * zstep,
                                       (u32x4){pk2(a[8 * z], a[8 * z + 1]), pk2(a[8 * z + 2], a[8 * z + 3]), pk2(a[8 * z + 4], a[8 * z + 5]), pk2(a[8 * z + 6], a[8 * z + 7])});
                            }
                    }
                }
            }
            LGKM0();
            TS(0, 25);
            publish_last(rw, sm, C_G1, is_conv ? 4 : 8, F_G1 + cbk * 16 + rt, tag, lane);
            __builtin_amdgcn_s_setprio(0);
            TS(0, 3);
            if (is_conv) {
                lds_wait(sm, C_G1, 4);
                wo_issue(wo, ct, 0, 12, wave, 4, lane);
            }
        }
    }
    if (!is_conv) {
        LGKM0(); BAR();
        if (ko >= 0) {
            const int h = ko >> 1, half = ko & 1;
            wo_issue(wo, ct, 0, 12, wave, 8, lane);
            kvo_block(sm, wave, lane, h, half, rw, tag, ws);
            TS(0, 26);
            VMCNT(0); BAR();
            if (tid == 0) set_flag(rw, F_O + half * 32 + h, tag);
            TS(0, 11);
            wo_issue(wo, ct, 12, 20, wave, 8, lane);
        } else {
            wo_issue(wo, ct, 0, 20, wave, 8, lane);
            if (ko == -2 && wave == 0) {
                wait_flags(rw, (int)F_G1 + lane, (int)F_G1 + 64 + lane, tag);
                wait_flags(rw, (int)F_G1 + 128 + lane, (int)F_G1 + 192 + lane, tag);
                if (lane == 0) set_flag(rw, F_EPOCH, tag);
            }
        }
    } else {
        VMCNT(0); BAR();
        if (wave >= 4) wo_issue(wo, ct, 12, 20, wave - 4, 4, lane);
    }
    TS(0, 16);
    final_block(sm, wave, lane, tid, st, ct, x, out, rw, tag, ws);
    TS(0, 19);
}

extern "C" void kernel_launch(void* const* d_in, const int* in_sizes, int n_in, void* d_out, int out_size, void* d_ws, size_t ws_size, hipStream_t stream) {
    const u16* x = (const u16*)d_in[0]; const u16* wqkv = (const u16*)d_in[1]; const u16* wo = (const u16*)d_in[2];
    const u16* kin = (const u16*)d_in[4]; const u16* vin = (const u16*)d_in[5]; const u16* shk = (const u16*)d_in[6];
    const u16* hk = (const u16*)d_in[7]; const u16* sD = (const u16*)d_in[8]; const u16* gw = (const u16*)d_in[9];
    (void)hipFuncSetAttribute((const void*)fused_kernel, hipFuncAttributeMaxDynamicSharedMemorySize, LDS_BYTES);
    fused_kernel<<<256, 512, LDS_BYTES, stream>>>(x, wqkv, wo, kin, vin, shk, hk, sD, gw, (u16*)d_out, (char*)d_ws);
}
```

```cpp
#include <hip/hip_runtime.h>
#include <stdint.h>

typedef unsigned short u16;
typedef unsigned int u32;
typedef __bf16 bf16x8 __attribute__((ext_vector_type(8)));
typedef __bf16 bf16x2 __attribute__((ext_vector_type(2)));
typedef float f32x4 __attribute__((ext_vector_type(4)));
typedef float f32x2 __attribute__((ext_vector_type(2)));
typedef float f32x16 __attribute__((ext_vector_type(16)));
typedef u32 u32x4 __attribute__((ext_vector_type(4)));
typedef u32 u32x2 __attribute__((ext_vector_type(2)));
typedef short s16x4 __attribute__((ext_vector_type(4)));
#define LAS __attribute__((address_space(3)))
#define GAS __attribute__((address_space(1)))
#define DI __device__ __forceinline__
typedef __amdgpu_buffer_rsrc_t rsrc_t;

constexpr int S = 2048, D = 640;
#define FS 256
#define POLL_SLEEP 1
#define ROT_ON 1

constexpr u32 WS_UT = 0;
constexpr u32 WS_YT = WS_UT + 2621440;
constexpr u32 WS_PK0 = WS_YT + 2621440;
constexpr u32 WS_V0 = WS_PK0 + 2097152;
constexpr u32 WS_XK = WS_V0 + 2097152;
constexpr u32 WS_XV = WS_XK + 524288;
constexpr u32 WS_P2 = WS_XV + 524288;
constexpr u32 WS_O = WS_P2 + 2621440;
constexpr u32 WS_G = WS_O + 2621440;
constexpr u32 WS_FLAG = WS_G + 8192;
constexpr u32 F_P0 = 0;
constexpr u32 F_G1 = 320;
constexpr u32 F_GATE = 560;
constexpr u32 F_O = 576;
constexpr u32 F_Y = 640;
constexpr u32 F_EPOCH = 896, F_TMO = 928;
constexpr u32 WS_STAMP = WS_FLAG + 1024 * FS;
constexpr u32 WS_BYTES = WS_STAMP + 256 * 32 * 16;

#define TS(w, id) do {} while (0)

DI u32 pk2(float a, float b) { f32x2 t = {a, b}; return __builtin_bit_cast(u32, __builtin_convertvector(t, bf16x2)); }
DI float blo(u32 u) { return __uint_as_float(u << 16); }
DI float bhi(u32 u) { return __uint_as_float(u & 0xffff0000u); }
DI float rbf(float f) { return blo(pk2(f, f)); }
DI bf16x8 asbf(u32x4 v) { return __builtin_bit_cast(bf16x8, v); }
DI f32x4 mfma16(u32x4 a, u32x4 b, f32x4 c) { return __builtin_amdgcn_mfma_f32_16x16x32_bf16(asbf(a), asbf(b), c, 0, 0, 0); }
DI f32x16 mfma32(u32x4 a, u32x4 b, f32x16 c) { return __builtin_amdgcn_mfma_f32_32x32x16_bf16(asbf(a), asbf(b), c, 0, 0, 0); }

DI void glds16(const void* sbase, u32 voff, u32 lds) {
    asm volatile("s_mov_b32 m0, %2\n\ts_nop 0\n\tglobal_load_lds_dwordx4 %0, %1" :: "v"(voff), "s"(sbase), "s"(lds) : "memory");
}
DI void glds16nt(const void* sbase, u32 voff, u32 lds) {
    asm volatile("s_mov_b32 m0, %2\n\ts_nop 0\n\tglobal_load_lds_dwordx4 %0, %1 nt" :: "v"(voff), "s"(sbase), "s"(lds) : "memory");
}
DI void touch(const void* sbase, u32 voff) {
    asm volatile("s_mov_b32 m0, %2\n\ts_nop 0\n\tglobal_load_lds_dword %0, %1" :: "v"(voff), "s"(sbase), "s"(156928u) : "memory");
}
DI void touch_tlb(const void* sbase, u32 voff) {
    asm volatile("s_mov_b32 m0, %2\n\ts_nop 0\n\tglobal_load_lds_dword %0, %1 sc1" :: "v"(voff), "s"(sbase), "s"(156928u) : "memory");
}
#define NT_KV 0
#define NT_X 0
#define NT_H 0
#define PREF1 1
#define PREF2 0
#define PREFC 1
#define CODE_LINES 314
#define PREFT 2
#define PREFKV 0
#define PREF3 1
#define PREFTIN 1
#define QDELAY 0
DI void gld64(u32x2& d, const void* sbase, u32 voff) { asm volatile("global_load_dwordx2 %0, %1, %2" : "=v"(d) : "v"(voff), "s"(sbase) : "memory"); }
DI void gld128(u32x4& d, const void* sbase, u32 voff) { asm volatile("global_load_dwordx4 %0, %1, %2" : "=v"(d) : "v"(voff), "s"(sbase) : "memory"); }
DI void gld128c(u32x4& d, const void* sbase, u32 voff) { asm volatile("global_load_dwordx4 %0, %1, %2 sc1" : "=v"(d) : "v"(voff), "s"(sbase) : "memory"); }
DI void gld32c(u32& d, const void* sbase, u32 voff) { asm volatile("global_load_dword %0, %1, %2 sc1" : "=v"(d) : "v"(voff), "s"(sbase) : "memory"); }
#define PIN(x) asm volatile("" : "+v"(x))
#define SB() __builtin_amdgcn_sched_barrier(0)
DI u32x4 ld128c(rsrc_t r, u32 off) { return __builtin_amdgcn_raw_buffer_load_b128(r, off, 0, 16); }
DI u32x2 ld64c(rsrc_t r, u32 off) { return __builtin_amdgcn_raw_buffer_load_b64(r, off, 0, 16); }
DI u32 ld32c(rsrc_t r, u32 off) { return __builtin_amdgcn_raw_buffer_load_b32(r, off, 0, 16); }
DI void st128c(rsrc_t r, u32 off, u32x4 v) { __builtin_amdgcn_raw_buffer_store_b128(v, r, off, 0, 16); }
DI void st64c(rsrc_t r, u32 off, u32x2 v) { __builtin_amdgcn_raw_buffer_store_b64(v, r, off, 0, 16); }
DI void st32c(rsrc_t r, u32 off, u32 v) { __builtin_amdgcn_raw_buffer_store_b32(v, r, off, 0, 16); }
DI u32x4 lds128(LAS char* sm, u32 a) { return *(LAS u32x4*)(sm + a); }
DI u32x2 lds64(LAS char* sm, u32 a) { return *(LAS u32x2*)(sm + a); }
DI u32x2 ldstr(LAS char* sm, u32 a) { return __builtin_bit_cast(u32x2, __builtin_amdgcn_ds_read_tr16_b64_v4i16((LAS s16x4*)(sm + a))); }
#define VMCNT(n) asm volatile("s_waitcnt vmcnt(" #n ")" ::: "memory")
#define LGKM0() asm volatile("s_waitcnt lgkmcnt(0)" ::: "memory")
#define BAR() do { asm volatile("" ::: "memory"); __builtin_amdgcn_s_barrier(); asm volatile("" ::: "memory"); } while (0)
#define MEMF() asm volatile("" ::: "memory")

DI void set_flag(rsrc_t r, u32 idx, u32 tag) { st32c(r, WS_FLAG + idx * FS, tag); }
DI void wait_flags(rsrc_t r, int idx0, int idx1, u32 tag) {
    unsigned long long t0 = 0;
    for (u32 it = 0;; it++) {
        u32 a = tag, b = tag;
        if (idx0 >= 0) a = ld32c(r, WS_FLAG + idx0 * FS);
        if (idx1 >= 0) b = ld32c(r, WS_FLAG + idx1 * FS);
        if (__all(a == tag && b == tag)) break;
        if (POLL_SLEEP) __builtin_amdgcn_s_sleep(POLL_SLEEP);
        if ((it & 63u) == 0u) {
            const unsigned long long t = __builtin_amdgcn_s_memrealtime();
            if (it == 0) t0 = t;
            else if (t - t0 > 2000000ull) { set_flag(r, F_TMO, tag); break; }
        }
    }
    MEMF();
}
DI void lds_arrive(LAS char* sm, u32 cnt, int lane) { LGKM0(); if (lane == 0) __hip_atomic_fetch_add((LAS u32*)(sm + cnt), 1u, __ATOMIC_RELAXED, __HIP_MEMORY_SCOPE_WORKGROUP); MEMF(); }
DI void lds_wait(LAS char* sm, u32 cnt, u32 n) { MEMF(); while (*(volatile LAS u32*)(sm + cnt) < n) __builtin_amdgcn_s_sleep(0); MEMF(); }
DI void publish_last(rsrc_t r, LAS char* sm, u32 cnt, u32 nw, u32 flag, u32 tag, int lane) {
    VMCNT(0);
    if (lane == 0) {
        const u32 old = __hip_atomic_fetch_add((LAS u32*)(sm + cnt), 1u, __ATOMIC_RELAXED, __HIP_MEMORY_SCOPE_WORKGROUP);
        if (old == nw - 1) set_flag(r, flag, tag);
    }
    MEMF();
}

DI u32 phi2(float a, float b) {
    const f32x2 t = {a, b}, s = t * 1.4426950408889634f;
    const float ea = __builtin_amdgcn_fmed3f(__builtin_amdgcn_exp2f(s.x), 0.f, 1.f), eb = __builtin_amdgcn_fmed3f(__builtin_amdgcn_exp2f(s.y), 0.f, 1.f);
    const float ma = __int_as_float(max(__float_as_int(a), 0)), mb = __int_as_float(max(__float_as_int(b), 0));
    const f32x2 r = ((f32x2){ea, eb} + (f32x2){ma, mb}) + 1e-6f;
    return pk2(r.x, r.y);
}

constexpr int K1_STAGE = 32768;
constexpr int P0_K = 131072, P0_V = P0_K + 9216, P0_OUT = P0_V + 8192;
constexpr int L_CNT = 156672;
constexpr int C_G1 = L_CNT, C_Y = L_CNT + 4, C_P0A = L_CNT + 8, C_P0B = L_CNT + 12, C_P0C = L_CNT + 16, C_FY = L_CNT + 20, C_FY2 = L_CNT + 24, C_FO = L_CNT + 28, C_FR = L_CNT + 32, C_XF = L_CNT + 36;
constexpr int CV_PITCH = 4160;
constexpr int CV_U = 4 * CV_PITCH;
constexpr int CV_WAVE = CV_U + 4608;
constexpr int CV_BASE = 65536;
constexpr int WO_HI = 65536;
constexpr int F_YY = 110592;
constexpr int F_Y2 = F_YY + 10240;
constexpr int F_GG = F_Y2 + 11264;
constexpr int KO_RED = 110592;
constexpr int KO_KV = KO_RED + 32768;
constexpr int RED_PITCH = 336;
constexpr int LDS_BYTES = 160 * 1024;
DI u32 p0_flag(int tile) { return F_P0 + (tile % 10) * 32 + tile / 10; }
DI constexpr u32 wo_off(int kap) { return kap < 12 ? kap * 5120 : WO_HI + (kap - 12) * 5120; }

DI void p0_issue(const u16* kin, const u16* vin, int tile, int lw, int lane, u32 base) {
    const int cg = tile % 10, rg = tile / 10, s0 = rg * 64;
    const u32 col = cg * 128 + (lane & 7) * 16;
#pragma unroll
    for (int z = 0; z < 2; z++) {
        const int n = 2 * lw + z;
        int r = s0 - 4 + n * 8 + (lane >> 3); r = r < 0 ? 0 : r;
        (NT_KV ? glds16nt : glds16)(kin, (u32)(r * 1280) + col, base + n * 1024);
        (NT_KV ? glds16nt : glds16)(vin, (u32)((s0 + n * 8 + (lane >> 3)) * 1280) + col, base + 9216 + n * 1024);
    }
    {
        int r = s0 + 60 + (lane >> 3); r = r > S - 1 ? S - 1 : r;
        (NT_KV ? glds16nt : glds16)(kin, (u32)(r * 1280) + col, base + 8 * 1024);
    }
}
DI float dot2(u32 a, u32 b, float c) { return __builtin_amdgcn_fdot2_f32_bf16(__builtin_bit_cast(bf16x2, a), __builtin_bit_cast(bf16x2, b), c, false); }
DI void p0_compute(LAS char* sm, u32x2 w4, int tile, int wave, int lane, u32 base, u32 outb, char* ws = nullptr) {
    const int s0 = (tile / 10) * 64;
    const int G = wave * 4 + (lane >> 4), cb = G & 3, i = lane & 15, q = i >> 2, p = i & 3;
    const u32 wa = __builtin_amdgcn_alignbit(w4.y, w4.y, 16), wb = __builtin_amdgcn_alignbit(w4.x, w4.x, 16);
    const int ch = 16 * cb + i;
    u32x2 kq[2][3], vq[2][2];
#pragma unroll
    for (int z = 0; z < 2; z++) {
        const int o = (G >> 2) + 4 * z;
        const u32 ka = base + (8 * o + q) * 128 + 32 * cb + 8 * p, va = ka + 9216;
        kq[z][0] = ldstr(sm, ka); kq[z][1] = ldstr(sm, ka + 512); kq[z][2] = ldstr(sm, ka + 1024);
        vq[z][0] = ldstr(sm, va); vq[z][1] = ldstr(sm, va + 512);
    }
#pragma unroll
    for (int z = 0; z < 2; z++) {
        const int o = (G >> 2) + 4 * z;
        u32 K[6] = {kq[z][0].x, kq[z][0].y, kq[z][1].x, kq[z][1].y, kq[z][2].x, kq[z][2].y};
        if (s0 == 0 && o == 0) { K[0] = 0; K[1] = 0; }
        u32 Sh[5];
#pragma unroll
        for (int e = 0; e < 5; e++) Sh[e] = __builtin_amdgcn_alignbit(K[e + 1], K[e], 16);
        const u32 V[4] = {vq[z][0].x, vq[z][0].y, vq[z][1].x, vq[z][1].y};
        u32 r[4];
#pragma unroll
        for (int e = 0; e < 4; e++) {
            const float k0 = dot2(Sh[e], wa, dot2(Sh[e + 1], wb, 0.f)), k1 = dot2(K[e + 1], wa, dot2(K[e + 2], wb, 0.f));
            r[e] = pk2(k0 * blo(V[e]), k1 * bhi(V[e]));
        }
        *(LAS u32x4*)(sm + outb + ch * 128 + ((o ^ (ch & 7)) * 16)) = (u32x4){r[0], r[1], r[2], r[3]};
    }
}
DI void p0_store(LAS char* sm, rsrc_t rw, int tile, int t256, u32 outb) {
    const int cg = tile % 10, s0 = (tile / 10) * 64;
#pragma unroll
    for (int z = 0; z < 2; z++) {
        const int ch2 = (t256 >> 3) + 32 * z, o2 = t256 & 7;
        const u32x4 t4 = lds128(sm, outb + ch2 * 128 + ((o2 ^ (ch2 & 7)) * 16));
        st128c(rw, WS_UT + (cg * 64 + ch2) * 4096 + (s0 + 8 * o2) * 2, t4);
    }
}
DI void p0_weights(u32x2& w4, const u16* shk, int tile, int wave, int lane) {
    const int cg = tile % 10, cb = (wave * 4 + (lane >> 4)) & 3;
    gld64(w4, shk, (u32)((cg * 64 + 16 * cb + (lane & 15)) * 8));
}

struct K1Set { u32x4 xa[2][2]; u32x2 wl[2][2], wh[2][2]; };
struct K1Addr { u32 x[2][2]; u32 w[2][2]; };
template <bool QT, int WO, int XO>
DI void k1_phase(LAS char* sm, f32x16 (&acc)[2][2], const K1Set& cur, K1Set& nxt, const u32 (&xb)[2], const u32 (&wb)[2][2]) {
#pragma unroll
    for (int n = 0; n < 8; n++) {
        const int ks = n >> 2, i = (n >> 1) & 1, jj = n & 1;
        const u32x4 wf = {cur.wl[ks][jj].x, cur.wl[ks][jj].y, cur.wh[ks][jj].x, cur.wh[ks][jj].y};
        acc[i][jj] = QT ? mfma32(wf, cur.xa[ks][i], acc[i][jj]) : mfma32(cur.xa[ks][i], wf, acc[i][jj]);
        if (n == 0) { nxt.wl[0][0] = ldstr(sm, wb[0][0] + WO); nxt.wh[0][0] = ldstr(sm, wb[0][1] + WO); }
        if (n == 1) { nxt.xa[0][0] = lds128(sm, xb[0] + XO); nxt.wl[0][1] = ldstr(sm, wb[1][0] + WO); }
        if (n == 2) { nxt.wh[0][1] = ldstr(sm, wb[1][1] + WO); nxt.xa[0][1] = lds128(sm, xb[0] + XO + 4096); }
        if (n == 3) { nxt.wl[1][0] = ldstr(sm, wb[0][0] + WO + 4096); nxt.wh[1][0] = ldstr(sm, wb[0][1] + WO + 4096); }
        if (n == 4) { nxt.xa[1][0] = lds128(sm, xb[1] + XO); nxt.wl[1][1] = ldstr(sm, wb[1][0] + WO + 4096); }
        if (n == 5) { nxt.wh[1][1] = ldstr(sm, wb[1][1] + WO + 4096); nxt.xa[1][1] = lds128(sm, xb[1] + XO + 4096); }
        SB();
    }
}
template <bool QT, int ST>
DI void k1_chunk(LAS char* sm, f32x16 (&acc)[2][2], K1Set& s0, K1Set& s1, const K1Addr& a0, const K1Addr& a1, bool bar, bool drain = false) {
    constexpr int SN = (ST + 1) & 3;
    const K1Addr& ac = ST < 2 ? a0 : a1; const K1Addr& an = SN < 2 ? a0 : a1;
    k1_phase<QT, (ST & 1) * K1_STAGE + 8192, (ST & 1) * K1_STAGE + 16384>(sm, acc, s0, s1, ac.x[1], ac.w);
    if (drain) VMCNT(0);
    if (bar) { LGKM0(); BAR(); }
    k1_phase<QT, (SN & 1) * K1_STAGE, (SN & 1) * K1_STAGE + 16384>(sm, acc, s1, s0, an.x[0], an.w);
}
template <bool QT>
DI void k1_loop(LAS char* sm, const K1Addr& a0, f32x16 (&acc)[2][2], rsrc_t rw, u32 pflag, u32 tag, int wave, int lane, const u16* hk, const u16* wo, int n240, u32x2 w4) {
    K1Addr a1;
#pragma unroll
    for (int i = 0; i < 2; i++)
#pragma unroll
        for (int z = 0; z < 2; z++) { a1.x[i][z] = a0.x[i][z] + 65536; a1.w[i][z] = a0.w[i][z] + 65536; }
    K1Set s0, s1;
#pragma unroll
    for (int ks = 0; ks < 2; ks++)
#pragma unroll
        for (int jj = 0; jj < 2; jj++) {
            s0.wl[ks][jj] = ldstr(sm, a0.w[jj][0] + ks * 4096); s0.wh[ks][jj] = ldstr(sm, a0.w[jj][1] + ks * 4096);
            s0.xa[ks][jj] = lds128(sm, a0.x[0][ks] + 16384 + jj * 4096);
        }
    SB();
#pragma unroll 1
    for (int c = 0; c < 8; c += 4) {
        k1_chunk<QT, 0>(sm, acc, s0, s1, a0, a1, true);
        if (c == 4) p0_store(sm, rw, n240, wave * 64 + lane, P0_OUT);
        k1_chunk<QT, 1>(sm, acc, s0, s1, a0, a1, true);
        k1_chunk<QT, 2>(sm, acc, s0, s1, a0, a1, true);
        k1_chunk<QT, 3>(sm, acc, s0, s1, a0, a1, true, c == 4);
        if (c == 4 && wave == 0 && lane == 0) set_flag(rw, pflag, tag);
        if (c == 0) { VMCNT(0); PIN(w4); p0_compute(sm, w4, n240, wave, lane, P0_K, P0_OUT); }
        if (PREF2 && c == 0 && wave >= 2) {
            if (wave == 2) { const u32 l_ = n240 * 86 + lane; touch(hk, (l_ < 20480 ? l_ : 20479) * 128); }
            else { const u32 l_ = n240 * 86 + 64 + lane, m_ = n240 * 27 + lane - 22;
                   if (lane < 22) touch(hk, (l_ < 20480 ? l_ : 20479) * 128); else if (lane < 49) touch(wo, (m_ < 6400 ? m_ : 6399) * 128); }
        }
    }
    k1_chunk<QT, 0>(sm, acc, s0, s1, a0, a1, true);
    k1_chunk<QT, 1>(sm, acc, s0, s1, a0, a1, false);
}

DI void kv_addr(bool isk, int j5, int rt, int wm, int wn, int jj, int r32, int hh, u32& dst, u32& istep, u32& zstep) {
    const int cc = 128 * j5 + 64 * wn + 32 * jj + r32, h = cc / 20, d = cc - 20 * h;
    const int sb32 = rt * 4 + 2 * wm;
    const bool lo = d < 16;
    dst = lo ? (isk ? WS_PK0 : WS_V0) + (((h * 64 + sb32) * 4 + hh) * 128 + d * 8) * 2
             : (isk ? WS_XK : WS_XV) + (((h * 64 + sb32) * 4 + hh) * 32 + (d - 16) * 8) * 2;
    istep = lo ? 1024 : 256; zstep = lo ? 512 : 128;
}
constexpr int K_XFER = 65536;

DI void wo_issue(const u16* wo, int ct, int k0, int k1, int w, int nw, int lane) {
    u32 vo[5], vs[5];
#pragma unroll
    for (int i = 0; i < 5; i++) {
        const int n = 64 * i + lane, rho = n / 10, c = n - 10 * rho, g = 2 * (rho >> 4) + ((rho >> 2) & 1), r8 = 4 * ((rho >> 3) & 1) + (rho & 3);
        vo[i] = (u32)((8 * (g < 2 ? g : g + 1) + r8) * 1280 + ct * 160 + c * 16);
        vs[i] = (u32)((40 * g + 16 + r8) * 1280 + ct * 160 + c * 16);
    }
#pragma unroll 1
    for (int kap = k0 + w; kap < k1; kap += nw) {
        const u32 dst = kap < 12 ? kap * 5120 : WO_HI + (kap - 12) * 5120;
        if (kap < 16) {
            const char* src = (const char*)wo + (size_t)kap * 51200;
#pragma unroll
            for (int i = 0; i < 5; i++) glds16(src, vo[i], dst + i * 1024);
        } else {
            const char* src = (const char*)wo + (size_t)(kap - 16) * 204800;
#pragma unroll
            for (int i = 0; i < 5; i++) glds16(src, vs[i], dst + i * 1024);
        }
    }
}

struct CvSet { u32x2 fl[8], fh[8]; u32x4 g[8]; };
DI void cv_load(LAS char* sm, CvSet& s, u32 fa, u32 ga) {
#pragma unroll
    for (int k1 = 0; k1 < 8; k1++) { s.fl[k1] = ldstr(sm, fa + 512 * k1 + 8); s.fh[k1] = ldstr(sm, fa + 512 * k1); s.g[k1] = lds128(sm, ga + 512 * k1); }
}
DI void cv_phase(LAS char* sm, f32x4 (&Y)[8], const CvSet& cur, CvSet& nxt, u32 fa, u32 ga) {
    PIN(fa); PIN(ga);
    SB();
    int n = 0;
#pragma unroll
    for (int e = 0; e < 8; e++)
#pragma unroll
        for (int k1 = 0; k1 + e < 8; k1++) {
            const u32x4 F = {cur.fl[k1].x, cur.fl[k1].y, cur.fh[k1].x, cur.fh[k1].y};
            Y[k1 + e] = mfma16(F, cur.g[e], Y[k1 + e]);
            if (n == 0) nxt.g[0] = lds128(sm, ga);
            else if (n <= 16) { const int r = (n - 1) >> 1; if ((n - 1) & 1) nxt.fh[r] = ldstr(sm, fa + 512 * r); else nxt.fl[r] = ldstr(sm, fa + 512 * r + 8); }
            else if (n < 24) nxt.g[n - 16] = lds128(sm, ga + 512 * (n - 16));
            n++;
            SB();
        }
}
struct CvIn { u32x4 hd[4]; u32x2 hm[4]; };
DI void conv_fetch_h(CvIn& in, const u16* hk, int c, int lane) {
    const char* hb = (const char*)hk + (size_t)c * 4096;
#pragma unroll
    for (int z = 0; z < 4; z++) {
        const int i = lane + 64 * z;
        if (NT_H) asm volatile("global_load_dwordx4 %0, %1, %2 nt" : "=v"(in.hd[z]) : "v"((u32)(i * 16)), "s"(hb) : "memory"); else gld128(in.hd[z], hb, (u32)(i * 16));
        gld64(in.hm[z], hb, (u32)(i > 0 ? i * 16 - 8 : 0));
    }
}
DI void conv_build_h(LAS char* sm0, CvIn& in, int w, int lane) {
    LAS char* sm = sm0 + CV_BASE + w * CV_WAVE;
#pragma unroll
    for (int z = 0; z < 4; z++) { PIN(in.hd[z]); PIN(in.hm[z]); }
    if (lane < 32) *(LAS u32x4*)(sm + CV_U + lane * 16) = (u32x4){0, 0, 0, 0};
    if (lane < 8) *(LAS u32x4*)(sm + (lane >> 1) * CV_PITCH + (lane & 1) * 16) = (u32x4){0, 0, 0, 0};
#pragma unroll
    for (int z = 0; z < 4; z++) {
        const int i = lane + 64 * z;
        u32 m2 = in.hm[z].x, m1 = in.hm[z].y;
        if (i == 0) { m2 = 0; m1 = 0; }
        const u32 d0 = in.hd[z].x, d1 = in.hd[z].y, d2 = in.hd[z].z, d3 = in.hd[z].w;
        const u32 om2 = __builtin_amdgcn_alignbit(m1, m2, 16), om1 = __builtin_amdgcn_alignbit(d0, m1, 16), o0 = __builtin_amdgcn_alignbit(d1, d0, 16),
                  o1 = __builtin_amdgcn_alignbit(d2, d1, 16), o2 = __builtin_amdgcn_alignbit(d3, d2, 16);
        LAS char* p = sm + 32 + i * 16;
        *(LAS u32x4*)(p) = (u32x4){d0, d1, d2, d3};
        *(LAS u32x4*)(p + CV_PITCH) = (u32x4){om1, o0, o1, o2};
        *(LAS u32x4*)(p + 2 * CV_PITCH) = (u32x4){m1, d0, d1, d2};
        *(LAS u32x4*)(p + 3 * CV_PITCH) = (u32x4){om2, om1, o0, o1};
    }
}
DI void conv_run(LAS char* sm0, float Dc, int w, int lane, int c, rsrc_t rw, char* ws) {
    LAS char* sm = sm0;
    LGKM0();
    TS(4, 9);
    const int fr = lane & 15, g = lane >> 4, q = fr >> 2, p = lane & 3;
    sm = sm0;
    const u32 fa = CV_BASE + w * CV_WAVE + q * CV_PITCH + 2 * (32 - 8 * g + 4 * p) - 8;
    const u32 ga = CV_BASE + w * CV_WAVE + CV_U + 480 + 32 * fr + 16 * g;
    f32x4 Y[8];
#pragma unroll
    for (int i = 0; i < 8; i++) Y[i] = (f32x4){0, 0, 0, 0};
    CvSet A, B;
    cv_load(sm, A, fa, ga);
    SB();
#pragma unroll 1
    for (int k0 = 0; k0 < 8; k0 += 2) {
        cv_phase(sm, Y, A, B, fa + 64 * (k0 + 1), ga - 64 * (k0 + 1));
        cv_phase(sm, Y, B, A, fa + 64 * (k0 + 2), ga - 64 * (k0 + 2));
    }
    TS(4, 10);
    sm = sm0 + CV_BASE + w * CV_WAVE;
    {
        u32x2 u4[8];
#pragma unroll
        for (int ib = 0; ib < 8; ib++) u4[ib] = lds64(sm, CV_U + 512 + 512 * ib + 32 * fr + 8 * g);
#pragma unroll
        for (int ib = 0; ib < 8; ib++) {
            const u32x2 r = {pk2(fmaf(blo(u4[ib].x), Dc, Y[ib][0]), fmaf(bhi(u4[ib].x), Dc, Y[ib][1])), pk2(fmaf(blo(u4[ib].y), Dc, Y[ib][2]), fmaf(bhi(u4[ib].y), Dc, Y[ib][3]))};
            *(LAS u32x2*)(sm + 512 * ib + 32 * fr + 8 * g) = r;
        }
    }
    LGKM0();
    u32x4 y8[4];
#pragma unroll
    for (int z = 0; z < 4; z++) y8[z] = lds128(sm, z * 1024 + lane * 16);
#pragma unroll
    for (int z = 0; z < 4; z++) st128c(rw, WS_YT + c * 4096 + z * 1024 + lane * 16, y8[z]);
}

DI void kvo_block(LAS char* sm, int wave, int lane, int h, int half, rsrc_t rw, u32 tag, char* ws) {
    const int fr = lane & 15, g = lane >> 4;
    const int j0 = (20 * h) >> 7, j1 = (20 * h + 19) >> 7;
    wait_flags(rw, lane < 8 ? (int)F_G1 + 16 * (((lane & 2) ? 10 : 5) + ((lane & 1) ? j1 : j0)) + 2 * wave + (lane >> 2)
                            : lane < 10 ? (int)F_G1 + 16 * ((lane & 1) ? j1 : j0) + 8 * half + wave : -1, -1, tag);
    TS(0, 8);
    const u32 pk0 = WS_PK0 + (h * 64 + 8 * wave) * 1024 + lane * 16, v0 = pk0 + (WS_V0 - WS_PK0);
    const u32 xx = ((fr & 4) ? WS_XV : WS_XK) + ((h * 64 + 8 * wave + (fr >> 3)) * 4 + g) * 64 + (fr & 3) * 16;
    u32x4 A[8], B[8], X[4], Q[8];
#pragma unroll
    for (int i = 0; i < 8; i++) { A[i] = ld128c(rw, pk0 + i * 1024); B[i] = ld128c(rw, v0 + i * 1024); if ((i & 1) == 0) X[i >> 1] = ld128c(rw, xx + (i >> 1) * 512); }
    const int odd = h & 1, pr = h >> 1;
    const int u5 = (g < 3 ? g : 2) + 2 * odd;
    const int sb0 = half * 64 + wave * 8;
    const u32 pq = WS_P2 + (((pr * 128 + sb0) * 5 + u5) * 128 + fr * 8) * 2;
    MEMF(); SB();
    f32x4 a00 = {0, 0, 0, 0}, a0x = a00, ax0 = a00, axx = a00;
    const u32 cst = fr == 8 ? 0x3F803F80u : 0u;
#pragma unroll
    for (int i = 0; i < 8; i++) {
        u32x4 xv = X[i >> 1];
        if (i & 1) {
#pragma unroll
            for (int e = 0; e < 4; e++) xv[e] = (u32)__builtin_amdgcn_update_dpp(0, (int)xv[e], 0x108, 0xf, 0xf, false);
        }
        if (fr >= 8) xv = (u32x4){cst, cst, cst, cst};
        a00 = mfma16(A[i], B[i], a00); a0x = mfma16(A[i], xv, a0x); ax0 = mfma16(xv, B[i], ax0); axx = mfma16(xv, xv, axx);
    }
    PIN(a00); PIN(a0x); PIN(ax0); PIN(axx);
    SB();
#pragma unroll
    for (int i = 0; i < 8; i++) Q[i] = ld128c(rw, pq + i * 1280);
    MEMF(); SB();
    TS(0, 9);
    *(LAS f32x4*)(sm + KO_RED + ((wave * 4 + 0) * 64 + lane) * 16) = a00;
    *(LAS f32x4*)(sm + KO_RED + ((wave * 4 + 1) * 64 + lane) * 16) = a0x;
    *(LAS f32x4*)(sm + KO_RED + ((wave * 4 + 2) * 64 + lane) * 16) = ax0;
    *(LAS f32x4*)(sm + KO_RED + ((wave * 4 + 3) * 64 + lane) * 16) = axx;
    if (wave >= 4 && (wave - 4) * 64 + lane < 189) *(LAS u32x2*)(sm + KO_KV + ((wave - 4) * 64 + lane) * 8) = (u32x2){0, 0};
    LGKM0(); BAR();
    if (wave < 4) {
        const int tile = wave;
        f32x4 s = *(LAS f32x4*)(sm + KO_RED + (tile * 64 + lane) * 16);
#pragma unroll
        for (int w2 = 1; w2 < 8; w2++) s += *(LAS f32x4*)(sm + KO_RED + ((w2 * 4 + tile) * 64 + lane) * 16);
        const int e = (tile & 1) ? 12 + fr : fr;
        const bool colok = (tile & 1) ? (fr >= 4 && fr <= 8) : true;
        const bool rowok = (tile & 2) ? (g == 0) : true;
        const int d0 = (tile & 2) ? 16 : 4 * g;
        if (colok && rowok) *(LAS u32x2*)(sm + KO_KV + e * 72 + (d0 + 4) * 2) = (u32x2){pk2(s[0], s[1]), pk2(s[2], s[3])};
    }
    LGKM0(); BAR();
    u32x4 A0, A1[4], A2;
    {
        const u32 dofs = KO_KV + (8 * g - 4 * odd + 4) * 2;
        const u32 r0 = (4 * odd + fr) * 72, r1 = ((odd ? 0 : 16) + (fr & 3)) * 72;
        const u32x2 p0 = lds64(sm, dofs + r0), p1 = lds64(sm, dofs + r0 + 8);
        const u32x2 q0 = lds64(sm, dofs + r1), q1 = lds64(sm, dofs + r1 + 8);
        const u32x2 z0 = lds64(sm, dofs + 20 * 72), z1 = lds64(sm, dofs + 20 * 72 + 8);
        A0 = (u32x4){p0.x, p0.y, p1.x, p1.y}; A2 = (u32x4){z0.x, z0.y, z1.x, z1.y};
#pragma unroll
        for (int z = 0; z < 4; z++) { const bool m = (fr >> 2) == z; A1[z] = (u32x4){m ? q0.x : 0u, m ? q0.y : 0u, m ? q1.x : 0u, m ? q1.y : 0u}; }
    }
    TS(0, 10);
    const u32 offA = WS_O + ((pr * 4 + 2 * odd + (g >> 1)) * 128 + fr * 8) * 2 + (g & 1) * 20480;
    const u32 offX = WS_O + (((16 + (pr >> 2)) * 4 + (pr & 3)) * 128 + odd * 64 + fr * 4) * 2;
    const f32x4 zero = {0, 0, 0, 0};
#pragma unroll
    for (int i4 = 0; i4 < 2; i4++) {
        f32x4 n1 = zero; float rden[4]; u32x2 ov[4];
#pragma unroll
        for (int z = 0; z < 4; z++) {
            const int i = 4 * i4 + z;
            const f32x4 n0 = mfma16(A0, Q[i], zero);
            const f32x4 dn = mfma16(A2, Q[i], zero);
            n1 = mfma16(A1[z], Q[i], n1);
            const float rd = __builtin_amdgcn_rcpf(dn[0] + 1e-6f);
            rden[z] = rd;
            ov[z] = (u32x2){pk2(n0[0] * rd, n0[1] * rd), pk2(n0[2] * rd, n0[3] * rd)};
        }
#pragma unroll
        for (int z = 0; z < 4; z += 2) {
            auto rx = __builtin_amdgcn_permlane16_swap(ov[z].x, ov[z + 1].x, false, false);
            auto ry = __builtin_amdgcn_permlane16_swap(ov[z].y, ov[z + 1].y, false, false);
            st128c(rw, offA + (sb0 + 4 * i4 + z) * 20480, (u32x4){rx[0], ry[0], rx[1], ry[1]});
        }
        const float rd = g == 0 ? rden[0] : g == 1 ? rden[1] : g == 2 ? rden[2] : rden[3];
        st64c(rw, offX + (sb0 + 4 * i4 + g) * 20480, (u32x2){pk2(n1[0] * rd, n1[1] * rd), pk2(n1[2] * rd, n1[3] * rd)});
    }
}

DI void gate_block(int wave, int lane, int m, const u16* x, const u16* gw, rsrc_t rw) {
    const int sub = lane & 15, r4 = lane >> 4;
    u32x4 gq[5], xr[4][5];
#pragma unroll
    for (int jj = 0; jj < 5; jj++) gq[jj] = *(const GAS u32x4*)((const GAS char*)gw + (sub + 16 * jj) * 16);
#pragma unroll
    for (int it = 0; it < 4; it++)
#pragma unroll
        for (int jj = 0; jj < 5; jj++) xr[it][jj] = *(const GAS u32x4*)((const GAS char*)x + (size_t)(128 * m + 16 * wave + 4 * it + r4) * 1280 + (sub + 16 * jj) * 16);
    MEMF(); SB();
#pragma unroll
    for (int it = 0; it < 4; it++) {
        float a = 0.f;
#pragma unroll
        for (int jj = 0; jj < 5; jj++)
#pragma unroll
            for (int e = 0; e < 4; e++) { a = fmaf(blo(xr[it][jj][e]), blo(gq[jj][e]), a); a = fmaf(bhi(xr[it][jj][e]), bhi(gq[jj][e]), a); }
        a += __shfl_xor(a, 1); a += __shfl_xor(a, 2); a += __shfl_xor(a, 4); a += __shfl_xor(a, 8);
        a = rbf(a);
        const float g = rbf(__builtin_amdgcn_rcpf(1.f + __builtin_amdgcn_exp2f(-1.4426950408889634f * a)));
        if (sub == 0) st32c(rw, WS_G + (128 * m + 16 * wave + 4 * it + r4) * 4, __float_as_uint(g));
    }
}

DI void final_block(LAS char* sm, int wave, int lane, int tid, int st, int ct, const u16* x, u16* out, rsrc_t rw, u32 tag, char* ws) {
    const int fr = lane & 15, g = lane >> 4;
    u32x4 xv[2];
#pragma unroll
    for (int r = 0; r < 2; r++) {
        int it = tid + 512 * r; it = it < 640 ? it : 639;
        const int s = it / 10, u = it - 10 * s;
        xv[r] = *(const GAS u32x4*)((const GAS char*)x + (size_t)(st * 64 + s) * 1280 + ct * 160 + u * 16);
    }
    VMCNT(0); BAR();
    if (wave < 4) {
        const int kh = wave & 1, sh = wave >> 1;
        wait_flags(rw, lane < 16 ? (int)F_O + (st >> 4) * 32 + 16 * kh + lane : -1, -1, tag);
        TS(0, 17);
        const u32 ob = WS_O + ((st * 4 + 2 * sh) * 20 + 8 * kh) * 1024 + lane * 16;
        const u32 os = WS_O + ((st * 4 + 2 * sh) * 20 + 16 + 2 * kh) * 1024 + g * 256 + fr * 8;
        u32x4 B[2][10];
#pragma unroll
        for (int kl = 0; kl < 8; kl++) { B[0][kl] = ld128c(rw, ob + kl * 1024); B[1][kl] = ld128c(rw, ob + 20480 + kl * 1024); }
#pragma unroll
        for (int kl = 0; kl < 2; kl++)
#pragma unroll
            for (int z = 0; z < 2; z++) {
                const u32x2 h0 = ld64c(rw, os + z * 20480 + kl * 1024), h1 = ld64c(rw, os + z * 20480 + kl * 1024 + 128);
                B[z][8 + kl] = (u32x4){h0.x, h0.y, h1.x, h1.y};
            }
        MEMF(); SB();
        f32x4 acc[5][2];
#pragma unroll
        for (int cb = 0; cb < 5; cb++) { acc[cb][0] = (f32x4){0, 0, 0, 0}; acc[cb][1] = (f32x4){0, 0, 0, 0}; }
        const u32 aoff = (16 * (g >> 1) + 4 * (g & 1) + (fr >> 2)) * 160 + (lane & 3) * 8;
        u32x2 al[2][5], ah[2][5];
#define F_KAP(kl) ((kl) < 8 ? (kl) : 8 + (kl))
#define F_READ(buf, kl) do { const u32 b0_ = kh ? wo_off(F_KAP(kl) + ((kl) < 8 ? 8 : 2)) : wo_off(F_KAP(kl)); \
            _Pragma("unroll") for (int cb = 0; cb < 5; cb++) { al[buf][cb] = ldstr(sm, b0_ + aoff + cb * 32); ah[buf][cb] = ldstr(sm, b0_ + aoff + cb * 32 + 1280); } } while (0)
        F_READ(0, 0);
#pragma unroll
        for (int kl = 0; kl < 10; kl++) {
            if (kl < 9) F_READ((kl + 1) & 1, kl + 1);
            SB();
#pragma unroll
            for (int cb = 0; cb < 5; cb++) {
                const u32x4 a = {al[kl & 1][cb].x, al[kl & 1][cb].y, ah[kl & 1][cb].x, ah[kl & 1][cb].y};
                acc[cb][0] = mfma16(a, B[0][kl], acc[cb][0]); acc[cb][1] = mfma16(a, B[1][kl], acc[cb][1]);
            }
            SB();
        }
        TS(0, 18);
        lds_arrive(sm, C_FR, lane); lds_wait(sm, C_FR, 4);
#pragma unroll
        for (int cb = 0; cb < 5; cb++)
#pragma unroll
            for (int z = 0; z < 2; z++)
                *(LAS f32x4*)(sm + kh * (64 * RED_PITCH) + (16 * (2 * sh + z) + fr) * RED_PITCH + (16 * cb + 4 * g) * 4) = acc[cb][z];
    } else {
        if (wave == 4) { wait_flags(rw, lane < 20 ? (int)F_Y + ct * 32 + lane : lane == 32 ? (int)F_GATE + (st >> 1) : -1, -1, tag); lds_arrive(sm, C_FY, lane); }
        else lds_wait(sm, C_FY, 1);
        const int t = tid - 256;
        u32x4 yv[3];
#pragma unroll
        for (int r = 0; r < 3; r++) { int id = t + 256 * r; id = id < 640 ? id : 639; yv[r] = ld128c(rw, WS_YT + (80 * ct + (id >> 3)) * 4096 + st * 128 + (id & 7) * 16); }
#pragma unroll
        for (int r = 0; r < 3; r++) { const int id = t + 256 * r; if (id < 640) *(LAS u32x4*)(sm + F_YY + id * 16) = yv[r]; }
        if (wave == 4) *(LAS u32*)(sm + F_GG + lane * 4) = ld32c(rw, WS_G + (st * 64 + lane) * 4);
        lds_arrive(sm, C_FY2, lane); lds_wait(sm, C_FY2, 4);
        const int sb = wave - 4, q = fr >> 2, p = lane & 3;
        u32x2 tt[5];
#pragma unroll
        for (int cb = 0; cb < 5; cb++) tt[cb] = ldstr(sm, F_YY + (16 * cb + 4 * g + q) * 128 + (16 * sb + 4 * p) * 2);
#pragma unroll
        for (int cb = 0; cb < 5; cb++) *(LAS u32x2*)(sm + F_Y2 + (16 * sb + fr) * 176 + (16 * cb + 4 * g) * 2) = tt[cb];
    }
    LGKM0(); BAR();
#pragma unroll
    for (int r = 0; r < 2; r++) {
        const int it = tid + 512 * r;
        if (it < 640) {
            const int s = it / 10, u = it - 10 * s;
            const u32 ra = s * RED_PITCH + u * 32;
            const f32x4 p0 = *(LAS f32x4*)(sm + ra), p1 = *(LAS f32x4*)(sm + ra + 16), q0 = *(LAS f32x4*)(sm + 64 * RED_PITCH + ra), q1 = *(LAS f32x4*)(sm + 64 * RED_PITCH + ra + 16);
            const u32x4 y8 = lds128(sm, F_Y2 + s * 176 + u * 16);
            const float gg = *(LAS float*)(sm + F_GG + s * 4), omg = 1.f - gg;
            float o[8];
#pragma unroll
            for (int e = 0; e < 4; e++) {
                const float a0 = (e < 2 ? p0[2 * e] + q0[2 * e] : p1[2 * e - 4] + q1[2 * e - 4]), a1 = (e < 2 ? p0[2 * e + 1] + q0[2 * e + 1] : p1[2 * e - 3] + q1[2 * e - 3]);
                const float b0 = omg * blo(xv[r][e]), b1 = omg * bhi(xv[r][e]);
                o[2 * e] = fmaf(b0, blo(y8[e]), gg * a0); o[2 * e + 1] = fmaf(b1, bhi(y8[e]), gg * a1);
            }
            { const u32x4 ov_ = {pk2(o[0], o[1]), pk2(o[2], o[3]), pk2(o[4], o[5]), pk2(o[6], o[7])};
              asm volatile("global_store_dwordx4 %0, %1, %2 nt\n\ts_nop 1" :: "v"((u32)((st * 64 + s) * 1280 + ct * 160 + u * 16)), "v"(ov_), "s"(out) : "memory"); }
        }
    }
}

__global__ __launch_bounds__(512, 2) void fused_kernel(const u16* __restrict__ x, const u16* __restrict__ w, const u16* __restrict__ wo, const u16* __restrict__ kin,
                                                        const u16* __restrict__ vin, const u16* __restrict__ shk, const u16* __restrict__ hk, const u16* __restrict__ sD,
                                                        const u16* __restrict__ gw, u16* __restrict__ out, char* ws) {
    extern __shared__ __attribute__((aligned(16))) char smem[];
    LAS char* sm = (LAS char*)smem;
    const int tid = threadIdx.x, lane = tid & 63, wave = __builtin_amdgcn_readfirstlane(tid >> 6);
    const int b = blockIdx.x, xcd = b & 7, j = b >> 3;
    const rsrc_t rw = __builtin_amdgcn_make_buffer_rsrc(ws, 0, WS_BYTES, 0x00020000);
    asm volatile("" :: "s"(x), "s"(w), "s"(wo), "s"(kin), "s"(vin), "s"(shk), "s"(hk), "s"(sD), "s"(gw), "s"(out));
    if (PREF1 && wave < 3 && j < 30) {
        if (wave == 0) touch(x, (u32)((xcd * 2560 + j * 80 + lane) * 128));
        else if (wave == 1) { if (lane < 16) touch(x, (u32)((xcd * 2560 + j * 80 + 64 + lane) * 128)); else touch(w, (u32)(((xcd * 30 + j) * 80 + lane - 16) * 128)); }
        else if (lane < 32) touch(w, (u32)(((xcd * 30 + j) * 80 + 48 + lane) * 128));
        else if (PREFTIN && lane < 42) touch(kin, (u32)((lane - 32) * 262144 + (j & 7) * 128));
        else if (PREFTIN && lane < 52) touch(vin, (u32)((lane - 42) * 262144 + (j & 7) * 128));
    }
    if (PREFKV && wave < 2 && j < 30) {
        const int tile_ = xcd * 30 + j;
        touch(wave == 0 ? kin : vin, (u32)(((tile_ / 10) * 64 + lane) * 1280 + (tile_ % 10) * 128));
    }
    if (PREF3 && wave < 2 && j < 30) {
        const int n240 = xcd * 30 + j;
        if (wave == 0) { const u32 l_ = n240 * 86 + lane; touch(hk, (l_ < 20480 ? l_ : 20479) * 128); }
        else { const u32 l_ = n240 * 86 + 64 + lane, m_ = n240 * 27 + lane - 22;
               if (lane < 22) touch(hk, (l_ < 20480 ? l_ : 20479) * 128); else if (lane < 49) touch(wo, (m_ < 6400 ? m_ : 6399) * 128); }
    }
    if (PREFC && wave == 3) {
        unsigned long long pc;
        asm volatile("s_getpc_b64 %0" : "=s"(pc));
        pc &= ~127ull;
        const int ln = PREFC == 1 ? j * 10 + lane : lane;
        if (PREFC == 1) { if (lane < 10 && ln < CODE_LINES) touch((const void*)pc, (u32)(ln * 128)); }
        else {
#pragma unroll
            for (int z = 0; z < 5; z++) if (z * 64 + lane < CODE_LINES) touch((const void*)pc, (u32)((z * 64 + lane) * 128));
        }
    }
    if (PREFT && wave == 2) {
        if (PREFT == 1) { touch_tlb(ws, (u32)(lane * 262144)); if (lane < 40) touch_tlb(out, (u32)(lane * 65536)); }
        else { if (lane < 8) touch_tlb(ws, (u32)((j * 8 + lane) * 65536) % (WS_STAMP - 1024)); if (lane < 40) touch_tlb(out, (u32)(lane * 65536)); }
    }
    u32 tag;
    asm volatile("s_load_dword %0, %1, 0x0\n\ts_waitcnt lgkmcnt(0)" : "=s"(tag) : "s"(ws + WS_FLAG + F_EPOCH * FS) : "memory");
    tag += 1u;
    if (tid < 16) *(LAS u32*)(sm + L_CNT + tid * 4) = 0u;
    TS(0, 0);
    const int st = 4 * xcd + (j >> 3), ct = j & 7;
    int ko = -1;
    bool is_conv = false;

    if (j >= 30) {
        const int m = xcd * 2 + (j - 30);
        ko = -100;
        {
            const int grp = wave >> 2, w4i = wave & 3;
            u32x2 w4[3];
#pragma unroll
            for (int t = 0; t < 3; t++) { const int tt = 2 * t + grp < 5 ? 2 * t + grp : 4; p0_weights(w4[t], shk, 240 + 5 * m + tt, w4i, lane); }
#pragma unroll
            for (int t = 0; t < 3; t++) if (2 * t + grp < 5) p0_issue(kin, vin, 240 + 5 * m + 2 * t + grp, w4i, lane, (2 * t + grp) * 17408);
            VMCNT(0);
#pragma unroll
            for (int t = 0; t < 3; t++) PIN(w4[t]);
            BAR();
#pragma unroll
            for (int t = 0; t < 3; t++)
                if (2 * t + grp < 5) p0_compute(sm, w4[t], 240 + 5 * m + 2 * t + grp, w4i, lane, (2 * t + grp) * 17408, 90112 + (2 * t + grp) * 8192);
            LGKM0(); BAR();
            {
                u32x4 t4[3][2];
#pragma unroll
                for (int t = 0; t < 3; t++)
#pragma unroll
                    for (int z = 0; z < 2; z++) { const int ch2 = ((tid & 255) >> 3) + 32 * z, o2 = tid & 7, tt = 2 * t + grp < 5 ? 2 * t + grp : 4; t4[t][z] = lds128(sm, 90112 + tt * 8192 + ch2 * 128 + ((o2 ^ (ch2 & 7)) * 16)); }
#pragma unroll
                for (int t = 0; t < 3; t++)
                    if (2 * t + grp < 5) {
                        const int tile = 240 + 5 * m + 2 * t + grp, cg = tile % 10, s0 = (tile / 10) * 64;
#pragma unroll
                        for (int z = 0; z < 2; z++) { const int ch2 = ((tid & 255) >> 3) + 32 * z, o2 = tid & 7; st128c(rw, WS_UT + (cg * 64 + ch2) * 4096 + (s0 + 8 * o2) * 2, t4[t][z]); }
                    }
            }
        }
        VMCNT(0); BAR();
        if (tid < 5) set_flag(rw, p0_flag(240 + 5 * m + tid), tag);
        TS(0, 1);
        gate_block(wave, lane, m, x, gw, rw);
        VMCNT(0); LGKM0(); BAR();
        if (tid == 0) set_flag(rw, F_GATE + m, tag);
        TS(0, 3);
    } else {
        const int rt = 2 * xcd + j / 15, cbk = j % 15, tile = xcd * 30 + j;
        is_conv = cbk < 5 || cbk >= 10;
        if (!is_conv) { const int ki = rt * 5 + (cbk - 5); ko = ki < 64 ? ki : -2 - (ki - 64); }
        if (wave >= 4) {
            const int lw = wave - 4;
            u32 vx[4], vw[4];
#pragma unroll
            for (int n2 = 0; n2 < 4; n2++) {
                const int n = 4 * lw + n2;
                const int row = 8 * n + (lane >> 3), ch = (lane & 7) ^ ((row >> 1) & 7);
                vx[n2] = (u32)((rt * 128 + row) * 1280 + ch * 16);
                const int kr = 4 * n + (lane >> 4), f = ((kr & 3) << 2) | ((kr >> 2) & 3), cw = (lane & 15) ^ f;
                vw[n2] = (u32)(kr * 3840 + cbk * 256 + cw * 16);
            }
            const int rot = ((rt >> 1) + cbk) % 10;
#define K1_ISSUE(c) do { const u32 st_ = ((c) & 3) * K1_STAGE + 4 * lw * 1024; int kc_ = (c) + ROT_ON * rot; kc_ = kc_ >= 10 ? kc_ - 10 : kc_; \
            const char* xb_ = (const char*)x + kc_ * 128; const char* wp_ = (const char*)w + (size_t)kc_ * 64 * 3840; \
            _Pragma("unroll") for (int n2 = 0; n2 < 4; n2++) { if (NT_X) glds16nt(xb_, vx[n2], st_ + 16384 + n2 * 1024); else glds16(xb_, vx[n2], st_ + 16384 + n2 * 1024); glds16(wp_, vw[n2], st_ + n2 * 1024); } } while (0)
            K1_ISSUE(0); K1_ISSUE(1);
            VMCNT(8); BAR();
            K1_ISSUE(2); K1_ISSUE(3);
            p0_issue(kin, vin, tile, lw, lane, P0_K);
#pragma unroll 1
            for (int c = 0; c < 6; c++) {
                if (c < 3) VMCNT(21); else VMCNT(16);
                BAR();
                K1_ISSUE(c + 4);
            }
            const int cv = rt * 10 + (cbk < 5 ? cbk : cbk - 5), c = 4 * cv + lw;
            if (is_conv) {
                CvIn in; u32 fl; u32x4 u[4];
                const int fidx = (int)F_P0 + (cv >> 4) * 32 + (lane & 31);
                VMCNT(16); BAR();
                VMCNT(8); BAR();
                conv_fetch_h(in, hk, c, lane);
                gld32c(fl, ws, WS_FLAG + fidx * FS);
                VMCNT(9); BAR();
                TS(4, 4);
                VMCNT(0); PIN(fl);
                const bool ready = __all(fl == tag);
                if (ready) {
#pragma unroll
                    for (int z = 0; z < 4; z++) gld128c(u[z], ws, WS_UT + c * 4096 + z * 1024 + lane * 16);
                }
                TS(4, 5);
                conv_build_h(sm, in, lw, lane);
                TS(4, 6);
                if (!ready) {
                    wait_flags(rw, fidx, -1, tag);
#pragma unroll
                    for (int z = 0; z < 4; z++) gld128c(u[z], ws, WS_UT + c * 4096 + z * 1024 + lane * 16);
                }
                TS(4, 7);
                VMCNT(0);
#pragma unroll
                for (int z = 0; z < 4; z++) { PIN(u[z]); *(LAS u32x4*)(sm + CV_BASE + lw * CV_WAVE + CV_U + 512 + z * 1024 + lane * 16) = u[z]; }
                const float Dc = blo((u32)sD[c]);
                TS(4, 8);
                conv_run(sm, Dc, lw, lane, c, rw, ws);
                publish_last(rw, sm, C_Y, 4, F_Y + (cv / 20) * 32 + cv % 20, tag, lane);
                TS(4, 11);
            } else {
                VMCNT(16); BAR();
                VMCNT(8); BAR();
                VMCNT(0); BAR();
                const int r32 = lane & 31, hh = lane >> 5;
                u32 dst, istep, zstep; kv_addr(true, cbk - 5, rt, lw >> 1, lw & 1, 1, r32, hh, dst, istep, zstep);
                lds_wait(sm, C_XF + lw * 4, 1);
                f32x4 a[2][4];
#pragma unroll
                for (int i = 0; i < 2; i++)
#pragma unroll
                    for (int q4 = 0; q4 < 4; q4++) a[i][q4] = *(LAS f32x4*)(sm + K_XFER + lw * 8192 + ((i * 4 + q4) * 64 + lane) * 16);
#pragma unroll
                for (int i = 0; i < 2; i++)
#pragma unroll
                    for (int z = 0; z < 2; z++) {
                        const f32x4 a0 = a[i][2 * z], a1 = a[i][2 * z + 1];
                        st128c(rw, dst + i * istep + z * zstep, (u32x4){phi2(a0[0], a0[1]), phi2(a0[2], a0[3]), phi2(a1[0], a1[1]), phi2(a1[2], a1[3])});
                    }
                publish_last(rw, sm, C_G1, 8, F_G1 + cbk * 16 + rt, tag, lane);
            }
        } else {
            u32x2 w4; p0_weights(w4, shk, tile, wave, lane);
            const int wm = wave >> 1, wn = wave & 1, r32 = lane & 31, hh = lane >> 5;
            K1Addr a0;
#pragma unroll
            for (int ks = 0; ks < 2; ks++) {
                a0.x[0][ks] = (64 * wm + r32) * 128 + (((2 * ks + hh) ^ ((r32 >> 1) & 7)) * 16);
                a0.x[1][ks] = a0.x[0][ks] ^ 64;
            }
#pragma unroll
            for (int jj = 0; jj < 2; jj++)
#pragma unroll
                for (int t = 0; t < 2; t++) {
                    const int blk = (lane >> 4) & 1, q = (lane & 15) >> 2, p = lane & 3, row = 8 * hh + 4 * t + q, ch = 8 * wn + 4 * jj + 2 * blk + (p >> 1);
                    const int f = ((row & 3) << 2) | ((row >> 2) & 3);
                    a0.w[jj][t] = row * 256 + ((ch ^ f) * 16) + 8 * (p & 1);
                }
            f32x16 acc[2][2];
#pragma unroll
            for (int i = 0; i < 2; i++)
#pragma unroll
                for (int jj = 0; jj < 2; jj++)
#pragma unroll
                    for (int e = 0; e < 16; e++) acc[i][jj][e] = 0.f;
            BAR();
            TS(0, 1);
            if (cbk < 5) {
                k1_loop<true>(sm, a0, acc, rw, p0_flag(tile), tag, wave, lane, hk, wo, tile, w4);
                __builtin_amdgcn_s_setprio(2);
                TS(0, 2);
                u32x4 qv[2][2][2];
#pragma unroll
                for (int jj = 0; jj < 2; jj++)
#pragma unroll
                    for (int ap = 0; ap < 2; ap++)
#pragma unroll
                        for (int i = 0; i < 2; i++) {
                            const f32x16& a = acc[i][jj];
                            const u32 x0 = phi2(a[8 * ap], a[8 * ap + 1]), y0 = phi2(a[8 * ap + 2], a[8 * ap + 3]);
                            const u32 x1 = phi2(a[8 * ap + 4], a[8 * ap + 5]), y1 = phi2(a[8 * ap + 6], a[8 * ap + 7]);
                            auto rx = __builtin_amdgcn_permlane32_swap(x0, x1, false, false);
                            auto ry = __builtin_amdgcn_permlane32_swap(y0, y1, false, false);
                            qv[jj][ap][i] = (u32x4){rx[0], ry[0], rx[1], ry[1]};
                        }
                if (QDELAY) { __builtin_amdgcn_s_setprio(0); for (int z = 0; z < QDELAY; z++) __builtin_amdgcn_s_sleep(8); }
#pragma unroll
                for (int jj = 0; jj < 2; jj++)
#pragma unroll
                    for (int ap = 0; ap < 2; ap++) {
                        const int U = 16 * cbk + 8 * wn + 4 * jj + 2 * ap + hh, pr = U / 5, u5 = U - 5 * pr;
                        const u32 dst = WS_P2 + (((pr * 128 + rt * 8 + 4 * wm + (r32 >> 4)) * 5 + u5) * 128 + (r32 & 15) * 8) * 2;
#pragma unroll
                        for (int i = 0; i < 2; i++) st128c(rw, dst + i * 2560, qv[jj][ap][i]);
                    }
            } else {
                k1_loop<false>(sm, a0, acc, rw, p0_flag(tile), tag, wave, lane, hk, wo, tile, w4);
                __builtin_amdgcn_s_setprio(2);
                TS(0, 2);
                const bool isk = cbk < 10;
                const int j5 = isk ? cbk - 5 : cbk - 10;
                if (isk) {
#pragma unroll
                    for (int i = 0; i < 2; i++)
#pragma unroll
                        for (int q4 = 0; q4 < 4; q4++)
                            *(LAS f32x4*)(sm + K_XFER + wave * 8192 + ((i * 4 + q4) * 64 + lane) * 16) = (f32x4){acc[i][1][4 * q4], acc[i][1][4 * q4 + 1], acc[i][1][4 * q4 + 2], acc[i][1][4 * q4 + 3]};
                    lds_arrive(sm, C_XF + wave * 4, lane);
                    u32 dst, istep, zstep; kv_addr(true, j5, rt, wm, wn, 0, r32, hh, dst, istep, zstep);
#pragma unroll
                    for (int i = 0; i < 2; i++)
#pragma unroll
                        for (int z = 0; z < 2; z++) {
                            const f32x16& a = acc[i][0];
                            st128c(rw, dst + i * istep + z * zstep,
                                   (u32x4){phi2(a[8 * z], a[8 * z + 1]), phi2(a[8 * z + 2], a[8 * z + 3]), phi2(a[8 * z + 4], a[8 * z + 5]), phi2(a[8 * z + 6], a[8 * z + 7])});
                        }
                } else {
#pragma unroll
                    for (int jj = 0; jj < 2; jj++) {
                        u32 dst, istep, zstep; kv_addr(false, j5, rt, wm, wn, jj, r32, hh, dst, istep, zstep);
#pragma unroll
                        for (int i = 0; i < 2; i++)
#pragma unroll
                            for (int z = 0; z < 2; z++) {
                                const f32x16& a = acc[i][jj];
                                st128c(rw, dst + i * istep + z * zstep,
                                       (u32x4){pk2(a[8 * z], a[8 * z + 1]), pk2(a[8 * z + 2], a[8 * z + 3]), pk2(a[8 * z + 4], a[8 * z + 5]), pk2(a[8 * z + 6], a[8 * z + 7])});
                            }
                    }
                }
            }
            LGKM0();
            TS(0, 25);
            publish_last(rw, sm, C_G1, is_conv ? 4 : 8, F_G1 + cbk * 16 + rt, tag, lane);
            __builtin_amdgcn_s_setprio(0);
            TS(0, 3);
            if (is_conv) {
                lds_wait(sm, C_G1, 4);
                wo_issue(wo, ct, 0, 12, wave, 4, lane);
            }
        }
    }
    if (!is_conv) {
        LGKM0(); BAR();
        if (ko >= 0) {
            const int h = ko >> 1, half = ko & 1;
            wo_issue(wo, ct, 0, 12, wave, 8, lane);
            kvo_block(sm, wave, lane, h, half, rw, tag, ws);
            TS(0, 26);
            VMCNT(0); BAR();
            if (tid == 0) set_flag(rw, F_O + half * 32 + h, tag);
            TS(0, 11);
            wo_issue(wo, ct, 12, 20, wave, 8, lane);
        } else {
            wo_issue(wo, ct, 0, 20, wave, 8, lane);
            if (ko == -2 && wave == 0) {
                wait_flags(rw, (int)F_G1 + lane, (int)F_G1 + 64 + lane, tag);
                wait_flags(rw, (int)F_G1 + 128 + lane, (int)F_G1 + 192 + lane, tag);
                if (lane == 0) set_flag(rw, F_EPOCH, tag);
            }
        }
    } else {
        VMCNT(0); BAR();
        if (wave >= 4) wo_issue(wo, ct, 12, 20, wave - 4, 4, lane);
    }
    TS(0, 16);
    final_block(sm, wave, lane, tid, st, ct, x, out, rw, tag, ws);
    TS(0, 19);
}

extern "C" void kernel_launch(void* const* d_in, const int* in_sizes, int n_in, void* d_out, int out_size, void* d_ws, size_t ws_size, hipStream_t stream) {
    const u16* x = (const u16*)d_in[0]; const u16* wqkv = (const u16*)d_in[1]; const u16* wo = (const u16*)d_in[2];
    const u16* kin = (const u16*)d_in[4]; const u16* vin = (const u16*)d_in[5]; const u16* shk = (const u16*)d_in[6];
    const u16* hk = (const u16*)d_in[7]; const u16* sD = (const u16*)d_in[8]; const u16* gw = (const u16*)d_in[9];
    (void)hipFuncSetAttribute((const void*)fused_kernel, hipFuncAttributeMaxDynamicSharedMemorySize, LDS_BYTES);
    fused_kernel<<<256, 512, LDS_BYTES, stream>>>(x, wqkv, wo, kin, vin, shk, hk, sD, gw, (u16*)d_out, (char*)d_ws);
}
```

```cpp
#include <hip/hip_runtime.h>
#include <stdint.h>

typedef unsigned short u16;
typedef unsigned int u32;
typedef __bf16 bf16x8 __attribute__((ext_vector_type(8)));
typedef __bf16 bf16x2 __attribute__((ext_vector_type(2)));
typedef float f32x4 __attribute__((ext_vector_type(4)));
typedef float f32x2 __attribute__((ext_vector_type(2)));
typedef float f32x16 __attribute__((ext_vector_type(16)));
typedef u32 u32x4 __attribute__((ext_vector_type(4)));
typedef u32 u32x2 __attribute__((ext_vector_type(2)));
typedef short s16x4 __attribute__((ext_vector_type(4)));
#define LAS __attribute__((address_space(3)))
#define GAS __attribute__((address_space(1)))
#define DI __device__ __forceinline__
typedef __amdgpu_buffer_rsrc_t rsrc_t;

constexpr int S = 2048, D = 640;
#define FS 256
#define POLL_SLEEP 1
#define ROT_ON 1

constexpr u32 WS_UT = 0;
constexpr u32 WS_YT = WS_UT + 2621440;
constexpr u32 WS_PK0 = WS_YT + 2621440;
constexpr u32 WS_V0 = WS_PK0 + 2097152;
constexpr u32 WS_XK = WS_V0 + 2097152;
constexpr u32 WS_XV = WS_XK + 524288;
constexpr u32 WS_P2 = WS_XV + 524288;
constexpr u32 WS_O = WS_P2 + 2621440;
constexpr u32 WS_G = WS_O + 2621440;
constexpr u32 WS_FLAG = WS_G + 8192;
constexpr u32 F_P0 = 0;
constexpr u32 F_G1 = 320;
constexpr u32 F_GATE = 560;
constexpr u32 F_O = 576;
constexpr u32 F_Y = 640;
constexpr u32 F_EPOCH = 896, F_TMO = 928;
constexpr u32 WS_STAMP = WS_FLAG + 1024 * FS;
constexpr u32 WS_BYTES = WS_STAMP + 256 * 32 * 16;

#define TS(w, id) do {} while (0)

DI u32 pk2(float a, float b) { f32x2 t = {a, b}; return __builtin_bit_cast(u32, __builtin_convertvector(t, bf16x2)); }
DI float blo(u32 u) { return __uint_as_float(u << 16); }
DI float bhi(u32 u) { return __uint_as_float(u & 0xffff0000u); }
DI float rbf(float f) { return blo(pk2(f, f)); }
DI bf16x8 asbf(u32x4 v) { return __builtin_bit_cast(bf16x8, v); }
DI f32x4 mfma16(u32x4 a, u32x4 b, f32x4 c) { return __builtin_amdgcn_mfma_f32_16x16x32_bf16(asbf(a), asbf(b), c, 0, 0, 0); }
DI f32x16 mfma32(u32x4 a, u32x4 b, f32x16 c) { return __builtin_amdgcn_mfma_f32_32x32x16_bf16(asbf(a), asbf(b), c, 0, 0, 0); }

DI void glds16(const void* sbase, u32 voff, u32 lds) {
    asm volatile("s_mov_b32 m0, %2\n\ts_nop 0\n\tglobal_load_lds_dwordx4 %0, %1" :: "v"(voff), "s"(sbase), "s"(lds) : "memory");
}
DI void glds16nt(const void* sbase, u32 voff, u32 lds) {
    asm volatile("s_mov_b32 m0, %2\n\ts_nop 0\n\tglobal_load_lds_dwordx4 %0, %1 nt" :: "v"(voff), "s"(sbase), "s"(lds) : "memory");
}
DI void touch(const void* sbase, u32 voff) {
    asm volatile("s_mov_b32 m0, %2\n\ts_nop 0\n\tglobal_load_lds_dword %0, %1" :: "v"(voff), "s"(sbase), "s"(156928u) : "memory");
}
DI void touch_tlb(const void* sbase, u32 voff) {
    asm volatile("s_mov_b32 m0, %2\n\ts_nop 0\n\tglobal_load_lds_dword %0, %1 sc1" :: "v"(voff), "s"(sbase), "s"(156928u) : "memory");
}
#define NT_KV 0
#define NT_X 0
#define NT_H 0
#define PREF1 1
#define PREF2 0
#define PREFC 1
#define CODE_LINES 314
#define PREFT 2
#define PREFKV 0
#define PREF3 1
#define PREFTIN 1
#define QDELAY 0
DI void gld64(u32x2& d, const void* sbase, u32 voff) { asm volatile("global_load_dwordx2 %0, %1, %2" : "=v"(d) : "v"(voff), "s"(sbase) : "memory"); }
DI void gld128(u32x4& d, const void* sbase, u32 voff) { asm volatile("global_load_dwordx4 %0, %1, %2" : "=v"(d) : "v"(voff), "s"(sbase) : "memory"); }
DI void gld128c(u32x4& d, const void* sbase, u32 voff) { asm volatile("global_load_dwordx4 %0, %1, %2 sc1" : "=v"(d) : "v"(voff), "s"(sbase) : "memory"); }
DI void gld32c(u32& d, const void* sbase, u32 voff) { asm volatile("global_load_dword %0, %1, %2 sc1" : "=v"(d) : "v"(voff), "s"(sbase) : "memory"); }
#define PIN(x) asm volatile("" : "+v"(x))
#define SB() __builtin_amdgcn_sched_barrier(0)
DI u32x4 ld128c(rsrc_t r, u32 off) { return __builtin_amdgcn_raw_buffer_load_b128(r, off, 0, 16); }
DI u32x2 ld64c(rsrc_t r, u32 off) { return __builtin_amdgcn_raw_buffer_load_b64(r, off, 0, 16); }
DI u32 ld32c(rsrc_t r, u32 off) { return __builtin_amdgcn_raw_buffer_load_b32(r, off, 0, 16); }
DI void st128c(rsrc_t r, u32 off, u32x4 v) { __builtin_amdgcn_raw_buffer_store_b128(v, r, off, 0, 16); }
DI void st64c(rsrc_t r, u32 off, u32x2 v) { __builtin_amdgcn_raw_buffer_store_b64(v, r, off, 0, 16); }
DI void st32c(rsrc_t r, u32 off, u32 v) { __builtin_amdgcn_raw_buffer_store_b32(v, r, off, 0, 16); }
DI u32x4 lds128(LAS char* sm, u32 a) { return *(LAS u32x4*)(sm + a); }
DI u32x2 lds64(LAS char* sm, u32 a) { return *(LAS u32x2*)(sm + a); }
DI u32x2 ldstr(LAS char* sm, u32 a) { return __builtin_bit_cast(u32x2, __builtin_amdgcn_ds_read_tr16_b64_v4i16((LAS s16x4*)(sm + a))); }
#define VMCNT(n) asm volatile("s_waitcnt vmcnt(" #n ")" ::: "memory")
#define LGKM0() asm volatile("s_waitcnt lgkmcnt(0)" ::: "memory")
#define BAR() do { asm volatile("" ::: "memory"); __builtin_amdgcn_s_barrier(); asm volatile("" ::: "memory"); } while (0)
#define MEMF() asm volatile("" ::: "memory")

DI void set_flag(rsrc_t r, u32 idx, u32 tag) { st32c(r, WS_FLAG + idx * FS, tag); }
DI void wait_flags(rsrc_t r, int idx0, int idx1, u32 tag) {
    unsigned long long t0 = 0;
    for (u32 it = 0;; it++) {
        u32 a = tag, b = tag;
        if (idx0 >= 0) a = ld32c(r, WS_FLAG + idx0 * FS);
        if (idx1 >= 0) b = ld32c(r, WS_FLAG + idx1 * FS);
        if (__all(a == tag && b == tag)) break;
        if (POLL_SLEEP) __builtin_amdgcn_s_sleep(POLL_SLEEP);
        if ((it & 63u) == 0u) {
            const unsigned long long t = __builtin_amdgcn_s_memrealtime();
            if (it == 0) t0 = t;
            else if (t - t0 > 2000000ull) { set_flag(r, F_TMO, tag); break; }
        }
    }
    MEMF();
}
DI void lds_arrive(LAS char* sm, u32 cnt, int lane) { LGKM0(); if (lane == 0) __hip_atomic_fetch_add((LAS u32*)(sm + cnt), 1u, __ATOMIC_RELAXED, __HIP_MEMORY_SCOPE_WORKGROUP); MEMF(); }
DI void lds_wait(LAS char* sm, u32 cnt, u32 n) { MEMF(); while (*(volatile LAS u32*)(sm + cnt) < n) __builtin_amdgcn_s_sleep(0); MEMF(); }
DI void publish_last(rsrc_t r, LAS char* sm, u32 cnt, u32 nw, u32 flag, u32 tag, int lane) {
    VMCNT(0);
    if (lane == 0) {
        const u32 old = __hip_atomic_fetch_add((LAS u32*)(sm + cnt), 1u, __ATOMIC_RELAXED, __HIP_MEMORY_SCOPE_WORKGROUP);
        if (old == nw - 1) set_flag(r, flag, tag);
    }
    MEMF();
}

DI u32 phi2(float a, float b) {
    const f32x2 t = {a, b}, s = t * 1.4426950408889634f;
    const float ea = __builtin_amdgcn_fmed3f(__builtin_amdgcn_exp2f(s.x), 0.f, 1.f), eb = __builtin_amdgcn_fmed3f(__builtin_amdgcn_exp2f(s.y), 0.f, 1.f);
    const float ma = __int_as_float(max(__float_as_int(a), 0)), mb = __int_as_float(max(__float_as_int(b), 0));
    const f32x2 r = ((f32x2){ea, eb} + (f32x2){ma, mb}) + 1e-6f;
    return pk2(r.x, r.y);
}

constexpr int K1_STAGE = 32768;
constexpr int P0_K = 131072, P0_V = P0_K + 9216, P0_OUT = P0_V + 8192;
constexpr int L_CNT = 156672;
constexpr int C_G1 = L_CNT, C_Y = L_CNT + 4, C_P0A = L_CNT + 8, C_P0B = L_CNT + 12, C_P0C = L_CNT + 16, C_FY = L_CNT + 20, C_FY2 = L_CNT + 24, C_FO = L_CNT + 28, C_FR = L_CNT + 32, C_XF = L_CNT + 36;
constexpr int CV_PITCH = 4160;
constexpr int CV_U = 4 * CV_PITCH;
constexpr int CV_WAVE = CV_U + 4608;
constexpr int CV_BASE = 65536;
constexpr int WO_HI = 65536;
constexpr int F_YY = 110592;
constexpr int F_Y2 = F_YY + 10240;
constexpr int F_GG = F_Y2 + 11264;
constexpr int KO_RED = 110592;
constexpr int KO_KV = KO_RED + 32768;
constexpr int RED_PITCH = 336;
constexpr int LDS_BYTES = 160 * 1024;
DI u32 p0_flag(int tile) { return F_P0 + (tile % 10) * 32 + tile / 10; }
DI constexpr u32 wo_off(int kap) { return kap < 12 ? kap * 5120 : WO_HI + (kap - 12) * 5120; }

DI void p0_issue(const u16* kin, const u16* vin, int tile, int lw, int lane, u32 base) {
    const int cg = tile % 10, rg = tile / 10, s0 = rg * 64;
    const u32 col = cg * 128 + (lane & 7) * 16;
#pragma unroll
    for (int z = 0; z < 2; z++) {
        const int n = 2 * lw + z;
        int r = s0 - 4 + n * 8 + (lane >> 3); r = r < 0 ? 0 : r;
        (NT_KV ? glds16nt : glds16)(kin, (u32)(r * 1280) + col, base + n * 1024);
        (NT_KV ? glds16nt : glds16)(vin, (u32)((s0 + n * 8 + (lane >> 3)) * 1280) + col, base + 9216 + n * 1024);
    }
    {
        int r = s0 + 60 + (lane >> 3); r = r > S - 1 ? S - 1 : r;
        (NT_KV ? glds16nt : glds16)(kin, (u32)(r * 1280) + col, base + 8 * 1024);
    }
}
DI float dot2(u32 a, u32 b, float c) { return __builtin_amdgcn_fdot2_f32_bf16(__builtin_bit_cast(bf16x2, a), __builtin_bit_cast(bf16x2, b), c, false); }
DI void p0_compute(LAS char* sm, u32x2 w4, int tile, int wave, int lane, u32 base, u32 outb, char* ws = nullptr) {
    const int s0 = (tile / 10) * 64;
    const int G = wave * 4 + (lane >> 4), cb = G & 3, i = lane & 15, q = i >> 2, p = i & 3;
    const u32 wa = __builtin_amdgcn_alignbit(w4.y, w4.y, 16), wb = __builtin_amdgcn_alignbit(w4.x, w4.x, 16);
    const int ch = 16 * cb + i;
    u32x2 kq[2][3], vq[2][2];
#pragma unroll
    for (int z = 0; z < 2; z++) {
        const int o = (G >> 2) + 4 * z;
        const u32 ka = base + (8 * o + q) * 128 + 32 * cb + 8 * p, va = ka + 9216;
        kq[z][0] = ldstr(sm, ka); kq[z][1] = ldstr(sm, ka + 512); kq[z][2] = ldstr(sm, ka + 1024);
        vq[z][0] = ldstr(sm, va); vq[z][1] = ldstr(sm, va + 512);
    }
#pragma unroll
    for (int z = 0; z < 2; z++) {
        const int o = (G >> 2) + 4 * z;
        u32 K[6] = {kq[z][0].x, kq[z][0].y, kq[z][1].x, kq[z][1].y, kq[z][2].x, kq[z][2].y};
        if (s0 == 0 && o == 0) { K[0] = 0; K[1] = 0; }
        u32 Sh[5];
#pragma unroll
        for (int e = 0; e < 5; e++) Sh[e] = __builtin_amdgcn_alignbit(K[e + 1], K[e], 16);
        const u32 V[4] = {vq[z][0].x, vq[z][0].y, vq[z][1].x, vq[z][1].y};
        u32 r[4];
#pragma unroll
        for (int e = 0; e < 4; e++) {
            const float k0 = dot2(Sh[e], wa, dot2(Sh[e + 1], wb, 0.f)), k1 = dot2(K[e + 1], wa, dot2(K[e + 2], wb, 0.f));
            r[e] = pk2(k0 * blo(V[e]), k1 * bhi(V[e]));
        }
        *(LAS u32x4*)(sm + outb + ch * 128 + ((o ^ (ch & 7)) * 16)) = (u32x4){r[0], r[1], r[2], r[3]};
    }
}
DI void p0_store(LAS char* sm, rsrc_t rw, int tile, int t256, u32 outb) {
    const int cg = tile % 10, s0 = (tile / 10) * 64;
#pragma unroll
    for (int z = 0; z < 2; z++) {
        const int ch2 = (t256 >> 3) + 32 * z, o2 = t256 & 7;
        const u32x4 t4 = lds128(sm, outb + ch2 * 128 + ((o2 ^ (ch2 & 7)) * 16));
        st128c(rw, WS_UT + (cg * 64 + ch2) * 4096 + (s0 + 8 * o2) * 2, t4);
    }
}
DI void p0_weights(u32x2& w4, const u16* shk, int tile, int wave, int lane) {
    const int cg = tile % 10, cb = (wave * 4 + (lane >> 4)) & 3;
    gld64(w4, shk, (u32)((cg * 64 + 16 * cb + (lane & 15)) * 8));
}

struct K1Set { u32x4 xa[2][2]; u32x2 wl[2][2], wh[2][2]; };
struct K1Addr { u32 x[2][2]; u32 w[2][2]; };
template <bool QT, int WO, int XO>
DI void k1_phase(LAS char* sm, f32x16 (&acc)[2][2], const K1Set& cur, K1Set& nxt, const u32 (&xb)[2], const u32 (&wb)[2][2]) {
#pragma unroll
    for (int n = 0; n < 8; n++) {
        const int ks = n >> 2, i = (n >> 1) & 1, jj = n & 1;
        const u32x4 wf = {cur.wl[ks][jj].x, cur.wl[ks][jj].y, cur.wh[ks][jj].x, cur.wh[ks][jj].y};
        acc[i][jj] = QT ? mfma32(wf, cur.xa[ks][i], acc[i][jj]) : mfma32(cur.xa[ks][i], wf, acc[i][jj]);
        if (n == 0) { nxt.wl[0][0] = ldstr(sm, wb[0][0] + WO); nxt.wh[0][0] = ldstr(sm, wb[0][1] + WO); }
        if (n == 1) { nxt.xa[0][0] = lds128(sm, xb[0] + XO); nxt.wl[0][1] = ldstr(sm, wb[1][0] + WO); }
        if (n == 2) { nxt.wh[0][1] = ldstr(sm, wb[1][1] + WO); nxt.xa[0][1] = lds128(sm, xb[0] + XO + 4096); }
        if (n == 3) { nxt.wl[1][0] = ldstr(sm, wb[0][0] + WO + 4096); nxt.wh[1][0] = ldstr(sm, wb[0][1] + WO + 4096); }
        if (n == 4) { nxt.xa[1][0] = lds128(sm, xb[1] + XO); nxt.wl[1][1] = ldstr(sm, wb[1][0] + WO + 4096); }
        if (n == 5) { nxt.wh[1][1] = ldstr(sm, wb[1][1] + WO + 4096); nxt.xa[1][1] = lds128(sm, xb[1] + XO + 4096); }
        SB();
    }
}
template <bool QT, int ST>
DI void k1_chunk(LAS char* sm, f32x16 (&acc)[2][2], K1Set& s0, K1Set& s1, const K1Addr& a0, const K1Addr& a1, bool bar, bool drain = false) {
    constexpr int SN = (ST + 1) & 3;
    const K1Addr& ac = ST < 2 ? a0 : a1; const K1Addr& an = SN < 2 ? a0 : a1;
    k1_phase<QT, (ST & 1) * K1_STAGE + 8192, (ST & 1) * K1_STAGE + 16384>(sm, acc, s0, s1, ac.x[1], ac.w);
    if (drain) VMCNT(0);
    if (bar) { LGKM0(); BAR(); }
    k1_phase<QT, (SN & 1) * K1_STAGE, (SN & 1) * K1_STAGE + 16384>(sm, acc, s1, s0, an.x[0], an.w);
}
template <bool QT>
DI void k1_loop(LAS char* sm, const K1Addr& a0, f32x16 (&acc)[2][2], rsrc_t rw, u32 pflag, u32 tag, int wave, int lane, const u16* hk, const u16* wo, int n240, u32x2 w4) {
    K1Addr a1;
#pragma unroll
    for (int i = 0; i < 2; i++)
#pragma unroll
        for (int z = 0; z < 2; z++) { a1.x[i][z] = a0.x[i][z] + 65536; a1.w[i][z] = a0.w[i][z] + 65536; }
    K1Set s0, s1;
#pragma unroll
    for (int ks = 0; ks < 2; ks++)
#pragma unroll
        for (int jj = 0; jj < 2; jj++) {
            s0.wl[ks][jj] = ldstr(sm, a0.w[jj][0] + ks * 4096); s0.wh[ks][jj] = ldstr(sm, a0.w[jj][1] + ks * 4096);
            s0.xa[ks][jj] = lds128(sm, a0.x[0][ks] + 16384 + jj * 4096);
        }
    SB();
#pragma unroll 1
    for (int c = 0; c < 8; c += 4) {
        k1_chunk<QT, 0>(sm, acc, s0, s1, a0, a1, true);
        if (c == 4) p0_store(sm, rw, n240, wave * 64 + lane, P0_OUT);
        k1_chunk<QT, 1>(sm, acc, s0, s1, a0, a1, true);
        k1_chunk<QT, 2>(sm, acc, s0, s1, a0, a1, true);
        k1_chunk<QT, 3>(sm, acc, s0, s1, a0, a1, true, c == 4);
        if (c == 4 && wave == 0 && lane == 0) set_flag(rw, pflag, tag);
        if (c == 0) { VMCNT(0); PIN(w4); p0_compute(sm, w4, n240, wave, lane, P0_K, P0_OUT); }
        if (PREF2 && c == 0 && wave >= 2) {
            if (wave == 2) { const u32 l_ = n240 * 86 + lane; touch(hk, (l_ < 20480 ? l_ : 20479) * 128); }
            else { const u32 l_ = n240 * 86 + 64 + lane, m_ = n240 * 27 + lane - 22;
                   if (lane < 22) touch(hk, (l_ < 20480 ? l_ : 20479) * 128); else if (lane < 49) touch(wo, (m_ < 6400 ? m_ : 6399) * 128); }
        }
    }
    k1_chunk<QT, 0>(sm, acc, s0, s1, a0, a1, true);
    k1_chunk<QT, 1>(sm, acc, s0, s1, a0, a1, false);
}

DI void kv_addr(bool isk, int j5, int rt, int wm, int wn, int jj, int r32, int hh, u32& dst, u32& istep, u32& zstep) {
    const int cc = 128 * j5 + 64 * wn + 32 * jj + r32, h = cc / 20, d = cc - 20 * h;
    const int sb32 = rt * 4 + 2 * wm;
    const bool lo = d < 16;
    dst = lo ? (isk ? WS_PK0 : WS_V0) + (((h * 64 + sb32) * 4 + hh) * 128 + d * 8) * 2
             : (isk ? WS_XK : WS_XV) + (((h * 64 + sb32) * 4 + hh) * 32 + (d - 16) * 8) * 2;
    istep = lo ? 1024 : 256; zstep = lo ? 512 : 128;
}
constexpr int K_XFER = 65536;

DI void wo_issue(const u16* wo, int ct, int k0, int k1, int w, int nw, int lane) {
    u32 vo[5], vs[5];
#pragma unroll
    for (int i = 0; i < 5; i++) {
        const int n = 64 * i + lane, rho = n / 10, c = n - 10 * rho, g = 2 * (rho >> 4) + ((rho >> 2) & 1), r8 = 4 * ((rho >> 3) & 1) + (rho & 3);
        vo[i] = (u32)((8 * (g < 2 ? g : g + 1) + r8) * 1280 + ct * 160 + c * 16);
        vs[i] = (u32)((40 * g + 16 + r8) * 1280 + ct * 160 + c * 16);
    }
#pragma unroll 1
    for (int kap = k0 + w; kap < k1; kap += nw) {
        const u32 dst = kap < 12 ? kap * 5120 : WO_HI + (kap - 12) * 5120;
        if (kap < 16) {
            const char* src = (const char*)wo + (size_t)kap * 51200;
#pragma unroll
            for (int i = 0; i < 5; i++) glds16(src, vo[i], dst + i * 1024);
        } else {
            const char* src = (const char*)wo + (size_t)(kap - 16) * 204800;
#pragma unroll
            for (int i = 0; i < 5; i++) glds16(src, vs[i], dst + i * 1024);
        }
    }
}

struct CvSet { u32x2 fl[8], fh[8]; u32x4 g[8]; };
DI void cv_load(LAS char* sm, CvSet& s, u32 fa, u32 ga) {
#pragma unroll
    for (int k1 = 0; k1 < 8; k1++) { s.fl[k1] = ldstr(sm, fa + 512 * k1 + 8); s.fh[k1] = ldstr(sm, fa + 512 * k1); s.g[k1] = lds128(sm, ga + 512 * k1); }
}
DI void cv_phase(LAS char* sm, f32x4 (&Y)[8], const CvSet& cur, CvSet& nxt, u32 fa, u32 ga) {
    PIN(fa); PIN(ga);
    SB();
    int n = 0;
#pragma unroll
    for (int e = 0; e < 8; e++)
#pragma unroll
        for (int k1 = 0; k1 + e < 8; k1++) {
            const u32x4 F = {cur.fl[k1].x, cur.fl[k1].y, cur.fh[k1].x, cur.fh[k1].y};
            Y[k1 + e] = mfma16(F, cur.g[e], Y[k1 + e]);
            if (n == 0) nxt.g[0] = lds128(sm, ga);
            else if (n <= 16) { const int r = (n - 1) >> 1; if ((n - 1) & 1) nxt.fh[r] = ldstr(sm, fa + 512 * r); else nxt.fl[r] = ldstr(sm, fa + 512 * r + 8); }
            else if (n < 24) nxt.g[n - 16] = lds128(sm, ga + 512 * (n - 16));
            n++;
            SB();
        }
}
struct CvIn { u32x4 hd[4]; u32x2 hm[4]; };
DI void conv_fetch_h(CvIn& in, const u16* hk, int c, int lane) {
    const char* hb = (const char*)hk + (size_t)c * 4096;
#pragma unroll
    for (int z = 0; z < 4; z++) {
        const int i = lane + 64 * z;
        if (NT_H) asm volatile("global_load_dwordx4 %0, %1, %2 nt" : "=v"(in.hd[z]) : "v"((u32)(i * 16)), "s"(hb) : "memory"); else gld128(in.hd[z], hb, (u32)(i * 16));
        gld64(in.hm[z], hb, (u32)(i > 0 ? i * 16 - 8 : 0));
    }
}
DI void conv_build_h(LAS char* sm0, CvIn& in, int w, int lane) {
    LAS char* sm = sm0 + CV_BASE + w * CV_WAVE;
#pragma unroll
    for (int z = 0; z < 4; z++) { PIN(in.hd[z]); PIN(in.hm[z]); }
    if (lane < 32) *(LAS u32x4*)(sm + CV_U + lane * 16) = (u32x4){0, 0, 0, 0};
    if (lane < 8) *(LAS u32x4*)(sm + (lane >> 1) * CV_PITCH + (lane & 1) * 16) = (u32x4){0, 0, 0, 0};
#pragma unroll
    for (int z = 0; z < 4; z++) {
        const int i = lane + 64 * z;
        u32 m2 = in.hm[z].x, m1 = in.hm[z].y;
        if (i == 0) { m2 = 0; m1 = 0; }
        const u32 d0 = in.hd[z].x, d1 = in.hd[z].y, d2 = in.hd[z].z, d3 = in.hd[z].w;
        const u32 om2 = __builtin_amdgcn_alignbit(m1, m2, 16), om1 = __builtin_amdgcn_alignbit(d0, m1, 16), o0 = __builtin_amdgcn_alignbit(d1, d0, 16),
                  o1 = __builtin_amdgcn_alignbit(d2, d1, 16), o2 = __builtin_amdgcn_alignbit(d3, d2, 16);
        LAS char* p = sm + 32 + i * 16;
        *(LAS u32x4*)(p) = (u32x4){d0, d1, d2, d3};
        *(LAS u32x4*)(p + CV_PITCH) = (u32x4){om1, o0, o1, o2};
        *(LAS u32x4*)(p + 2 * CV_PITCH) = (u32x4){m1, d0, d1, d2};
        *(LAS u32x4*)(p + 3 * CV_PITCH) = (u32x4){om2, om1, o0, o1};
    }
}
DI void conv_run(LAS char* sm0, float Dc, int w, int lane, int c, rsrc_t rw, char* ws) {
    LAS char* sm = sm0;
    LGKM0();
    TS(4, 9);
    const int fr = lane & 15, g = lane >> 4, q = fr >> 2, p = lane & 3;
    sm = sm0;
    const u32 fa = CV_BASE + w * CV_WAVE + q * CV_PITCH + 2 * (32 - 8 * g + 4 * p) - 8;
    const u32 ga = CV_BASE + w * CV_WAVE + CV_U + 480 + 32 * fr + 16 * g;
    f32x4 Y[8];
#pragma unroll
    for (int i = 0; i < 8; i++) Y[i] = (f32x4){0, 0, 0, 0};
    CvSet A, B;
    cv_load(sm, A, fa, ga);
    SB();
#pragma unroll 1
    for (int k0 = 0; k0 < 8; k0 += 2) {
        cv_phase(sm, Y, A, B, fa + 64 * (k0 + 1), ga - 64 * (k0 + 1));
        cv_phase(sm, Y, B, A, fa + 64 * (k0 + 2), ga - 64 * (k0 + 2));
    }
    TS(4, 10);
    sm = sm0 + CV_BASE + w * CV_WAVE;
    {
        u32x2 u4[8];
#pragma unroll
        for (int ib = 0; ib < 8; ib++) u4[ib] = lds64(sm, CV_U + 512 + 512 * ib + 32 * fr + 8 * g);
#pragma unroll
        for (int ib = 0; ib < 8; ib++) {
            const u32 y0 = pk2(Y[ib][0], Y[ib][1]), y1 = pk2(Y[ib][2], Y[ib][3]);
            const u32 d0 = pk2(blo(u4[ib].x) * Dc, bhi(u4[ib].x) * Dc), d1 = pk2(blo(u4[ib].y) * Dc, bhi(u4[ib].y) * Dc);
            const u32x2 r = {pk2(blo(y0) + blo(d0), bhi(y0) + bhi(d0)), pk2(blo(y1) + blo(d1), bhi(y1) + bhi(d1))};
            *(LAS u32x2*)(sm + 512 * ib + 32 * fr + 8 * g) = r;
        }
    }
    LGKM0();
    u32x4 y8[4];
#pragma unroll
    for (int z = 0; z < 4; z++) y8[z] = lds128(sm, z * 1024 + lane * 16);
#pragma unroll
    for (int z = 0; z < 4; z++) st128c(rw, WS_YT + c * 4096 + z * 1024 + lane * 16, y8[z]);
}

DI void kvo_block(LAS char* sm, int wave, int lane, int h, int half, rsrc_t rw, u32 tag, char* ws) {
    const int fr = lane & 15, g = lane >> 4;
    const int j0 = (20 * h) >> 7, j1 = (20 * h + 19) >> 7;
    wait_flags(rw, lane < 8 ? (int)F_G1 + 16 * (((lane & 2) ? 10 : 5) + ((lane & 1) ? j1 : j0)) + 2 * wave + (lane >> 2)
                            : lane < 10 ? (int)F_G1 + 16 * ((lane & 1) ? j1 : j0) + 8 * half + wave : -1, -1, tag);
    TS(0, 8);
    const u32 pk0 = WS_PK0 + (h * 64 + 8 * wave) * 1024 + lane * 16, v0 = pk0 + (WS_V0 - WS_PK0);
    const u32 xx = ((fr & 4) ? WS_XV : WS_XK) + ((h * 64 + 8 * wave + (fr >> 3)) * 4 + g) * 64 + (fr & 3) * 16;
    u32x4 A[8], B[8], X[4], Q[8];
#pragma unroll
    for (int i = 0; i < 8; i++) { A[i] = ld128c(rw, pk0 + i * 1024); B[i] = ld128c(rw, v0 + i * 1024); if ((i & 1) == 0) X[i >> 1] = ld128c(rw, xx + (i >> 1) * 512); }
    const int odd = h & 1, pr = h >> 1;
    const int u5 = (g < 3 ? g : 2) + 2 * odd;
    const int sb0 = half * 64 + wave * 8;
    const u32 pq = WS_P2 + (((pr * 128 + sb0) * 5 + u5) * 128 + fr * 8) * 2;
    MEMF(); SB();
    f32x4 a00 = {0, 0, 0, 0}, a0x = a00, ax0 = a00, axx = a00;
    const u32 cst = fr == 8 ? 0x3F803F80u : 0u;
#pragma unroll
    for (int i = 0; i < 8; i++) {
        u32x4 xv = X[i >> 1];
        if (i & 1) {
#pragma unroll
            for (int e = 0; e < 4; e++) xv[e] = (u32)__builtin_amdgcn_update_dpp(0, (int)xv[e], 0x108, 0xf, 0xf, false);
        }
        if (fr >= 8) xv = (u32x4){cst, cst, cst, cst};
        a00 = mfma16(A[i], B[i], a00); a0x = mfma16(A[i], xv, a0x); ax0 = mfma16(xv, B[i], ax0); axx = mfma16(xv, xv, axx);
    }
    PIN(a00); PIN(a0x); PIN(ax0); PIN(axx);
    SB();
#pragma unroll
    for (int i = 0; i < 8; i++) Q[i] = ld128c(rw, pq + i * 1280);
    MEMF(); SB();
    TS(0, 9);
    *(LAS f32x4*)(sm + KO_RED + ((wave * 4 + 0) * 64 + lane) * 16) = a00;
    *(LAS f32x4*)(sm + KO_RED + ((wave * 4 + 1) * 64 + lane) * 16) = a0x;
    *(LAS f32x4*)(sm + KO_RED + ((wave * 4 + 2) * 64 + lane) * 16) = ax0;
    *(LAS f32x4*)(sm + KO_RED + ((wave * 4 + 3) * 64 + lane) * 16) = axx;
    if (wave >= 4 && (wave - 4) * 64 + lane < 189) *(LAS u32x2*)(sm + KO_KV + ((wave - 4) * 64 + lane) * 8) = (u32x2){0, 0};
    LGKM0(); BAR();
    if (wave < 4) {
        const int tile = wave;
        f32x4 s = *(LAS f32x4*)(sm + KO_RED + (tile * 64 + lane) * 16);
#pragma unroll
        for (int w2 = 1; w2 < 8; w2++) s += *(LAS f32x4*)(sm + KO_RED + ((w2 * 4 + tile) * 64 + lane) * 16);
        const int e = (tile & 1) ? 12 + fr : fr;
        const bool colok = (tile & 1) ? (fr >= 4 && fr <= 8) : true;
        const bool rowok = (tile & 2) ? (g == 0) : true;
        const int d0 = (tile & 2) ? 16 : 4 * g;
        if (colok && rowok) *(LAS u32x2*)(sm + KO_KV + e * 72 + (d0 + 4) * 2) = (u32x2){pk2(s[0], s[1]), pk2(s[2], s[3])};
    }
    LGKM0(); BAR();
    u32x4 A0, A1[4], A2;
    {
        const u32 dofs = KO_KV + (8 * g - 4 * odd + 4) * 2;
        const u32 r0 = (4 * odd + fr) * 72, r1 = ((odd ? 0 : 16) + (fr & 3)) * 72;
        const u32x2 p0 = lds64(sm, dofs + r0), p1 = lds64(sm, dofs + r0 + 8);
        const u32x2 q0 = lds64(sm, dofs + r1), q1 = lds64(sm, dofs + r1 + 8);
        const u32x2 z0 = lds64(sm, dofs + 20 * 72), z1 = lds64(sm, dofs + 20 * 72 + 8);
        A0 = (u32x4){p0.x, p0.y, p1.x, p1.y}; A2 = (u32x4){z0.x, z0.y, z1.x, z1.y};
#pragma unroll
        for (int z = 0; z < 4; z++) { const bool m = (fr >> 2) == z; A1[z] = (u32x4){m ? q0.x : 0u, m ? q0.y : 0u, m ? q1.x : 0u, m ? q1.y : 0u}; }
    }
    TS(0, 10);
    const u32 offA = WS_O + ((pr * 4 + 2 * odd + (g >> 1)) * 128 + fr * 8) * 2 + (g & 1) * 20480;
    const u32 offX = WS_O + (((16 + (pr >> 2)) * 4 + (pr & 3)) * 128 + odd * 64 + fr * 4) * 2;
    const f32x4 zero = {0, 0, 0, 0};
#pragma unroll
    for (int i4 = 0; i4 < 2; i4++) {
        f32x4 n1 = zero; float rden[4]; u32x2 ov[4];
#pragma unroll
        for (int z = 0; z < 4; z++) {
            const int i = 4 * i4 + z;
            const f32x4 n0 = mfma16(A0, Q[i], zero);
            const f32x4 dn = mfma16(A2, Q[i], zero);
            n1 = mfma16(A1[z], Q[i], n1);
            const float rd = __builtin_amdgcn_rcpf(dn[0] + 1e-6f);
            rden[z] = rd;
            ov[z] = (u32x2){pk2(n0[0] * rd, n0[1] * rd), pk2(n0[2] * rd, n0[3] * rd)};
        }
#pragma unroll
        for (int z = 0; z < 4; z += 2) {
            auto rx = __builtin_amdgcn_permlane16_swap(ov[z].x, ov[z + 1].x, false, false);
            auto ry = __builtin_amdgcn_permlane16_swap(ov[z].y, ov[z + 1].y, false, false);
            st128c(rw, offA + (sb0 + 4 * i4 + z) * 20480, (u32x4){rx[0], ry[0], rx[1], ry[1]});
        }
        const float rd = g == 0 ? rden[0] : g == 1 ? rden[1] : g == 2 ? rden[2] : rden[3];
        st64c(rw, offX + (sb0 + 4 * i4 + g) * 20480, (u32x2){pk2(n1[0] * rd, n1[1] * rd), pk2(n1[2] * rd, n1[3] * rd)});
    }
}

DI void gate_block(int wave, int lane, int m, const u16* x, const u16* gw, rsrc_t rw) {
    const int sub = lane & 15, r4 = lane >> 4;
    u32x4 gq[5], xr[4][5];
#pragma unroll
    for (int jj = 0; jj < 5; jj++) gq[jj] = *(const GAS u32x4*)((const GAS char*)gw + (sub + 16 * jj) * 16);
#pragma unroll
    for (int it = 0; it < 4; it++)
#pragma unroll
        for (int jj = 0; jj < 5; jj++) xr[it][jj] = *(const GAS u32x4*)((const GAS char*)x + (size_t)(128 * m + 16 * wave + 4 * it + r4) * 1280 + (sub + 16 * jj) * 16);
    MEMF(); SB();
#pragma unroll
    for (int it = 0; it < 4; it++) {
        float a = 0.f;
#pragma unroll
        for (int jj = 0; jj < 5; jj++)
#pragma unroll
            for (int e = 0; e < 4; e++) { a = fmaf(blo(xr[it][jj][e]), blo(gq[jj][e]), a); a = fmaf(bhi(xr[it][jj][e]), bhi(gq[jj][e]), a); }
        a += __shfl_xor(a, 1); a += __shfl_xor(a, 2); a += __shfl_xor(a, 4); a += __shfl_xor(a, 8);
        a = rbf(a);
        const float g = rbf(__builtin_amdgcn_rcpf(1.f + __builtin_amdgcn_exp2f(-1.4426950408889634f * a)));
        if (sub == 0) st32c(rw, WS_G + (128 * m + 16 * wave + 4 * it + r4) * 4, __float_as_uint(g));
    }
}

DI void final_block(LAS char* sm, int wave, int lane, int tid, int st, int ct, const u16* x, u16* out, rsrc_t rw, u32 tag, char* ws) {
    const int fr = lane & 15, g = lane >> 4;
    u32x4 xv[2];
#pragma unroll
    for (int r = 0; r < 2; r++) {
        int it = tid + 512 * r; it = it < 640 ? it : 639;
        const int s = it / 10, u = it - 10 * s;
        xv[r] = *(const GAS u32x4*)((const GAS char*)x + (size_t)(st * 64 + s) * 1280 + ct * 160 + u * 16);
    }
    VMCNT(0); BAR();
    if (wave < 4) {
        const int kh = wave & 1, sh = wave >> 1;
        wait_flags(rw, lane < 16 ? (int)F_O + (st >> 4) * 32 + 16 * kh + lane : -1, -1, tag);
        TS(0, 17);
        const u32 ob = WS_O + ((st * 4 + 2 * sh) * 20 + 8 * kh) * 1024 + lane * 16;
        const u32 os = WS_O + ((st * 4 + 2 * sh) * 20 + 16 + 2 * kh) * 1024 + g * 256 + fr * 8;
        u32x4 B[2][10];
#pragma unroll
        for (int kl = 0; kl < 8; kl++) { B[0][kl] = ld128c(rw, ob + kl * 1024); B[1][kl] = ld128c(rw, ob + 20480 + kl * 1024); }
#pragma unroll
        for (int kl = 0; kl < 2; kl++)
#pragma unroll
            for (int z = 0; z < 2; z++) {
                const u32x2 h0 = ld64c(rw, os + z * 20480 + kl * 1024), h1 = ld64c(rw, os + z * 20480 + kl * 1024 + 128);
                B[z][8 + kl] = (u32x4){h0.x, h0.y, h1.x, h1.y};
            }
        MEMF(); SB();
        f32x4 acc[5][2];
#pragma unroll
        for (int cb = 0; cb < 5; cb++) { acc[cb][0] = (f32x4){0, 0, 0, 0}; acc[cb][1] = (f32x4){0, 0, 0, 0}; }
        const u32 aoff = (16 * (g >> 1) + 4 * (g & 1) + (fr >> 2)) * 160 + (lane & 3) * 8;
        u32x2 al[2][5], ah[2][5];
#define F_KAP(kl) ((kl) < 8 ? (kl) : 8 + (kl))
#define F_READ(buf, kl) do { const u32 b0_ = kh ? wo_off(F_KAP(kl) + ((kl) < 8 ? 8 : 2)) : wo_off(F_KAP(kl)); \
            _Pragma("unroll") for (int cb = 0; cb < 5; cb++) { al[buf][cb] = ldstr(sm, b0_ + aoff + cb * 32); ah[buf][cb] = ldstr(sm, b0_ + aoff + cb * 32 + 1280); } } while (0)
        F_READ(0, 0);
#pragma unroll
        for (int kl = 0; kl < 10; kl++) {
            if (kl < 9) F_READ((kl + 1) & 1, kl + 1);
            SB();
#pragma unroll
            for (int cb = 0; cb < 5; cb++) {
                const u32x4 a = {al[kl & 1][cb].x, al[kl & 1][cb].y, ah[kl & 1][cb].x, ah[kl & 1][cb].y};
                acc[cb][0] = mfma16(a, B[0][kl], acc[cb][0]); acc[cb][1] = mfma16(a, B[1][kl], acc[cb][1]);
            }
            SB();
        }
        TS(0, 18);
        lds_arrive(sm, C_FR, lane); lds_wait(sm, C_FR, 4);
#pragma unroll
        for (int cb = 0; cb < 5; cb++)
#pragma unroll
            for (int z = 0; z < 2; z++)
                *(LAS f32x4*)(sm + kh * (64 * RED_PITCH) + (16 * (2 * sh + z) + fr) * RED_PITCH + (16 * cb + 4 * g) * 4) = acc[cb][z];
    } else {
        if (wave == 4) { wait_flags(rw, lane < 20 ? (int)F_Y + ct * 32 + lane : lane == 32 ? (int)F_GATE + (st >> 1) : -1, -1, tag); lds_arrive(sm, C_FY, lane); }
        else lds_wait(sm, C_FY, 1);
        const int t = tid - 256;
        u32x4 yv[3];
#pragma unroll
        for (int r = 0; r < 3; r++) { int id = t + 256 * r; id = id < 640 ? id : 639; yv[r] = ld128c(rw, WS_YT + (80 * ct + (id >> 3)) * 4096 + st * 128 + (id & 7) * 16); }
#pragma unroll
        for (int r = 0; r < 3; r++) { const int id = t + 256 * r; if (id < 640) *(LAS u32x4*)(sm + F_YY + id * 16) = yv[r]; }
        if (wave == 4) *(LAS u32*)(sm + F_GG + lane * 4) = ld32c(rw, WS_G + (st * 64 + lane) * 4);
        lds_arrive(sm, C_FY2, lane); lds_wait(sm, C_FY2, 4);
        const int sb = wave - 4, q = fr >> 2, p = lane & 3;
        u32x2 tt[5];
#pragma unroll
        for (int cb = 0; cb < 5; cb++) tt[cb] = ldstr(sm, F_YY + (16 * cb + 4 * g + q) * 128 + (16 * sb + 4 * p) * 2);
#pragma unroll
        for (int cb = 0; cb < 5; cb++) *(LAS u32x2*)(sm + F_Y2 + (16 * sb + fr) * 176 + (16 * cb + 4 * g) * 2) = tt[cb];
    }
    LGKM0(); BAR();
#pragma unroll
    for (int r = 0; r < 2; r++) {
        const int it = tid + 512 * r;
        if (it < 640) {
            const int s = it / 10, u = it - 10 * s;
            const u32 ra = s * RED_PITCH + u * 32;
            const f32x4 p0 = *(LAS f32x4*)(sm + ra), p1 = *(LAS f32x4*)(sm + ra + 16), q0 = *(LAS f32x4*)(sm + 64 * RED_PITCH + ra), q1 = *(LAS f32x4*)(sm + 64 * RED_PITCH + ra + 16);
            const u32x4 y8 = lds128(sm, F_Y2 + s * 176 + u * 16);
            const float gg = *(LAS float*)(sm + F_GG + s * 4), omg = rbf(1.f - gg);
            float o[8];
#pragma unroll
            for (int e = 0; e < 4; e++) {
                const float a0 = (e < 2 ? p0[2 * e] + q0[2 * e] : p1[2 * e - 4] + q1[2 * e - 4]), a1 = (e < 2 ? p0[2 * e + 1] + q0[2 * e + 1] : p1[2 * e - 3] + q1[2 * e - 3]);
                const u32 bb = pk2(blo(xv[r][e]) * blo(y8[e]), bhi(xv[r][e]) * bhi(y8[e]));
                o[2 * e] = fmaf(omg, blo(bb), gg * a0); o[2 * e + 1] = fmaf(omg, bhi(bb), gg * a1);
            }
            { const u32x4 ov_ = {pk2(o[0], o[1]), pk2(o[2], o[3]), pk2(o[4], o[5]), pk2(o[6], o[7])};
              asm volatile("global_store_dwordx4 %0, %1, %2 nt\n\ts_nop 1" :: "v"((u32)((st * 64 + s) * 1280 + ct * 160 + u * 16)), "v"(ov_), "s"(out) : "memory"); }
        }
    }
}

__global__ __launch_bounds__(512, 2) void fused_kernel(const u16* __restrict__ x, const u16* __restrict__ w, const u16* __restrict__ wo, const u16* __restrict__ kin,
                                                        const u16* __restrict__ vin, const u16* __restrict__ shk, const u16* __restrict__ hk, const u16* __restrict__ sD,
                                                        const u16* __restrict__ gw, u16* __restrict__ out, char* ws) {
    extern __shared__ __attribute__((aligned(16))) char smem[];
    LAS char* sm = (LAS char*)smem;
    const int tid = threadIdx.x, lane = tid & 63, wave = __builtin_amdgcn_readfirstlane(tid >> 6);
    const int b = blockIdx.x, xcd = b & 7, j = b >> 3;
    const rsrc_t rw = __builtin_amdgcn_make_buffer_rsrc(ws, 0, WS_BYTES, 0x00020000);
    asm volatile("" :: "s"(x), "s"(w), "s"(wo), "s"(kin), "s"(vin), "s"(shk), "s"(hk), "s"(sD), "s"(gw), "s"(out));
    if (PREF1 && wave < 3 && j < 30) {
        if (wave == 0) touch(x, (u32)((xcd * 2560 + j * 80 + lane) * 128));
        else if (wave == 1) { if (lane < 16) touch(x, (u32)((xcd * 2560 + j * 80 + 64 + lane) * 128)); else touch(w, (u32)(((xcd * 30 + j) * 80 + lane - 16) * 128)); }
        else if (lane < 32) touch(w, (u32)(((xcd * 30 + j) * 80 + 48 + lane) * 128));
        else if (PREFTIN && lane < 42) touch(kin, (u32)((lane - 32) * 262144 + (j & 7) * 128));
        else if (PREFTIN && lane < 52) touch(vin, (u32)((lane - 42) * 262144 + (j & 7) * 128));
    }
    if (PREFKV && wave < 2 && j < 30) {
        const int tile_ = xcd * 30 + j;
        touch(wave == 0 ? kin : vin, (u32)(((tile_ / 10) * 64 + lane) * 1280 + (tile_ % 10) * 128));
    }
    if (PREF3 && wave < 2 && j < 30) {
        const int n240 = xcd * 30 + j;
        if (wave == 0) { const u32 l_ = n240 * 86 + lane; touch(hk, (l_ < 20480 ? l_ : 20479) * 128); }
        else { const u32 l_ = n240 * 86 + 64 + lane, m_ = n240 * 27 + lane - 22;
               if (lane < 22) touch(hk, (l_ < 20480 ? l_ : 20479) * 128); else if (lane < 49) touch(wo, (m_ < 6400 ? m_ : 6399) * 128); }
    }
    if (PREFC && wave == 3) {
        unsigned long long pc;
        asm volatile("s_getpc_b64 %0" : "=s"(pc));
        pc &= ~127ull;
        const int ln = PREFC == 1 ? j * 10 + lane : lane;
        if (PREFC == 1) { if (lane < 10 && ln < CODE_LINES) touch((const void*)pc, (u32)(ln * 128)); }
        else {
#pragma unroll
            for (int z = 0; z < 5; z++) if (z * 64 + lane < CODE_LINES) touch((const void*)pc, (u32)((z * 64 + lane) * 128));
        }
    }
    if (PREFT && wave == 2) {
        if (PREFT == 1) { touch_tlb(ws, (u32)(lane * 262144)); if (lane < 40) touch_tlb(out, (u32)(lane * 65536)); }
        else { if (lane < 8) touch_tlb(ws, (u32)((j * 8 + lane) * 65536) % (WS_STAMP - 1024)); if (lane < 40) touch_tlb(out, (u32)(lane * 65536)); }
    }
    u32 tag;
    asm volatile("s_load_dword %0, %1, 0x0\n\ts_waitcnt lgkmcnt(0)" : "=s"(tag) : "s"(ws + WS_FLAG + F_EPOCH * FS) : "memory");
    tag += 1u;
    if (tid < 16) *(LAS u32*)(sm + L_CNT + tid * 4) = 0u;
    TS(0, 0);
    const int st = 4 * xcd + (j >> 3), ct = j & 7;
    int ko = -1;
    bool is_conv = false;

    if (j >= 30) {
        const int m = xcd * 2 + (j - 30);
        ko = -100;
        {
            const int grp = wave >> 2, w4i = wave & 3;
            u32x2 w4[3];
#pragma unroll
            for (int t = 0; t < 3; t++) { const int tt = 2 * t + grp < 5 ? 2 * t + grp : 4; p0_weights(w4[t], shk, 240 + 5 * m + tt, w4i, lane); }
#pragma unroll
            for (int t = 0; t < 3; t++) if (2 * t + grp < 5) p0_issue(kin, vin, 240 + 5 * m + 2 * t + grp, w4i, lane, (2 * t + grp) * 17408);
            VMCNT(0);
#pragma unroll
            for (int t = 0; t < 3; t++) PIN(w4[t]);
            BAR();
#pragma unroll
            for (int t = 0; t < 3; t++)
                if (2 * t + grp < 5) p0_compute(sm, w4[t], 240 + 5 * m + 2 * t + grp, w4i, lane, (2 * t + grp) * 17408, 90112 + (2 * t + grp) * 8192);
            LGKM0(); BAR();
            {
                u32x4 t4[3][2];
#pragma unroll
                for (int t = 0; t < 3; t++)
#pragma unroll
                    for (int z = 0; z < 2; z++) { const int ch2 = ((tid & 255) >> 3) + 32 * z, o2 = tid & 7, tt = 2 * t + grp < 5 ? 2 * t + grp : 4; t4[t][z] = lds128(sm, 90112 + tt * 8192 + ch2 * 128 + ((o2 ^ (ch2 & 7)) * 16)); }
#pragma unroll
                for (int t = 0; t < 3; t++)
                    if (2 * t + grp < 5) {
                        const int tile = 240 + 5 * m + 2 * t + grp, cg = tile % 10, s0 = (tile / 10) * 64;
#pragma unroll
                        for (int z = 0; z < 2; z++) { const int ch2 = ((tid & 255) >> 3) + 32 * z, o2 = tid & 7; st128c(rw, WS_UT + (cg * 64 + ch2) * 4096 + (s0 + 8 * o2) * 2, t4[t][z]); }
                    }
            }
        }
        VMCNT(0); BAR();
        if (tid < 5) set_flag(rw, p0_flag(240 + 5 * m + tid), tag);
        TS(0, 1);
        gate_block(wave, lane, m, x, gw, rw);
        VMCNT(0); LGKM0(); BAR();
        if (tid == 0) set_flag(rw, F_GATE + m, tag);
        TS(0, 3);
    } else {
        const int rt = 2 * xcd + j / 15, cbk = j % 15, tile = xcd * 30 + j;
        is_conv = cbk < 5 || cbk >= 10;
        if (!is_conv) { const int ki = rt * 5 + (cbk - 5); ko = ki < 64 ? ki : -2 - (ki - 64); }
        if (wave >= 4) {
            const int lw = wave - 4;
            u32 vx[4], vw[4];
#pragma unroll
            for (int n2 = 0; n2 < 4; n2++) {
                const int n = 4 * lw + n2;
                const int row = 8 * n + (lane >> 3), ch = (lane & 7) ^ ((row >> 1) & 7);
                vx[n2] = (u32)((rt * 128 + row) * 1280 + ch * 16);
                const int kr = 4 * n + (lane >> 4), f = ((kr & 3) << 2) | ((kr >> 2) & 3), cw = (lane & 15) ^ f;
                vw[n2] = (u32)(kr * 3840 + cbk * 256 + cw * 16);
            }
            const int rot = ((rt >> 1) + cbk) % 10;
#define K1_ISSUE(c) do { const u32 st_ = ((c) & 3) * K1_STAGE + 4 * lw * 1024; int kc_ = (c) + ROT_ON * rot; kc_ = kc_ >= 10 ? kc_ - 10 : kc_; \
            const char* xb_ = (const char*)x + kc_ * 128; const char* wp_ = (const char*)w + (size_t)kc_ * 64 * 3840; \
            _Pragma("unroll") for (int n2 = 0; n2 < 4; n2++) { if (NT_X) glds16nt(xb_, vx[n2], st_ + 16384 + n2 * 1024); else glds16(xb_, vx[n2], st_ + 16384 + n2 * 1024); glds16(wp_, vw[n2], st_ + n2 * 1024); } } while (0)
            K1_ISSUE(0); K1_ISSUE(1);
            VMCNT(8); BAR();
            K1_ISSUE(2); K1_ISSUE(3);
            p0_issue(kin, vin, tile, lw, lane, P0_K);
#pragma unroll 1
            for (int c = 0; c < 6; c++) {
                if (c < 3) VMCNT(21); else VMCNT(16);
                BAR();
                K1_ISSUE(c + 4);
            }
            const int cv = rt * 10 + (cbk < 5 ? cbk : cbk - 5), c = 4 * cv + lw;
            if (is_conv) {
                CvIn in; u32 fl; u32x4 u[4];
                const int fidx = (int)F_P0 + (cv >> 4) * 32 + (lane & 31);
                VMCNT(16); BAR();
                VMCNT(8); BAR();
                conv_fetch_h(in, hk, c, lane);
                gld32c(fl, ws, WS_FLAG + fidx * FS);
                VMCNT(9); BAR();
                TS(4, 4);
                VMCNT(0); PIN(fl);
                const bool ready = __all(fl == tag);
                if (ready) {
#pragma unroll
                    for (int z = 0; z < 4; z++) gld128c(u[z], ws, WS_UT + c * 4096 + z * 1024 + lane * 16);
                }
                TS(4, 5);
                conv_build_h(sm, in, lw, lane);
                TS(4, 6);
                if (!ready) {
                    wait_flags(rw, fidx, -1, tag);
#pragma unroll
                    for (int z = 0; z < 4; z++) gld128c(u[z], ws, WS_UT + c * 4096 + z * 1024 + lane * 16);
                }
                TS(4, 7);
                VMCNT(0);
#pragma unroll
                for (int z = 0; z < 4; z++) { PIN(u[z]); *(LAS u32x4*)(sm + CV_BASE + lw * CV_WAVE + CV_U + 512 + z * 1024 + lane * 16) = u[z]; }
                const float Dc = blo((u32)sD[c]);
                TS(4, 8);
                conv_run(sm, Dc, lw, lane, c, rw, ws);
                publish_last(rw, sm, C_Y, 4, F_Y + (cv / 20) * 32 + cv % 20, tag, lane);
                TS(4, 11);
            } else {
                VMCNT(16); BAR();
                VMCNT(8); BAR();
                VMCNT(0); BAR();
                const int r32 = lane & 31, hh = lane >> 5;
                u32 dst, istep, zstep; kv_addr(true, cbk - 5, rt, lw >> 1, lw & 1, 1, r32, hh, dst, istep, zstep);
                lds_wait(sm, C_XF + lw * 4, 1);
                f32x4 a[2][4];
#pragma unroll
                for (int i = 0; i < 2; i++)
#pragma unroll
                    for (int q4 = 0; q4 < 4; q4++) a[i][q4] = *(LAS f32x4*)(sm + K_XFER + lw * 8192 + ((i * 4 + q4) * 64 + lane) * 16);
#pragma unroll
                for (int i = 0; i < 2; i++)
#pragma unroll
                    for (int z = 0; z < 2; z++) {
                        const f32x4 a0 = a[i][2 * z], a1 = a[i][2 * z + 1];
                        st128c(rw, dst + i * istep + z * zstep, (u32x4){phi2(a0[0], a0[1]), phi2(a0[2], a0[3]), phi2(a1[0], a1[1]), phi2(a1[2], a1[3])});
                    }
                publish_last(rw, sm, C_G1, 8, F_G1 + cbk * 16 + rt, tag, lane);
            }
        } else {
            u32x2 w4; p0_weights(w4, shk, tile, wave, lane);
            const int wm = wave >> 1, wn = wave & 1, r32 = lane & 31, hh = lane >> 5;
            K1Addr a0;
#pragma unroll
            for (int ks = 0; ks < 2; ks++) {
                a0.x[0][ks] = (64 * wm + r32) * 128 + (((2 * ks + hh) ^ ((r32 >> 1) & 7)) * 16);
                a0.x[1][ks] = a0.x[0][ks] ^ 64;
            }
#pragma unroll
            for (int jj = 0; jj < 2; jj++)
#pragma unroll
                for (int t = 0; t < 2; t++) {
                    const int blk = (lane >> 4) & 1, q = (lane & 15) >> 2, p = lane & 3, row = 8 * hh + 4 * t + q, ch = 8 * wn + 4 * jj + 2 * blk + (p >> 1);
                    const int f = ((row & 3) << 2) | ((row >> 2) & 3);
                    a0.w[jj][t] = row * 256 + ((ch ^ f) * 16) + 8 * (p & 1);
                }
            f32x16 acc[2][2];
#pragma unroll
            for (int i = 0; i < 2; i++)
#pragma unroll
                for (int jj = 0; jj < 2; jj++)
#pragma unroll
                    for (int e = 0; e < 16; e++) acc[i][jj][e] = 0.f;
            BAR();
            TS(0, 1);
            if (cbk < 5) {
                k1_loop<true>(sm, a0, acc, rw, p0_flag(tile), tag, wave, lane, hk, wo, tile, w4);
                __builtin_amdgcn_s_setprio(2);
                TS(0, 2);
                u32x4 qv[2][2][2];
#pragma unroll
                for (int jj = 0; jj < 2; jj++)
#pragma unroll
                    for (int ap = 0; ap < 2; ap++)
#pragma unroll
                        for (int i = 0; i < 2; i++) {
                            const f32x16& a = acc[i][jj];
                            const u32 x0 = phi2(a[8 * ap], a[8 * ap + 1]), y0 = phi2(a[8 * ap + 2], a[8 * ap + 3]);
                            const u32 x1 = phi2(a[8 * ap + 4], a[8 * ap + 5]), y1 = phi2(a[8 * ap + 6], a[8 * ap + 7]);
                            auto rx = __builtin_amdgcn_permlane32_swap(x0, x1, false, false);
                            auto ry = __builtin_amdgcn_permlane32_swap(y0, y1, false, false);
                            qv[jj][ap][i] = (u32x4){rx[0], ry[0], rx[1], ry[1]};
                        }
                if (QDELAY) { __builtin_amdgcn_s_setprio(0); for (int z = 0; z < QDELAY; z++) __builtin_amdgcn_s_sleep(8); }
#pragma unroll
                for (int jj = 0; jj < 2; jj++)
#pragma unroll
                    for (int ap = 0; ap < 2; ap++) {
                        const int U = 16 * cbk + 8 * wn + 4 * jj + 2 * ap + hh, pr = U / 5, u5 = U - 5 * pr;
                        const u32 dst = WS_P2 + (((pr * 128 + rt * 8 + 4 * wm + (r32 >> 4)) * 5 + u5) * 128 + (r32 & 15) * 8) * 2;
#pragma unroll
                        for (int i = 0; i < 2; i++) st128c(rw, dst + i * 2560, qv[jj][ap][i]);
                    }
            } else {
                k1_loop<false>(sm, a0, acc, rw, p0_flag(tile), tag, wave, lane, hk, wo, tile, w4);
                __builtin_amdgcn_s_setprio(2);
                TS(0, 2);
                const bool isk = cbk < 10;
                const int j5 = isk ? cbk - 5 : cbk - 10;
                if (isk) {
#pragma unroll
                    for (int i = 0; i < 2; i++)
#pragma unroll
                        for (int q4 = 0; q4 < 4; q4++)
                            *(LAS f32x4*)(sm + K_XFER + wave * 8192 + ((i * 4 + q4) * 64 + lane) * 16) = (f32x4){acc[i][1][4 * q4], acc[i][1][4 * q4 + 1], acc[i][1][4 * q4 + 2], acc[i][1][4 * q4 + 3]};
                    lds_arrive(sm, C_XF + wave * 4, lane);
                    u32 dst, istep, zstep; kv_addr(true, j5, rt, wm, wn, 0, r32, hh, dst, istep, zstep);
#pragma unroll
                    for (int i = 0; i < 2; i++)
#pragma unroll
                        for (int z = 0; z < 2; z++) {
                            const f32x16& a = acc[i][0];
                            st128c(rw, dst + i * istep + z * zstep,
                                   (u32x4){phi2(a[8 * z], a[8 * z + 1]), phi2(a[8 * z + 2], a[8 * z + 3]), phi2(a[8 * z + 4], a[8 * z + 5]), phi2(a[8 * z + 6], a[8 * z + 7])});
                        }
                } else {
#pragma unroll
                    for (int jj = 0; jj < 2; jj++) {
                        u32 dst, istep, zstep; kv_addr(false, j5, rt, wm, wn, jj, r32, hh, dst, istep, zstep);
#pragma unroll
                        for (int i = 0; i < 2; i++)
#pragma unroll
                            for (int z = 0; z < 2; z++) {
                                const f32x16& a = acc[i][jj];
                                st128c(rw, dst + i * istep + z * zstep,
                                       (u32x4){pk2(a[8 * z], a[8 * z + 1]), pk2(a[8 * z + 2], a[8 * z + 3]), pk2(a[8 * z + 4], a[8 * z + 5]), pk2(a[8 * z + 6], a[8 * z + 7])});
                            }
                    }
                }
            }
            LGKM0();
            TS(0, 25);
            publish_last(rw, sm, C_G1, is_conv ? 4 : 8, F_G1 + cbk * 16 + rt, tag, lane);
            __builtin_amdgcn_s_setprio(0);
            TS(0, 3);
            if (is_conv) {
                lds_wait(sm, C_G1, 4);
                wo_issue(wo, ct, 0, 12, wave, 4, lane);
            }
        }
    }
    if (!is_conv) {
        LGKM0(); BAR();
        if (ko >= 0) {
            const int h = ko >> 1, half = ko & 1;
            wo_issue(wo, ct, 0, 12, wave, 8, lane);
            kvo_block(sm, wave, lane, h, half, rw, tag, ws);
            TS(0, 26);
            VMCNT(0); BAR();
            if (tid == 0) set_flag(rw, F_O + half * 32 + h, tag);
            TS(0, 11);
            wo_issue(wo, ct, 12, 20, wave, 8, lane);
        } else {
            wo_issue(wo, ct, 0, 20, wave, 8, lane);
            if (ko == -2 && wave == 0) {
                wait_flags(rw, (int)F_G1 + lane, (int)F_G1 + 64 + lane, tag);
                wait_flags(rw, (int)F_G1 + 128 + lane, (int)F_G1 + 192 + lane, tag);
                if (lane == 0) set_flag(rw, F_EPOCH, tag);
            }
        }
    } else {
        VMCNT(0); BAR();
        if (wave >= 4) wo_issue(wo, ct, 12, 20, wave - 4, 4, lane);
    }
    TS(0, 16);
    final_block(sm, wave, lane, tid, st, ct, x, out, rw, tag, ws);
    TS(0, 19);
}

extern "C" void kernel_launch(void* const* d_in, const int* in_sizes, int n_in, void* d_out, int out_size, void* d_ws, size_t ws_size, hipStream_t stream) {
    const u16* x = (const u16*)d_in[0]; const u16* wqkv = (const u16*)d_in[1]; const u16* wo = (const u16*)d_in[2];
    const u16* kin = (const u16*)d_in[4]; const u16* vin = (const u16*)d_in[5]; const u16* shk = (const u16*)d_in[6];
    const u16* hk = (const u16*)d_in[7]; const u16* sD = (const u16*)d_in[8]; const u16* gw = (const u16*)d_in[9];
    (void)hipFuncSetAttribute((const void*)fused_kernel, hipFuncAttributeMaxDynamicSharedMemorySize, LDS_BYTES);
    fused_kernel<<<256, 512, LDS_BYTES, stream>>>(x, wqkv, wo, kin, vin, shk, hk, sD, gw, (u16*)d_out, (char*)d_ws);
}
```

```cpp
#define GPROBE 31
#include <hip/hip_runtime.h>
#include <stdint.h>

typedef unsigned short u16;
typedef unsigned int u32;
typedef __bf16 bf16x8 __attribute__((ext_vector_type(8)));
typedef __bf16 bf16x2 __attribute__((ext_vector_type(2)));
typedef float f32x4 __attribute__((ext_vector_type(4)));
typedef float f32x2 __attribute__((ext_vector_type(2)));
typedef float f32x16 __attribute__((ext_vector_type(16)));
typedef u32 u32x4 __attribute__((ext_vector_type(4)));
typedef u32 u32x2 __attribute__((ext_vector_type(2)));
typedef short s16x4 __attribute__((ext_vector_type(4)));
#define LAS __attribute__((address_space(3)))
#define GAS __attribute__((address_space(1)))
#define DI __device__ __forceinline__
typedef __amdgpu_buffer_rsrc_t rsrc_t;

constexpr int S = 2048, D = 640;
#define FS 256
#define POLL_SLEEP 1
#define ROT_ON 1

constexpr u32 WS_UT = 0;
constexpr u32 WS_YT = WS_UT + 2621440;
constexpr u32 WS_PK0 = WS_YT + 2621440;
constexpr u32 WS_V0 = WS_PK0 + 2097152;
constexpr u32 WS_XK = WS_V0 + 2097152;
constexpr u32 WS_XV = WS_XK + 524288;
constexpr u32 WS_P2 = WS_XV + 524288;
constexpr u32 WS_O = WS_P2 + 2621440;
constexpr u32 WS_G = WS_O + 2621440;
constexpr u32 WS_FLAG = WS_G + 8192;
constexpr u32 F_P0 = 0;
constexpr u32 F_G1 = 320;
constexpr u32 F_GATE = 560;
constexpr u32 F_O = 576;
constexpr u32 F_Y = 640;
constexpr u32 F_EPOCH = 896, F_TMO = 928;
constexpr u32 WS_STAMP = WS_FLAG + 1024 * FS;
constexpr u32 WS_BYTES = WS_STAMP + 256 * 32 * 16;

extern __shared__ __attribute__((aligned(16))) char smem_gp[];
#define TS(w, id) do { if ((id) == GPROBE && threadIdx.x == 64 * (w) && (false)) { *(volatile LAS unsigned long long*)((LAS char*)smem_gp + 156672 + 64) = __builtin_amdgcn_s_memrealtime(); } } while (0)

DI u32 pk2(float a, float b) { f32x2 t = {a, b}; return __builtin_bit_cast(u32, __builtin_convertvector(t, bf16x2)); }
DI float blo(u32 u) { return __uint_as_float(u << 16); }
DI float bhi(u32 u) { return __uint_as_float(u & 0xffff0000u); }
DI float rbf(float f) { return blo(pk2(f, f)); }
DI bf16x8 asbf(u32x4 v) { return __builtin_bit_cast(bf16x8, v); }
DI f32x4 mfma16(u32x4 a, u32x4 b, f32x4 c) { return __builtin_amdgcn_mfma_f32_16x16x32_bf16(asbf(a), asbf(b), c, 0, 0, 0); }
DI f32x16 mfma32(u32x4 a, u32x4 b, f32x16 c) { return __builtin_amdgcn_mfma_f32_32x32x16_bf16(asbf(a), asbf(b), c, 0, 0, 0); }

DI void glds16(const void* sbase, u32 voff, u32 lds) {
    asm volatile("s_mov_b32 m0, %2\n\ts_nop 0\n\tglobal_load_lds_dwordx4 %0, %1" :: "v"(voff), "s"(sbase), "s"(lds) : "memory");
}
DI void glds16nt(const void* sbase, u32 voff, u32 lds) {
    asm volatile("s_mov_b32 m0, %2\n\ts_nop 0\n\tglobal_load_lds_dwordx4 %0, %1 nt" :: "v"(voff), "s"(sbase), "s"(lds) : "memory");
}
DI void touch(const void* sbase, u32 voff) {
    asm volatile("s_mov_b32 m0, %2\n\ts_nop 0\n\tglobal_load_lds_dword %0, %1" :: "v"(voff), "s"(sbase), "s"(156928u) : "memory");
}
DI void touch_tlb(const void* sbase, u32 voff) {
    asm volatile("s_mov_b32 m0, %2\n\ts_nop 0\n\tglobal_load_lds_dword %0, %1 sc1" :: "v"(voff), "s"(sbase), "s"(156928u) : "memory");
}
#define NT_KV 0
#define NT_X 0
#define NT_H 0
#define PREF1 1
#define PREF2 0
#define PREFC 1
#define CODE_LINES 314
#define PREFT 2
#define PREFKV 0
#define PREF3 1
#define PREFTIN 1
#define QDELAY 0
DI void gld64(u32x2& d, const void* sbase, u32 voff) { asm volatile("global_load_dwordx2 %0, %1, %2" : "=v"(d) : "v"(voff), "s"(sbase) : "memory"); }
DI void gld128(u32x4& d, const void* sbase, u32 voff) { asm volatile("global_load_dwordx4 %0, %1, %2" : "=v"(d) : "v"(voff), "s"(sbase) : "memory"); }
DI void gld128c(u32x4& d, const void* sbase, u32 voff) { asm volatile("global_load_dwordx4 %0, %1, %2 sc1" : "=v"(d) : "v"(voff), "s"(sbase) : "memory"); }
DI void gld32c(u32& d, const void* sbase, u32 voff) { asm volatile("global_load_dword %0, %1, %2 sc1" : "=v"(d) : "v"(voff), "s"(sbase) : "memory"); }
#define PIN(x) asm volatile("" : "+v"(x))
#define SB() __builtin_amdgcn_sched_barrier(0)
DI u32x4 ld128c(rsrc_t r, u32 off) { return __builtin_amdgcn_raw_buffer_load_b128(r, off, 0, 16); }
DI u32x2 ld64c(rsrc_t r, u32 off) { return __builtin_amdgcn_raw_buffer_load_b64(r, off, 0, 16); }
DI u32 ld32c(rsrc_t r, u32 off) { return __builtin_amdgcn_raw_buffer_load_b32(r, off, 0, 16); }
DI void st128c(rsrc_t r, u32 off, u32x4 v) { __builtin_amdgcn_raw_buffer_store_b128(v, r, off, 0, 16); }
DI void st64c(rsrc_t r, u32 off, u32x2 v) { __builtin_amdgcn_raw_buffer_store_b64(v, r, off, 0, 16); }
DI void st32c(rsrc_t r, u32 off, u32 v) { __builtin_amdgcn_raw_buffer_store_b32(v, r, off, 0, 16); }
DI u32x4 lds128(LAS char* sm, u32 a) { return *(LAS u32x4*)(sm + a); }
DI u32x2 lds64(LAS char* sm, u32 a) { return *(LAS u32x2*)(sm + a); }
DI u32x2 ldstr(LAS char* sm, u32 a) { return __builtin_bit_cast(u32x2, __builtin_amdgcn_ds_read_tr16_b64_v4i16((LAS s16x4*)(sm + a))); }
#define VMCNT(n) asm volatile("s_waitcnt vmcnt(" #n ")" ::: "memory")
#define LGKM0() asm volatile("s_waitcnt lgkmcnt(0)" ::: "memory")
#define BAR() do { asm volatile("" ::: "memory"); __builtin_amdgcn_s_barrier(); asm volatile("" ::: "memory"); } while (0)
#define MEMF() asm volatile("" ::: "memory")

DI void set_flag(rsrc_t r, u32 idx, u32 tag) { st32c(r, WS_FLAG + idx * FS, tag); }
DI void wait_flags(rsrc_t r, int idx0, int idx1, u32 tag) {
    unsigned long long t0 = 0;
    for (u32 it = 0;; it++) {
        u32 a = tag, b = tag;
        if (idx0 >= 0) a = ld32c(r, WS_FLAG + idx0 * FS);
        if (idx1 >= 0) b = ld32c(r, WS_FLAG + idx1 * FS);
        if (__all(a == tag && b == tag)) break;
        if (POLL_SLEEP) __builtin_amdgcn_s_sleep(POLL_SLEEP);
        if ((it & 63u) == 0u) {
            const unsigned long long t = __builtin_amdgcn_s_memrealtime();
            if (it == 0) t0 = t;
            else if (t - t0 > 2000000ull) { set_flag(r, F_TMO, tag); break; }
        }
    }
    MEMF();
}
DI void lds_arrive(LAS char* sm, u32 cnt, int lane) { LGKM0(); if (lane == 0) __hip_atomic_fetch_add((LAS u32*)(sm + cnt), 1u, __ATOMIC_RELAXED, __HIP_MEMORY_SCOPE_WORKGROUP); MEMF(); }
DI void lds_wait(LAS char* sm, u32 cnt, u32 n) { MEMF(); while (*(volatile LAS u32*)(sm + cnt) < n) __builtin_amdgcn_s_sleep(0); MEMF(); }
DI void publish_last(rsrc_t r, LAS char* sm, u32 cnt, u32 nw, u32 flag, u32 tag, int lane) {
    VMCNT(0);
    if (lane == 0) {
        const u32 old = __hip_atomic_fetch_add((LAS u32*)(sm + cnt), 1u, __ATOMIC_RELAXED, __HIP_MEMORY_SCOPE_WORKGROUP);
        if (old == nw - 1) set_flag(r, flag, tag);
    }
    MEMF();
}

DI u32 phi2(float a, float b) {
    const f32x2 t = {a, b}, s = t * 1.4426950408889634f;
    const float ea = __builtin_amdgcn_fmed3f(__builtin_amdgcn_exp2f(s.x), 0.f, 1.f), eb = __builtin_amdgcn_fmed3f(__builtin_amdgcn_exp2f(s.y), 0.f, 1.f);
    const float ma = __int_as_float(max(__float_as_int(a), 0)), mb = __int_as_float(max(__float_as_int(b), 0));
    const f32x2 r = ((f32x2){ea, eb} + (f32x2){ma, mb}) + 1e-6f;
    return pk2(r.x, r.y);
}

constexpr int K1_STAGE = 32768;
constexpr int P0_K = 131072, P0_V = P0_K + 9216, P0_OUT = P0_V + 8192;
constexpr int L_CNT = 156672;
constexpr int C_G1 = L_CNT, C_Y = L_CNT + 4, C_P0A = L_CNT + 8, C_P0B = L_CNT + 12, C_P0C = L_CNT + 16, C_FY = L_CNT + 20, C_FY2 = L_CNT + 24, C_FO = L_CNT + 28, C_FR = L_CNT + 32, C_XF = L_CNT + 36;
constexpr int CV_PITCH = 4160;
constexpr int CV_U = 4 * CV_PITCH;
constexpr int CV_WAVE = CV_U + 4608;
constexpr int CV_BASE = 65536;
constexpr int WO_HI = 65536;
constexpr int F_YY = 110592;
constexpr int F_Y2 = F_YY + 10240;
constexpr int F_GG = F_Y2 + 11264;
constexpr int KO_RED = 110592;
constexpr int KO_KV = KO_RED + 32768;
constexpr int RED_PITCH = 336;
constexpr int LDS_BYTES = 160 * 1024;
DI u32 p0_flag(int tile) { return F_P0 + (tile % 10) * 32 + tile / 10; }
DI constexpr u32 wo_off(int kap) { return kap < 12 ? kap * 5120 : WO_HI + (kap - 12) * 5120; }

DI void p0_issue(const u16* kin, const u16* vin, int tile, int lw, int lane, u32 base) {
    const int cg = tile % 10, rg = tile / 10, s0 = rg * 64;
    const u32 col = cg * 128 + (lane & 7) * 16;
#pragma unroll
    for (int z = 0; z < 2; z++) {
        const int n = 2 * lw + z;
        int r = s0 - 4 + n * 8 + (lane >> 3); r = r < 0 ? 0 : r;
        (NT_KV ? glds16nt : glds16)(kin, (u32)(r * 1280) + col, base + n * 1024);
        (NT_KV ? glds16nt : glds16)(vin, (u32)((s0 + n * 8 + (lane >> 3)) * 1280) + col, base + 9216 + n * 1024);
    }
    {
        int r = s0 + 60 + (lane >> 3); r = r > S - 1 ? S - 1 : r;
        (NT_KV ? glds16nt : glds16)(kin, (u32)(r * 1280) + col, base + 8 * 1024);
    }
}
DI float dot2(u32 a, u32 b, float c) { return __builtin_amdgcn_fdot2_f32_bf16(__builtin_bit_cast(bf16x2, a), __builtin_bit_cast(bf16x2, b), c, false); }
DI void p0_compute(LAS char* sm, u32x2 w4, int tile, int wave, int lane, u32 base, u32 outb, char* ws = nullptr) {
    const int s0 = (tile / 10) * 64;
    const int G = wave * 4 + (lane >> 4), cb = G & 3, i = lane & 15, q = i >> 2, p = i & 3;
    const u32 wa = __builtin_amdgcn_alignbit(w4.y, w4.y, 16), wb = __builtin_amdgcn_alignbit(w4.x, w4.x, 16);
    const int ch = 16 * cb + i;
    u32x2 kq[2][3], vq[2][2];
#pragma unroll
    for (int z = 0; z < 2; z++) {
        const int o = (G >> 2) + 4 * z;
        const u32 ka = base + (8 * o + q) * 128 + 32 * cb + 8 * p, va = ka + 9216;
        kq[z][0] = ldstr(sm, ka); kq[z][1] = ldstr(sm, ka + 512); kq[z][2] = ldstr(sm, ka + 1024);
        vq[z][0] = ldstr(sm, va); vq[z][1] = ldstr(sm, va + 512);
    }
#pragma unroll
    for (int z = 0; z < 2; z++) {
        const int o = (G >> 2) + 4 * z;
        u32 K[6] = {kq[z][0].x, kq[z][0].y, kq[z][1].x, kq[z][1].y, kq[z][2].x, kq[z][2].y};
        if (s0 == 0 && o == 0) { K[0] = 0; K[1] = 0; }
        u32 Sh[5];
#pragma unroll
        for (int e = 0; e < 5; e++) Sh[e] = __builtin_amdgcn_alignbit(K[e + 1], K[e], 16);
        const u32 V[4] = {vq[z][0].x, vq[z][0].y, vq[z][1].x, vq[z][1].y};
        u32 r[4];
#pragma unroll
        for (int e = 0; e < 4; e++) {
            const float k0 = dot2(Sh[e], wa, dot2(Sh[e + 1], wb, 0.f)), k1 = dot2(K[e + 1], wa, dot2(K[e + 2], wb, 0.f));
            r[e] = pk2(k0 * blo(V[e]), k1 * bhi(V[e]));
        }
        *(LAS u32x4*)(sm + outb + ch * 128 + ((o ^ (ch & 7)) * 16)) = (u32x4){r[0], r[1], r[2], r[3]};
    }
}
DI void p0_store(LAS char* sm, rsrc_t rw, int tile, int t256, u32 outb) {
    const int cg = tile % 10, s0 = (tile / 10) * 64;
#pragma unroll
    for (int z = 0; z < 2; z++) {
        const int ch2 = (t256 >> 3) + 32 * z, o2 = t256 & 7;
        const u32x4 t4 = lds128(sm, outb + ch2 * 128 + ((o2 ^ (ch2 & 7)) * 16));
        st128c(rw, WS_UT + (cg * 64 + ch2) * 4096 + (s0 + 8 * o2) * 2, t4);
    }
}
DI void p0_weights(u32x2& w4, const u16* shk, int tile, int wave, int lane) {
    const int cg = tile % 10, cb = (wave * 4 + (lane >> 4)) & 3;
    gld64(w4, shk, (u32)((cg * 64 + 16 * cb + (lane & 15)) * 8));
}

struct K1Set { u32x4 xa[2][2]; u32x2 wl[2][2], wh[2][2]; };
struct K1Addr { u32 x[2][2]; u32 w[2][2]; };
template <bool QT, int WO, int XO>
DI void k1_phase(LAS char* sm, f32x16 (&acc)[2][2], const K1Set& cur, K1Set& nxt, const u32 (&xb)[2], const u32 (&wb)[2][2]) {
#pragma unroll
    for (int n = 0; n < 8; n++) {
        const int ks = n >> 2, i = (n >> 1) & 1, jj = n & 1;
        const u32x4 wf = {cur.wl[ks][jj].x, cur.wl[ks][jj].y, cur.wh[ks][jj].x, cur.wh[ks][jj].y};
        acc[i][jj] = QT ? mfma32(wf, cur.xa[ks][i], acc[i][jj]) : mfma32(cur.xa[ks][i], wf, acc[i][jj]);
        if (n == 0) { nxt.wl[0][0] = ldstr(sm, wb[0][0] + WO); nxt.wh[0][0] = ldstr(sm, wb[0][1] + WO); }
        if (n == 1) { nxt.xa[0][0] = lds128(sm, xb[0] + XO); nxt.wl[0][1] = ldstr(sm, wb[1][0] + WO); }
        if (n == 2) { nxt.wh[0][1] = ldstr(sm, wb[1][1] + WO); nxt.xa[0][1] = lds128(sm, xb[0] + XO + 4096); }
        if (n == 3) { nxt.wl[1][0] = ldstr(sm, wb[0][0] + WO + 4096); nxt.wh[1][0] = ldstr(sm, wb[0][1] + WO + 4096); }
        if (n == 4) { nxt.xa[1][0] = lds128(sm, xb[1] + XO); nxt.wl[1][1] = ldstr(sm, wb[1][0] + WO + 4096); }
        if (n == 5) { nxt.wh[1][1] = ldstr(sm, wb[1][1] + WO + 4096); nxt.xa[1][1] = lds128(sm, xb[1] + XO + 4096); }
        SB();
    }
}
template <bool QT, int ST>
DI void k1_chunk(LAS char* sm, f32x16 (&acc)[2][2], K1Set& s0, K1Set& s1, const K1Addr& a0, const K1Addr& a1, bool bar, bool drain = false) {
    constexpr int SN = (ST + 1) & 3;
    const K1Addr& ac = ST < 2 ? a0 : a1; const K1Addr& an = SN < 2 ? a0 : a1;
    k1_phase<QT, (ST & 1) * K1_STAGE + 8192, (ST & 1) * K1_STAGE + 16384>(sm, acc, s0, s1, ac.x[1], ac.w);
    if (drain) VMCNT(0);
    if (bar) { LGKM0(); BAR(); }
    k1_phase<QT, (SN & 1) * K1_STAGE, (SN & 1) * K1_STAGE + 16384>(sm, acc, s1, s0, an.x[0], an.w);
}
template <bool QT>
DI void k1_loop(LAS char* sm, const K1Addr& a0, f32x16 (&acc)[2][2], rsrc_t rw, u32 pflag, u32 tag, int wave, int lane, const u16* hk, const u16* wo, int n240, u32x2 w4) {
    K1Addr a1;
#pragma unroll
    for (int i = 0; i < 2; i++)
#pragma unroll
        for (int z = 0; z < 2; z++) { a1.x[i][z] = a0.x[i][z] + 65536; a1.w[i][z] = a0.w[i][z] + 65536; }
    K1Set s0, s1;
#pragma unroll
    for (int ks = 0; ks < 2; ks++)
#pragma unroll
        for (int jj = 0; jj < 2; jj++) {
            s0.wl[ks][jj] = ldstr(sm, a0.w[jj][0] + ks * 4096); s0.wh[ks][jj] = ldstr(sm, a0.w[jj][1] + ks * 4096);
            s0.xa[ks][jj] = lds128(sm, a0.x[0][ks] + 16384 + jj * 4096);
        }
    SB();
#pragma unroll 1
    for (int c = 0; c < 8; c += 4) {
        k1_chunk<QT, 0>(sm, acc, s0, s1, a0, a1, true);
        if (c == 4) p0_store(sm, rw, n240, wave * 64 + lane, P0_OUT);
        k1_chunk<QT, 1>(sm, acc, s0, s1, a0, a1, true);
        k1_chunk<QT, 2>(sm, acc, s0, s1, a0, a1, true);
        k1_chunk<QT, 3>(sm, acc, s0, s1, a0, a1, true, c == 4);
        if (c == 4 && wave == 0 && lane == 0) set_flag(rw, pflag, tag);
        if (c == 0) { VMCNT(0); PIN(w4); p0_compute(sm, w4, n240, wave, lane, P0_K, P0_OUT); }
        if (PREF2 && c == 0 && wave >= 2) {
            if (wave == 2) { const u32 l_ = n240 * 86 + lane; touch(hk, (l_ < 20480 ? l_ : 20479) * 128); }
            else { const u32 l_ = n240 * 86 + 64 + lane, m_ = n240 * 27 + lane - 22;
                   if (lane < 22) touch(hk, (l_ < 20480 ? l_ : 20479) * 128); else if (lane < 49) touch(wo, (m_ < 6400 ? m_ : 6399) * 128); }
        }
    }
    k1_chunk<QT, 0>(sm, acc, s0, s1, a0, a1, true);
    k1_chunk<QT, 1>(sm, acc, s0, s1, a0, a1, false);
}

DI void kv_addr(bool isk, int j5, int rt, int wm, int wn, int jj, int r32, int hh, u32& dst, u32& istep, u32& zstep) {
    const int cc = 128 * j5 + 64 * wn + 32 * jj + r32, h = cc / 20, d = cc - 20 * h;
    const int sb32 = rt * 4 + 2 * wm;
    const bool lo = d < 16;
    dst = lo ? (isk ? WS_PK0 : WS_V0) + (((h * 64 + sb32) * 4 + hh) * 128 + d * 8) * 2
             : (isk ? WS_XK : WS_XV) + (((h * 64 + sb32) * 4 + hh) * 32 + (d - 16) * 8) * 2;
    istep = lo ? 1024 : 256; zstep = lo ? 512 : 128;
}
constexpr int K_XFER = 65536;

DI void wo_issue(const u16* wo, int ct, int k0, int k1, int w, int nw, int lane) {
    u32 vo[5], vs[5];
#pragma unroll
    for (int i = 0; i < 5; i++) {
        const int n = 64 * i + lane, rho = n / 10, c = n - 10 * rho, g = 2 * (rho >> 4) + ((rho >> 2) & 1), r8 = 4 * ((rho >> 3) & 1) + (rho & 3);
        vo[i] = (u32)((8 * (g < 2 ? g : g + 1) + r8) * 1280 + ct * 160 + c * 16);
        vs[i] = (u32)((40 * g + 16 + r8) * 1280 + ct * 160 + c * 16);
    }
#pragma unroll 1
    for (int kap = k0 + w; kap < k1; kap += nw) {
        const u32 dst = kap < 12 ? kap * 5120 : WO_HI + (kap - 12) * 5120;
        if (kap < 16) {
            const char* src = (const char*)wo + (size_t)kap * 51200;
#pragma unroll
            for (int i = 0; i < 5; i++) glds16(src, vo[i], dst + i * 1024);
        } else {
            const char* src = (const char*)wo + (size_t)(kap - 16) * 204800;
#pragma unroll
            for (int i = 0; i < 5; i++) glds16(src, vs[i], dst + i * 1024);
        }
    }
}

struct CvSet { u32x2 fl[8], fh[8]; u32x4 g[8]; };
DI void cv_load(LAS char* sm, CvSet& s, u32 fa, u32 ga) {
#pragma unroll
    for (int k1 = 0; k1 < 8; k1++) { s.fl[k1] = ldstr(sm, fa + 512 * k1 + 8); s.fh[k1] = ldstr(sm, fa + 512 * k1); s.g[k1] = lds128(sm, ga + 512 * k1); }
}
DI void cv_phase(LAS char* sm, f32x4 (&Y)[8], const CvSet& cur, CvSet& nxt, u32 fa, u32 ga) {
    PIN(fa); PIN(ga);
    SB();
    int n = 0;
#pragma unroll
    for (int e = 0; e < 8; e++)
#pragma unroll
        for (int k1 = 0; k1 + e < 8; k1++) {
            const u32x4 F = {cur.fl[k1].x, cur.fl[k1].y, cur.fh[k1].x, cur.fh[k1].y};
            Y[k1 + e] = mfma16(F, cur.g[e], Y[k1 + e]);
            if (n == 0) nxt.g[0] = lds128(sm, ga);
            else if (n <= 16) { const int r = (n - 1) >> 1; if ((n - 1) & 1) nxt.fh[r] = ldstr(sm, fa + 512 * r); else nxt.fl[r] = ldstr(sm, fa + 512 * r + 8); }
            else if (n < 24) nxt.g[n - 16] = lds128(sm, ga + 512 * (n - 16));
            n++;
            SB();
        }
}
struct CvIn { u32x4 hd[4]; u32x2 hm[4]; };
DI void conv_fetch_h(CvIn& in, const u16* hk, int c, int lane) {
    const char* hb = (const char*)hk + (size_t)c * 4096;
#pragma unroll
    for (int z = 0; z < 4; z++) {
        const int i = lane + 64 * z;
        if (NT_H) asm volatile("global_load_dwordx4 %0, %1, %2 nt" : "=v"(in.hd[z]) : "v"((u32)(i * 16)), "s"(hb) : "memory"); else gld128(in.hd[z], hb, (u32)(i * 16));
        gld64(in.hm[z], hb, (u32)(i > 0 ? i * 16 - 8 : 0));
    }
}
DI void conv_build_h(LAS char* sm0, CvIn& in, int w, int lane) {
    LAS char* sm = sm0 + CV_BASE + w * CV_WAVE;
#pragma unroll
    for (int z = 0; z < 4; z++) { PIN(in.hd[z]); PIN(in.hm[z]); }
    if (lane < 32) *(LAS u32x4*)(sm + CV_U + lane * 16) = (u32x4){0, 0, 0, 0};
    if (lane < 8) *(LAS u32x4*)(sm + (lane >> 1) * CV_PITCH + (lane & 1) * 16) = (u32x4){0, 0, 0, 0};
#pragma unroll
    for (int z = 0; z < 4; z++) {
        const int i = lane + 64 * z;
        u32 m2 = in.hm[z].x, m1 = in.hm[z].y;
        if (i == 0) { m2 = 0; m1 = 0; }
        const u32 d0 = in.hd[z].x, d1 = in.hd[z].y, d2 = in.hd[z].z, d3 = in.hd[z].w;
        const u32 om2 = __builtin_amdgcn_alignbit(m1, m2, 16), om1 = __builtin_amdgcn_alignbit(d0, m1, 16), o0 = __builtin_amdgcn_alignbit(d1, d0, 16),
                  o1 = __builtin_amdgcn_alignbit(d2, d1, 16), o2 = __builtin_amdgcn_alignbit(d3, d2, 16);
        LAS char* p = sm + 32 + i * 16;
        *(LAS u32x4*)(p) = (u32x4){d0, d1, d2, d3};
        *(LAS u32x4*)(p + CV_PITCH) = (u32x4){om1, o0, o1, o2};
        *(LAS u32x4*)(p + 2 * CV_PITCH) = (u32x4){m1, d0, d1, d2};
        *(LAS u32x4*)(p + 3 * CV_PITCH) = (u32x4){om2, om1, o0, o1};
    }
}
DI void conv_run(LAS char* sm0, float Dc, int w, int lane, int c, rsrc_t rw, char* ws) {
    LAS char* sm = sm0;
    LGKM0();
    TS(4, 9);
    const int fr = lane & 15, g = lane >> 4, q = fr >> 2, p = lane & 3;
    sm = sm0;
    const u32 fa = CV_BASE + w * CV_WAVE + q * CV_PITCH + 2 * (32 - 8 * g + 4 * p) - 8;
    const u32 ga = CV_BASE + w * CV_WAVE + CV_U + 480 + 32 * fr + 16 * g;
    f32x4 Y[8];
#pragma unroll
    for (int i = 0; i < 8; i++) Y[i] = (f32x4){0, 0, 0, 0};
    CvSet A, B;
    cv_load(sm, A, fa, ga);
    SB();
#pragma unroll 1
    for (int k0 = 0; k0 < 8; k0 += 2) {
        cv_phase(sm, Y, A, B, fa + 64 * (k0 + 1), ga - 64 * (k0 + 1));
        cv_phase(sm, Y, B, A, fa + 64 * (k0 + 2), ga - 64 * (k0 + 2));
    }
    TS(4, 10);
    sm = sm0 + CV_BASE + w * CV_WAVE;
    {
        u32x2 u4[8];
#pragma unroll
        for (int ib = 0; ib < 8; ib++) u4[ib] = lds64(sm, CV_U + 512 + 512 * ib + 32 * fr + 8 * g);
#pragma unroll
        for (int ib = 0; ib < 8; ib++) {
            const u32x2 r = {pk2(fmaf(blo(u4[ib].x), Dc, Y[ib][0]), fmaf(bhi(u4[ib].x), Dc, Y[ib][1])), pk2(fmaf(blo(u4[ib].y), Dc, Y[ib][2]), fmaf(bhi(u4[ib].y), Dc, Y[ib][3]))};
            *(LAS u32x2*)(sm + 512 * ib + 32 * fr + 8 * g) = r;
        }
    }
    LGKM0();
    u32x4 y8[4];
#pragma unroll
    for (int z = 0; z < 4; z++) y8[z] = lds128(sm, z * 1024 + lane * 16);
#pragma unroll
    for (int z = 0; z < 4; z++) st128c(rw, WS_YT + c * 4096 + z * 1024 + lane * 16, y8[z]);
}

DI void kvo_block(LAS char* sm, int wave, int lane, int h, int half, rsrc_t rw, u32 tag, char* ws) {
    const int fr = lane & 15, g = lane >> 4;
    const int j0 = (20 * h) >> 7, j1 = (20 * h + 19) >> 7;
    wait_flags(rw, lane < 8 ? (int)F_G1 + 16 * (((lane & 2) ? 10 : 5) + ((lane & 1) ? j1 : j0)) + 2 * wave + (lane >> 2)
                            : lane < 10 ? (int)F_G1 + 16 * ((lane & 1) ? j1 : j0) + 8 * half + wave : -1, -1, tag);
    TS(0, 8);
    const u32 pk0 = WS_PK0 + (h * 64 + 8 * wave) * 1024 + lane * 16, v0 = pk0 + (WS_V0 - WS_PK0);
    const u32 xx = ((fr & 4) ? WS_XV : WS_XK) + ((h * 64 + 8 * wave + (fr >> 3)) * 4 + g) * 64 + (fr & 3) * 16;
    u32x4 A[8], B[8], X[4], Q[8];
#pragma unroll
    for (int i = 0; i < 8; i++) { A[i] = ld128c(rw, pk0 + i * 1024); B[i] = ld128c(rw, v0 + i * 1024); if ((i & 1) == 0) X[i >> 1] = ld128c(rw, xx + (i >> 1) * 512); }
    const int odd = h & 1, pr = h >> 1;
    const int u5 = (g < 3 ? g : 2) + 2 * odd;
    const int sb0 = half * 64 + wave * 8;
    const u32 pq = WS_P2 + (((pr * 128 + sb0) * 5 + u5) * 128 + fr * 8) * 2;
    MEMF(); SB();
    f32x4 a00 = {0, 0, 0, 0}, a0x = a00, ax0 = a00, axx = a00;
    const u32 cst = fr == 8 ? 0x3F803F80u : 0u;
#pragma unroll
    for (int i = 0; i < 8; i++) {
        u32x4 xv = X[i >> 1];
        if (i & 1) {
#pragma unroll
            for (int e = 0; e < 4; e++) xv[e] = (u32)__builtin_amdgcn_update_dpp(0, (int)xv[e], 0x108, 0xf, 0xf, false);
        }
        if (fr >= 8) xv = (u32x4){cst, cst, cst, cst};
        a00 = mfma16(A[i], B[i], a00); a0x = mfma16(A[i], xv, a0x); ax0 = mfma16(xv, B[i], ax0); axx = mfma16(xv, xv, axx);
    }
    PIN(a00); PIN(a0x); PIN(ax0); PIN(axx);
    SB();
#pragma unroll
    for (int i = 0; i < 8; i++) Q[i] = ld128c(rw, pq + i * 1280);
    MEMF(); SB();
    TS(0, 9);
    *(LAS f32x4*)(sm + KO_RED + ((wave * 4 + 0) * 64 + lane) * 16) = a00;
    *(LAS f32x4*)(sm + KO_RED + ((wave * 4 + 1) * 64 + lane) * 16) = a0x;
    *(LAS f32x4*)(sm + KO_RED + ((wave * 4 + 2) * 64 + lane) * 16) = ax0;
    *(LAS f32x4*)(sm + KO_RED + ((wave * 4 + 3) * 64 + lane) * 16) = axx;
    if (wave >= 4 && (wave - 4) * 64 + lane < 189) *(LAS u32x2*)(sm + KO_KV + ((wave - 4) * 64 + lane) * 8) = (u32x2){0, 0};
    LGKM0(); BAR();
    if (wave < 4) {
        const int tile = wave;
        f32x4 s = *(LAS f32x4*)(sm + KO_RED + (tile * 64 + lane) * 16);
#pragma unroll
        for (int w2 = 1; w2 < 8; w2++) s += *(LAS f32x4*)(sm + KO_RED + ((w2 * 4 + tile) * 64 + lane) * 16);
        const int e = (tile & 1) ? 12 + fr : fr;
        const bool colok = (tile & 1) ? (fr >= 4 && fr <= 8) : true;
        const bool rowok = (tile & 2) ? (g == 0) : true;
        const int d0 = (tile & 2) ? 16 : 4 * g;
        if (colok && rowok) *(LAS u32x2*)(sm + KO_KV + e * 72 + (d0 + 4) * 2) = (u32x2){pk2(s[0], s[1]), pk2(s[2], s[3])};
    }
    LGKM0(); BAR();
    u32x4 A0, A1[4], A2;
    {
        const u32 dofs = KO_KV + (8 * g - 4 * odd + 4) * 2;
        const u32 r0 = (4 * odd + fr) * 72, r1 = ((odd ? 0 : 16) + (fr & 3)) * 72;
        const u32x2 p0 = lds64(sm, dofs + r0), p1 = lds64(sm, dofs + r0 + 8);
        const u32x2 q0 = lds64(sm, dofs + r1), q1 = lds64(sm, dofs + r1 + 8);
        const u32x2 z0 = lds64(sm, dofs + 20 * 72), z1 = lds64(sm, dofs + 20 * 72 + 8);
        A0 = (u32x4){p0.x, p0.y, p1.x, p1.y}; A2 = (u32x4){z0.x, z0.y, z1.x, z1.y};
#pragma unroll
        for (int z = 0; z < 4; z++) { const bool m = (fr >> 2) == z; A1[z] = (u32x4){m ? q0.x : 0u, m ? q0.y : 0u, m ? q1.x : 0u, m ? q1.y : 0u}; }
    }
    TS(0, 10);
    const u32 offA = WS_O + ((pr * 4 + 2 * odd + (g >> 1)) * 128 + fr * 8) * 2 + (g & 1) * 20480;
    const u32 offX = WS_O + (((16 + (pr >> 2)) * 4 + (pr & 3)) * 128 + odd * 64 + fr * 4) * 2;
    const f32x4 zero = {0, 0, 0, 0};
#pragma unroll
    for (int i4 = 0; i4 < 2; i4++) {
        f32x4 n1 = zero; float rden[4]; u32x2 ov[4];
#pragma unroll
        for (int z = 0; z < 4; z++) {
            const int i = 4 * i4 + z;
            const f32x4 n0 = mfma16(A0, Q[i], zero);
            const f32x4 dn = mfma16(A2, Q[i], zero);
            n1 = mfma16(A1[z], Q[i], n1);
            const float rd = __builtin_amdgcn_rcpf(dn[0] + 1e-6f);
            rden[z] = rd;
            ov[z] = (u32x2){pk2(n0[0] * rd, n0[1] * rd), pk2(n0[2] * rd, n0[3] * rd)};
        }
#pragma unroll
        for (int z = 0; z < 4; z += 2) {
            auto rx = __builtin_amdgcn_permlane16_swap(ov[z].x, ov[z + 1].x, false, false);
            auto ry = __builtin_amdgcn_permlane16_swap(ov[z].y, ov[z + 1].y, false, false);
            st128c(rw, offA + (sb0 + 4 * i4 + z) * 20480, (u32x4){rx[0], ry[0], rx[1], ry[1]});
        }
        const float rd = g == 0 ? rden[0] : g == 1 ? rden[1] : g == 2 ? rden[2] : rden[3];
        st64c(rw, offX + (sb0 + 4 * i4 + g) * 20480, (u32x2){pk2(n1[0] * rd, n1[1] * rd), pk2(n1[2] * rd, n1[3] * rd)});
    }
}

DI void gate_block(int wave, int lane, int m, const u16* x, const u16* gw, rsrc_t rw) {
    const int sub = lane & 15, r4 = lane >> 4;
    u32x4 gq[5], xr[4][5];
#pragma unroll
    for (int jj = 0; jj < 5; jj++) gq[jj] = *(const GAS u32x4*)((const GAS char*)gw + (sub + 16 * jj) * 16);
#pragma unroll
    for (int it = 0; it < 4; it++)
#pragma unroll
        for (int jj = 0; jj < 5; jj++) xr[it][jj] = *(const GAS u32x4*)((const GAS char*)x + (size_t)(128 * m + 16 * wave + 4 * it + r4) * 1280 + (sub + 16 * jj) * 16);
    MEMF(); SB();
#pragma unroll
    for (int it = 0; it < 4; it++) {
        float a = 0.f;
#pragma unroll
        for (int jj = 0; jj < 5; jj++)
#pragma unroll
            for (int e = 0; e < 4; e++) { a = fmaf(blo(xr[it][jj][e]), blo(gq[jj][e]), a); a = fmaf(bhi(xr[it][jj][e]), bhi(gq[jj][e]), a); }
        a += __shfl_xor(a, 1); a += __shfl_xor(a, 2); a += __shfl_xor(a, 4); a += __shfl_xor(a, 8);
        a = rbf(a);
        const float g = rbf(__builtin_amdgcn_rcpf(1.f + __builtin_amdgcn_exp2f(-1.4426950408889634f * a)));
        if (sub == 0) st32c(rw, WS_G + (128 * m + 16 * wave + 4 * it + r4) * 4, __float_as_uint(g));
    }
}

DI void final_block(LAS char* sm, int wave, int lane, int tid, int st, int ct, const u16* x, u16* out, rsrc_t rw, u32 tag, char* ws) {
    const int fr = lane & 15, g = lane >> 4;
    u32x4 xv[2];
#pragma unroll
    for (int r = 0; r < 2; r++) {
        int it = tid + 512 * r; it = it < 640 ? it : 639;
        const int s = it / 10, u = it - 10 * s;
        xv[r] = *(const GAS u32x4*)((const GAS char*)x + (size_t)(st * 64 + s) * 1280 + ct * 160 + u * 16);
    }
    VMCNT(0); BAR();
    if (wave < 4) {
        const int kh = wave & 1, sh = wave >> 1;
        wait_flags(rw, lane < 16 ? (int)F_O + (st >> 4) * 32 + 16 * kh + lane : -1, -1, tag);
        TS(0, 17);
        const u32 ob = WS_O + ((st * 4 + 2 * sh) * 20 + 8 * kh) * 1024 + lane * 16;
        const u32 os = WS_O + ((st * 4 + 2 * sh) * 20 + 16 + 2 * kh) * 1024 + g * 256 + fr * 8;
        u32x4 B[2][10];
#pragma unroll
        for (int kl = 0; kl < 8; kl++) { B[0][kl] = ld128c(rw, ob + kl * 1024); B[1][kl] = ld128c(rw, ob + 20480 + kl * 1024); }
#pragma unroll
        for (int kl = 0; kl < 2; kl++)
#pragma unroll
            for (int z = 0; z < 2; z++) {
                const u32x2 h0 = ld64c(rw, os + z * 20480 + kl * 1024), h1 = ld64c(rw, os + z * 20480 + kl * 1024 + 128);
                B[z][8 + kl] = (u32x4){h0.x, h0.y, h1.x, h1.y};
            }
        MEMF(); SB();
        f32x4 acc[5][2];
#pragma unroll
        for (int cb = 0; cb < 5; cb++) { acc[cb][0] = (f32x4){0, 0, 0, 0}; acc[cb][1] = (f32x4){0, 0, 0, 0}; }
        const u32 aoff = (16 * (g >> 1) + 4 * (g & 1) + (fr >> 2)) * 160 + (lane & 3) * 8;
        u32x2 al[2][5], ah[2][5];
#define F_KAP(kl) ((kl) < 8 ? (kl) : 8 + (kl))
#define F_READ(buf, kl) do { const u32 b0_ = kh ? wo_off(F_KAP(kl) + ((kl) < 8 ? 8 : 2)) : wo_off(F_KAP(kl)); \
            _Pragma("unroll") for (int cb = 0; cb < 5; cb++) { al[buf][cb] = ldstr(sm, b0_ + aoff + cb * 32); ah[buf][cb] = ldstr(sm, b0_ + aoff + cb * 32 + 1280); } } while (0)
        F_READ(0, 0);
#pragma unroll
        for (int kl = 0; kl < 10; kl++) {
            if (kl < 9) F_READ((kl + 1) & 1, kl + 1);
            SB();
#pragma unroll
            for (int cb = 0; cb < 5; cb++) {
                const u32x4 a = {al[kl & 1][cb].x, al[kl & 1][cb].y, ah[kl & 1][cb].x, ah[kl & 1][cb].y};
                acc[cb][0] = mfma16(a, B[0][kl], acc[cb][0]); acc[cb][1] = mfma16(a, B[1][kl], acc[cb][1]);
            }
            SB();
        }
        TS(0, 18);
        lds_arrive(sm, C_FR, lane); lds_wait(sm, C_FR, 4);
#pragma unroll
        for (int cb = 0; cb < 5; cb++)
#pragma unroll
            for (int z = 0; z < 2; z++)
                *(LAS f32x4*)(sm + kh * (64 * RED_PITCH) + (16 * (2 * sh + z) + fr) * RED_PITCH + (16 * cb + 4 * g) * 4) = acc[cb][z];
    } else {
        if (wave == 4) { wait_flags(rw, lane < 20 ? (int)F_Y + ct * 32 + lane : lane == 32 ? (int)F_GATE + (st >> 1) : -1, -1, tag); lds_arrive(sm, C_FY, lane); }
        else lds_wait(sm, C_FY, 1);
        const int t = tid - 256;
        u32x4 yv[3];
#pragma unroll
        for (int r = 0; r < 3; r++) { int id = t + 256 * r; id = id < 640 ? id : 639; yv[r] = ld128c(rw, WS_YT + (80 * ct + (id >> 3)) * 4096 + st * 128 + (id & 7) * 16); }
#pragma unroll
        for (int r = 0; r < 3; r++) { const int id = t + 256 * r; if (id < 640) *(LAS u32x4*)(sm + F_YY + id * 16) = yv[r]; }
        if (wave == 4) *(LAS u32*)(sm + F_GG + lane * 4) = ld32c(rw, WS_G + (st * 64 + lane) * 4);
        lds_arrive(sm, C_FY2, lane); lds_wait(sm, C_FY2, 4);
        const int sb = wave - 4, q = fr >> 2, p = lane & 3;
        u32x2 tt[5];
#pragma unroll
        for (int cb = 0; cb < 5; cb++) tt[cb] = ldstr(sm, F_YY + (16 * cb + 4 * g + q) * 128 + (16 * sb + 4 * p) * 2);
#pragma unroll
        for (int cb = 0; cb < 5; cb++) *(LAS u32x2*)(sm + F_Y2 + (16 * sb + fr) * 176 + (16 * cb + 4 * g) * 2) = tt[cb];
    }
    LGKM0(); BAR();
#pragma unroll
    for (int r = 0; r < 2; r++) {
        const int it = tid + 512 * r;
        if (it < 640) {
            const int s = it / 10, u = it - 10 * s;
            const u32 ra = s * RED_PITCH + u * 32;
            const f32x4 p0 = *(LAS f32x4*)(sm + ra), p1 = *(LAS f32x4*)(sm + ra + 16), q0 = *(LAS f32x4*)(sm + 64 * RED_PITCH + ra), q1 = *(LAS f32x4*)(sm + 64 * RED_PITCH + ra + 16);
            const u32x4 y8 = lds128(sm, F_Y2 + s * 176 + u * 16);
            const float gg = *(LAS float*)(sm + F_GG + s * 4), omg = 1.f - gg;
            float o[8];
#pragma unroll
            for (int e = 0; e < 4; e++) {
                const float a0 = (e < 2 ? p0[2 * e] + q0[2 * e] : p1[2 * e - 4] + q1[2 * e - 4]), a1 = (e < 2 ? p0[2 * e + 1] + q0[2 * e + 1] : p1[2 * e - 3] + q1[2 * e - 3]);
                const float b0 = omg * blo(xv[r][e]), b1 = omg * bhi(xv[r][e]);
                o[2 * e] = fmaf(b0, blo(y8[e]), gg * a0); o[2 * e + 1] = fmaf(b1, bhi(y8[e]), gg * a1);
            }
            { const u32x4 ov_ = {pk2(o[0], o[1]), pk2(o[2], o[3]), pk2(o[4], o[5]), pk2(o[6], o[7])};
              asm volatile("global_store_dwordx4 %0, %1, %2 nt\n\ts_nop 1" :: "v"((u32)((st * 64 + s) * 1280 + ct * 160 + u * 16)), "v"(ov_), "s"(out) : "memory"); }
        }
    }
}

__global__ __launch_bounds__(512, 2) void fused_kernel(const u16* __restrict__ x, const u16* __restrict__ w, const u16* __restrict__ wo, const u16* __restrict__ kin,
                                                        const u16* __restrict__ vin, const u16* __restrict__ shk, const u16* __restrict__ hk, const u16* __restrict__ sD,
                                                        const u16* __restrict__ gw, u16* __restrict__ out, char* ws) {
    const unsigned long long t_entry = __builtin_amdgcn_s_memrealtime();
    extern __shared__ __attribute__((aligned(16))) char smem[];
    LAS char* sm = (LAS char*)smem;
    const int tid = threadIdx.x, lane = tid & 63, wave = __builtin_amdgcn_readfirstlane(tid >> 6);
    const int b = blockIdx.x, xcd = b & 7, j = b >> 3;
    const rsrc_t rw = __builtin_amdgcn_make_buffer_rsrc(ws, 0, WS_BYTES, 0x00020000);
    asm volatile("" :: "s"(x), "s"(w), "s"(wo), "s"(kin), "s"(vin), "s"(shk), "s"(hk), "s"(sD), "s"(gw), "s"(out));
    if (PREF1 && wave < 3 && j < 30) {
        if (wave == 0) touch(x, (u32)((xcd * 2560 + j * 80 + lane) * 128));
        else if (wave == 1) { if (lane < 16) touch(x, (u32)((xcd * 2560 + j * 80 + 64 + lane) * 128)); else touch(w, (u32)(((xcd * 30 + j) * 80 + lane - 16) * 128)); }
        else if (lane < 32) touch(w, (u32)(((xcd * 30 + j) * 80 + 48 + lane) * 128));
        else if (PREFTIN && lane < 42) touch(kin, (u32)((lane - 32) * 262144 + (j & 7) * 128));
        else if (PREFTIN && lane < 52) touch(vin, (u32)((lane - 42) * 262144 + (j & 7) * 128));
    }
    if (PREFKV && wave < 2 && j < 30) {
        const int tile_ = xcd * 30 + j;
        touch(wave == 0 ? kin : vin, (u32)(((tile_ / 10) * 64 + lane) * 1280 + (tile_ % 10) * 128));
    }
    if (PREF3 && wave < 2 && j < 30) {
        const int n240 = xcd * 30 + j;
        if (wave == 0) { const u32 l_ = n240 * 86 + lane; touch(hk, (l_ < 20480 ? l_ : 20479) * 128); }
        else { const u32 l_ = n240 * 86 + 64 + lane, m_ = n240 * 27 + lane - 22;
               if (lane < 22) touch(hk, (l_ < 20480 ? l_ : 20479) * 128); else if (lane < 49) touch(wo, (m_ < 6400 ? m_ : 6399) * 128); }
    }
    if (PREFC && wave == 3) {
        unsigned long long pc;
        asm volatile("s_getpc_b64 %0" : "=s"(pc));
        pc &= ~127ull;
        const int ln = PREFC == 1 ? j * 10 + lane : lane;
        if (PREFC == 1) { if (lane < 10 && ln < CODE_LINES) touch((const void*)pc, (u32)(ln * 128)); }
        else {
#pragma unroll
            for (int z = 0; z < 5; z++) if (z * 64 + lane < CODE_LINES) touch((const void*)pc, (u32)((z * 64 + lane) * 128));
        }
    }
    if (PREFT && wave == 2) {
        if (PREFT == 1) { touch_tlb(ws, (u32)(lane * 262144)); if (lane < 40) touch_tlb(out, (u32)(lane * 65536)); }
        else { if (lane < 8) touch_tlb(ws, (u32)((j * 8 + lane) * 65536) % (WS_STAMP - 1024)); if (lane < 40) touch_tlb(out, (u32)(lane * 65536)); }
    }
    u32 tag;
    asm volatile("s_load_dword %0, %1, 0x0\n\ts_waitcnt lgkmcnt(0)" : "=s"(tag) : "s"(ws + WS_FLAG + F_EPOCH * FS) : "memory");
    tag += 1u;
    if (tid < 16) *(LAS u32*)(sm + L_CNT + tid * 4) = 0u;
    if (tid == 0) *(volatile LAS unsigned long long*)(sm + 156672 + 64) = 0ull;
    TS(0, 0);
    const int st = 4 * xcd + (j >> 3), ct = j & 7;
    int ko = -1;
    bool is_conv = false;

    if (j >= 30) {
        const int m = xcd * 2 + (j - 30);
        ko = -100;
        {
            const int grp = wave >> 2, w4i = wave & 3;
            u32x2 w4[3];
#pragma unroll
            for (int t = 0; t < 3; t++) { const int tt = 2 * t + grp < 5 ? 2 * t + grp : 4; p0_weights(w4[t], shk, 240 + 5 * m + tt, w4i, lane); }
#pragma unroll
            for (int t = 0; t < 3; t++) if (2 * t + grp < 5) p0_issue(kin, vin, 240 + 5 * m + 2 * t + grp, w4i, lane, (2 * t + grp) * 17408);
            VMCNT(0);
#pragma unroll
            for (int t = 0; t < 3; t++) PIN(w4[t]);
            BAR();
#pragma unroll
            for (int t = 0; t < 3; t++)
                if (2 * t + grp < 5) p0_compute(sm, w4[t], 240 + 5 * m + 2 * t + grp, w4i, lane, (2 * t + grp) * 17408, 90112 + (2 * t + grp) * 8192);
            LGKM0(); BAR();
            {
                u32x4 t4[3][2];
#pragma unroll
                for (int t = 0; t < 3; t++)
#pragma unroll
                    for (int z = 0; z < 2; z++) { const int ch2 = ((tid & 255) >> 3) + 32 * z, o2 = tid & 7, tt = 2 * t + grp < 5 ? 2 * t + grp : 4; t4[t][z] = lds128(sm, 90112 + tt * 8192 + ch2 * 128 + ((o2 ^ (ch2 & 7)) * 16)); }
#pragma unroll
                for (int t = 0; t < 3; t++)
                    if (2 * t + grp < 5) {
                        const int tile = 240 + 5 * m + 2 * t + grp, cg = tile % 10, s0 = (tile / 10) * 64;
#pragma unroll
                        for (int z = 0; z < 2; z++) { const int ch2 = ((tid & 255) >> 3) + 32 * z, o2 = tid & 7; st128c(rw, WS_UT + (cg * 64 + ch2) * 4096 + (s0 + 8 * o2) * 2, t4[t][z]); }
                    }
            }
        }
        VMCNT(0); BAR();
        if (tid < 5) set_flag(rw, p0_flag(240 + 5 * m + tid), tag);
        TS(0, 1);
        gate_block(wave, lane, m, x, gw, rw);
        VMCNT(0); LGKM0(); BAR();
        if (tid == 0) set_flag(rw, F_GATE + m, tag);
        TS(0, 3);
    } else {
        const int rt = 2 * xcd + j / 15, cbk = j % 15, tile = xcd * 30 + j;
        is_conv = cbk < 5 || cbk >= 10;
        if (!is_conv) { const int ki = rt * 5 + (cbk - 5); ko = ki < 64 ? ki : -2 - (ki - 64); }
        if (wave >= 4) {
            const int lw = wave - 4;
            u32 vx[4], vw[4];
#pragma unroll
            for (int n2 = 0; n2 < 4; n2++) {
                const int n = 4 * lw + n2;
                const int row = 8 * n + (lane >> 3), ch = (lane & 7) ^ ((row >> 1) & 7);
                vx[n2] = (u32)((rt * 128 + row) * 1280 + ch * 16);
                const int kr = 4 * n + (lane >> 4), f = ((kr & 3) << 2) | ((kr >> 2) & 3), cw = (lane & 15) ^ f;
                vw[n2] = (u32)(kr * 3840 + cbk * 256 + cw * 16);
            }
            const int rot = ((rt >> 1) + cbk) % 10;
#define K1_ISSUE(c) do { const u32 st_ = ((c) & 3) * K1_STAGE + 4 * lw * 1024; int kc_ = (c) + ROT_ON * rot; kc_ = kc_ >= 10 ? kc_ - 10 : kc_; \
            const char* xb_ = (const char*)x + kc_ * 128; const char* wp_ = (const char*)w + (size_t)kc_ * 64 * 3840; \
            _Pragma("unroll") for (int n2 = 0; n2 < 4; n2++) { if (NT_X) glds16nt(xb_, vx[n2], st_ + 16384 + n2 * 1024); else glds16(xb_, vx[n2], st_ + 16384 + n2 * 1024); glds16(wp_, vw[n2], st_ + n2 * 1024); } } while (0)
            K1_ISSUE(0); K1_ISSUE(1);
            VMCNT(8); BAR();
            K1_ISSUE(2); K1_ISSUE(3);
            p0_issue(kin, vin, tile, lw, lane, P0_K);
#pragma unroll 1
            for (int c = 0; c < 6; c++) {
                if (c < 3) VMCNT(21); else VMCNT(16);
                BAR();
                K1_ISSUE(c + 4);
            }
            const int cv = rt * 10 + (cbk < 5 ? cbk : cbk - 5), c = 4 * cv + lw;
            if (is_conv) {
                CvIn in; u32 fl; u32x4 u[4];
                const int fidx = (int)F_P0 + (cv >> 4) * 32 + (lane & 31);
                VMCNT(16); BAR();
                VMCNT(8); BAR();
                conv_fetch_h(in, hk, c, lane);
                gld32c(fl, ws, WS_FLAG + fidx * FS);
                VMCNT(9); BAR();
                TS(4, 4);
                VMCNT(0); PIN(fl);
                const bool ready = __all(fl == tag);
                if (ready) {
#pragma unroll
                    for (int z = 0; z < 4; z++) gld128c(u[z], ws, WS_UT + c * 4096 + z * 1024 + lane * 16);
                }
                TS(4, 5);
                conv_build_h(sm, in, lw, lane);
                TS(4, 6);
                if (!ready) {
                    wait_flags(rw, fidx, -1, tag);
#pragma unroll
                    for (int z = 0; z < 4; z++) gld128c(u[z], ws, WS_UT + c * 4096 + z * 1024 + lane * 16);
                }
                TS(4, 7);
                VMCNT(0);
#pragma unroll
                for (int z = 0; z < 4; z++) { PIN(u[z]); *(LAS u32x4*)(sm + CV_BASE + lw * CV_WAVE + CV_U + 512 + z * 1024 + lane * 16) = u[z]; }
                const float Dc = blo((u32)sD[c]);
                TS(4, 8);
                conv_run(sm, Dc, lw, lane, c, rw, ws);
                publish_last(rw, sm, C_Y, 4, F_Y + (cv / 20) * 32 + cv % 20, tag, lane);
                TS(4, 11);
            } else {
                VMCNT(16); BAR();
                VMCNT(8); BAR();
                VMCNT(0); BAR();
                const int r32 = lane & 31, hh = lane >> 5;
                u32 dst, istep, zstep; kv_addr(true, cbk - 5, rt, lw >> 1, lw & 1, 1, r32, hh, dst, istep, zstep);
                lds_wait(sm, C_XF + lw * 4, 1);
                f32x4 a[2][4];
#pragma unroll
                for (int i = 0; i < 2; i++)
#pragma unroll
                    for (int q4 = 0; q4 < 4; q4++) a[i][q4] = *(LAS f32x4*)(sm + K_XFER + lw * 8192 + ((i * 4 + q4) * 64 + lane) * 16);
#pragma unroll
                for (int i = 0; i < 2; i++)
#pragma unroll
                    for (int z = 0; z < 2; z++) {
                        const f32x4 a0 = a[i][2 * z], a1 = a[i][2 * z + 1];
                        st128c(rw, dst + i * istep + z * zstep, (u32x4){phi2(a0[0], a0[1]), phi2(a0[2], a0[3]), phi2(a1[0], a1[1]), phi2(a1[2], a1[3])});
                    }
                publish_last(rw, sm, C_G1, 8, F_G1 + cbk * 16 + rt, tag, lane);
            }
        } else {
            u32x2 w4; p0_weights(w4, shk, tile, wave, lane);
            const int wm = wave >> 1, wn = wave & 1, r32 = lane & 31, hh = lane >> 5;
            K1Addr a0;
#pragma unroll
            for (int ks = 0; ks < 2; ks++) {
                a0.x[0][ks] = (64 * wm + r32) * 128 + (((2 * ks + hh) ^ ((r32 >> 1) & 7)) * 16);
                a0.x[1][ks] = a0.x[0][ks] ^ 64;
            }
#pragma unroll
            for (int jj = 0; jj < 2; jj++)
#pragma unroll
                for (int t = 0; t < 2; t++) {
                    const int blk = (lane >> 4) & 1, q = (lane & 15) >> 2, p = lane & 3, row = 8 * hh + 4 * t + q, ch = 8 * wn + 4 * jj + 2 * blk + (p >> 1);
                    const int f = ((row & 3) << 2) | ((row >> 2) & 3);
                    a0.w[jj][t] = row * 256 + ((ch ^ f) * 16) + 8 * (p & 1);
                }
            f32x16 acc[2][2];
#pragma unroll
            for (int i = 0; i < 2; i++)
#pragma unroll
                for (int jj = 0; jj < 2; jj++)
#pragma unroll
                    for (int e = 0; e < 16; e++) acc[i][jj][e] = 0.f;
            BAR();
            TS(0, 1);
            if (cbk < 5) {
                k1_loop<true>(sm, a0, acc, rw, p0_flag(tile), tag, wave, lane, hk, wo, tile, w4);
                __builtin_amdgcn_s_setprio(2);
                TS(0, 2);
                u32x4 qv[2][2][2];
#pragma unroll
                for (int jj = 0; jj < 2; jj++)
#pragma unroll
                    for (int ap = 0; ap < 2; ap++)
#pragma unroll
                        for (int i = 0; i < 2; i++) {
                            const f32x16& a = acc[i][jj];
                            const u32 x0 = phi2(a[8 * ap], a[8 * ap + 1]), y0 = phi2(a[8 * ap + 2], a[8 * ap + 3]);
                            const u32 x1 = phi2(a[8 * ap + 4], a[8 * ap + 5]), y1 = phi2(a[8 * ap + 6], a[8 * ap + 7]);
                            auto rx = __builtin_amdgcn_permlane32_swap(x0, x1, false, false);
                            auto ry = __builtin_amdgcn_permlane32_swap(y0, y1, false, false);
                            qv[jj][ap][i] = (u32x4){rx[0], ry[0], rx[1], ry[1]};
                        }
                if (QDELAY) { __builtin_amdgcn_s_setprio(0); for (int z = 0; z < QDELAY; z++) __builtin_amdgcn_s_sleep(8); }
#pragma unroll
                for (int jj = 0; jj < 2; jj++)
#pragma unroll
                    for (int ap = 0; ap < 2; ap++) {
                        const int U = 16 * cbk + 8 * wn + 4 * jj + 2 * ap + hh, pr = U / 5, u5 = U - 5 * pr;
                        const u32 dst = WS_P2 + (((pr * 128 + rt * 8 + 4 * wm + (r32 >> 4)) * 5 + u5) * 128 + (r32 & 15) * 8) * 2;
#pragma unroll
                        for (int i = 0; i < 2; i++) st128c(rw, dst + i * 2560, qv[jj][ap][i]);
                    }
            } else {
                k1_loop<false>(sm, a0, acc, rw, p0_flag(tile), tag, wave, lane, hk, wo, tile, w4);
                __builtin_amdgcn_s_setprio(2);
                TS(0, 2);
                const bool isk = cbk < 10;
                const int j5 = isk ? cbk - 5 : cbk - 10;
                if (isk) {
#pragma unroll
                    for (int i = 0; i < 2; i++)
#pragma unroll
                        for (int q4 = 0; q4 < 4; q4++)
                            *(LAS f32x4*)(sm + K_XFER + wave * 8192 + ((i * 4 + q4) * 64 + lane) * 16) = (f32x4){acc[i][1][4 * q4], acc[i][1][4 * q4 + 1], acc[i][1][4 * q4 + 2], acc[i][1][4 * q4 + 3]};
                    lds_arrive(sm, C_XF + wave * 4, lane);
                    u32 dst, istep, zstep; kv_addr(true, j5, rt, wm, wn, 0, r32, hh, dst, istep, zstep);
#pragma unroll
                    for (int i = 0; i < 2; i++)
#pragma unroll
                        for (int z = 0; z < 2; z++) {
                            const f32x16& a = acc[i][0];
                            st128c(rw, dst + i * istep + z * zstep,
                                   (u32x4){phi2(a[8 * z], a[8 * z + 1]), phi2(a[8 * z + 2], a[8 * z + 3]), phi2(a[8 * z + 4], a[8 * z + 5]), phi2(a[8 * z + 6], a[8 * z + 7])});
                        }
                } else {
#pragma unroll
                    for (int jj = 0; jj < 2; jj++) {
                        u32 dst, istep, zstep; kv_addr(false, j5, rt, wm, wn, jj, r32, hh, dst, istep, zstep);
#pragma unroll
                        for (int i = 0; i < 2; i++)
#pragma unroll
                            for (int z = 0; z < 2; z++) {
                                const f32x16& a = acc[i][jj];
                                st128c(rw, dst + i * istep + z * zstep,
                                       (u32x4){pk2(a[8 * z], a[8 * z + 1]), pk2(a[8 * z + 2], a[8 * z + 3]), pk2(a[8 * z + 4], a[8 * z + 5]), pk2(a[8 * z + 6], a[8 * z + 7])});
                            }
                    }
                }
            }
            LGKM0();
            TS(0, 25);
            publish_last(rw, sm, C_G1, is_conv ? 4 : 8, F_G1 + cbk * 16 + rt, tag, lane);
            __builtin_amdgcn_s_setprio(0);
            TS(0, 3);
            if (is_conv) {
                lds_wait(sm, C_G1, 4);
                wo_issue(wo, ct, 0, 12, wave, 4, lane);
            }
        }
    }
    if (!is_conv) {
        LGKM0(); BAR();
        if (ko >= 0) {
            const int h = ko >> 1, half = ko & 1;
            wo_issue(wo, ct, 0, 12, wave, 8, lane);
            kvo_block(sm, wave, lane, h, half, rw, tag, ws);
            TS(0, 26);
            VMCNT(0); BAR();
            if (tid == 0) set_flag(rw, F_O + half * 32 + h, tag);
            TS(0, 11);
            wo_issue(wo, ct, 12, 20, wave, 8, lane);
        } else {
            wo_issue(wo, ct, 0, 20, wave, 8, lane);
            if (ko == -2 && wave == 0) {
                wait_flags(rw, (int)F_G1 + lane, (int)F_G1 + 64 + lane, tag);
                wait_flags(rw, (int)F_G1 + 128 + lane, (int)F_G1 + 192 + lane, tag);
                if (lane == 0) set_flag(rw, F_EPOCH, tag);
            }
        }
    } else {
        VMCNT(0); BAR();
        if (wave >= 4) wo_issue(wo, ct, 12, 20, wave - 4, 4, lane);
    }
    TS(0, 16);
    final_block(sm, wave, lane, tid, st, ct, x, out, rw, tag, ws);

    VMCNT(0); BAR();
    if (tid == 0) {
        const unsigned long long te_ = *(volatile LAS unsigned long long*)(sm + 156672 + 64);
        st64c(rw, WS_STAMP + blockIdx.x * 64, __builtin_bit_cast(u32x2, t_entry)); st64c(rw, WS_STAMP + blockIdx.x * 64 + 8, __builtin_bit_cast(u32x2, te_));
        VMCNT(0);
        st32c(rw, WS_STAMP + blockIdx.x * 64 + 16, tag);
    }
    if (blockIdx.x == 51 && wave == 0) {
        unsigned long long mn = ~0ull, mx = 0;
        for (int q = 0; q < 4; q++) {
            while (ld32c(rw, WS_STAMP + (q * 64 + lane) * 64 + 16) != tag) __builtin_amdgcn_s_sleep(1);
            const unsigned long long a_ = __builtin_bit_cast(unsigned long long, __builtin_amdgcn_raw_buffer_load_b64(rw, WS_STAMP + (q * 64 + lane) * 64, 0, 16));
            const unsigned long long e2_ = __builtin_bit_cast(unsigned long long, __builtin_amdgcn_raw_buffer_load_b64(rw, WS_STAMP + (q * 64 + lane) * 64 + 8, 0, 16));
            mn = a_ < mn ? a_ : mn; mx = e2_ > mx ? e2_ : mx; }
        for (int o = 1; o < 64; o <<= 1) { const unsigned long long a_ = __shfl_xor(mn, o), b_ = __shfl_xor(mx, o); mn = a_ < mn ? a_ : mn; mx = b_ > mx ? b_ : mx; }
        const unsigned long long d_ = mx > mn ? mx - mn : 0;
        const unsigned long long e_ = __builtin_amdgcn_s_memrealtime() + 2 * d_;
        while (__builtin_amdgcn_s_memrealtime() < e_) __builtin_amdgcn_s_sleep(1);
    }
    TS(0, 19);
}

extern "C" void kernel_launch(void* const* d_in, const int* in_sizes, int n_in, void* d_out, int out_size, void* d_ws, size_t ws_size, hipStream_t stream) {
    const u16* x = (const u16*)d_in[0]; const u16* wqkv = (const u16*)d_in[1]; const u16* wo = (const u16*)d_in[2];
    const u16* kin = (const u16*)d_in[4]; const u16* vin = (const u16*)d_in[5]; const u16* shk = (const u16*)d_in[6];
    const u16* hk = (const u16*)d_in[7]; const u16* sD = (const u16*)d_in[8]; const u16* gw = (const u16*)d_in[9];
    (void)hipFuncSetAttribute((const void*)fused_kernel, hipFuncAttributeMaxDynamicSharedMemorySize, LDS_BYTES);
    fused_kernel<<<256, 512, LDS_BYTES, stream>>>(x, wqkv, wo, kin, vin, shk, hk, sD, gw, (u16*)d_out, (char*)d_ws);
}
```

```cpp
#include <hip/hip_runtime.h>
#include <stdint.h>

typedef unsigned short u16;
typedef unsigned int u32;
typedef __bf16 bf16x8 __attribute__((ext_vector_type(8)));
typedef __bf16 bf16x2 __attribute__((ext_vector_type(2)));
typedef float f32x4 __attribute__((ext_vector_type(4)));
typedef float f32x2 __attribute__((ext_vector_type(2)));
typedef float f32x16 __attribute__((ext_vector_type(16)));
typedef u32 u32x4 __attribute__((ext_vector_type(4)));
typedef u32 u32x2 __attribute__((ext_vector_type(2)));
typedef short s16x4 __attribute__((ext_vector_type(4)));
#define LAS __attribute__((address_space(3)))
#define GAS __attribute__((address_space(1)))
#define DI __device__ __forceinline__
typedef __amdgpu_buffer_rsrc_t rsrc_t;

constexpr int S = 2048;
constexpr u32 FS = 256;

constexpr u32 WS_UT = 0;
constexpr u32 WS_YT = WS_UT + 2621440;
constexpr u32 WS_PK0 = WS_YT + 2621440;
constexpr u32 WS_V0 = WS_PK0 + 2097152;
constexpr u32 WS_XK = WS_V0 + 2097152;
constexpr u32 WS_XV = WS_XK + 524288;
constexpr u32 WS_P2 = WS_XV + 524288;
constexpr u32 WS_O = WS_P2 + 2621440;
constexpr u32 WS_G = WS_O + 2621440;
constexpr u32 WS_FLAG = WS_G + 8192;
constexpr u32 F_P0 = 0;
constexpr u32 F_G1 = 320;
constexpr u32 F_GATE = 560;
constexpr u32 F_O = 576;
constexpr u32 F_Y = 640;
constexpr u32 F_EPOCH = 896, F_TMO = 928;
constexpr u32 WS_BYTES = WS_FLAG + 1024 * FS;

DI u32 pk2(float a, float b) { f32x2 t = {a, b}; return __builtin_bit_cast(u32, __builtin_convertvector(t, bf16x2)); }
DI float blo(u32 u) { return __uint_as_float(u << 16); }
DI float bhi(u32 u) { return __uint_as_float(u & 0xffff0000u); }
DI float rbf(float f) { return blo(pk2(f, f)); }
DI bf16x8 asbf(u32x4 v) { return __builtin_bit_cast(bf16x8, v); }
DI f32x4 mfma16(u32x4 a, u32x4 b, f32x4 c) { return __builtin_amdgcn_mfma_f32_16x16x32_bf16(asbf(a), asbf(b), c, 0, 0, 0); }
DI f32x16 mfma32(u32x4 a, u32x4 b, f32x16 c) { return __builtin_amdgcn_mfma_f32_32x32x16_bf16(asbf(a), asbf(b), c, 0, 0, 0); }

DI void glds16(const void* sbase, u32 voff, u32 lds) {
    asm volatile("s_mov_b32 m0, %2\n\ts_nop 0\n\tglobal_load_lds_dwordx4 %0, %1" :: "v"(voff), "s"(sbase), "s"(lds) : "memory");
}
DI void touch(const void* sbase, u32 voff) {
    asm volatile("s_mov_b32 m0, %2\n\ts_nop 0\n\tglobal_load_lds_dword %0, %1" :: "v"(voff), "s"(sbase), "s"(156928u) : "memory");
}
DI void touch_tlb(const void* sbase, u32 voff) {
    asm volatile("s_mov_b32 m0, %2\n\ts_nop 0\n\tglobal_load_lds_dword %0, %1 sc1" :: "v"(voff), "s"(sbase), "s"(156928u) : "memory");
}
constexpr int CODE_LINES = 322;
DI void gld64(u32x2& d, const void* sbase, u32 voff) { asm volatile("global_load_dwordx2 %0, %1, %2" : "=v"(d) : "v"(voff), "s"(sbase) : "memory"); }
DI void gld128(u32x4& d, const void* sbase, u32 voff) { asm volatile("global_load_dwordx4 %0, %1, %2" : "=v"(d) : "v"(voff), "s"(sbase) : "memory"); }
#define PIN(x) asm volatile("" : "+v"(x))
#define SB() __builtin_amdgcn_sched_barrier(0)
DI u32x4 ld128c(rsrc_t r, u32 off) { return __builtin_amdgcn_raw_buffer_load_b128(r, off, 0, 16); }
DI u32x2 ld64c(rsrc_t r, u32 off) { return __builtin_amdgcn_raw_buffer_load_b64(r, off, 0, 16); }
DI u32 ld32c(rsrc_t r, u32 off) { return __builtin_amdgcn_raw_buffer_load_b32(r, off, 0, 16); }
DI void st128c(rsrc_t r, u32 off, u32x4 v) { __builtin_amdgcn_raw_buffer_store_b128(v, r, off, 0, 16); }
DI void st64c(rsrc_t r, u32 off, u32x2 v) { __builtin_amdgcn_raw_buffer_store_b64(v, r, off, 0, 16); }
DI void st32c(rsrc_t r, u32 off, u32 v) { __builtin_amdgcn_raw_buffer_store_b32(v, r, off, 0, 16); }
DI u32x4 lds128(LAS char* sm, u32 a) { return *(LAS u32x4*)(sm + a); }
DI u32x2 lds64(LAS char* sm, u32 a) { return *(LAS u32x2*)(sm + a); }
DI u32x2 ldstr(LAS char* sm, u32 a) { return __builtin_bit_cast(u32x2, __builtin_amdgcn_ds_read_tr16_b64_v4i16((LAS s16x4*)(sm + a))); }
#define VMCNT(n) asm volatile("s_waitcnt vmcnt(" #n ")" ::: "memory")
#define LGKM0() asm volatile("s_waitcnt lgkmcnt(0)" ::: "memory")
#define BAR() do { asm volatile("" ::: "memory"); __builtin_amdgcn_s_barrier(); asm volatile("" ::: "memory"); } while (0)
#define MEMF() asm volatile("" ::: "memory")

DI void set_flag(rsrc_t r, u32 idx, u32 tag) { st32c(r, WS_FLAG + idx * FS, tag); }
DI void wait_flags(rsrc_t r, int idx0, int idx1, u32 tag) {
    unsigned long long t0 = 0;
    for (u32 it = 0;; it++) {
        u32 a = tag, b = tag;
        if (idx0 >= 0) a = ld32c(r, WS_FLAG + idx0 * FS);
        if (idx1 >= 0) b = ld32c(r, WS_FLAG + idx1 * FS);
        if (__all(a == tag && b == tag)) break;
        __builtin_amdgcn_s_sleep(1);
        if ((it & 63u) == 0u) {
            const unsigned long long t = __builtin_amdgcn_s_memrealtime();
            if (it == 0) t0 = t;
            else if (t - t0 > 2000000ull) { set_flag(r, F_TMO, tag); break; }
        }
    }
    MEMF();
}
DI void lds_arrive(LAS char* sm, u32 cnt, int lane) { LGKM0(); if (lane == 0) __hip_atomic_fetch_add((LAS u32*)(sm + cnt), 1u, __ATOMIC_RELAXED, __HIP_MEMORY_SCOPE_WORKGROUP); MEMF(); }
DI void lds_wait(LAS char* sm, u32 cnt, u32 n) { MEMF(); while (*(volatile LAS u32*)(sm + cnt) < n) __builtin_amdgcn_s_sleep(0); MEMF(); }
DI void publish_last(rsrc_t r, LAS char* sm, u32 cnt, u32 nw, u32 flag, u32 tag, int lane) {
    VMCNT(0);
    if (lane == 0) {
        const u32 old = __hip_atomic_fetch_add((LAS u32*)(sm + cnt), 1u, __ATOMIC_RELAXED, __HIP_MEMORY_SCOPE_WORKGROUP);
        if (old == nw - 1) set_flag(r, flag, tag);
    }
    MEMF();
}

DI u32 phi2(float a, float b) {
    const f32x2 t = {a, b}, s = t * 1.4426950408889634f;
    const float ea = __builtin_amdgcn_fmed3f(__builtin_amdgcn_exp2f(s.x), 0.f, 1.f), eb = __builtin_amdgcn_fmed3f(__builtin_amdgcn_exp2f(s.y), 0.f, 1.f);
    const float ma = __int_as_float(max(__float_as_int(a), 0)), mb = __int_as_float(max(__float_as_int(b), 0));
    const f32x2 r = ((f32x2){ea, eb} + (f32x2){ma, mb}) + 1e-6f;
    return pk2(r.x, r.y);
}

constexpr int K1_STAGE = 32768;
constexpr int P0_K = 131072, P0_V = P0_K + 9216, P0_OUT = P0_V + 8192;
constexpr int L_CNT = 156672;
constexpr int C_G1 = L_CNT, C_Y = L_CNT + 4, C_FY = L_CNT + 8, C_FY2 = L_CNT + 12, C_FR = L_CNT + 16, C_XF = L_CNT + 20;
constexpr int CV_PITCH = 4160;
constexpr int CV_U = 4 * CV_PITCH;
constexpr int CV_WAVE = CV_U + 4608;
constexpr int CV_BASE = 65536;
constexpr int WO_HI = 65536;
constexpr int F_YY = 110592;
constexpr int F_Y2 = F_YY + 10240;
constexpr int F_GG = F_Y2 + 11264;
constexpr int KO_RED = 110592;
constexpr int KO_KV = KO_RED + 32768;
constexpr int RED_PITCH = 336;
constexpr int LDS_BYTES = 160 * 1024;
DI u32 p0_flag(int tile) { return F_P0 + (tile % 10) * 32 + tile / 10; }
DI constexpr u32 wo_off(int kap) { return kap < 12 ? kap * 5120 : WO_HI + (kap - 12) * 5120; }

DI void p0_issue(const u16* kin, const u16* vin, int tile, int lw, int lane, u32 base) {
    const int cg = tile % 10, rg = tile / 10, s0 = rg * 64;
    const u32 col = cg * 128 + (lane & 7) * 16;
#pragma unroll
    for (int z = 0; z < 2; z++) {
        const int n = 2 * lw + z;
        int r = s0 - 4 + n * 8 + (lane >> 3); r = r < 0 ? 0 : r;
        glds16(kin, (u32)(r * 1280) + col, base + n * 1024);
        glds16(vin, (u32)((s0 + n * 8 + (lane >> 3)) * 1280) + col, base + 9216 + n * 1024);
    }
    {
        int r = s0 + 60 + (lane >> 3); r = r > S - 1 ? S - 1 : r;
        glds16(kin, (u32)(r * 1280) + col, base + 8 * 1024);
    }
}
DI float dot2(u32 a, u32 b, float c) { return __builtin_amdgcn_fdot2_f32_bf16(__builtin_bit_cast(bf16x2, a), __builtin_bit_cast(bf16x2, b), c, false); }
DI void p0_compute(LAS char* sm, u32x2 w4, int tile, int wave, int lane, u32 base, u32 outb) {
    const int s0 = (tile / 10) * 64;
    const int G = wave * 4 + (lane >> 4), cb = G & 3, i = lane & 15, q = i >> 2, p = i & 3;
    const u32 wa = __builtin_amdgcn_alignbit(w4.y, w4.y, 16), wb = __builtin_amdgcn_alignbit(w4.x, w4.x, 16);
    const int ch = 16 * cb + i;
    u32x2 kq[2][3], vq[2][2];
#pragma unroll
    for (int z = 0; z < 2; z++) {
        const int o = (G >> 2) + 4 * z;
        const u32 ka = base + (8 * o + q) * 128 + 32 * cb + 8 * p, va = ka + 9216;
        kq[z][0] = ldstr(sm, ka); kq[z][1] = ldstr(sm, ka + 512); kq[z][2] = ldstr(sm, ka + 1024);
        vq[z][0] = ldstr(sm, va); vq[z][1] = ldstr(sm, va + 512);
    }
#pragma unroll
    for (int z = 0; z < 2; z++) {
        const int o = (G >> 2) + 4 * z;
        u32 K[6] = {kq[z][0].x, kq[z][0].y, kq[z][1].x, kq[z][1].y, kq[z][2].x, kq[z][2].y};
        if (s0 == 0 && o == 0) { K[0] = 0; K[1] = 0; }
        u32 Sh[5];
#pragma unroll
        for (int e = 0; e < 5; e++) Sh[e] = __builtin_amdgcn_alignbit(K[e + 1], K[e], 16);
        const u32 V[4] = {vq[z][0].x, vq[z][0].y, vq[z][1].x, vq[z][1].y};
        u32 r[4];
#pragma unroll
        for (int e = 0; e < 4; e++) {
            const float k0 = dot2(Sh[e], wa, dot2(Sh[e + 1], wb, 0.f)), k1 = dot2(K[e + 1], wa, dot2(K[e + 2], wb, 0.f));
            r[e] = pk2(k0 * blo(V[e]), k1 * bhi(V[e]));
        }
        *(LAS u32x4*)(sm + outb + ch * 128 + ((o ^ (ch & 7)) * 16)) = (u32x4){r[0], r[1], r[2], r[3]};
    }
}
DI void p0_store(LAS char* sm, rsrc_t rw, int tile, int t256, u32 outb) {
    const int cg = tile % 10, s0 = (tile / 10) * 64;
#pragma unroll
    for (int z = 0; z < 2; z++) {
        const int ch2 = (t256 >> 3) + 32 * z, o2 = t256 & 7;
        const u32x4 t4 = lds128(sm, outb + ch2 * 128 + ((o2 ^ (ch2 & 7)) * 16));
        st128c(rw, WS_UT + (cg * 64 + ch2) * 4096 + (s0 + 8 * o2) * 2, t4);
    }
}
DI void p0_weights(u32x2& w4, const u16* shk, int tile, int wave, int lane) {
    const int cg = tile % 10, cb = (wave * 4 + (lane >> 4)) & 3;
    gld64(w4, shk, (u32)((cg * 64 + 16 * cb + (lane & 15)) * 8));
}

struct K1Set { u32x4 xa[2][2]; u32x2 wl[2][2], wh[2][2]; };
struct K1Addr { u32 x[2][2]; u32 w[2][2]; };
template <bool QT, int WO, int XO>
DI void k1_phase(LAS char* sm, f32x16 (&acc)[2][2], const K1Set& cur, K1Set& nxt, const u32 (&xb)[2], const u32 (&wb)[2][2]) {
#pragma unroll
    for (int n = 0; n < 8; n++) {
        const int ks = n >> 2, i = (n >> 1) & 1, jj = n & 1;
        const u32x4 wf = {cur.wl[ks][jj].x, cur.wl[ks][jj].y, cur.wh[ks][jj].x, cur.wh[ks][jj].y};
        acc[i][jj] = QT ? mfma32(wf, cur.xa[ks][i], acc[i][jj]) : mfma32(cur.xa[ks][i], wf, acc[i][jj]);
        if (n == 0) { nxt.wl[0][0] = ldstr(sm, wb[0][0] + WO); nxt.wh[0][0] = ldstr(sm, wb[0][1] + WO); }
        if (n == 1) { nxt.xa[0][0] = lds128(sm, xb[0] + XO); nxt.wl[0][1] = ldstr(sm, wb[1][0] + WO); }
        if (n == 2) { nxt.wh[0][1] = ldstr(sm, wb[1][1] + WO); nxt.xa[0][1] = lds128(sm, xb[0] + XO + 4096); }
        if (n == 3) { nxt.wl[1][0] = ldstr(sm, wb[0][0] + WO + 4096); nxt.wh[1][0] = ldstr(sm, wb[0][1] + WO + 4096); }
        if (n == 4) { nxt.xa[1][0] = lds128(sm, xb[1] + XO); nxt.wl[1][1] = ldstr(sm, wb[1][0] + WO + 4096); }
        if (n == 5) { nxt.wh[1][1] = ldstr(sm, wb[1][1] + WO + 4096); nxt.xa[1][1] = lds128(sm, xb[1] + XO + 4096); }
        SB();
    }
}
template <bool QT, int ST>
DI void k1_chunk(LAS char* sm, f32x16 (&acc)[2][2], K1Set& s0, K1Set& s1, const K1Addr& a0, const K1Addr& a1, bool bar, bool drain = false) {
    constexpr int SN = (ST + 1) & 3;
    const K1Addr& ac = ST < 2 ? a0 : a1; const K1Addr& an = SN < 2 ? a0 : a1;
    k1_phase<QT, (ST & 1) * K1_STAGE + 8192, (ST & 1) * K1_STAGE + 16384>(sm, acc, s0, s1, ac.x[1], ac.w);
    if (drain) VMCNT(0);
    if (bar) { LGKM0(); BAR(); }
    k1_phase<QT, (SN & 1) * K1_STAGE, (SN & 1) * K1_STAGE + 16384>(sm, acc, s1, s0, an.x[0], an.w);
}
template <bool QT>
DI void k1_loop(LAS char* sm, const K1Addr& a0, f32x16 (&acc)[2][2], rsrc_t rw, u32 pflag, u32 tag, int wave, int lane, int tile, u32x2 w4) {
    K1Addr a1;
#pragma unroll
    for (int i = 0; i < 2; i++)
#pragma unroll
        for (int z = 0; z < 2; z++) { a1.x[i][z] = a0.x[i][z] + 65536; a1.w[i][z] = a0.w[i][z] + 65536; }
    K1Set s0, s1;
#pragma unroll
    for (int ks = 0; ks < 2; ks++)
#pragma unroll
        for (int jj = 0; jj < 2; jj++) {
            s0.wl[ks][jj] = ldstr(sm, a0.w[jj][0] + ks * 4096); s0.wh[ks][jj] = ldstr(sm, a0.w[jj][1] + ks * 4096);
            s0.xa[ks][jj] = lds128(sm, a0.x[0][ks] + 16384 + jj * 4096);
        }
    SB();
#pragma unroll 1
    for (int c = 0; c < 8; c += 4) {
        k1_chunk<QT, 0>(sm, acc, s0, s1, a0, a1, true);
        if (c == 4) p0_store(sm, rw, tile, wave * 64 + lane, P0_OUT);
        k1_chunk<QT, 1>(sm, acc, s0, s1, a0, a1, true);
        k1_chunk<QT, 2>(sm, acc, s0, s1, a0, a1, true);
        k1_chunk<QT, 3>(sm, acc, s0, s1, a0, a1, true, c == 4);
        if (c == 4 && wave == 0 && lane == 0) set_flag(rw, pflag, tag);
        if (c == 0) p0_compute(sm, w4, tile, wave, lane, P0_K, P0_OUT);
    }
    k1_chunk<QT, 0>(sm, acc, s0, s1, a0, a1, true);
    k1_chunk<QT, 1>(sm, acc, s0, s1, a0, a1, false);
}

DI void kv_addr(bool isk, int j5, int rt, int wm, int wn, int jj, int r32, int hh, u32& dst, u32& istep, u32& zstep) {
    const int cc = 128 * j5 + 64 * wn + 32 * jj + r32, h = cc / 20, d = cc - 20 * h;
    const int sb32 = rt * 4 + 2 * wm;
    const bool lo = d < 16;
    dst = lo ? (isk ? WS_PK0 : WS_V0) + (((h * 64 + sb32) * 4 + hh) * 128 + d * 8) * 2
             : (isk ? WS_XK : WS_XV) + (((h * 64 + sb32) * 4 + hh) * 32 + (d - 16) * 8) * 2;
    istep = lo ? 1024 : 256; zstep = lo ? 512 : 128;
}
constexpr int K_XFER = 65536;

DI void wo_issue(const u16* wo, int ct, int k0, int k1, int w, int nw, int lane) {
    u32 vo[5], vs[5];
#pragma unroll
    for (int i = 0; i < 5; i++) {
        const int n = 64 * i + lane, rho = n / 10, c = n - 10 * rho, g = 2 * (rho >> 4) + ((rho >> 2) & 1), r8 = 4 * ((rho >> 3) & 1) + (rho & 3);
        vo[i] = (u32)((8 * (g < 2 ? g : g + 1) + r8) * 1280 + ct * 160 + c * 16);
        vs[i] = (u32)((40 * g + 16 + r8) * 1280 + ct * 160 + c * 16);
    }
#pragma unroll 1
    for (int kap = k0 + w; kap < k1; kap += nw) {
        const u32 dst = kap < 12 ? kap * 5120 : WO_HI + (kap - 12) * 5120;
        if (kap < 16) {
            const char* src = (const char*)wo + (size_t)kap * 51200;
#pragma unroll
            for (int i = 0; i < 5; i++) glds16(src, vo[i], dst + i * 1024);
        } else {
            const char* src = (const char*)wo + (size_t)(kap - 16) * 204800;
#pragma unroll
            for (int i = 0; i < 5; i++) glds16(src, vs[i], dst + i * 1024);
        }
    }
}

struct CvSet { u32x2 fl[8], fh[8]; u32x4 g[8]; };
DI void cv_load(LAS char* sm, CvSet& s, u32 fa, u32 ga) {
#pragma unroll
    for (int k1 = 0; k1 < 8; k1++) { s.fl[k1] = ldstr(sm, fa + 512 * k1 + 8); s.fh[k1] = ldstr(sm, fa + 512 * k1); s.g[k1] = lds128(sm, ga + 512 * k1); }
}
DI void cv_phase(LAS char* sm, f32x4 (&Y)[8], const CvSet& cur, CvSet& nxt, u32 fa, u32 ga) {
    PIN(fa); PIN(ga);
    SB();
    int n = 0;
#pragma unroll
    for (int e = 0; e < 8; e++)
#pragma unroll
        for (int k1 = 0; k1 + e < 8; k1++) {
            const u32x4 F = {cur.fl[k1].x, cur.fl[k1].y, cur.fh[k1].x, cur.fh[k1].y};
            Y[k1 + e] = mfma16(F, cur.g[e], Y[k1 + e]);
            if (n == 0) nxt.g[0] = lds128(sm, ga);
            else if (n <= 16) { const int r = (n - 1) >> 1; if ((n - 1) & 1) nxt.fh[r] = ldstr(sm, fa + 512 * r); else nxt.fl[r] = ldstr(sm, fa + 512 * r + 8); }
            else if (n < 24) nxt.g[n - 16] = lds128(sm, ga + 512 * (n - 16));
            n++;
            SB();
        }
}
struct CvIn { u32x4 hd[4]; u32x2 hm[4]; };
DI void conv_fetch_h(CvIn& in, const u16* hk, int c, int lane) {
    const char* hb = (const char*)hk + (size_t)c * 4096;
#pragma unroll
    for (int z = 0; z < 4; z++) {
        const int i = lane + 64 * z;
        gld128(in.hd[z], hb, (u32)(i * 16));
        gld64(in.hm[z], hb, (u32)(i > 0 ? i * 16 - 8 : 0));
    }
}
DI void conv_build_h(LAS char* sm0, CvIn& in, int w, int lane) {
    LAS char* sm = sm0 + CV_BASE + w * CV_WAVE;
#pragma unroll
    for (int z = 0; z < 4; z++) { PIN(in.hd[z]); PIN(in.hm[z]); }
    if (lane < 32) *(LAS u32x4*)(sm + CV_U + lane * 16) = (u32x4){0, 0, 0, 0};
    if (lane < 8) *(LAS u32x4*)(sm + (lane >> 1) * CV_PITCH + (lane & 1) * 16) = (u32x4){0, 0, 0, 0};
#pragma unroll
    for (int z = 0; z < 4; z++) {
        const int i = lane + 64 * z;
        u32 m2 = in.hm[z].x, m1 = in.hm[z].y;
        if (i == 0) { m2 = 0; m1 = 0; }
        const u32 d0 = in.hd[z].x, d1 = in.hd[z].y, d2 = in.hd[z].z, d3 = in.hd[z].w;
        const u32 om2 = __builtin_amdgcn_alignbit(m1, m2, 16), om1 = __builtin_amdgcn_alignbit(d0, m1, 16), o0 = __builtin_amdgcn_alignbit(d1, d0, 16),
                  o1 = __builtin_amdgcn_alignbit(d2, d1, 16), o2 = __builtin_amdgcn_alignbit(d3, d2, 16);
        LAS char* p = sm + 32 + i * 16;
        *(LAS u32x4*)(p) = (u32x4){d0, d1, d2, d3};
        *(LAS u32x4*)(p + CV_PITCH) = (u32x4){om1, o0, o1, o2};
        *(LAS u32x4*)(p + 2 * CV_PITCH) = (u32x4){m1, d0, d1, d2};
        *(LAS u32x4*)(p + 3 * CV_PITCH) = (u32x4){om2, om1, o0, o1};
    }
}
DI void conv_run(LAS char* sm0, float Dc, int w, int lane, int c, rsrc_t rw) {
    LAS char* sm = sm0;
    LGKM0();
    const int fr = lane & 15, g = lane >> 4, q = fr >> 2, p = lane & 3;
    const u32 fa = CV_BASE + w * CV_WAVE + q * CV_PITCH + 2 * (32 - 8 * g + 4 * p) - 8;
    const u32 ga = CV_BASE + w * CV_WAVE + CV_U + 480 + 32 * fr + 16 * g;
    f32x4 Y[8];
#pragma unroll
    for (int i = 0; i < 8; i++) Y[i] = (f32x4){0, 0, 0, 0};
    CvSet A, B;
    cv_load(sm, A, fa, ga);
    SB();
#pragma unroll 1
    for (int k0 = 0; k0 < 8; k0 += 2) {
        cv_phase(sm, Y, A, B, fa + 64 * (k0 + 1), ga - 64 * (k0 + 1));
        cv_phase(sm, Y, B, A, fa + 64 * (k0 + 2), ga - 64 * (k0 + 2));
    }
    sm = sm0 + CV_BASE + w * CV_WAVE;
    {
        u32x2 u4[8];
#pragma unroll
        for (int ib = 0; ib < 8; ib++) u4[ib] = lds64(sm, CV_U + 512 + 512 * ib + 32 * fr + 8 * g);
#pragma unroll
        for (int ib = 0; ib < 8; ib++) {
            const u32 y0 = pk2(Y[ib][0], Y[ib][1]), y1 = pk2(Y[ib][2], Y[ib][3]);
            const u32 d0 = pk2(blo(u4[ib].x) * Dc, bhi(u4[ib].x) * Dc), d1 = pk2(blo(u4[ib].y) * Dc, bhi(u4[ib].y) * Dc);
            const u32x2 r = {pk2(blo(y0) + blo(d0), bhi(y0) + bhi(d0)), pk2(blo(y1) + blo(d1), bhi(y1) + bhi(d1))};
            *(LAS u32x2*)(sm + 512 * ib + 32 * fr + 8 * g) = r;
        }
    }
    LGKM0();
    u32x4 y8[4];
#pragma unroll
    for (int z = 0; z < 4; z++) y8[z] = lds128(sm, z * 1024 + lane * 16);
#pragma unroll
    for (int z = 0; z < 4; z++) st128c(rw, WS_YT + c * 4096 + z * 1024 + lane * 16, y8[z]);
}

DI void kvo_block(LAS char* sm, int wave, int lane, int h, int half, rsrc_t rw, u32 tag) {
    const int fr = lane & 15, g = lane >> 4;
    const int j0 = (20 * h) >> 7, j1 = (20 * h + 19) >> 7;
    wait_flags(rw, lane < 8 ? (int)F_G1 + 16 * (((lane & 2) ? 10 : 5) + ((lane & 1) ? j1 : j0)) + 2 * wave + (lane >> 2)
                            : lane < 10 ? (int)F_G1 + 16 * ((lane & 1) ? j1 : j0) + 8 * half + wave : -1, -1, tag);
    const u32 pk0 = WS_PK0 + (h * 64 + 8 * wave) * 1024 + lane * 16, v0 = pk0 + (WS_V0 - WS_PK0);
    const u32 xx = ((fr & 4) ? WS_XV : WS_XK) + ((h * 64 + 8 * wave + (fr >> 3)) * 4 + g) * 64 + (fr & 3) * 16;
    u32x4 A[8], B[8], X[4], Q[8];
#pragma unroll
    for (int i = 0; i < 8; i++) { A[i] = ld128c(rw, pk0 + i * 1024); B[i] = ld128c(rw, v0 + i * 1024); if ((i & 1) == 0) X[i >> 1] = ld128c(rw, xx + (i >> 1) * 512); }
    const int odd = h & 1, pr = h >> 1;
    const int u5 = (g < 3 ? g : 2) + 2 * odd;
    const int sb0 = half * 64 + wave * 8;
    const u32 pq = WS_P2 + (((pr * 128 + sb0) * 5 + u5) * 128 + fr * 8) * 2;
    MEMF(); SB();
    f32x4 a00 = {0, 0, 0, 0}, a0x = a00, ax0 = a00, axx = a00;
    const u32 cst = fr == 8 ? 0x3F803F80u : 0u;
#pragma unroll
    for (int i = 0; i < 8; i++) {
        u32x4 xv = X[i >> 1];
        if (i & 1) {
#pragma unroll
            for (int e = 0; e < 4; e++) xv[e] = (u32)__builtin_amdgcn_update_dpp(0, (int)xv[e], 0x108, 0xf, 0xf, false);
        }
        if (fr >= 8) xv = (u32x4){cst, cst, cst, cst};
        a00 = mfma16(A[i], B[i], a00); a0x = mfma16(A[i], xv, a0x); ax0 = mfma16(xv, B[i], ax0); axx = mfma16(xv, xv, axx);
    }
    PIN(a00); PIN(a0x); PIN(ax0); PIN(axx);
    SB();
#pragma unroll
    for (int i = 0; i < 8; i++) Q[i] = ld128c(rw, pq + i * 1280);
    MEMF(); SB();
    *(LAS f32x4*)(sm + KO_RED + ((wave * 4 + 0) * 64 + lane) * 16) = a00;
    *(LAS f32x4*)(sm + KO_RED + ((wave * 4 + 1) * 64 + lane) * 16) = a0x;
    *(LAS f32x4*)(sm + KO_RED + ((wave * 4 + 2) * 64 + lane) * 16) = ax0;
    *(LAS f32x4*)(sm + KO_RED + ((wave * 4 + 3) * 64 + lane) * 16) = axx;
    if (wave >= 4 && (wave - 4) * 64 + lane < 189) *(LAS u32x2*)(sm + KO_KV + ((wave - 4) * 64 + lane) * 8) = (u32x2){0, 0};
    LGKM0(); BAR();
    if (wave < 4) {
        const int tile = wave;
        f32x4 s = *(LAS f32x4*)(sm + KO_RED + (tile * 64 + lane) * 16);
#pragma unroll
        for (int w2 = 1; w2 < 8; w2++) s += *(LAS f32x4*)(sm + KO_RED + ((w2 * 4 + tile) * 64 + lane) * 16);
        const int e = (tile & 1) ? 12 + fr : fr;
        const bool colok = (tile & 1) ? (fr >= 4 && fr <= 8) : true;
        const bool rowok = (tile & 2) ? (g == 0) : true;
        const int d0 = (tile & 2) ? 16 : 4 * g;
        if (colok && rowok) *(LAS u32x2*)(sm + KO_KV + e * 72 + (d0 + 4) * 2) = (u32x2){pk2(s[0], s[1]), pk2(s[2], s[3])};
    }
    LGKM0(); BAR();
    u32x4 A0, A1[4], A2;
    {
        const u32 dofs = KO_KV + (8 * g - 4 * odd + 4) * 2;
        const u32 r0 = (4 * odd + fr) * 72, r1 = ((odd ? 0 : 16) + (fr & 3)) * 72;
        const u32x2 p0 = lds64(sm, dofs + r0), p1 = lds64(sm, dofs + r0 + 8);
        const u32x2 q0 = lds64(sm, dofs + r1), q1 = lds64(sm, dofs + r1 + 8);
        const u32x2 z0 = lds64(sm, dofs + 20 * 72), z1 = lds64(sm, dofs + 20 * 72 + 8);
        A0 = (u32x4){p0.x, p0.y, p1.x, p1.y}; A2 = (u32x4){z0.x, z0.y, z1.x, z1.y};
#pragma unroll
        for (int z = 0; z < 4; z++) { const bool m = (fr >> 2) == z; A1[z] = (u32x4){m ? q0.x : 0u, m ? q0.y : 0u, m ? q1.x : 0u, m ? q1.y : 0u}; }
    }
    const u32 offA = WS_O + ((pr * 4 + 2 * odd + (g >> 1)) * 128 + fr * 8) * 2 + (g & 1) * 20480;
    const u32 offX = WS_O + (((16 + (pr >> 2)) * 4 + (pr & 3)) * 128 + odd * 64 + fr * 4) * 2;
    const f32x4 zero = {0, 0, 0, 0};
#pragma unroll
    for (int i4 = 0; i4 < 2; i4++) {
        f32x4 n1 = zero; float rden[4]; u32x2 ov[4];
#pragma unroll
        for (int z = 0; z < 4; z++) {
            const int i = 4 * i4 + z;
            const f32x4 n0 = mfma16(A0, Q[i], zero);
            const f32x4 dn = mfma16(A2, Q[i], zero);
            n1 = mfma16(A1[z], Q[i], n1);
            const float rd = __builtin_amdgcn_rcpf(dn[0] + 1e-6f);
            rden[z] = rd;
            ov[z] = (u32x2){pk2(n0[0] * rd, n0[1] * rd), pk2(n0[2] * rd, n0[3] * rd)};
        }
#pragma unroll
        for (int z = 0; z < 4; z += 2) {
            auto rx = __builtin_amdgcn_permlane16_swap(ov[z].x, ov[z + 1].x, false, false);
            auto ry = __builtin_amdgcn_permlane16_swap(ov[z].y, ov[z + 1].y, false, false);
            st128c(rw, offA + (sb0 + 4 * i4 + z) * 20480, (u32x4){rx[0], ry[0], rx[1], ry[1]});
        }
        const float rd = g == 0 ? rden[0] : g == 1 ? rden[1] : g == 2 ? rden[2] : rden[3];
        st64c(rw, offX + (sb0 + 4 * i4 + g) * 20480, (u32x2){pk2(n1[0] * rd, n1[1] * rd), pk2(n1[2] * rd, n1[3] * rd)});
    }
}

DI void gate_block(int wave, int lane, int m, const u16* x, const u16* gw, rsrc_t rw) {
    const int sub = lane & 15, r4 = lane >> 4;
    u32x4 gq[5], xr[4][5];
#pragma unroll
    for (int jj = 0; jj < 5; jj++) gq[jj] = *(const GAS u32x4*)((const GAS char*)gw + (sub + 16 * jj) * 16);
#pragma unroll
    for (int it = 0; it < 4; it++)
#pragma unroll
        for (int jj = 0; jj < 5; jj++) xr[it][jj] = *(const GAS u32x4*)((const GAS char*)x + (size_t)(128 * m + 16 * wave + 4 * it + r4) * 1280 + (sub + 16 * jj) * 16);
    MEMF(); SB();
#pragma unroll
    for (int it = 0; it < 4; it++) {
        float a = 0.f;
#pragma unroll
        for (int jj = 0; jj < 5; jj++)
#pragma unroll
            for (int e = 0; e < 4; e++) { a = fmaf(blo(xr[it][jj][e]), blo(gq[jj][e]), a); a = fmaf(bhi(xr[it][jj][e]), bhi(gq[jj][e]), a); }
        a += __shfl_xor(a, 1); a += __shfl_xor(a, 2); a += __shfl_xor(a, 4); a += __shfl_xor(a, 8);
        a = rbf(a);
        const float g = rbf(__builtin_amdgcn_rcpf(1.f + __builtin_amdgcn_exp2f(-1.4426950408889634f * a)));
        if (sub == 0) st32c(rw, WS_G + (128 * m + 16 * wave + 4 * it + r4) * 4, __float_as_uint(g));
    }
}

DI void final_block(LAS char* sm, int wave, int lane, int tid, int st, int ct, const u16* x, u16* out, rsrc_t rw, u32 tag) {
    const int fr = lane & 15, g = lane >> 4;
    u32x4 xv[2];
#pragma unroll
    for (int r = 0; r < 2; r++) {
        int it = tid + 512 * r; it = it < 640 ? it : 639;
        const int s = it / 10, u = it - 10 * s;
        xv[r] = *(const GAS u32x4*)((const GAS char*)x + (size_t)(st * 64 + s) * 1280 + ct * 160 + u * 16);
    }
    VMCNT(0); BAR();
    if (wave < 4) {
        const int kh = wave & 1, sh = wave >> 1;
        wait_flags(rw, lane < 16 ? (int)F_O + (st >> 4) * 32 + 16 * kh + lane : -1, -1, tag);
        const u32 ob = WS_O + ((st * 4 + 2 * sh) * 20 + 8 * kh) * 1024 + lane * 16;
        const u32 os = WS_O + ((st * 4 + 2 * sh) * 20 + 16 + 2 * kh) * 1024 + g * 256 + fr * 8;
        u32x4 B[2][10];
#pragma unroll
        for (int kl = 0; kl < 8; kl++) { B[0][kl] = ld128c(rw, ob + kl * 1024); B[1][kl] = ld128c(rw, ob + 20480 + kl * 1024); }
#pragma unroll
        for (int kl = 0; kl < 2; kl++)
#pragma unroll
            for (int z = 0; z < 2; z++) {
                const u32x2 h0 = ld64c(rw, os + z * 20480 + kl * 1024), h1 = ld64c(rw, os + z * 20480 + kl * 1024 + 128);
                B[z][8 + kl] = (u32x4){h0.x, h0.y, h1.x, h1.y};
            }
        MEMF(); SB();
        f32x4 acc[5][2];
#pragma unroll
        for (int cb = 0; cb < 5; cb++) { acc[cb][0] = (f32x4){0, 0, 0, 0}; acc[cb][1] = (f32x4){0, 0, 0, 0}; }
        const u32 aoff = (16 * (g >> 1) + 4 * (g & 1) + (fr >> 2)) * 160 + (lane & 3) * 8;
        u32x2 al[2][5], ah[2][5];
#define F_KAP(kl) ((kl) < 8 ? (kl) : 8 + (kl))
#define F_READ(buf, kl) do { const u32 b0_ = kh ? wo_off(F_KAP(kl) + ((kl) < 8 ? 8 : 2)) : wo_off(F_KAP(kl)); \
            _Pragma("unroll") for (int cb = 0; cb < 5; cb++) { al[buf][cb] = ldstr(sm, b0_ + aoff + cb * 32); ah[buf][cb] = ldstr(sm, b0_ + aoff + cb * 32 + 1280); } } while (0)
        F_READ(0, 0);
#pragma unroll
        for (int kl = 0; kl < 10; kl++) {
            if (kl < 9) F_READ((kl + 1) & 1, kl + 1);
            SB();
#pragma unroll
            for (int cb = 0; cb < 5; cb++) {
                const u32x4 a = {al[kl & 1][cb].x, al[kl & 1][cb].y, ah[kl & 1][cb].x, ah[kl & 1][cb].y};
                acc[cb][0] = mfma16(a, B[0][kl], acc[cb][0]); acc[cb][1] = mfma16(a, B[1][kl], acc[cb][1]);
            }
            SB();
        }
        lds_arrive(sm, C_FR, lane); lds_wait(sm, C_FR, 4);
#pragma unroll
        for (int cb = 0; cb < 5; cb++)
#pragma unroll
            for (int z = 0; z < 2; z++)
                *(LAS f32x4*)(sm + kh * (64 * RED_PITCH) + (16 * (2 * sh + z) + fr) * RED_PITCH + (16 * cb + 4 * g) * 4) = acc[cb][z];
    } else {
        if (wave == 4) { wait_flags(rw, lane < 20 ? (int)F_Y + ct * 32 + lane : lane == 32 ? (int)F_GATE + (st >> 1) : -1, -1, tag); lds_arrive(sm, C_FY, lane); }
        else lds_wait(sm, C_FY, 1);
        const int t = tid - 256;
        u32x4 yv[3];
#pragma unroll
        for (int r = 0; r < 3; r++) { int id = t + 256 * r; id = id < 640 ? id : 639; yv[r] = ld128c(rw, WS_YT + (80 * ct + (id >> 3)) * 4096 + st * 128 + (id & 7) * 16); }
#pragma unroll
        for (int r = 0; r < 3; r++) { const int id = t + 256 * r; if (id < 640) *(LAS u32x4*)(sm + F_YY + id * 16) = yv[r]; }
        if (wave == 4) *(LAS u32*)(sm + F_GG + lane * 4) = ld32c(rw, WS_G + (st * 64 + lane) * 4);
        lds_arrive(sm, C_FY2, lane); lds_wait(sm, C_FY2, 4);
        const int sb = wave - 4, q = fr >> 2, p = lane & 3;
        u32x2 tt[5];
#pragma unroll
        for (int cb = 0; cb < 5; cb++) tt[cb] = ldstr(sm, F_YY + (16 * cb + 4 * g + q) * 128 + (16 * sb + 4 * p) * 2);
#pragma unroll
        for (int cb = 0; cb < 5; cb++) *(LAS u32x2*)(sm + F_Y2 + (16 * sb + fr) * 176 + (16 * cb + 4 * g) * 2) = tt[cb];
    }
    LGKM0(); BAR();
#pragma unroll
    for (int r = 0; r < 2; r++) {
        const int it = tid + 512 * r;
        if (it < 640) {
            const int s = it / 10, u = it - 10 * s;
            const u32 ra = s * RED_PITCH + u * 32;
            const f32x4 p0 = *(LAS f32x4*)(sm + ra), p1 = *(LAS f32x4*)(sm + ra + 16), q0 = *(LAS f32x4*)(sm + 64 * RED_PITCH + ra), q1 = *(LAS f32x4*)(sm + 64 * RED_PITCH + ra + 16);
            const u32x4 y8 = lds128(sm, F_Y2 + s * 176 + u * 16);
            const float gg = *(LAS float*)(sm + F_GG + s * 4), omg = rbf(1.f - gg);
            float o[8];
#pragma unroll
            for (int e = 0; e < 4; e++) {
                const float a0 = (e < 2 ? p0[2 * e] + q0[2 * e] : p1[2 * e - 4] + q1[2 * e - 4]), a1 = (e < 2 ? p0[2 * e + 1] + q0[2 * e + 1] : p1[2 * e - 3] + q1[2 * e - 3]);
                const u32 bb = pk2(blo(xv[r][e]) * blo(y8[e]), bhi(xv[r][e]) * bhi(y8[e]));
                o[2 * e] = fmaf(omg, blo(bb), gg * a0); o[2 * e + 1] = fmaf(omg, bhi(bb), gg * a1);
            }
            { const u32x4 ov_ = {pk2(o[0], o[1]), pk2(o[2], o[3]), pk2(o[4], o[5]), pk2(o[6], o[7])};
              asm volatile("global_store_dwordx4 %0, %1, %2 nt\n\ts_nop 1" :: "v"((u32)((st * 64 + s) * 1280 + ct * 160 + u * 16)), "v"(ov_), "s"(out) : "memory"); }
        }
    }
}

__global__ __launch_bounds__(512, 2) void fused_kernel(const u16* __restrict__ x, const u16* __restrict__ w, const u16* __restrict__ wo, const u16* __restrict__ kin,
                                                        const u16* __restrict__ vin, const u16* __restrict__ shk, const u16* __restrict__ hk, const u16* __restrict__ sD,
                                                        const u16* __restrict__ gw, u16* __restrict__ out, char* ws) {
    extern __shared__ __attribute__((aligned(16))) char smem[];
    LAS char* sm = (LAS char*)smem;
    const int tid = threadIdx.x, lane = tid & 63, wave = __builtin_amdgcn_readfirstlane(tid >> 6);
    const int b = blockIdx.x, xcd = b & 7, j = b >> 3;
    const rsrc_t rw = __builtin_amdgcn_make_buffer_rsrc(ws, 0, WS_BYTES, 0x00020000);
    asm volatile("" :: "s"(x), "s"(w), "s"(wo), "s"(kin), "s"(vin), "s"(shk), "s"(hk), "s"(sD), "s"(gw), "s"(out));
    if (j < 30) {
        const int n240 = xcd * 30 + j;
        if (wave == 0) touch(x, (u32)((xcd * 2560 + j * 80 + lane) * 128));
        else if (wave == 1) { if (lane < 16) touch(x, (u32)((xcd * 2560 + j * 80 + 64 + lane) * 128)); else touch(w, (u32)((n240 * 80 + lane - 16) * 128)); }
        else if (wave == 2) {
            if (lane < 32) touch(w, (u32)((n240 * 80 + 48 + lane) * 128));
            else if (lane < 42) touch(kin, (u32)((lane - 32) * 262144 + (j & 7) * 128));
            else if (lane < 52) touch(vin, (u32)((lane - 42) * 262144 + (j & 7) * 128));
            else if (lane == 53) touch(sD, 0u);
        }
        if (wave == 0) { const u32 l_ = n240 * 86 + lane; touch(hk, (l_ < 20480 ? l_ : 20479) * 128); }
        else if (wave == 1) {
            const u32 l_ = n240 * 86 + 64 + lane, m_ = n240 * 27 + lane - 22;
            if (lane < 22) touch(hk, (l_ < 20480 ? l_ : 20479) * 128); else if (lane < 49) touch(wo, (m_ < 6400 ? m_ : 6399) * 128);
        }
    }
    if (wave == 3) {
        unsigned long long pc;
        asm volatile("s_getpc_b64 %0" : "=s"(pc));
        pc &= ~127ull;
        const int ln = j * 11 + lane;
        if (lane < 11 && ln < CODE_LINES) touch((const void*)pc, (u32)(ln * 128));
    }
    if (wave == 2) {
        if (lane < 8) touch_tlb(ws, (u32)((j * 8 + lane) * 65536) % (WS_BYTES - 1024));
        if (lane < 40) touch_tlb(out, (u32)(lane * 65536));
    }
    u32 tag;
    asm volatile("s_load_dword %0, %1, 0x0\n\ts_waitcnt lgkmcnt(0)" : "=s"(tag) : "s"(ws + WS_FLAG + F_EPOCH * FS) : "memory");
    tag += 1u;
    if (tid < 16) *(LAS u32*)(sm + L_CNT + tid * 4) = 0u;
    const int st = 4 * xcd + (j >> 3), ct = j & 7;
    int ko = -1;
    bool is_conv = false;

    if (j >= 30) {
        const int m = xcd * 2 + (j - 30);
        ko = -100;
        {
            const int grp = wave >> 2, w4i = wave & 3;
            u32x2 w4[3];
#pragma unroll
            for (int t = 0; t < 3; t++) if (2 * t + grp < 5) p0_issue(kin, vin, 240 + 5 * m + 2 * t + grp, w4i, lane, (2 * t + grp) * 17408);
#pragma unroll
            for (int t = 0; t < 3; t++) { const int tt = 2 * t + grp < 5 ? 2 * t + grp : 4; p0_weights(w4[t], shk, 240 + 5 * m + tt, w4i, lane); }
            VMCNT(0);
#pragma unroll
            for (int t = 0; t < 3; t++) PIN(w4[t]);
            BAR();
#pragma unroll
            for (int t = 0; t < 3; t++)
                if (2 * t + grp < 5) p0_compute(sm, w4[t], 240 + 5 * m + 2 * t + grp, w4i, lane, (2 * t + grp) * 17408, 90112 + (2 * t + grp) * 8192);
            LGKM0(); BAR();
            {
                u32x4 t4[3][2];
#pragma unroll
                for (int t = 0; t < 3; t++)
#pragma unroll
                    for (int z = 0; z < 2; z++) { const int ch2 = ((tid & 255) >> 3) + 32 * z, o2 = tid & 7, tt = 2 * t + grp < 5 ? 2 * t + grp : 4; t4[t][z] = lds128(sm, 90112 + tt * 8192 + ch2 * 128 + ((o2 ^ (ch2 & 7)) * 16)); }
#pragma unroll
                for (int t = 0; t < 3; t++)
                    if (2 * t + grp < 5) {
                        const int tile = 240 + 5 * m + 2 * t + grp, cg = tile % 10, s0 = (tile / 10) * 64;
#pragma unroll
                        for (int z = 0; z < 2; z++) { const int ch2 = ((tid & 255) >> 3) + 32 * z, o2 = tid & 7; st128c(rw, WS_UT + (cg * 64 + ch2) * 4096 + (s0 + 8 * o2) * 2, t4[t][z]); }
                    }
            }
        }
        VMCNT(0); BAR();
        if (tid < 5) set_flag(rw, p0_flag(240 + 5 * m + tid), tag);
        gate_block(wave, lane, m, x, gw, rw);
        VMCNT(0); LGKM0(); BAR();
        if (tid == 0) set_flag(rw, F_GATE + m, tag);
    } else {
        const int rt = 2 * xcd + j / 15, cbk = j % 15, tile = xcd * 30 + j;
        is_conv = cbk < 5 || cbk >= 10;
        if (!is_conv) { const int ki = rt * 5 + (cbk - 5); ko = ki < 64 ? ki : -2 - (ki - 64); }
        if (wave >= 4) {
            const int lw = wave - 4;
            u32 vx[4], vw[4];
#pragma unroll
            for (int n2 = 0; n2 < 4; n2++) {
                const int n = 4 * lw + n2;
                const int row = 8 * n + (lane >> 3), ch = (lane & 7) ^ ((row >> 1) & 7);
                vx[n2] = (u32)((rt * 128 + row) * 1280 + ch * 16);
                const int kr = 4 * n + (lane >> 4), f = ((kr & 3) << 2) | ((kr >> 2) & 3), cw = (lane & 15) ^ f;
                vw[n2] = (u32)(kr * 3840 + cbk * 256 + cw * 16);
            }
            const int rot = ((rt >> 1) + cbk) % 10;
#define K1_ISSUE(c) do { const u32 st_ = ((c) & 3) * K1_STAGE + 4 * lw * 1024; int kc_ = (c) + rot; kc_ = kc_ >= 10 ? kc_ - 10 : kc_; \
            const char* xb_ = (const char*)x + kc_ * 128; const char* wp_ = (const char*)w + (size_t)kc_ * 64 * 3840; \
            _Pragma("unroll") for (int n2 = 0; n2 < 4; n2++) { glds16(xb_, vx[n2], st_ + 16384 + n2 * 1024); glds16(wp_, vw[n2], st_ + n2 * 1024); } } while (0)
            K1_ISSUE(0); K1_ISSUE(1);
            VMCNT(8); BAR();
            K1_ISSUE(2); K1_ISSUE(3);
            p0_issue(kin, vin, tile, lw, lane, P0_K);
#pragma unroll 1
            for (int c = 0; c < 6; c++) {
                if (c < 3) VMCNT(21); else VMCNT(16);
                BAR();
                K1_ISSUE(c + 4);
            }
            const int cv = rt * 10 + (cbk < 5 ? cbk : cbk - 5), c = 4 * cv + lw;
            if (is_conv) {
                CvIn in;
                const int fidx = (int)F_P0 + (cv >> 4) * 32 + (lane & 31);
                VMCNT(16); BAR();
                VMCNT(8); BAR();
                conv_fetch_h(in, hk, c, lane);
                const u32 fl = ld32c(rw, WS_FLAG + fidx * FS);
                MEMF(); SB();
                VMCNT(9); BAR();
                VMCNT(0);
#pragma unroll
                for (int z = 0; z < 4; z++) { PIN(in.hd[z]); PIN(in.hm[z]); }
                const u32 uo = WS_UT + c * 4096 + lane * 16, ul = CV_BASE + lw * CV_WAVE + CV_U + 512 + lane * 16;
                if (__all(fl == tag)) {
                    u32x4 u[4];
#pragma unroll
                    for (int z = 0; z < 4; z++) u[z] = ld128c(rw, uo + z * 1024);
                    MEMF(); SB();
                    conv_build_h(sm, in, lw, lane);
#pragma unroll
                    for (int z = 0; z < 4; z++) *(LAS u32x4*)(sm + ul + z * 1024) = u[z];
                } else {
                    conv_build_h(sm, in, lw, lane);
                    wait_flags(rw, fidx, -1, tag);
                    u32x4 u[4];
#pragma unroll
                    for (int z = 0; z < 4; z++) u[z] = ld128c(rw, uo + z * 1024);
#pragma unroll
                    for (int z = 0; z < 4; z++) *(LAS u32x4*)(sm + ul + z * 1024) = u[z];
                }
                const float Dc = blo((u32)sD[c]);
                conv_run(sm, Dc, lw, lane, c, rw);
                publish_last(rw, sm, C_Y, 4, F_Y + (cv / 20) * 32 + cv % 20, tag, lane);
            } else {
                VMCNT(16); BAR();
                VMCNT(8); BAR();
                VMCNT(0); BAR();
                const int r32 = lane & 31, hh = lane >> 5;
                u32 dst, istep, zstep; kv_addr(true, cbk - 5, rt, lw >> 1, lw & 1, 1, r32, hh, dst, istep, zstep);
                lds_wait(sm, C_XF + lw * 4, 1);
                f32x4 a[2][4];
#pragma unroll
                for (int i = 0; i < 2; i++)
#pragma unroll
                    for (int q4 = 0; q4 < 4; q4++) a[i][q4] = *(LAS f32x4*)(sm + K_XFER + lw * 8192 + ((i * 4 + q4) * 64 + lane) * 16);
#pragma unroll
                for (int i = 0; i < 2; i++)
#pragma unroll
                    for (int z = 0; z < 2; z++) {
                        const f32x4 a0 = a[i][2 * z], a1 = a[i][2 * z + 1];
                        st128c(rw, dst + i * istep + z * zstep, (u32x4){phi2(a0[0], a0[1]), phi2(a0[2], a0[3]), phi2(a1[0], a1[1]), phi2(a1[2], a1[3])});
                    }
                publish_last(rw, sm, C_G1, 8, F_G1 + cbk * 16 + rt, tag, lane);
            }
        } else {
            u32x2 w4; p0_weights(w4, shk, tile, wave, lane);
            const int wm = wave >> 1, wn = wave & 1, r32 = lane & 31, hh = lane >> 5;
            K1Addr a0;
#pragma unroll
            for (int ks = 0; ks < 2; ks++) {
                a0.x[0][ks] = (64 * wm + r32) * 128 + (((2 * ks + hh) ^ ((r32 >> 1) & 7)) * 16);
                a0.x[1][ks] = a0.x[0][ks] ^ 64;
            }
#pragma unroll
            for (int jj = 0; jj < 2; jj++)
#pragma unroll
                for (int t = 0; t < 2; t++) {
                    const int blk = (lane >> 4) & 1, q = (lane & 15) >> 2, p = lane & 3, row = 8 * hh + 4 * t + q, ch = 8 * wn + 4 * jj + 2 * blk + (p >> 1);
                    const int f = ((row & 3) << 2) | ((row >> 2) & 3);
                    a0.w[jj][t] = row * 256 + ((ch ^ f) * 16) + 8 * (p & 1);
                }
            f32x16 acc[2][2];
#pragma unroll
            for (int i = 0; i < 2; i++)
#pragma unroll
                for (int jj = 0; jj < 2; jj++)
#pragma unroll
                    for (int e = 0; e < 16; e++) acc[i][jj][e] = 0.f;
            VMCNT(0); PIN(w4);
            BAR();
            if (cbk < 5) {
                k1_loop<true>(sm, a0, acc, rw, p0_flag(tile), tag, wave, lane, tile, w4);
                __builtin_amdgcn_s_setprio(2);
                u32x4 qv[2][2][2];
#pragma unroll
                for (int jj = 0; jj < 2; jj++)
#pragma unroll
                    for (int ap = 0; ap < 2; ap++)
#pragma unroll
                        for (int i = 0; i < 2; i++) {
                            const f32x16& a = acc[i][jj];
                            const u32 x0 = phi2(a[8 * ap], a[8 * ap + 1]), y0 = phi2(a[8 * ap + 2], a[8 * ap + 3]);
                            const u32 x1 = phi2(a[8 * ap + 4], a[8 * ap + 5]), y1 = phi2(a[8 * ap + 6], a[8 * ap + 7]);
                            auto rx = __builtin_amdgcn_permlane32_swap(x0, x1, false, false);
                            auto ry = __builtin_amdgcn_permlane32_swap(y0, y1, false, false);
                            qv[jj][ap][i] = (u32x4){rx[0], ry[0], rx[1], ry[1]};
                        }
#pragma unroll
                for (int jj = 0; jj < 2; jj++)
#pragma unroll
                    for (int ap = 0; ap < 2; ap++) {
                        const int U = 16 * cbk + 8 * wn + 4 * jj + 2 * ap + hh, pr = U / 5, u5 = U - 5 * pr;
                        const u32 dst = WS_P2 + (((pr * 128 + rt * 8 + 4 * wm + (r32 >> 4)) * 5 + u5) * 128 + (r32 & 15) * 8) * 2;
#pragma unroll
                        for (int i = 0; i < 2; i++) st128c(rw, dst + i * 2560, qv[jj][ap][i]);
                    }
            } else {
                k1_loop<false>(sm, a0, acc, rw, p0_flag(tile), tag, wave, lane, tile, w4);
                __builtin_amdgcn_s_setprio(2);
                const bool isk = cbk < 10;
                const int j5 = isk ? cbk - 5 : cbk - 10;
                if (isk) {
#pragma unroll
                    for (int i = 0; i < 2; i++)
#pragma unroll
                        for (int q4 = 0; q4 < 4; q4++)
                            *(LAS f32x4*)(sm + K_XFER + wave * 8192 + ((i * 4 + q4) * 64 + lane) * 16) = (f32x4){acc[i][1][4 * q4], acc[i][1][4 * q4 + 1], acc[i][1][4 * q4 + 2], acc[i][1][4 * q4 + 3]};
                    lds_arrive(sm, C_XF + wave * 4, lane);
                    u32 dst, istep, zstep; kv_addr(true, j5, rt, wm, wn, 0, r32, hh, dst, istep, zstep);
#pragma unroll
                    for (int i = 0; i < 2; i++)
#pragma unroll
                        for (int z = 0; z < 2; z++) {
                            const f32x16& a = acc[i][0];
                            st128c(rw, dst + i * istep + z * zstep,
                                   (u32x4){phi2(a[8 * z], a[8 * z + 1]), phi2(a[8 * z + 2], a[8 * z + 3]), phi2(a[8 * z + 4], a[8 * z + 5]), phi2(a[8 * z + 6], a[8 * z + 7])});
                        }
                } else {
#pragma unroll
                    for (int jj = 0; jj < 2; jj++) {
                        u32 dst, istep, zstep; kv_addr(false, j5, rt, wm, wn, jj, r32, hh, dst, istep, zstep);
#pragma unroll
                        for (int i = 0; i < 2; i++)
#pragma unroll
                            for (int z = 0; z < 2; z++) {
                                const f32x16& a = acc[i][jj];
                                st128c(rw, dst + i * istep + z * zstep,
                                       (u32x4){pk2(a[8 * z], a[8 * z + 1]), pk2(a[8 * z + 2], a[8 * z + 3]), pk2(a[8 * z + 4], a[8 * z + 5]), pk2(a[8 * z + 6], a[8 * z + 7])});
                            }
                    }
                }
            }
            LGKM0();
            publish_last(rw, sm, C_G1, is_conv ? 4 : 8, F_G1 + cbk * 16 + rt, tag, lane);
            __builtin_amdgcn_s_setprio(0);
            if (is_conv) {
                lds_wait(sm, C_G1, 4);
                wo_issue(wo, ct, 0, 12, wave, 4, lane);
            }
        }
    }
    if (!is_conv) {
        LGKM0(); BAR();
        if (ko >= 0) {
            const int h = ko >> 1, half = ko & 1;
            wo_issue(wo, ct, 0, 12, wave, 8, lane);
            kvo_block(sm, wave, lane, h, half, rw, tag);
            VMCNT(0); BAR();
            if (tid == 0) set_flag(rw, F_O + half * 32 + h, tag);
            wo_issue(wo, ct, 12, 20, wave, 8, lane);
        } else {
            wo_issue(wo, ct, 0, 20, wave, 8, lane);
            if (ko == -2 && wave == 0) {
                wait_flags(rw, (int)F_G1 + lane, (int)F_G1 + 64 + lane, tag);
                wait_flags(rw, (int)F_G1 + 128 + lane, (int)F_G1 + 192 + lane, tag);
                if (lane == 0) set_flag(rw, F_EPOCH, tag);
            }
        }
    } else {
        VMCNT(0); BAR();
        if (wave >= 4) wo_issue(wo, ct, 12, 20, wave - 4, 4, lane);
    }
    final_block(sm, wave, lane, tid, st, ct, x, out, rw, tag);
}

extern "C" void kernel_launch(void* const* d_in, const int* in_sizes, int n_in, void* d_out, int out_size, void* d_ws, size_t ws_size, hipStream_t stream) {
    const u16* x = (const u16*)d_in[0]; const u16* wqkv = (const u16*)d_in[1]; const u16* wo = (const u16*)d_in[2];
    const u16* kin = (const u16*)d_in[4]; const u16* vin = (const u16*)d_in[5]; const u16* shk = (const u16*)d_in[6];
    const u16* hk = (const u16*)d_in[7]; const u16* sD = (const u16*)d_in[8]; const u16* gw = (const u16*)d_in[9];
    (void)hipFuncSetAttribute((const void*)fused_kernel, hipFuncAttributeMaxDynamicSharedMemorySize, LDS_BYTES);
    fused_kernel<<<256, 512, LDS_BYTES, stream>>>(x, wqkv, wo, kin, vin, shk, hk, sD, gw, (u16*)d_out, (char*)d_ws);
}
```
